```python
import torch

def _softmax(x, axis=-1):
    return torch.softmax(x, dim=axis)
def _gelu(x):
    return torch.nn.functional.gelu(x, approximate="tanh")
def _silu(x):
    return torch.nn.functional.silu(x)
def _sigmoid(x):
    return torch.sigmoid(x)
def _layernorm(x, axis=-1, eps=1e-5):
    m = x.mean(dim=axis, keepdim=True)
    v = x.var(dim=axis, keepdim=True, unbiased=False)
    return (x - m) / torch.sqrt(v + eps)
def _scatter_add(tgt, idx, src, axis=0):
    return tgt.clone().index_add_(axis, idx, src)
def _segment_sum(src, seg, n):
    out = torch.zeros((n,) + src.shape[1:], dtype=src.dtype)
    return out.index_add_(0, seg, src)
def _pad(x, pw):
    flat = tuple(v for pair in reversed(pw) for v in pair)
    return torch.nn.functional.pad(x, flat)

kernel_name = 'sequential_preLN__ssm_conv_s4d__moe_arch_mod__prompt'

def setup_inputs():

    _w = (lambda a: (lambda t: t.to(torch.bfloat16) if t.is_floating_point() else t)(torch.as_tensor(a)))
    def _A_setup():
        Bsz, L, D, N = 1, 2048, 576, 128
        return {
            "u": _w((lambda *s: torch.randn(*s, dtype=torch.bfloat16) * 0.2)(Bsz, L, D)),
            "log_A_real": _w((lambda *s: torch.randn(*s, dtype=torch.bfloat16) * 6.4)(D, N) * 0.5),
            "A_imag": _w((lambda *s: torch.randn(*s, dtype=torch.bfloat16) * 0.2)(D, N) * 3.14),
            "C_real": _w((lambda *s: torch.randn(*s, dtype=torch.bfloat16) * 0.2)(D, N) / N ** 0.5),
            "C_imag": _w((lambda *s: torch.randn(*s, dtype=torch.bfloat16) * 0.2)(D, N) / N ** 0.5),
            "log_dt": _w((lambda *s: torch.randn(*s, dtype=torch.bfloat16) * 0.2)(D) * 0.5 - 4.0),
            "D_skip": _w((lambda *s: torch.randn(*s, dtype=torch.bfloat16) * 0.2)(D)),
        }
    _A_in = _A_setup()

    def _B_setup():
        B, S, D, F, C = 1, 2048, 576, 4608, 8
        return {
            "x": _w((lambda *s: torch.randn(*s, dtype=torch.bfloat16) * 0.2)(B, S, D)),
            "w_r": _w((lambda *s: torch.randn(*s, dtype=torch.bfloat16) * 0.2)(D)),
            "w_gate": _w((lambda *s: torch.randn(*s, dtype=torch.bfloat16) * 0.2)(D, F)),
            "w_up": _w((lambda *s: torch.randn(*s, dtype=torch.bfloat16) * 0.2)(D, F)),
            "w_down": _w((lambda *s: torch.randn(*s, dtype=torch.bfloat16) * 0.2)(F, D)),
        }
    _B_in = _B_setup()

    return {
        "A_u": _A_in["u"],
        "A_log_A_real": _A_in["log_A_real"],
        "A_A_imag": _A_in["A_imag"],
        "A_C_real": _A_in["C_real"],
        "A_C_imag": _A_in["C_imag"],
        "A_log_dt": _A_in["log_dt"],
        "A_D_skip": _A_in["D_skip"],
        "B_x": _B_in["x"],
        "B_w_r": _B_in["w_r"],
        "B_w_gate": _B_in["w_gate"],
        "B_w_up": _B_in["w_up"],
        "B_w_down": _B_in["w_down"],
    }

def reference(A_u, A_log_A_real, A_A_imag, A_C_real, A_C_imag, A_log_dt, A_D_skip, B_x, B_w_r, B_w_gate, B_w_up, B_w_down):
    def _A_ref(u, log_A_real, A_imag, C_real, C_imag, log_dt, D_skip):
        L = u.shape[1]
        dt = torch.exp(log_dt)
        A = -torch.exp(log_A_real) + 1j * A_imag
        C = (C_real + 1j * C_imag) * dt[:, None]
        pos = (lambda x, dt: x.to(dt))(torch.arange(L), log_dt.dtype)
        P = torch.exp((A * dt[:, None])[:, :, None] * pos[None, None, :])
        K = 2.0 * torch.einsum("dn,dnl->dl", C, P).real
        L2 = 2 * L
        u_t = (lambda x, *d: x.permute(*d))(u, 0, 2, 1)
        Kf = (lambda x, *a, **k: torch.fft.rfft(x.float(), *a, **k))(K, L2)
        Uf = (lambda x, *a, **k: torch.fft.rfft(x.float(), *a, **k))(u_t, L2)
        y = (lambda x, *a, **k: torch.fft.irfft(x, *a, **k).to(torch.bfloat16))(Uf * Kf[None, :, :], L2)[:, :, :L]
        y = (lambda x, *d: x.permute(*d))(y, 0, 2, 1)
        return y + u * D_skip[None, None, :]

    def _B_ref(x, w_r, w_gate, w_up, w_down):
        B, S, D = x.shape
        C = 8
        r = x @ w_r
        idx = torch.argsort(-r, axis=-1)[:, :C]
        idx_e = (lambda x, n, axis: torch.repeat_interleave(x, n, dim=axis))(idx.reshape(B, C, 1), D, axis=2)
        x_sel = (lambda x, i, axis: torch.take_along_dim(x, i, dim=axis))(x, idx_e, axis=1)
        g = _silu(x_sel @ w_gate)
        u = x_sel @ w_up
        y = (g * u) @ w_down
        r_sel = _sigmoid((lambda x, i, axis: torch.take_along_dim(x, i, dim=axis))(r, idx, axis=1))
        y = y * r_sel.reshape(B, C, 1)
        b_off = (torch.arange(B) * S).reshape(B, 1)
        flat_idx = (idx + b_off).reshape(B * C)
        zero = (x * 0.0).reshape(B * S, D)
        upd = _scatter_add(zero, flat_idx, y.reshape(B * C, D), axis=0)
        return x + upd.reshape(B, S, D)

    x = A_u
    h = x + _A_ref(_layernorm(x, axis=-1), A_log_A_real, A_A_imag, A_C_real, A_C_imag, A_log_dt, A_D_skip)
    h2 = h + _B_ref(_layernorm(h, axis=-1), B_w_r, B_w_gate, B_w_up, B_w_down)
    return h2
```

```cpp
#include <hip/hip_runtime.h>
#include <stdint.h>

namespace {

constexpr int SEQ = 2048;
constexpr int DM = 576;
constexpr int NM = 128;
constexpr int FF = 4608;
constexpr int CAPK = 8;

typedef unsigned short u16;
#define LAS __attribute__((address_space(3)))
using half8 = __attribute__((ext_vector_type(8))) _Float16;
using half4v = __attribute__((ext_vector_type(4))) _Float16;
using half2v = __attribute__((ext_vector_type(2))) _Float16;
using bf2v = __attribute__((ext_vector_type(2))) __bf16;
using f32x2 = __attribute__((ext_vector_type(2))) float;
using f32x4 = __attribute__((ext_vector_type(4))) float;
using u32x2 = __attribute__((ext_vector_type(2))) unsigned;
using u32x4 = __attribute__((ext_vector_type(4))) unsigned;

constexpr size_t WS_UB = 0;
constexpr size_t WS_ZT = WS_UB + (size_t)DM * SEQ * 2;
constexpr size_t WS_H = WS_ZT + (size_t)DM * SEQ * 2;
constexpr size_t WS_LNH = WS_H + (size_t)DM * SEQ * 2;
constexpr size_t WS_R = WS_LNH + (size_t)DM * SEQ * 2;
constexpr size_t WS_IDX = WS_R + (size_t)SEQ * 4;
constexpr size_t WS_PART = WS_IDX + 256;
constexpr size_t WS_TS = 64u << 20;

#define TSW(k, s)
#define TC(k, s)
#define TS(k, s)

__device__ __forceinline__ float bf2f(unsigned v) { return __uint_as_float(v << 16); }
__device__ __forceinline__ float bflo(unsigned v) { return __uint_as_float(v << 16); }
__device__ __forceinline__ float bfhi(unsigned v) { return __uint_as_float(v & 0xffff0000u); }
__device__ __forceinline__ unsigned pkbf(float a, float b) {
  f32x2 f = {a, b};
  return __builtin_bit_cast(unsigned, __builtin_convertvector(f, bf2v));
}
__device__ __forceinline__ unsigned pkh(float a, float b) {
  f32x2 f = {a, b};
  return __builtin_bit_cast(unsigned, __builtin_convertvector(f, half2v));
}
__device__ __forceinline__ u16 f2bf(float f) { return (u16)(pkbf(f, f) & 0xffffu); }
__device__ __forceinline__ float rbf(float f) { return bflo(pkbf(f, f)); }

template <int CTRL>
__device__ __forceinline__ float dppf(float v) {
  return __int_as_float(__builtin_amdgcn_update_dpp(0, __float_as_int(v), CTRL, 0xf, 0xf, true));
}
__device__ __forceinline__ float red16_sum(float v) {
  v += dppf<0xB1>(v);
  v += dppf<0x4E>(v);
  v += dppf<0x141>(v);
  v += dppf<0x140>(v);
  return v;
}
__device__ __forceinline__ float red32_sum(float v) {
  v = red16_sum(v);
  auto r = __builtin_amdgcn_permlane16_swap(__float_as_uint(v), __float_as_uint(v), false, false);
  return __uint_as_float(r[0]) + __uint_as_float(r[1]);
}
__device__ __forceinline__ float wave_sum(float v) {
  v = red32_sum(v);
  auto r = __builtin_amdgcn_permlane32_swap(__float_as_uint(v), __float_as_uint(v), false, false);
  return __uint_as_float(r[0]) + __uint_as_float(r[1]);
}

__device__ __forceinline__ void ln_stats(const float* v, int lane, float& mbf, float& sbf) {
  float s = 0.f;
#pragma unroll
  for (int k = 0; k < 5; k++) {
    bool ok = (lane + 64 * k) < 288;
    s += ok ? (v[2 * k] + v[2 * k + 1]) : 0.f;
  }
  s = wave_sum(s);
  const float mean = s * (1.0f / DM);
  float q = 0.f;
#pragma unroll
  for (int k = 0; k < 5; k++) {
    bool ok = (lane + 64 * k) < 288;
    float d0 = v[2 * k] - mean, d1 = v[2 * k + 1] - mean;
    q += ok ? (d0 * d0 + d1 * d1) : 0.f;
  }
  q = wave_sum(q);
  mbf = rbf(mean);
  const float vbf = rbf(q * (1.0f / DM));
  sbf = rbf(sqrtf(rbf(vbf + 1e-5f)));
}

__device__ __forceinline__ void ln1_load(const u16* __restrict__ x, int blk, unsigned (&xw)[9]) {
  const int wave = threadIdx.x >> 6, lane = threadIdx.x & 63;
  const unsigned* xr = (const unsigned*)(x + (size_t)(blk * 8 + wave * 2 + (lane >> 5)) * DM) + (lane & 31);
#pragma unroll
  for (int k = 0; k < 9; k++) xw[k] = xr[32 * k];
}
__device__ __forceinline__ void ln1_compute(const unsigned (&xw)[9], unsigned* tl) {
  const int wave = threadIdx.x >> 6, lane = threadIdx.x & 63;
  float s = 0.f;
#pragma unroll
  for (int k = 0; k < 9; k++) s += bflo(xw[k]) + bfhi(xw[k]);
  const float mean = red32_sum(s) * (1.0f / DM);
  float q = 0.f;
#pragma unroll
  for (int k = 0; k < 9; k++) {
    const float d0 = bflo(xw[k]) - mean, d1 = bfhi(xw[k]) - mean;
    q = fmaf(d0, d0, q);
    q = fmaf(d1, d1, q);
  }
  q = red32_sum(q);
  const float mbf = rbf(mean);
  const float sbf = rbf(__builtin_amdgcn_sqrtf(rbf(rbf(q * (1.0f / DM)) + 1e-5f)));
  const float rinv = __builtin_amdgcn_rcpf(sbf);
  const unsigned sel = lane < 32 ? 0x05040100u : 0x07060302u;
#pragma unroll
  for (int k = 0; k < 9; k++) {
    const unsigned d = pkbf(bflo(xw[k]) - mbf, bfhi(xw[k]) - mbf);
    const unsigned n = pkbf(bflo(d) * rinv, bfhi(d) * rinv);
    const unsigned hp = pkh(bflo(n), bfhi(n));
    auto r = __builtin_amdgcn_permlane32_swap(hp, hp, false, false);
    tl[wave * DM + 2 * ((lane & 31) + 32 * k) + (lane >> 5)] = __builtin_amdgcn_perm(r[1], r[0], sel);
  }
}

__global__ __launch_bounds__(256) void k_ln1(const u16* __restrict__ x, char* __restrict__ ws) {
  __shared__ unsigned tl[4 * DM];
  TS(0, 0);
  unsigned xw[9];
  ln1_load(x, blockIdx.x, xw);
  ln1_compute(xw, tl);
  __syncthreads();
  u32x4* dst = (u32x4*)(ws + WS_UB) + (size_t)blockIdx.x * DM;
  for (int ch = threadIdx.x; ch < DM; ch += 256) {
    u32x4 o = {tl[ch], tl[DM + ch], tl[2 * DM + ch], tl[3 * DM + ch]};
    dst[ch] = o;
  }
  TS(0, 1);
}

constexpr int TAB_STRIDE = 544;
constexpr int TAB_ROWS = 57;
constexpr int SM_TAB = 0;
constexpr int SM_US = TAB_ROWS * TAB_STRIDE;
constexpr int UPAD = 256;
constexpr int SM_KC = SM_US + (UPAD + SEQ) * 2;
constexpr int KC_RD0 = SM_KC;
constexpr int KC_RD1 = KC_RD0 + 304 * 4;
constexpr int KC_R5 = KC_RD1 + 304 * 4;
constexpr int KC_R3 = KC_R5 + 64 * 4;
constexpr int KC_R9 = KC_R3 + 64 * 4;
constexpr int KC_R7 = KC_R9 + 96 * 4;
constexpr int KC_F1024 = KC_R7 + 96 * 4;
constexpr int KC_DUMP = KC_F1024 + 4;
constexpr int SM_END = KC_F1024 + 16;
constexpr int KC_CHUNKS = (SM_END - SM_KC) / 16;
constexpr int SM_XCH = 0;

typedef f32x2 cf;
struct cstep { cf s, r; };
__device__ __forceinline__ cstep mkstep(cf a) { return cstep{a, cf{-a[1], a[0]}}; }
__device__ __forceinline__ cf cmul(cf a, const cstep& b) {
  return __builtin_elementwise_fma(cf{a[0], a[0]}, b.s, cf{a[1], a[1]} * b.r);
}
__device__ __forceinline__ unsigned pkc(cf a) { return __builtin_bit_cast(unsigned, __builtin_convertvector(a, half2v)); }
__device__ __forceinline__ cf cexp_p2(float p, float ch, float cl, float wh, float wl) {
  float mag = __builtin_amdgcn_exp2f(p * ch);
  mag = fmaf(mag, (p * 0.693147181f) * cl, mag);
  const float rh = p * wh;
  const float fr = fmaf(p, wl, rh - rintf(rh));
  return cf{mag * __builtin_amdgcn_cosf(fr), mag * __builtin_amdgcn_sinf(fr)};
}
__device__ __forceinline__ float exp_acc(float x) {
  const float th = x * 1.44269502f;
  const float tl = fmaf(x, 1.92596299e-8f, fmaf(x, 1.44269502f, -th));
  const float e = __builtin_amdgcn_exp2f(th);
  return fmaf(e, tl * 0.693147181f, e);
}
__device__ __forceinline__ unsigned wave_max_u32(unsigned v) {
  v = max(v, (unsigned)__builtin_amdgcn_update_dpp(0, (int)v, 0x111, 0xf, 0xf, false));
  v = max(v, (unsigned)__builtin_amdgcn_update_dpp(0, (int)v, 0x112, 0xf, 0xf, false));
  v = max(v, (unsigned)__builtin_amdgcn_update_dpp(0, (int)v, 0x114, 0xf, 0xf, false));
  v = max(v, (unsigned)__builtin_amdgcn_update_dpp(0, (int)v, 0x118, 0xf, 0xf, false));
  v = max(v, (unsigned)__builtin_amdgcn_update_dpp(0, (int)v, 0x142, 0xa, 0xf, false));
  v = max(v, (unsigned)__builtin_amdgcn_update_dpp(0, (int)v, 0x143, 0xc, 0xf, false));
  return (unsigned)__builtin_amdgcn_readlane((int)v, 63);
}

#define MFMA16(a, b, c) __builtin_amdgcn_mfma_f32_16x16x32_f16((a), (b), (c), 0, 0, 0)

__device__ __forceinline__ half8 ld_frag(const char* p) { return *(const half8*)p; }
__device__ __forceinline__ const LAS unsigned* opaque_lds(const char* p) {
  const LAS unsigned* q = (const LAS unsigned*)p;
  asm volatile("" : "+v"(q));
  return q;
}
template <int O2>
__device__ __forceinline__ half8 ld_toep(const LAS unsigned* q) {
  u32x4 v = {q[0], q[1], q[O2], q[O2 + 1]};
  return __builtin_bit_cast(half8, v);
}
__device__ __forceinline__ half8 band_frag(int lane, unsigned pat, unsigned val2) {
  const int bits = (int)((pat << (lane & 15)) >> (8 * (lane >> 4)));
  u32x4 v;
#pragma unroll
  for (int j = 0; j < 4; j++) {
    const unsigned lo = (unsigned)__builtin_amdgcn_sbfe(bits, 2 * j, 1), hi = (unsigned)__builtin_amdgcn_sbfe(bits, 2 * j + 1, 1);
    v[j] = __builtin_amdgcn_perm(hi, lo, 0x05040100u) & val2;
  }
  return __builtin_bit_cast(half8, v);
}
#define SCHED_FENCE() __builtin_amdgcn_sched_barrier(0)
__device__ __forceinline__ half8 pack_win(f32x4 a, f32x4 b) {
  u32x4 v = {pkh(a[0], a[1]), pkh(a[2], a[3]), pkh(b[0], b[1]), pkh(b[2], b[3])};
  return __builtin_bit_cast(half8, v);
}

template <int NT, int S0>
__device__ __forceinline__ void conv_dense(const LAS unsigned* tb, const char* ub, f32x4 (&acc)[3]) {
  constexpr int NS = 17 + 8 * (NT - 1);
  constexpr int PF = 6;
  half8 td[17], u[NS];
  auto ldt = [&](int i) { td[i] = ld_toep<2>(tb + 16 * (16 - i)); };
  auto issue = [&](int s) {
    if (s < NS) {
      if (s <= 16) ldt(16 - s);
      if (S0 == 9 && s <= 16) ldt(24 - s);
      if (S0 == 9 && s == 16) ldt(16);
      u[s] = ld_frag(ub + 32 * (-33 + 2 * s));
    }
  };
#pragma unroll
  for (int s = S0; s < S0 + PF; s++) issue(s);
  SCHED_FENCE();
#pragma unroll
  for (int s = S0; s < NS; s++) {
    issue(s + PF);
#pragma unroll
    for (int t = 0; t < NT; t++) {
      const int i = 16 + 8 * t - s;
      if (i >= 0 && i <= 16) acc[t] = MFMA16(td[i], u[s], acc[t]);
    }
    SCHED_FENCE();
  }
}

__global__ __launch_bounds__(256, 3) void k_conv(const u16* __restrict__ lar, const u16* __restrict__ aim,
                                                 const u16* __restrict__ crl, const u16* __restrict__ cim,
                                                 const u16* __restrict__ ldt, const u16* __restrict__ dskip,
                                                 char* __restrict__ ws) {
  __shared__ __attribute__((aligned(16))) char smem[SM_END];
  TS(1, 0);
  TC(1, 14);
  const int tid = threadIdx.x, lane = tid & 63;
  const int wave = __builtin_amdgcn_readfirstlane(tid >> 6);
  const int d = (blockIdx.x & 7) * 72 + (blockIdx.x >> 3);
  const int n = tid & 127;
  const unsigned v_lar = lar[d * NM + n], v_aim = aim[d * NM + n];
  const unsigned v_cr = crl[d * NM + n], v_ci = cim[d * NM + n];
  const unsigned v_cr2 = crl[d * NM + (n ^ 64)], v_ci2 = cim[d * NM + (n ^ 64)];
  const unsigned v_ldt = ldt[d], v_dsk = dskip[d];
  const u32x4 ureg = *((const u32x4*)(ws + WS_UB) + (size_t)tid * DM + d);
  {
    const u32x4 z = {0u, 0u, 0u, 0u};
    if (tid < KC_CHUNKS) *(u32x4*)(smem + SM_KC + 16 * tid) = z;
    if (tid < UPAD / 8) *(u32x4*)(smem + SM_US + 16 * tid) = z;
  }
  half8 bA = band_frag(lane, wave == 3 ? 0x1fu << 6 : 0x1ffu << 4, 0x3c003c00u);
  half8 bB = band_frag(lane, wave == 3 ? 0x7u << 11 : 0x7fu << 5, 0x3c003c00u);
  half8 bte = band_frag(lane, 0x3u << 12, 0x3c003c00u);
  asm volatile("" : "+v"(bA), "+v"(bB), "+v"(bte));
  SCHED_FENCE();
  const float dt = rbf(exp_acc(bf2f(v_ldt)));
  const float are = -rbf(exp_acc(bf2f(v_lar))) * dt;
  const float aie = bf2f(v_aim) * dt;
  const float ch = are * 1.44269502f;
  const float cl = fmaf(are, 1.92596299e-8f, fmaf(are, 1.44269502f, -ch));
  const float wh = aie * 0.159154936f;
  const float wl = fmaf(aie, 7.13048051e-9f, fmaf(aie, 0.159154936f, -wh));
  const unsigned cmax = wave_max_u32(max(max(v_cr & 0x7fffu, v_ci & 0x7fffu), max(v_cr2 & 0x7fffu, v_ci2 & 0x7fffu)));
  int se = 385 - (int)(cmax >> 7) - (int)(__float_as_uint(dt) >> 23);
  se = min(max(se, 2), 252);
  const float scale = __uint_as_float((unsigned)se << 23), inv_scale = __uint_as_float((unsigned)(254 - se) << 23);
  const cf cc = {bf2f(v_cr) * dt * (2.0f * scale), -bf2f(v_ci) * dt * (2.0f * scale)};
  unsigned* tab = (unsigned*)(smem + SM_TAB) + n;
  constexpr int TS4 = TAB_STRIDE / 4;
  if (wave < 2) {
    const cstep e1 = mkstep(cexp_p2(1.f, ch, cl, -wh, -wl)), e128 = mkstep(cexp_p2(128.f, ch, cl, -wh, -wl));
    const cstep e2 = mkstep(cmul(e1.s, e1)), e4 = mkstep(cmul(e2.s, e2));
    cf b = cc;
#pragma unroll
    for (int a = 0; a < 8; a++) {
      const int cnt = a < 2 ? 8 : (a < 4 ? 4 : 2);
      const int r0 = a < 2 ? 8 * a : (a < 4 ? 16 + 4 * (a - 2) : 24 + 2 * (a - 4));
      const cstep st = a < 2 ? e1 : (a < 4 ? e2 : e4);
      cf v = b;
#pragma unroll
      for (int j = 0; j < cnt; j++) {
        tab[(r0 + j) * TS4] = pkc(v);
        if (j + 1 < cnt) v = cmul(v, st);
      }
      if (a < 7) b = cmul(b, e128);
    }
  } else {
    const cstep e8 = mkstep(cexp_p2(8.f, ch, cl, wh, wl)), e128 = mkstep(cexp_p2(128.f, ch, cl, -wh, -wl));
    cf b = cmul(cc, mkstep(cexp_p2(1024.f, ch, cl, -wh, -wl)));
    cf h = e8.s;
    tab[41 * TS4] = pkh(1.f, 0.f);
#pragma unroll
    for (int i = 0; i < 15; i++) {
      tab[(42 + i) * TS4] = pkc(h);
      if (i < 14) h = cmul(h, e8);
      if (i < 9) {
        tab[(32 + i) * TS4] = pkc(b);
        if (i < 8) b = cmul(b, e128);
      }
    }
  }
  TS(1, 1);
  __syncthreads();
  const int c15 = lane & 15, g = lane >> 4;
  *(u32x4*)(smem + SM_US + 2 * UPAD + 16 * tid) = ureg;
  if (wave < 3) {
    f32x4 f = {0.f, 0.f, 0.f, 0.f};
    const char* pa = smem + SM_TAB + (16 * wave + c15) * TAB_STRIDE + 16 * g;
    const char* pb = smem + SM_TAB + (41 + c15) * TAB_STRIDE + 16 * g;
    half8 fa[8], fb[8];
#pragma unroll
    for (int ks = 0; ks < 8; ks++) { fa[ks] = ld_frag(pa + 64 * ks); fb[ks] = ld_frag(pb + 64 * ks); }
    SCHED_FENCE();
#pragma unroll
    for (int ks = 0; ks < 8; ks++) f = MFMA16(fa[ks], fb[ks], f);
    const unsigned f10 = pkh(f[1], f[0]), f32 = pkh(f[3], f[2]);
    LAS char* const L = (LAS char*)smem;
    auto w16 = [&](int off, unsigned v) { *(volatile LAS u16*)(L + off) = (u16)v; };
    auto w32 = [&](int off, unsigned v) { *(volatile LAS unsigned*)(L + off) = v; };
    auto w64 = [&](int off, unsigned a, unsigned b) { *(volatile LAS u32x2*)(L + off) = u32x2{a, b}; };
    const unsigned f21 = __builtin_amdgcn_alignbit(f10, f32, 16);
    if (wave == 0) {
      const int l0 = 2 * (128 * (g >> 1) + 4 * (g & 1) + 8 * c15);
      w64(KC_RD0 + 2 * 540 - l0, f32, f10);
      w16(KC_RD1 + 2 * 539 - l0, f32);
      w32(KC_RD1 + 2 * 540 - l0, f21);
      w16(KC_RD1 + 2 * 542 - l0, f10 >> 16);
    } else if (wave == 1) {
      const unsigned f00 = __builtin_amdgcn_perm(f10, f10, 0x03020302u);
      if (g < 2) {
        const int p0 = 2 * (256 + 128 * g + 8 * c15);
        const unsigned f22 = __builtin_amdgcn_perm(f32, f32, 0x03020302u);
        w16(KC_RD0 + 2 * 537 - p0, f32);
        w32(KC_RD0 + 2 * 538 - p0, f22);
        w64(KC_RD0 + 2 * 540 - p0, f21, f00);
        w64(KC_RD1 + 2 * 536 - p0, f32, f22);
        w32(KC_RD1 + 2 * 540 - p0, f10);
        w16(KC_RD1 + 2 * 542 - p0, f00);
        if (lane != 0) {
          w16(KC_RD0 + 2 * 544 - p0, f00);
          w16(KC_RD1 + 2 * 543 - p0, f00);
        }
      } else {
        const int m2 = 2 * (32 * g + c15);
        w16(KC_R3 + 2 * 159 - m2, f10);
        w16(KC_R5 + 2 * 160 - 32 - m2, f32 >> 16);
        w16(KC_R3 + 2 * 159 - 32 - m2, f32);
        if (lane != 32) {
          w16(KC_R5 + 2 * 160 - m2, f00);
        } else {
          w16(KC_RD0 + 2 * 29, f00);
          w32(KC_RD0 + 2 * 30, f00);
          w16(KC_RD0 + 2 * 32, f00);
          w64(KC_RD1 + 2 * 28, f00, f00);
        }
      }
    } else if (lane <= 32) {
      const int a0 = ((c15 & 1) ? KC_R7 + 2 * 63 : KC_R9 + 2 * 64) - 64 * g - (c15 & 14);
      w16(lane == 0 ? KC_R5 + 2 * 32 : a0, f10 >> 16);
      w16(lane == 0 ? KC_F1024 : a0 + 190, f10 >> 16);
      if (lane != 32) {
        w16(a0 - 16, f10);
        w16(a0 - 16 + 190, f10);
        w16(a0 - 32, f32 >> 16);
        w16(a0 - 32 + 190, f32 >> 16);
        w16(a0 - 48, f32);
        w16(a0 - 48 + 190, f32);
      }
    }
  }
  TS(1, 2);
  __syncthreads();
  const f32x4 zero4 = {0.f, 0.f, 0.f, 0.f};
  f32x4 acc[3] = {zero4, zero4, zero4};
  f32x4* xch = (f32x4*)(smem + SM_XCH) + lane;
  const char* uf16 = smem + SM_US + 2 * (UPAD + 16 * c15 + 8 * g);
  if (wave < 3) {
    {
      const int c = (c15 + 1) & 1;
      const LAS unsigned* tb = opaque_lds(smem + (c ? KC_RD1 : KC_RD0) + 2 * (543 - 16 - c15 + 8 * g - c) - 64 * 16);
      if (wave == 0) conv_dense<3, 9>(tb, uf16, acc);
      else if (wave == 1) conv_dense<3, 0>(tb, uf16 + 2 * 768, acc);
      else conv_dense<2, 0>(tb, uf16 + 2 * 1536, acc);
    }
    if (wave > 0) {
      const _Float16 f1024 = *(const _Float16*)(smem + KC_F1024);
      const half8 te = bte * f1024;
      if (wave == 1) {
        acc[1] = MFMA16(te, ld_frag(uf16 + 32 * (64 - 65)), acc[1]);
        acc[2] = MFMA16(te, ld_frag(uf16 + 32 * (80 - 65)), acc[2]);
      } else {
        acc[0] = MFMA16(te, ld_frag(uf16 + 32 * (96 - 65)), acc[0]);
        acc[1] = MFMA16(te, ld_frag(uf16 + 32 * (112 - 65)), acc[1]);
      }
    }
    if (wave == 2) {
      const half8 b9 = bA, b7 = bB;
      const int c = (c15 + 1) & 1;
      const LAS unsigned* t9 = opaque_lds(smem + KC_R9 + 192 * c + 2 * (127 - 64 - c15 + 4 * g - c) - 96);
      half8 T9[4], T7[4];
#pragma unroll
      for (int e = 0; e < 4; e++) {
        T9[e] = ld_toep<8>(t9 + 8 * (3 - e));
        T7[e] = ld_toep<8>(t9 + (KC_R7 - KC_R9) / 4 + 8 * (3 - e));
      }
      f32x4 a4 = zero4, a5 = zero4;
#pragma unroll
      for (int j = 0; j < 2; j++) {
        const char* ua = uf16 + 2 * 16 * 32 * j;
        const half8 X9 = pack_win(MFMA16(ld_frag(ua - 48), b9, zero4), MFMA16(ld_frag(ua + 512 - 48), b9, zero4));
        const half8 X7 = pack_win(MFMA16(ld_frag(ua - 32), b7, zero4), MFMA16(ld_frag(ua + 512 - 32), b7, zero4));
        if (j == 0) {
          a4 = MFMA16(X9, T9[0], a4); a4 = MFMA16(X7, T7[0], a4);
          a5 = MFMA16(X9, T9[1], a5); a5 = MFMA16(X7, T7[1], a5);
        }
        acc[0] = MFMA16(X9, T9[2 - 2 * j], acc[0]); acc[0] = MFMA16(X7, T7[2 - 2 * j], acc[0]);
        acc[1] = MFMA16(X9, T9[3 - 2 * j], acc[1]); acc[1] = MFMA16(X7, T7[3 - 2 * j], acc[1]);
      }
      xch[64 * 0] = a4;
      xch[64 * 1] = a5;
    }
  } else {
    const half8 b5 = bA, b3 = bB;
    const LAS unsigned* t5 = opaque_lds(smem + KC_R5 + 2 * (158 - 64 - 2 * c15 + 4 * g) - 128);
    half8 T5[3], T3[3];
#pragma unroll
    for (int e = 0; e < 3; e++) {
      T5[e] = ld_toep<8>(t5 + 16 * (2 - e));
      T3[e] = ld_toep<8>(t5 + (KC_R3 - KC_R5) / 4 + 16 * (2 - e));
    }
    const char* uf8 = smem + SM_US + 2 * (UPAD + 8 * c15 + 8 * g - 24);
    f32x4 a2[6];
#pragma unroll
    for (int i = 0; i < 6; i++) a2[i] = zero4;
    half8 uw[12];
#pragma unroll
    for (int j = 0; j < 12; j++) uw[j] = ld_frag(uf8 + 256 * j);
    SCHED_FENCE();
#pragma unroll
    for (int j = 0; j < 6; j++) {
      const half8 a0 = uw[2 * j], a1 = uw[2 * j + 1];
      const half8 X5 = pack_win(MFMA16(a0, b5, zero4), MFMA16(a1, b5, zero4));
      const half8 X3 = pack_win(MFMA16(a0, b3, zero4), MFMA16(a1, b3, zero4));
#pragma unroll
      for (int e = 0; e < 3; e++) {
        if (j + e < 6) {
          a2[j + e] = MFMA16(X5, T5[e], a2[j + e]);
          a2[j + e] = MFMA16(X3, T3[e], a2[j + e]);
        }
      }
      xch[64 * (2 + j)] = a2[j];
    }
  }
  TSW(1, 3);
  __syncthreads();
  if (wave < 3) {
    const float dsk = bf2f(v_dsk);
    u16* zrow = (u16*)(ws + WS_ZT) + (size_t)d * SEQ + 768 * wave + 16 * c15 + 4 * g;
    const char* ue = smem + SM_US + 2 * (UPAD + 768 * wave + 16 * c15 + 4 * g);
#pragma unroll
    for (int t = 0; t < 3; t++) {
      const int nt = 3 * wave + t;
      if (nt < 8) {
        f32x4 y = acc[t];
        if (nt >= 2) y += xch[64 * nt];
        if (nt == 4 || nt == 5) y += xch[64 * (nt - 4)];
        const half4v uu = *(const half4v*)(ue + 512 * t);
        const unsigned y01 = pkbf(y[0] * inv_scale, y[1] * inv_scale), y23 = pkbf(y[2] * inv_scale, y[3] * inv_scale);
        const unsigned s01 = pkbf((float)uu[0] * dsk, (float)uu[1] * dsk), s23 = pkbf((float)uu[2] * dsk, (float)uu[3] * dsk);
        u32x2 o = {pkbf(bflo(y01) + bflo(s01), bfhi(y01) + bfhi(s01)), pkbf(bflo(y23) + bflo(s23), bfhi(y23) + bfhi(s23))};
        *(u32x2*)(zrow + 256 * t) = o;
      }
    }
  }
  TS(1, 7);
  TC(1, 15);
}

constexpr int LN2_ROWS = 8;
constexpr int LN2_TS = 10;
__global__ __launch_bounds__(512) void k_ln2(const u16* __restrict__ x, const u16* __restrict__ zT,
                                             const u16* __restrict__ wr, u16* __restrict__ hbuf,
                                             u16* __restrict__ lnh, float* __restrict__ rbuf,
                                             u16* __restrict__ out, char* ws) {
  __shared__ __attribute__((aligned(16))) u16 T[DM * LN2_TS];
  TS(2, 0);
  const int rb = (blockIdx.x & 7) * 32 + (blockIdx.x >> 3);
  const int row0 = rb * LN2_ROWS;
  const int tid = threadIdx.x;
  const int wave = tid >> 6, lane = tid & 63;
  const int row = row0 + wave;
  unsigned xw[5], ww[5];
  {
    const unsigned* xr = (const unsigned*)(x + (size_t)row * DM);
#pragma unroll
    for (int k = 0; k < 5; k++) {
      const int idx = lane + 64 * k;
      xw[k] = (idx < 288) ? xr[idx] : 0u;
      ww[k] = (idx < 288) ? ((const unsigned*)wr)[idx] : 0u;
    }
  }
  for (int ch = tid; ch < DM; ch += 512) {
    const u32x4 v = *(const u32x4*)(zT + (size_t)ch * SEQ + row0);
    unsigned* dst = (unsigned*)(T + ch * LN2_TS);
#pragma unroll
    for (int j = 0; j < 4; j++) dst[j] = v[j];
  }
  __syncthreads();
  float v[10];
#pragma unroll
  for (int k = 0; k < 5; k++) {
    const int idx = lane + 64 * k;
    if (idx < 288) {
      v[2 * k] = rbf(bf2f(xw[k] & 0xffffu) + bf2f(T[(2 * idx) * LN2_TS + wave]));
      v[2 * k + 1] = rbf(bf2f(xw[k] >> 16) + bf2f(T[(2 * idx + 1) * LN2_TS + wave]));
    } else {
      v[2 * k] = 0.f;
      v[2 * k + 1] = 0.f;
    }
  }
  float mbf, sbf;
  ln_stats(v, lane, mbf, sbf);
  float dot = 0.f;
#pragma unroll
  for (int k = 0; k < 5; k++) {
    const int idx = lane + 64 * k;
    if (idx < 288) {
      const float n0 = rbf(rbf(v[2 * k] - mbf) / sbf);
      const float n1 = rbf(rbf(v[2 * k + 1] - mbf) / sbf);
      dot = fmaf(n0, bf2f(ww[k] & 0xffffu), dot);
      dot = fmaf(n1, bf2f(ww[k] >> 16), dot);
      ((unsigned*)hbuf)[(size_t)row * 288 + idx] = (unsigned)f2bf(v[2 * k]) | ((unsigned)f2bf(v[2 * k + 1]) << 16);
      ((unsigned*)lnh)[(size_t)row * 288 + idx] = (unsigned)f2bf(n0) | ((unsigned)f2bf(n1) << 16);
      ((unsigned*)out)[(size_t)row * 288 + idx] =
          (unsigned)f2bf(v[2 * k] + n0) | ((unsigned)f2bf(v[2 * k + 1] + n1) << 16);
    }
  }
  dot = wave_sum(dot);
  if (lane == 0) rbuf[row] = rbf(dot);
  TS(2, 1);
}

__device__ __forceinline__ unsigned r_key(float r, int idx) {
  const unsigned b = __float_as_uint(r) >> 16;
  const unsigned k16 = (b & 0x8000u) ? (~b & 0xffffu) : (b | 0x8000u);
  return (k16 << 16) | (unsigned)(SEQ - 1 - idx);
}
__device__ void top8(const float* __restrict__ rbuf, int* sel, unsigned* cand) {
  const int tid = threadIdx.x, lane = tid & 63, wave = tid >> 6;
  unsigned k[8];
#pragma unroll
  for (int j = 0; j < 8; j++) k[j] = r_key(rbuf[tid + 256 * j], tid + 256 * j);
#pragma unroll
  for (int it = 0; it < CAPK; it++) {
    unsigned m = k[0];
#pragma unroll
    for (int j = 1; j < 8; j++) m = max(m, k[j]);
    m = wave_max_u32(m);
    if (lane == 0) cand[wave * 8 + it] = m;
#pragma unroll
    for (int j = 0; j < 8; j++) k[j] = (k[j] == m) ? 0u : k[j];
  }
  __syncthreads();
  unsigned c = (lane < 32) ? cand[lane] : 0u;
#pragma unroll
  for (int it = 0; it < CAPK; it++) {
    const unsigned m = wave_max_u32(c);
    if (tid == 0) sel[it] = SEQ - 1 - (int)(m & 0xffffu);
    c = (c == m) ? 0u : c;
  }
  __syncthreads();
}

constexpr int FFN_W = 32;
constexpr int FFN_BLOCKS = FF / FFN_W;
__global__ __launch_bounds__(256) void k_ffn(const float* __restrict__ rbuf, const u16* __restrict__ lnh,
                                             const u16* __restrict__ wg, const u16* __restrict__ wu,
                                             const u16* __restrict__ wd, float* __restrict__ part,
                                             int* __restrict__ idx_out, char* ws) {
  TS(3, 0);
  __shared__ __attribute__((aligned(16))) float xs[DM * 8];
  __shared__ __attribute__((aligned(16))) float red[3 * 8 * DM];
  __shared__ __attribute__((aligned(16))) float gus[FFN_W * 8];
  __shared__ int sel[8];
  __shared__ unsigned cand[32];
  const int tid = threadIdx.x, lane = tid & 63, wave = tid >> 6;
  const int j = (blockIdx.x & 7) * 18 + (blockIdx.x >> 3);
  const int cl = lane & 15, rg = (lane >> 4) + 4 * wave;
  unsigned gw[DM / 16], uw[DM / 16];
#pragma unroll
  for (int it = 0; it < DM / 16; it++) {
    const int k = rg + 16 * it;
    gw[it] = *(const unsigned*)(wg + (size_t)k * FF + FFN_W * j + 2 * cl);
    uw[it] = *(const unsigned*)(wu + (size_t)k * FF + FFN_W * j + 2 * cl);
  }
  const int cc = tid % 72, gq = tid / 72;
  u32x4 dw[11];
#pragma unroll
  for (int i = 0; i < 11; i++) {
    const int f = gq + 3 * i;
    dw[i] = (tid < 216 && f < FFN_W) ? *(const u32x4*)(wd + (size_t)(FFN_W * j + f) * DM + 8 * cc) : u32x4{0u, 0u, 0u, 0u};
  }
  top8(rbuf, sel, cand);
  if (j == 0 && tid < 8) idx_out[tid] = sel[tid];
  for (int i = tid; i < DM * 4; i += 256) {
    const int tk = i / 288, k2 = i - tk * 288;
    const unsigned w = ((const unsigned*)lnh)[(size_t)sel[tk] * 288 + k2];
    xs[(2 * k2) * 8 + tk] = bf2f(w & 0xffffu);
    xs[(2 * k2 + 1) * 8 + tk] = bf2f(w >> 16);
  }
  __syncthreads();
  {
    float ag[16], au[16];
#pragma unroll
    for (int i = 0; i < 16; i++) { ag[i] = 0.f; au[i] = 0.f; }
#pragma unroll
    for (int it = 0; it < DM / 16; it++) {
      const int k = rg + 16 * it;
      const float g0 = bf2f(gw[it] & 0xffffu), g1 = bf2f(gw[it] >> 16);
      const float u0 = bf2f(uw[it] & 0xffffu), u1 = bf2f(uw[it] >> 16);
      const float4 xa = *(const float4*)(xs + k * 8), xb = *(const float4*)(xs + k * 8 + 4);
      const float xv[8] = {xa.x, xa.y, xa.z, xa.w, xb.x, xb.y, xb.z, xb.w};
#pragma unroll
      for (int tk = 0; tk < 8; tk++) {
        ag[2 * tk] = fmaf(xv[tk], g0, ag[2 * tk]);
        ag[2 * tk + 1] = fmaf(xv[tk], g1, ag[2 * tk + 1]);
        au[2 * tk] = fmaf(xv[tk], u0, au[2 * tk]);
        au[2 * tk + 1] = fmaf(xv[tk], u1, au[2 * tk + 1]);
      }
    }
#pragma unroll
    for (int tk = 0; tk < 8; tk++) {
      *(float2*)(red + ((rg * 2 + 0) * 8 + tk) * 32 + 2 * cl) = make_float2(ag[2 * tk], ag[2 * tk + 1]);
      *(float2*)(red + ((rg * 2 + 1) * 8 + tk) * 32 + 2 * cl) = make_float2(au[2 * tk], au[2 * tk + 1]);
    }
  }
  __syncthreads();
  {
    const int tk = tid >> 5, col = tid & 31;
    float gs = 0.f, us = 0.f;
#pragma unroll
    for (int rg = 0; rg < 16; rg++) {
      gs += red[((rg * 2 + 0) * 8 + tk) * 32 + col];
      us += red[((rg * 2 + 1) * 8 + tk) * 32 + col];
    }
    const float gp = rbf(gs);
    const float gq = rbf(gp / (1.0f + __expf(-gp)));
    gus[col * 8 + tk] = rbf(gq * rbf(us));
  }
  __syncthreads();
  if (tid < 216) {
    float acc[64];
#pragma unroll
    for (int i = 0; i < 64; i++) acc[i] = 0.f;
#pragma unroll
    for (int i = 0; i < 11; i++) {
      const int f = gq + 3 * i;
      if (f < FFN_W) {
        float w[8];
#pragma unroll
        for (int q = 0; q < 4; q++) { w[2 * q] = bf2f(dw[i][q] & 0xffffu); w[2 * q + 1] = bf2f(dw[i][q] >> 16); }
        const float4 ga = *(const float4*)(gus + f * 8), gb = *(const float4*)(gus + f * 8 + 4);
        const float gv[8] = {ga.x, ga.y, ga.z, ga.w, gb.x, gb.y, gb.z, gb.w};
#pragma unroll
        for (int tk = 0; tk < 8; tk++)
#pragma unroll
          for (int q = 0; q < 8; q++) acc[tk * 8 + q] = fmaf(gv[tk], w[q], acc[tk * 8 + q]);
      }
    }
#pragma unroll
    for (int tk = 0; tk < 8; tk++) {
      float* dst = red + (gq * 8 + tk) * DM + 8 * cc;
      *(float4*)dst = make_float4(acc[tk * 8], acc[tk * 8 + 1], acc[tk * 8 + 2], acc[tk * 8 + 3]);
      *(float4*)(dst + 4) = make_float4(acc[tk * 8 + 4], acc[tk * 8 + 5], acc[tk * 8 + 6], acc[tk * 8 + 7]);
    }
  }
  __syncthreads();
  for (int i = tid; i < 8 * DM; i += 256)
    part[(size_t)j * 8 * DM + i] = red[i] + red[8 * DM + i] + red[16 * DM + i];
  TS(3, 1);
}

__global__ __launch_bounds__(256) void k_final(const float* __restrict__ part, const int* __restrict__ idx,
                                               const float* __restrict__ rbuf, const u16* __restrict__ hbuf,
                                               const u16* __restrict__ lnh, u16* __restrict__ out, char* ws) {
  __shared__ float red[8][32];
  TS(4, 0);
  const int tk = blockIdx.x / 18, cb = blockIdx.x % 18;
  const int col = cb * 32 + (threadIdx.x & 31), gq = threadIdx.x >> 5;
  float s = 0.f;
#pragma unroll
  for (int i = 0; i < 18; i++) s += part[((size_t)(gq * 18 + i) * 8 + tk) * DM + col];
  red[gq][threadIdx.x & 31] = s;
  __syncthreads();
  if (threadIdx.x < 32) {
    float y = 0.f;
#pragma unroll
    for (int q = 0; q < 8; q++) y += red[q][threadIdx.x];
    const int row = idx[tk];
    const float rv = rbuf[row];
    const float sg = rbf(1.0f / (1.0f + __expf(-rv)));
    const float y2 = rbf(rbf(y) * sg);
    const float hv = bf2f(hbuf[(size_t)row * DM + col]);
    const float lv = bf2f(lnh[(size_t)row * DM + col]);
    out[(size_t)row * DM + col] = f2bf(hv + rbf(lv + y2));
  }
  TS(4, 1);
}

}

extern "C" void kernel_launch(void* const* d_in, const int* in_sizes, int n_in, void* d_out, int out_size,
                              void* d_ws, size_t ws_size, hipStream_t stream) {
  const u16* x = (const u16*)d_in[0];
  const u16* lar = (const u16*)d_in[1];
  const u16* aim = (const u16*)d_in[2];
  const u16* crl = (const u16*)d_in[3];
  const u16* cim = (const u16*)d_in[4];
  const u16* ldt = (const u16*)d_in[5];
  const u16* dsk = (const u16*)d_in[6];
  const u16* wr = (const u16*)d_in[8];
  const u16* wg = (const u16*)d_in[9];
  const u16* wu = (const u16*)d_in[10];
  const u16* wd = (const u16*)d_in[11];
  char* ws = (char*)d_ws;
  u16* zT = (u16*)(ws + WS_ZT);
  u16* hbuf = (u16*)(ws + WS_H);
  u16* lnh = (u16*)(ws + WS_LNH);
  float* rbuf = (float*)(ws + WS_R);
  int* idx = (int*)(ws + WS_IDX);
  float* part = (float*)(ws + WS_PART);
  u16* out = (u16*)d_out;
  k_ln1<<<256, 256, 0, stream>>>(x, ws);
  k_conv<<<DM, 256, 0, stream>>>(lar, aim, crl, cim, ldt, dsk, ws);
  k_ln2<<<SEQ / LN2_ROWS, 512, 0, stream>>>(x, zT, wr, hbuf, lnh, rbuf, out, ws);
  k_ffn<<<FFN_BLOCKS, 256, 0, stream>>>(rbuf, lnh, wg, wu, wd, part, idx, ws);
  k_final<<<8 * 18, 256, 0, stream>>>(part, idx, rbuf, hbuf, lnh, out, ws);
}
```

```cpp
#include <hip/hip_runtime.h>
#include <stdint.h>

namespace {

constexpr int SEQ = 2048;
constexpr int DM = 576;
constexpr int NM = 128;
constexpr int FF = 4608;

constexpr int NTHR = 768;
constexpr int NCONV = 192;
constexpr int NLN = 128;
constexpr int NFFN = 64;
constexpr int RED0 = 48;

typedef unsigned short u16;
#define LAS __attribute__((address_space(3)))
using half8 = __attribute__((ext_vector_type(8))) _Float16;
using half2v = __attribute__((ext_vector_type(2))) _Float16;
using bf2v = __attribute__((ext_vector_type(2))) __bf16;
using bf8v = __attribute__((ext_vector_type(8))) __bf16;
using s16x4 = __attribute__((ext_vector_type(4))) short;
using f32x2 = __attribute__((ext_vector_type(2))) float;
using f32x4 = __attribute__((ext_vector_type(4))) float;
using u32x2 = __attribute__((ext_vector_type(2))) unsigned;
using u32x4 = __attribute__((ext_vector_type(4))) unsigned;

constexpr int WS_EPOCH = 0;
constexpr int WS_RG = 4096;
constexpr int WS_XS = WS_RG + 8192;
constexpr int WS_HS = WS_XS + 8 * 1536;
constexpr int WS_UP = 65536;
constexpr int WS_ZP = WS_UP + NCONV * NLN * 128;
constexpr int WS_PART = WS_ZP + NCONV * NLN * 128;
constexpr int WS_END = WS_PART + NFFN * 8 * DM * 4;
constexpr size_t WS_TS = 64u << 20;
constexpr int SC1 = 16, NT = 2;
constexpr int SPIN_CAP = 1 << 16;

#define TS(k, s)

__device__ __forceinline__ float bflo(unsigned v) { return __uint_as_float(v << 16); }
__device__ __forceinline__ float bfhi(unsigned v) { return __uint_as_float(v & 0xffff0000u); }
__device__ __forceinline__ unsigned pkbf(float a, float b) {
  f32x2 f = {a, b};
  return __builtin_bit_cast(unsigned, __builtin_convertvector(f, bf2v));
}
__device__ __forceinline__ unsigned pkh(float a, float b) {
  f32x2 f = {a, b};
  return __builtin_bit_cast(unsigned, __builtin_convertvector(f, half2v));
}
__device__ __forceinline__ float rbf(float f) { return bflo(pkbf(f, f)); }

template <int CTRL>
__device__ __forceinline__ float dppf(float v) {
  return __int_as_float(__builtin_amdgcn_update_dpp(0, __float_as_int(v), CTRL, 0xf, 0xf, true));
}
__device__ __forceinline__ float swap16_sum(float v) {
  auto r = __builtin_amdgcn_permlane16_swap(__float_as_uint(v), __float_as_uint(v), false, false);
  return __uint_as_float(r[0]) + __uint_as_float(r[1]);
}
__device__ __forceinline__ float swap32_sum(float v) {
  auto r = __builtin_amdgcn_permlane32_swap(__float_as_uint(v), __float_as_uint(v), false, false);
  return __uint_as_float(r[0]) + __uint_as_float(r[1]);
}
__device__ __forceinline__ float red32_sum(float v) {
  v += dppf<0xB1>(v);
  v += dppf<0x4E>(v);
  v += dppf<0x141>(v);
  v += dppf<0x140>(v);
  return swap16_sum(v);
}
#define SCHED_FENCE() __builtin_amdgcn_sched_barrier(0)
#define DRAIN() asm volatile("s_waitcnt vmcnt(0)" ::: "memory")

__device__ __forceinline__ void ln_row(const unsigned (&v)[9], unsigned (&n)[9]) {
  float s = 0.f;
#pragma unroll
  for (int k = 0; k < 9; k++) s += bflo(v[k]) + bfhi(v[k]);
  const float mean = red32_sum(s) * (1.0f / DM);
  float q = 0.f;
#pragma unroll
  for (int k = 0; k < 9; k++) {
    const float d0 = bflo(v[k]) - mean, d1 = bfhi(v[k]) - mean;
    q = fmaf(d0, d0, q);
    q = fmaf(d1, d1, q);
  }
  q = red32_sum(q);
  const float mbf = rbf(mean);
  const float sbf = rbf(__builtin_amdgcn_sqrtf(rbf(rbf(q * (1.0f / DM)) + 1e-5f)));
  const float rinv = __builtin_amdgcn_rcpf(sbf);
#pragma unroll
  for (int k = 0; k < 9; k++) {
    const unsigned d = pkbf(bflo(v[k]) - mbf, bfhi(v[k]) - mbf);
    n[k] = pkbf(bflo(d) * rinv, bfhi(d) * rinv);
  }
}

constexpr int TAB_STRIDE = 544;
constexpr int TAB_ROWS = 57;
constexpr int SM_US = TAB_ROWS * TAB_STRIDE;
constexpr int UPAD = 256;
constexpr int SM_KC = SM_US + (UPAD + SEQ) * 2;
constexpr int KC_RD0 = SM_KC;
constexpr int KC_RD1 = KC_RD0 + 304 * 4;
constexpr int KC_R5 = KC_RD1 + 304 * 4;
constexpr int KC_R3 = KC_R5 + 64 * 4;
constexpr int KC_R9 = KC_R3 + 64 * 4;
constexpr int KC_R7 = KC_R9 + 96 * 4;
constexpr int KC_F1024 = KC_R7 + 96 * 4;
constexpr int CH_BYTES = KC_F1024 + 16;
constexpr int KC_CHUNKS = (CH_BYTES - SM_KC) / 16;
constexpr int YT = 1280;
constexpr int SM_YD = 0, SM_Y2 = 8 * YT - 2 * YT, SM_Y3 = 14 * YT - 4 * YT;
constexpr int SM_TL = 3 * CH_BYTES;
constexpr int TLS = 580;
constexpr int SM_MISC = SM_TL + 8 * TLS * 4;
constexpr int SM_TOTAL = SM_MISC + 128 + 1024;
constexpr int FS_STAGE = 0;
constexpr int DS = 1184;
constexpr int FS_XS = 72 * DS;
constexpr int FS_RED = FS_XS + 8 * DS;
constexpr int FS_GU = FS_RED + 4 * 9 * 512;
static_assert(FS_GU + 8 * 160 <= SM_TOTAL, "lds");

typedef f32x2 cf;
struct cstep { cf s, r; };
__device__ __forceinline__ cstep mkstep(cf a) { return cstep{a, cf{-a[1], a[0]}}; }
__device__ __forceinline__ cf cmul(cf a, const cstep& b) {
  cf t, o;
  asm("v_pk_mul_f32 %0, %1, %2 op_sel:[1,0]" : "=v"(t) : "v"(a), "v"(b.r));
  asm("v_pk_fma_f32 %0, %1, %2, %3 op_sel_hi:[0,1,1]" : "=v"(o) : "v"(a), "v"(b.s), "v"(t));
  return o;
}
__device__ __forceinline__ unsigned pkc(cf a) { return __builtin_bit_cast(unsigned, __builtin_convertvector(a, half2v)); }
__device__ __forceinline__ cf cexp_p2(float p, float ch, float cl, float wh, float wl) {
  float mag = __builtin_amdgcn_exp2f(p * ch);
  mag = fmaf(mag, (p * 0.693147181f) * cl, mag);
  const float rh = p * wh;
  const float fr = fmaf(p, wl, rh - rintf(rh));
  return cf{mag * __builtin_amdgcn_cosf(fr), mag * __builtin_amdgcn_sinf(fr)};
}
__device__ __forceinline__ float exp_acc(float x) {
  const float th = x * 1.44269502f;
  const float tl = fmaf(x, 1.92596299e-8f, fmaf(x, 1.44269502f, -th));
  const float e = __builtin_amdgcn_exp2f(th);
  return fmaf(e, tl * 0.693147181f, e);
}
__device__ __forceinline__ unsigned wave_max_u32(unsigned v) {
  v = max(v, (unsigned)__builtin_amdgcn_update_dpp(0, (int)v, 0x111, 0xf, 0xf, false));
  v = max(v, (unsigned)__builtin_amdgcn_update_dpp(0, (int)v, 0x112, 0xf, 0xf, false));
  v = max(v, (unsigned)__builtin_amdgcn_update_dpp(0, (int)v, 0x114, 0xf, 0xf, false));
  v = max(v, (unsigned)__builtin_amdgcn_update_dpp(0, (int)v, 0x118, 0xf, 0xf, false));
  v = max(v, (unsigned)__builtin_amdgcn_update_dpp(0, (int)v, 0x142, 0xa, 0xf, false));
  v = max(v, (unsigned)__builtin_amdgcn_update_dpp(0, (int)v, 0x143, 0xc, 0xf, false));
  return (unsigned)__builtin_amdgcn_readlane((int)v, 63);
}

#define MFMA16(a, b, c) __builtin_amdgcn_mfma_f32_16x16x32_f16((a), (b), (c), 0, 0, 0)
#define MFMAB(a, b, c) __builtin_amdgcn_mfma_f32_16x16x32_bf16((a), (b), (c), 0, 0, 0)

__device__ __forceinline__ half8 ld_frag(const LAS char* p) { return *(const LAS half8*)p; }
__device__ __forceinline__ const LAS unsigned* opaque_lds(const LAS char* p) {
  const LAS unsigned* q = (const LAS unsigned*)p;
  asm volatile("" : "+v"(q));
  return q;
}
template <int O2>
__device__ __forceinline__ half8 ld_toep(const LAS unsigned* q) {
  u32x4 v = {q[0], q[1], q[O2], q[O2 + 1]};
  return __builtin_bit_cast(half8, v);
}
__device__ __forceinline__ half8 band_frag(int lane, unsigned pat) {
  const int bits = (int)((pat << (lane & 15)) >> (8 * (lane >> 4)));
  u32x4 v;
#pragma unroll
  for (int j = 0; j < 4; j++) {
    const unsigned lo = (unsigned)__builtin_amdgcn_sbfe(bits, 2 * j, 1), hi = (unsigned)__builtin_amdgcn_sbfe(bits, 2 * j + 1, 1);
    v[j] = __builtin_amdgcn_perm(hi, lo, 0x05040100u) & 0x3c003c00u;
  }
  return __builtin_bit_cast(half8, v);
}
__device__ __forceinline__ half8 pack_win(f32x4 a, f32x4 b) {
  u32x4 v = {pkh(a[0], a[1]), pkh(a[2], a[3]), pkh(b[0], b[1]), pkh(b[2], b[3])};
  return __builtin_bit_cast(half8, v);
}

template <int NTL, int S0>
__device__ __forceinline__ void conv_dense(const half8 (&td)[17], const LAS char* ub, f32x4 (&acc)[3]) {
  constexpr int NS = 17 + 8 * (NTL - 1);
  constexpr int PF = 5;
  half8 u[NS];
#pragma unroll
  for (int s = S0; s < S0 + PF; s++) u[s] = ld_frag(ub + 32 * (-33 + 2 * s));
  SCHED_FENCE();
#pragma unroll
  for (int s = S0; s < NS; s++) {
    if (s + PF < NS) u[s + PF] = ld_frag(ub + 32 * (-33 + 2 * (s + PF)));
#pragma unroll
    for (int t = 0; t < NTL; t++) {
      const int i = 16 + 8 * t - s;
      if (i >= 0 && i <= 16) acc[t] = MFMA16(td[i], u[s], acc[t]);
    }
    SCHED_FENCE();
  }
}

typedef __amdgpu_buffer_rsrc_t rsrc_t;
__device__ __forceinline__ u32x4 ldg16(rsrc_t rs, int off) { return __builtin_amdgcn_raw_buffer_load_b128(rs, off, 0, SC1); }
__device__ __forceinline__ void stg16(rsrc_t rs, int off, u32x4 v) { __builtin_amdgcn_raw_buffer_store_b128(v, rs, off, 0, SC1); }
__device__ __forceinline__ void wait_ticks(int t) {
  const unsigned long long t0 = __builtin_amdgcn_s_memrealtime();
  while (__builtin_amdgcn_s_memrealtime() - t0 < (unsigned long long)t) __builtin_amdgcn_s_sleep(1);
}
__device__ __forceinline__ unsigned r_key(unsigned b, int row) {
  const unsigned k16 = (b & 0x8000u) ? (~b & 0xffffu) : (b | 0x8000u);
  return (k16 << 16) | (unsigned)(SEQ - 1 - row);
}

#define DLY_X 60
#define DLY_P 100

__device__ __forceinline__ void ffn_role(const u16* __restrict__ wg, const u16* __restrict__ wu, const u16* __restrict__ wd,
                                         char* __restrict__ ws, rsrc_t rs, unsigned tagv, LAS char* L) {
  const int tid = threadIdx.x, lane = tid & 63;
  const int wave = __builtin_amdgcn_readfirstlane(tid >> 6);
  const int fw = blockIdx.x - NCONV;
  const int j = (fw & 7) * 8 + (fw >> 3);
  const int r0 = tid / 18, rem = tid - r0 * 18;
  const int pc = rem >= 9 ? rem - 9 : rem;
  const char* gsrc = (const char*)(rem >= 9 ? wu : wg) + (size_t)r0 * (FF * 2) + j * 144 + pc * 16;
  u32x4 wa[7], wb[7], wc[7];
#pragma unroll
  for (int i = 0; i < 7; i++) {
    const bool ok = tid < 756 && r0 + 42 * i < 288;
    wa[i] = __builtin_nontemporal_load((const u32x4*)(ok ? gsrc + (size_t)(42 * i) * (FF * 2) : (const char*)wg));
  }
#pragma unroll
  for (int i = 0; i < 7; i++) {
    const bool ok = tid < 756 && r0 + 42 * i < 288;
    wb[i] = __builtin_nontemporal_load((const u32x4*)(ok ? gsrc + (size_t)(288 + 42 * i) * (FF * 2) : (const char*)wg));
  }
#pragma unroll
  for (int i = 0; i < 7; i++) {
    const int e = tid + NTHR * i;
    wc[i] = __builtin_nontemporal_load((const u32x4*)((const char*)wd + (size_t)j * 72 * 1152) + (e < 5184 ? e : 0));
  }
  SCHED_FENCE();
  if (tid < 8 * 4) *(LAS unsigned*)(L + FS_GU + (tid >> 2) * 160 + 144 + (tid & 3) * 4) = 0u;
  const int g = lane >> 4, tq = (lane & 15) >> 2, tp = lane & 3;
  const int tg = wave % 3, kq = wave / 3;
  s16x4 fa[3][5][2];
  auto stage_gu = [&](const u32x4 (&w)[7]) {
#pragma unroll
    for (int i = 0; i < 7; i++)
      if (tid < 756 && r0 + 42 * i < 288) *(LAS u32x4*)(L + FS_STAGE + (r0 + 42 * i) * 288 + rem * 16) = w[i];
  };
  auto read_gu = [&]() {
    const LAS char* p = L + FS_STAGE + (160 * (kq & 1) + 8 * g + tq) * 288 + (tp >> 1) * 144 + (tp & 1) * 8 + 48 * tg;
#pragma unroll
    for (int ks = 0; ks < 5; ks++) {
      const int kk = (ks == 4) ? ((kq & 1) ? 3 : 4) : ks;
#pragma unroll
      for (int t = 0; t < 3; t++)
#pragma unroll
        for (int h = 0; h < 2; h++)
          fa[t][ks][h] = __builtin_amdgcn_ds_read_tr16_b64_v4i16((LAS s16x4*)(p + (32 * kk + 4 * h) * 288 + 16 * t));
    }
  };
  stage_gu(wa);
  __syncthreads();
  if (kq < 2) read_gu();
  __syncthreads();
  stage_gu(wb);
  __syncthreads();
  if (kq >= 2) read_gu();
  __syncthreads();
  TS(1, 1);
#pragma unroll
  for (int i = 0; i < 7; i++) {
    const int e = tid + NTHR * i;
    const int row = e / 72;
    if (e < 5184) *(LAS u32x4*)(L + FS_STAGE + row * DS + (e - row * 72) * 16) = wc[i];
  }
  __syncthreads();
  s16x4 fd[3][5];
  {
    const LAS char* p = L + FS_STAGE + tq * DS + (48 * wave + 4 * tp) * 2;
#pragma unroll
    for (int t = 0; t < 3; t++) {
#pragma unroll
      for (int h = 0; h < 4; h++)
        fd[t][h] = __builtin_amdgcn_ds_read_tr16_b64_v4i16((LAS s16x4*)(p + (32 * (h >> 1) + 8 * g + 4 * (h & 1)) * DS + 32 * t));
      fd[t][4] = __builtin_amdgcn_ds_read_tr16_b64_v4i16((LAS s16x4*)(p + (64 + 4 * (g & 1)) * DS + 32 * t));
    }
  }
  const unsigned tag = __builtin_amdgcn_readfirstlane(tagv);
  TS(1, 2);
  if (wave == 0) {
    for (int spin = 0; spin < SPIN_CAP; spin++) {
        asm volatile("" ::: "memory");
      const unsigned t = __builtin_amdgcn_raw_buffer_load_b32(rs, WS_RG + (fw * 16) * 8 + 4, 0, SC1);
      if (__builtin_amdgcn_readfirstlane(t) == tag) break;
      __builtin_amdgcn_s_sleep(4);
    }
    wait_ticks(DLY_X);
  }
  __syncthreads();
  TS(1, 3);
  {
    u32x4 v;
    for (int spin = 0; spin < SPIN_CAP; spin++) {
        asm volatile("" ::: "memory");
      v = ldg16(rs, WS_XS + tid * 16);
      if (__all(v[3] == tag)) break;
      __builtin_amdgcn_s_sleep(2);
    }
    const int slot = tid / 96, rm = tid - slot * 96;
    LAS unsigned* dst = (LAS unsigned*)(L + FS_XS + slot * DS) + (rm & 31) + 96 * (rm >> 5);
    dst[0] = v[0];
    dst[32] = v[1];
    dst[64] = v[2];
  }
  __syncthreads();
  TS(1, 4);
  {
    const f32x4 zero4 = {0.f, 0.f, 0.f, 0.f};
    f32x4 acc[3] = {zero4, zero4, zero4};
    const LAS char* xp = L + FS_XS + (lane & 7) * DS + 16 * g + 64 * (9 * (kq >> 1) + 5 * (kq & 1));
    bf8v xf[5];
#pragma unroll
    for (int ks = 0; ks < 5; ks++) xf[ks] = *(const LAS bf8v*)(xp + 64 * ks);
#pragma unroll
    for (int ks = 0; ks < 5; ks++) {
      if (ks < 4 || !(kq & 1)) {
#pragma unroll
        for (int t = 0; t < 3; t++) {
          const bf8v a = __builtin_bit_cast(bf8v, __builtin_shufflevector(fa[t][ks][0], fa[t][ks][1], 0, 1, 2, 3, 4, 5, 6, 7));
          acc[t] = MFMAB(a, xf[ks], acc[t]);
        }
      }
    }
    if ((lane & 15) < 8) {
#pragma unroll
      for (int t = 0; t < 3; t++) *(LAS f32x4*)(L + FS_RED + ((kq * 9 + 3 * tg + t) * 8 + (lane & 7)) * 64 + 16 * g) = acc[t];
    }
  }
  __syncthreads();
  if (wave < 9) {
    const LAS float* rp = (const LAS float*)(L + FS_RED) + (wave * 8 + (lane >> 3)) * 16 + (lane & 7);
    const float gs = ((rp[0] + rp[1152]) + rp[2304]) + rp[3456];
    const float us = ((rp[8] + rp[1160]) + rp[2312]) + rp[3464];
    const float gp = rbf(gs);
    const float gq = rbf(gp * __builtin_amdgcn_rcpf(1.0f + __expf(-gp)));
    *(LAS u16*)(L + FS_GU + (lane >> 3) * 160 + (wave * 8 + (lane & 7)) * 2) = (u16)pkbf(gq * rbf(us), 0.f);
  }
  __syncthreads();
  {
    const LAS char* gp = L + FS_GU + (lane & 7) * 160;
    const bf8v b0 = *(const LAS bf8v*)(gp + 16 * g), b1 = *(const LAS bf8v*)(gp + 64 + 16 * g);
    const s16x4 b2 = *(const LAS s16x4*)(gp + 128 + 8 * g);
#pragma unroll
    for (int t = 0; t < 3; t++) {
      f32x4 acc = {0.f, 0.f, 0.f, 0.f};
      acc = __builtin_amdgcn_mfma_f32_16x16x16bf16_1k(fd[t][4], b2, acc, 0, 0, 0);
      f32x4 acc2 = {0.f, 0.f, 0.f, 0.f};
      acc2 = MFMAB(__builtin_bit_cast(bf8v, __builtin_shufflevector(fd[t][0], fd[t][1], 0, 1, 2, 3, 4, 5, 6, 7)), b0, acc2);
      acc2 = MFMAB(__builtin_bit_cast(bf8v, __builtin_shufflevector(fd[t][2], fd[t][3], 0, 1, 2, 3, 4, 5, 6, 7)), b1, acc2);
      u32x4 o;
#pragma unroll
      for (int r = 0; r < 4; r++) o[r] = min(__float_as_uint(acc[r] + acc2[r]), 0xfffffffeu);
      if ((lane & 15) < 8) stg16(rs, WS_PART + fw * (8 * DM * 4) + (3 * wave + t) * 512 + (lane & 7) * 64 + 16 * g, o);
    }
  }
  TS(1, 5);
}

__global__ __launch_bounds__(NTHR, 3) void k_all(const u16* __restrict__ x, const u16* __restrict__ lar,
                                                 const u16* __restrict__ aim, const u16* __restrict__ crl,
                                                 const u16* __restrict__ cim, const u16* __restrict__ ldt,
                                                 const u16* __restrict__ dskip, const u16* __restrict__ wr,
                                                 const u16* __restrict__ wg, const u16* __restrict__ wu,
                                                 const u16* __restrict__ wd, u16* __restrict__ out,
                                                 char* __restrict__ ws) {
  __shared__ __attribute__((aligned(16))) char smem[SM_TOTAL];
  LAS char* const L = (LAS char*)smem;
  const int tid = threadIdx.x, lane = tid & 63, b = blockIdx.x;
  const int wave = __builtin_amdgcn_readfirstlane(tid >> 6);
  const rsrc_t rs = __builtin_amdgcn_make_buffer_rsrc(ws, 0, WS_END, 0x00020000);
  const unsigned tagv = __builtin_amdgcn_raw_buffer_load_b32(rs, WS_EPOCH, 0, SC1) + 1u;
  if (b >= NCONV) {
    TS(1, 0);
    ffn_role(wg, wu, wd, ws, rs, tagv, L);
    return;
  }
  TS(0, 0);
  const bool is_ln = b < NLN;
  const bool ln_wave = is_ln && wave < 8;
  const int hl = lane & 31, rsel = lane >> 5;
  const int row = b * 16 + wave * 2 + rsel;
  unsigned xw[9];
  if (ln_wave) {
    const unsigned* xr = (const unsigned*)(x + (size_t)row * DM) + hl;
#pragma unroll
    for (int k = 0; k < 9; k++) xw[k] = xr[32 * k];
  }
  const int ch = wave >> 2, sub = (wave + ch) & 3;
  const int d = 3 * b + ch;
  const int n = (sub & 1) * 64 + lane;
  const unsigned v_lar = lar[d * NM + n], v_aim = aim[d * NM + n];
  const unsigned v_cr = crl[d * NM + n], v_ci = cim[d * NM + n];
  const unsigned v_cr2 = crl[d * NM + (n ^ 64)], v_ci2 = cim[d * NM + (n ^ 64)];
  const unsigned v_ldt = ldt[d], v_dsk = dskip[d];
  const int part_off = WS_PART + (tid >> 3) * (8 * DM * 4) + (b - RED0) * 128 + (tid & 7) * 16;
  if (b >= RED0 && tid < 512) stg16(rs, part_off, u32x4{~0u, ~0u, ~0u, ~0u});
  LAS char* const C = L + ch * CH_BYTES;
  {
    const int ct = tid & 255;
    const u32x4 z = {0u, 0u, 0u, 0u};
    if (ct < KC_CHUNKS) *(LAS u32x4*)(C + SM_KC + 16 * ct) = z;
    if (ct < UPAD / 8) *(LAS u32x4*)(C + SM_US + 16 * ct) = z;
    if (tid < 16) *(LAS unsigned*)(L + SM_MISC + 64 + 4 * tid) = 0u;
  }
  half8 bA = band_frag(lane, sub == 3 ? 0x1fu << 6 : 0x1ffu << 4);
  half8 bB = band_frag(lane, sub == 3 ? 0x7u << 11 : 0x7fu << 5);
  half8 bte = band_frag(lane, 0x3u << 12);
  asm volatile("" : "+v"(bA), "+v"(bB), "+v"(bte));
  SCHED_FENCE();
  const unsigned tag = __builtin_amdgcn_readfirstlane(tagv);
  LAS unsigned* const tl = (LAS unsigned*)(L + SM_TL);
  const unsigned hsel = rsel ? 0x07060302u : 0x05040100u;
  if (is_ln) {
    if (wave < 8) {
      unsigned un[9];
      ln_row(xw, un);
#pragma unroll
      for (int k = 0; k < 9; k++) {
        const unsigned hp = pkh(bflo(un[k]), bfhi(un[k]));
        auto r = __builtin_amdgcn_permlane32_swap(hp, hp, false, false);
        tl[wave * TLS + 2 * (hl + 32 * k) + rsel] = __builtin_amdgcn_perm(r[1], r[0], hsel);
      }
    }
    __syncthreads();
#pragma unroll
    for (int i = 0; i < 2; i++) {
      const int e = tid + NTHR * i;
      const LAS unsigned* s = tl + (e & 7) * TLS + 3 * (e >> 3);
      stg16(rs, WS_UP + ((e >> 3) * NLN + b) * 128 + (e & 7) * 16, u32x4{s[0], s[1], s[2], tag});
    }
  }
  TS(0, 1);
  float inv_scale;
  {
    const float dt = rbf(exp_acc(bflo(v_ldt)));
    const float are = -rbf(exp_acc(bflo(v_lar))) * dt;
    const float aie = bflo(v_aim) * dt;
    const float chh = are * 1.44269502f;
    const float cl = fmaf(are, 1.92596299e-8f, fmaf(are, 1.44269502f, -chh));
    const float wh = aie * 0.159154936f;
    const float wl = fmaf(aie, 7.13048051e-9f, fmaf(aie, 0.159154936f, -wh));
    const unsigned cmax = wave_max_u32(max(max(v_cr & 0x7fffu, v_ci & 0x7fffu), max(v_cr2 & 0x7fffu, v_ci2 & 0x7fffu)));
    int se = 385 - (int)(cmax >> 7) - (int)(__float_as_uint(dt) >> 23);
    se = min(max(se, 2), 252);
    const float scale2 = __uint_as_float((unsigned)(se + 1) << 23);
    inv_scale = __uint_as_float((unsigned)(254 - se) << 23);
    const cf cc = {bflo(v_cr) * dt * scale2, -bflo(v_ci) * dt * scale2};
    LAS unsigned* tab = (LAS unsigned*)C + n;
    constexpr int TS4 = TAB_STRIDE / 4;
    if (sub < 2) {
      const cstep e1 = mkstep(cexp_p2(1.f, chh, cl, -wh, -wl)), e128 = mkstep(cexp_p2(128.f, chh, cl, -wh, -wl));
      const cstep e2 = mkstep(cmul(e1.s, e1)), e4 = mkstep(cmul(e2.s, e2));
      cf bb = cc;
#pragma unroll
      for (int a = 0; a < 8; a++) {
        const int cnt = a < 2 ? 8 : (a < 4 ? 4 : 2);
        const int r0 = a < 2 ? 8 * a : (a < 4 ? 16 + 4 * (a - 2) : 24 + 2 * (a - 4));
        const cstep st = a < 2 ? e1 : (a < 4 ? e2 : e4);
        cf v = bb;
#pragma unroll
        for (int jj = 0; jj < cnt; jj++) {
          tab[(r0 + jj) * TS4] = pkc(v);
          if (jj + 1 < cnt) v = cmul(v, st);
        }
        if (a < 7) bb = cmul(bb, e128);
      }
    } else {
      const cstep e8 = mkstep(cexp_p2(8.f, chh, cl, wh, wl)), e128 = mkstep(cexp_p2(128.f, chh, cl, -wh, -wl));
      cf bb = cmul(cc, mkstep(cexp_p2(1024.f, chh, cl, -wh, -wl)));
      cf h = e8.s;
      tab[41 * TS4] = pkh(1.f, 0.f);
#pragma unroll
      for (int i = 0; i < 15; i++) {
        tab[(42 + i) * TS4] = pkc(h);
        if (i < 14) h = cmul(h, e8);
        if (i < 9) {
          tab[(32 + i) * TS4] = pkc(bb);
          if (i < 8) bb = cmul(bb, e128);
        }
      }
    }
  }
  __syncthreads();
  TS(0, 2);
  const int c15 = lane & 15, g = lane >> 4;
  if (sub < 3) {
    f32x4 f = {0.f, 0.f, 0.f, 0.f};
    const LAS char* pa = C + (16 * sub + c15) * TAB_STRIDE + 16 * g;
    const LAS char* pb = C + (41 + c15) * TAB_STRIDE + 16 * g;
    half8 fa[8], fb[8];
#pragma unroll
    for (int ks = 0; ks < 8; ks++) { fa[ks] = ld_frag(pa + 64 * ks); fb[ks] = ld_frag(pb + 64 * ks); }
    SCHED_FENCE();
#pragma unroll
    for (int ks = 0; ks < 8; ks++) f = MFMA16(fa[ks], fb[ks], f);
    const unsigned f10 = pkh(f[1], f[0]), f32 = pkh(f[3], f[2]);
    auto w16 = [&](int off, unsigned v) { *(volatile LAS u16*)(C + off) = (u16)v; };
    auto w32 = [&](int off, unsigned v) { *(volatile LAS unsigned*)(C + off) = v; };
    auto w64 = [&](int off, unsigned a, unsigned c) { *(volatile LAS u32x2*)(C + off) = u32x2{a, c}; };
    const unsigned f21 = __builtin_amdgcn_alignbit(f10, f32, 16);
    if (sub == 0) {
      const int l0 = 2 * (128 * (g >> 1) + 4 * (g & 1) + 8 * c15);
      w64(KC_RD0 + 2 * 540 - l0, f32, f10);
      w16(KC_RD1 + 2 * 539 - l0, f32);
      w32(KC_RD1 + 2 * 540 - l0, f21);
      w16(KC_RD1 + 2 * 542 - l0, f10 >> 16);
    } else if (sub == 1) {
      const unsigned f00 = __builtin_amdgcn_perm(f10, f10, 0x03020302u);
      if (g < 2) {
        const int p0 = 2 * (256 + 128 * g + 8 * c15);
        const unsigned f22 = __builtin_amdgcn_perm(f32, f32, 0x03020302u);
        w16(KC_RD0 + 2 * 537 - p0, f32);
        w32(KC_RD0 + 2 * 538 - p0, f22);
        w64(KC_RD0 + 2 * 540 - p0, f21, f00);
        w64(KC_RD1 + 2 * 536 - p0, f32, f22);
        w32(KC_RD1 + 2 * 540 - p0, f10);
        w16(KC_RD1 + 2 * 542 - p0, f00);
        if (lane != 0) {
          w16(KC_RD0 + 2 * 544 - p0, f00);
          w16(KC_RD1 + 2 * 543 - p0, f00);
        }
      } else {
        const int m2 = 2 * (32 * g + c15);
        w16(KC_R3 + 2 * 159 - m2, f10);
        w16(KC_R5 + 2 * 160 - 32 - m2, f32 >> 16);
        w16(KC_R3 + 2 * 159 - 32 - m2, f32);
        if (lane != 32) {
          w16(KC_R5 + 2 * 160 - m2, f00);
        } else {
          w16(KC_RD0 + 2 * 29, f00);
          w32(KC_RD0 + 2 * 30, f00);
          w16(KC_RD0 + 2 * 32, f00);
          w64(KC_RD1 + 2 * 28, f00, f00);
        }
      }
    } else if (lane <= 32) {
      const int a0 = ((c15 & 1) ? KC_R7 + 2 * 63 : KC_R9 + 2 * 64) - 64 * g - (c15 & 14);
      w16(lane == 0 ? KC_R5 + 2 * 32 : a0, f10 >> 16);
      w16(lane == 0 ? KC_F1024 : a0 + 190, f10 >> 16);
      if (lane != 32) {
        w16(a0 - 16, f10);
        w16(a0 - 16 + 190, f10);
        w16(a0 - 32, f32 >> 16);
        w16(a0 - 32 + 190, f32 >> 16);
        w16(a0 - 48, f32);
        w16(a0 - 48 + 190, f32);
      }
    }
  } else if (lane == 0) {
    *(LAS f32x2*)(C + KC_F1024 + 8) = f32x2{inv_scale, bflo(v_dsk)};
  }
  __syncthreads();
  TS(0, 3);
  half8 td[17];
  {
    const int c = (c15 + 1) & 1;
    if (sub < 3) {
      const LAS unsigned* tb = opaque_lds(C + (c ? KC_RD1 : KC_RD0) + 2 * (543 - 16 - c15 + 8 * g - c) - 64 * 16);
#pragma unroll
      for (int i = 0; i < 17; i++) td[i] = ld_toep<2>(tb + 16 * (16 - i));
      bte = bte * *(const LAS _Float16*)(C + KC_F1024);
    } else {
      const LAS unsigned* t5 = opaque_lds(C + KC_R5 + 2 * (158 - 64 - 2 * c15 + 4 * g) - 128);
#pragma unroll
      for (int e = 0; e < 3; e++) {
        td[e] = ld_toep<8>(t5 + 16 * (2 - e));
        td[3 + e] = ld_toep<8>(t5 + (KC_R3 - KC_R5) / 4 + 16 * (2 - e));
      }
#pragma unroll
      for (int e = 6; e < 17; e++) td[e] = td[0];
    }
  }
  DRAIN();
  TS(0, 4);
  {
    u32x4 v0, v1;
    const int off = WS_UP + b * (NLN * 128) + tid * 16;
    for (int spin = 0; spin < SPIN_CAP; spin++) {
        asm volatile("" ::: "memory");
      v0 = ldg16(rs, off);
      v1 = v0;
      if (wave < 4) v1 = ldg16(rs, off + NTHR * 16);
      if (__all(v0[3] == tag && v1[3] == tag)) break;
      __builtin_amdgcn_s_sleep(2);
    }
    LAS char* dst = L + SM_US + 2 * UPAD + 4 * tid;
#pragma unroll
    for (int c = 0; c < 3; c++) *(LAS unsigned*)(dst + c * CH_BYTES) = v0[c];
    if (wave < 4) {
#pragma unroll
      for (int c = 0; c < 3; c++) *(LAS unsigned*)(dst + c * CH_BYTES + 4 * NTHR) = v1[c];
    }
  }
  __syncthreads();
  TS(0, 5);
  {
    const f32x4 zero4 = {0.f, 0.f, 0.f, 0.f};
    const LAS char* uf16 = C + SM_US + 2 * (UPAD + 16 * c15 + 8 * g);
    LAS char* yb = C + g * 320 + c15 * 16;
    f32x4 acc[3] = {zero4, zero4, zero4};
    if (sub == 3) {
      const LAS char* uf8 = C + SM_US + 2 * (UPAD + 8 * c15 + 8 * g - 24);
      f32x4 a2[6];
#pragma unroll
      for (int i = 0; i < 6; i++) a2[i] = zero4;
      half8 uw[12];
#pragma unroll
      for (int jj = 0; jj < 4; jj++) uw[jj] = ld_frag(uf8 + 256 * jj);
      SCHED_FENCE();
#pragma unroll
      for (int jj = 0; jj < 6; jj++) {
        if (jj < 4) {
          uw[2 * jj + 4] = ld_frag(uf8 + 256 * (2 * jj + 4));
          uw[2 * jj + 5] = ld_frag(uf8 + 256 * (2 * jj + 5));
        }
        const half8 X5 = pack_win(MFMA16(uw[2 * jj], bA, zero4), MFMA16(uw[2 * jj + 1], bA, zero4));
        const half8 X3 = pack_win(MFMA16(uw[2 * jj], bB, zero4), MFMA16(uw[2 * jj + 1], bB, zero4));
#pragma unroll
        for (int e = 0; e < 3; e++) {
          if (jj + e < 6) {
            a2[jj + e] = MFMA16(X5, td[e], a2[jj + e]);
            a2[jj + e] = MFMA16(X3, td[3 + e], a2[jj + e]);
          }
        }
        if (jj > 0) *(LAS f32x4*)(yb + SM_Y2 + (1 + jj) * YT) = a2[jj - 1];
        SCHED_FENCE();
      }
      *(LAS f32x4*)(yb + SM_Y2 + 7 * YT) = a2[5];
    } else if (sub == 0) {
      conv_dense<3, 9>(td, uf16, acc);
    } else if (sub == 1) {
      acc[1] = MFMA16(bte, ld_frag(uf16 + 32 * (64 - 65)), acc[1]);
      acc[2] = MFMA16(bte, ld_frag(uf16 + 32 * (80 - 65)), acc[2]);
      conv_dense<3, 0>(td, uf16 + 2 * 768, acc);
    } else {
      acc[0] = MFMA16(bte, ld_frag(uf16 + 32 * (96 - 65)), acc[0]);
      acc[1] = MFMA16(bte, ld_frag(uf16 + 32 * (112 - 65)), acc[1]);
      conv_dense<2, 0>(td, uf16 + 2 * 1536, acc);
    }
    asm volatile("" ::: "memory");
    if (sub == 2) {
      const int c = (c15 + 1) & 1;
      const LAS unsigned* t9 = opaque_lds(C + KC_R9 + 192 * c + 2 * (127 - 64 - c15 + 4 * g - c) - 96);
      half8 T9[4], T7[4], uw[8];
#pragma unroll
      for (int i = 0; i < 4; i++) {
        uw[2 * i] = ld_frag(uf16 + 512 * i - 48);
        uw[2 * i + 1] = ld_frag(uf16 + 512 * i - 32);
      }
#pragma unroll
      for (int e = 0; e < 4; e++) {
        T9[e] = ld_toep<8>(t9 + 8 * (3 - e));
        T7[e] = ld_toep<8>(t9 + (KC_R7 - KC_R9) / 4 + 8 * (3 - e));
      }
      SCHED_FENCE();
      f32x4 a4 = zero4, a5 = zero4;
#pragma unroll
      for (int jj = 0; jj < 2; jj++) {
        const half8 X9 = pack_win(MFMA16(uw[4 * jj], bA, zero4), MFMA16(uw[4 * jj + 2], bA, zero4));
        const half8 X7 = pack_win(MFMA16(uw[4 * jj + 1], bB, zero4), MFMA16(uw[4 * jj + 3], bB, zero4));
        if (jj == 0) {
          a4 = MFMA16(X9, T9[0], a4); a4 = MFMA16(X7, T7[0], a4);
          a5 = MFMA16(X9, T9[1], a5); a5 = MFMA16(X7, T7[1], a5);
        }
        acc[0] = MFMA16(X9, T9[2 - 2 * jj], acc[0]); acc[0] = MFMA16(X7, T7[2 - 2 * jj], acc[0]);
        acc[1] = MFMA16(X9, T9[3 - 2 * jj], acc[1]); acc[1] = MFMA16(X7, T7[3 - 2 * jj], acc[1]);
      }
      *(LAS f32x4*)(yb + SM_Y3 + 4 * YT) = a4;
      *(LAS f32x4*)(yb + SM_Y3 + 5 * YT) = a5;
    }
    if (sub < 3) {
      LAS char* yd = yb + SM_YD + 3 * sub * YT;
      *(LAS f32x4*)(yd) = acc[0];
      *(LAS f32x4*)(yd + YT) = acc[1];
      if (sub < 2) *(LAS f32x4*)(yd + 2 * YT) = acc[2];
    }
  }
  unsigned wrw[9];
  if (ln_wave) {
#pragma unroll
    for (int k = 0; k < 9; k++) wrw[k] = ((const unsigned*)wr)[hl + 32 * k];
  }
  __syncthreads();
  TS(0, 6);
#pragma unroll
  for (int i = 0; i < 2; i++) {
    if (i == 0 || wave < 4) {
      const int e = tid + NTHR * i;
      const int nt = __builtin_amdgcn_readfirstlane(e >> 7);
      const int yo = nt * YT + ((e >> 1) & 3) * 320 + ((e >> 3) & 15) * 16 + (e & 1) * 8;
      u32x4 o;
#pragma unroll
      for (int c = 0; c < 3; c++) {
        const LAS char* cb = L + c * CH_BYTES;
        f32x2 y = *(const LAS f32x2*)(cb + SM_YD + yo);
        if (nt >= 2) y += *(const LAS f32x2*)(cb + SM_Y2 + yo);
        if (nt == 4 || nt == 5) y += *(const LAS f32x2*)(cb + SM_Y3 + yo);
        const f32x2 sd = *(const LAS f32x2*)(cb + KC_F1024 + 8);
        const half2v uu = *(const LAS half2v*)(cb + SM_US + 2 * UPAD + 4 * e);
        const float isc = sd[0], dsk = sd[1];
        const unsigned yb2 = pkbf(y[0] * isc, y[1] * isc);
        const unsigned sb2 = pkbf((float)uu[0] * dsk, (float)uu[1] * dsk);
        o[c] = pkbf(bflo(yb2) + bflo(sb2), bfhi(yb2) + bfhi(sb2));
      }
      o[3] = tag;
      stg16(rs, WS_ZP + ((e >> 3) * NCONV + b) * 128 + (e & 7) * 16, o);
    }
  }
  TS(0, 7);
  if (is_ln) {
    DRAIN();
    {
      u32x4 v0, v1;
      const int off = WS_ZP + b * (NCONV * 128) + tid * 16;
      for (int spin = 0; spin < SPIN_CAP; spin++) {
        asm volatile("" ::: "memory");
        v0 = ldg16(rs, off);
        v1 = ldg16(rs, off + NTHR * 16);
        if (__all(v0[3] == tag && v1[3] == tag)) break;
        __builtin_amdgcn_s_sleep(2);
      }
      LAS unsigned* dst = tl + (tid & 7) * TLS + 3 * (tid >> 3);
#pragma unroll
      for (int c = 0; c < 3; c++) { dst[c] = v0[c]; dst[288 + c] = v1[c]; }
    }
    __syncthreads();
    TS(0, 8);
    LAS unsigned* const okey = (LAS unsigned*)(L + SM_MISC);
    LAS unsigned* const cnt = okey + 16;
    unsigned hw[9], wn[9], rbits = 0;
    if (wave < 8) {
#pragma unroll
      for (int k = 0; k < 9; k++) {
        const u32x2 t2 = *(const LAS u32x2*)(tl + wave * TLS + 2 * (hl + 32 * k));
        const unsigned t0 = t2[0], t1 = t2[1];
        const unsigned z2 = __builtin_amdgcn_perm(t1, t0, hsel);
        hw[k] = pkbf(bflo(xw[k]) + bflo(z2), bfhi(xw[k]) + bfhi(z2));
      }
      ln_row(hw, wn);
      float dot = 0.f;
#pragma unroll
      for (int k = 0; k < 9; k++) {
        dot = fmaf(bflo(wn[k]), bflo(wrw[k]), dot);
        dot = fmaf(bfhi(wn[k]), bfhi(wrw[k]), dot);
      }
      dot = red32_sum(dot);
      rbits = pkbf(dot, dot) & 0xffffu;
      const unsigned rr = (unsigned)__builtin_amdgcn_readlane((int)rbits, 0) | ((unsigned)__builtin_amdgcn_readlane((int)rbits, 32) << 16);
      if (lane == 0) __builtin_amdgcn_raw_buffer_store_b64(u32x2{rr, tag}, rs, WS_RG + (b * 8 + wave) * 8, 0, SC1);
      if (hl == 0) okey[2 * wave + rsel] = r_key(rbits, row);
    }
    __syncthreads();
    TS(0, 9);
    int orank = 0;
    if (wave < 8) {
      const unsigned mykey = okey[c15];
#pragma unroll
      for (int i = 0; i < 16; i++) orank += okey[i] > mykey;
      DRAIN();
      u32x4 v;
      for (int spin = 0; spin < SPIN_CAP; spin++) {
        asm volatile("" ::: "memory");
        v = ldg16(rs, WS_RG + tid * 16);
        if (__all(v[1] == tag && v[3] == tag)) break;
        __builtin_amdgcn_s_sleep(1);
      }
      const unsigned k0 = r_key(v[0] & 0xffffu, 4 * tid), k1 = r_key(v[0] >> 16, 4 * tid + 1);
      const unsigned k2 = r_key(v[2] & 0xffffu, 4 * tid + 2), k3 = r_key(v[2] >> 16, 4 * tid + 3);
      int mine = 8;
      for (int c = 0; c < 8; c++) {
        const unsigned oc = (unsigned)__builtin_amdgcn_readlane((int)mykey, __builtin_ctzll(__ballot(orank == c)));
        const int s = __popcll(__ballot(k0 > oc)) + __popcll(__ballot(k1 > oc)) + __popcll(__ballot(k2 > oc)) + __popcll(__ballot(k3 > oc));
        if (s >= 8) break;
        mine = lane == c ? s : mine;
      }
      if (lane < 8) __hip_atomic_fetch_add(cnt + lane, (unsigned)mine, __ATOMIC_RELAXED, __HIP_MEMORY_SCOPE_WORKGROUP);
    }
    __syncthreads();
    TS(0, 10);
    if (wave < 8) {
      const int so = __shfl(orank, 2 * wave + rsel);
      const unsigned rank = so < 8 ? cnt[so] : 8u;
      if (rank < 8) {
#pragma unroll
        for (int m = 0; m < 3; m++) stg16(rs, WS_XS + rank * 1536 + m * 512 + hl * 16, u32x4{wn[3 * m], wn[3 * m + 1], wn[3 * m + 2], tag});
#pragma unroll
        for (int k = 0; k < 9; k++) stg16(rs, WS_HS + rank * 4608 + (hl + 32 * k) * 16, u32x4{hw[k], wn[k], ((unsigned)row << 16) | rbits, tag});
      } else {
        unsigned* orow = (unsigned*)(out + (size_t)row * DM) + hl;
#pragma unroll
        for (int k = 0; k < 9; k++) orow[32 * k] = pkbf(bflo(hw[k]) + bflo(wn[k]), bfhi(hw[k]) + bfhi(wn[k]));
      }
    }
  }
  if (b < RED0) {
    TS(0, 12);
    return;
  }
  {
    const int T = (b - RED0) >> 2, tp = (b - RED0) & 3;
    u32x4 hv = {0u, 0u, 0u, 0u};
    if (wave == 8) {
      for (int spin = 0; spin < SPIN_CAP; spin++) {
        asm volatile("" ::: "memory");
        hv = ldg16(rs, WS_HS + (2 * tp + ((lane >> 3) & 1)) * 4608 + (8 * T + (lane & 7)) * 16);
        if (__all(hv[3] == tag)) break;
        __builtin_amdgcn_s_sleep(4);
      }
      wait_ticks(DLY_P);
    }
    __syncthreads();
    TS(0, 11);
    LAS float* red = (LAS float*)(L + SM_MISC + 128);
    if (wave < 8) {
      u32x4 v;
      for (int spin = 0; spin < SPIN_CAP; spin++) {
        asm volatile("" ::: "memory");
        v = ldg16(rs, part_off);
        if (__all(v[0] != ~0u && v[1] != ~0u && v[2] != ~0u && v[3] != ~0u)) break;
        __builtin_amdgcn_s_sleep(2);
      }
      f32x4 s;
#pragma unroll
      for (int r = 0; r < 4; r++) {
        float a = __uint_as_float(v[r]);
        a += dppf<0x128>(a);
        a = swap16_sum(a);
        s[r] = swap32_sum(a);
      }
      if (lane < 8) *(LAS f32x4*)(red + wave * 32 + lane * 4) = s;
    }
    __syncthreads();
    if (wave == 8 && lane < 16) {
      float y0 = 0.f, y1 = 0.f;
#pragma unroll
      for (int w = 0; w < 8; w++) {
        const f32x2 p = *(const LAS f32x2*)(red + w * 32 + lane * 2);
        y0 += p[0];
        y1 += p[1];
      }
      const float sg = rbf(__builtin_amdgcn_rcpf(1.0f + __expf(-bflo(hv[2]))));
      const unsigned yb2 = pkbf(y0, y1);
      const unsigned y2 = pkbf(bflo(yb2) * sg, bfhi(yb2) * sg);
      const unsigned a2 = pkbf(bflo(hv[1]) + bflo(y2), bfhi(hv[1]) + bfhi(y2));
      ((unsigned*)out)[(size_t)((hv[2] >> 16) & (SEQ - 1)) * (DM / 2) + 8 * T + (lane & 7)] = pkbf(bflo(hv[0]) + bflo(a2), bfhi(hv[0]) + bfhi(a2));
      if (b == RED0 && lane == 0) __builtin_amdgcn_raw_buffer_store_b32(tag, rs, WS_EPOCH, 0, SC1);
    }
  }
  TS(0, 12);
}

}

extern "C" void kernel_launch(void* const* d_in, const int* in_sizes, int n_in, void* d_out, int out_size,
                              void* d_ws, size_t ws_size, hipStream_t stream) {
  k_all<<<NCONV + NFFN, NTHR, 0, stream>>>((const u16*)d_in[0], (const u16*)d_in[1], (const u16*)d_in[2], (const u16*)d_in[3],
                                           (const u16*)d_in[4], (const u16*)d_in[5], (const u16*)d_in[6], (const u16*)d_in[8],
                                           (const u16*)d_in[9], (const u16*)d_in[10], (const u16*)d_in[11], (u16*)d_out,
                                           (char*)d_ws);
}
```

```cpp
#include <hip/hip_runtime.h>
#include <stdint.h>

#define PRA 2
#define PRB 0
#define PFD 5
namespace {

constexpr int SEQ = 2048;
constexpr int DM = 576;
constexpr int NM = 128;
constexpr int FF = 4608;

constexpr int NTHR = 768;
constexpr int NCONV = 192;
constexpr int NLN = 128;
constexpr int NFFN = 64;
constexpr int RED0 = 48;

typedef unsigned short u16;
#define LAS __attribute__((address_space(3)))
using half8 = __attribute__((ext_vector_type(8))) _Float16;
using half2v = __attribute__((ext_vector_type(2))) _Float16;
using bf2v = __attribute__((ext_vector_type(2))) __bf16;
using bf8v = __attribute__((ext_vector_type(8))) __bf16;
using s16x4 = __attribute__((ext_vector_type(4))) short;
using f32x2 = __attribute__((ext_vector_type(2))) float;
using f32x4 = __attribute__((ext_vector_type(4))) float;
using u32x2 = __attribute__((ext_vector_type(2))) unsigned;
using u32x4 = __attribute__((ext_vector_type(4))) unsigned;

constexpr int WS_EPOCH = 0;
constexpr int WS_RG = 4096;
constexpr int WS_XS = WS_RG + 8192;
constexpr int WS_HS = WS_XS + 8 * 1536;
constexpr int WS_UP = 65536;
constexpr int WS_ZP = WS_UP + NCONV * NLN * 128;
constexpr int WS_PART = WS_ZP + NCONV * NLN * 128;
constexpr int WS_END = WS_PART + NFFN * 8 * DM * 4;
constexpr size_t WS_TS = 64u << 20;
constexpr int SC1 = 16, NT = 2;
constexpr int SPIN_CAP = 1 << 16;

#define TW(k)
#define TWDECL
#define TSX(k, s)
#define TWFLUSH()
#define TC(k, s)
#define TS(k, s)

__device__ __forceinline__ float bflo(unsigned v) { return __uint_as_float(v << 16); }
__device__ __forceinline__ float bfhi(unsigned v) { return __uint_as_float(v & 0xffff0000u); }
__device__ __forceinline__ unsigned pkbf(float a, float b) {
  f32x2 f = {a, b};
  return __builtin_bit_cast(unsigned, __builtin_convertvector(f, bf2v));
}
__device__ __forceinline__ unsigned pkh(float a, float b) {
  f32x2 f = {a, b};
  return __builtin_bit_cast(unsigned, __builtin_convertvector(f, half2v));
}
__device__ __forceinline__ float rbf(float f) { return __uint_as_float(pkbf(0.f, f)); }
__device__ __forceinline__ f32x2 rbf2(f32x2 v) { return f32x2{rbf(v[0]), rbf(v[1])}; }
__device__ __forceinline__ f32x2 unpk(unsigned v) { return f32x2{bflo(v), bfhi(v)}; }
__device__ __forceinline__ float dot2(unsigned a, unsigned b, float c) {
  return __builtin_amdgcn_fdot2_f32_bf16(__builtin_bit_cast(bf2v, a), __builtin_bit_cast(bf2v, b), c, false);
}
__device__ __forceinline__ f32x2 h2_times(unsigned h2, float s) {
  float a, b;
  asm("v_fma_mix_f32 %0, %1, %2, 0 op_sel_hi:[1,0,0]" : "=v"(a) : "v"(h2), "s"(s));
  asm("v_fma_mix_f32 %0, %1, %2, 0 op_sel:[1,0,0] op_sel_hi:[1,0,0]" : "=v"(b) : "v"(h2), "s"(s));
  return f32x2{a, b};
}
__device__ __forceinline__ float hadd(f32x2 v) {
  float a = v[0], b = v[1];
  asm volatile("" : "+v"(a), "+v"(b));
  return a + b;
}

template <int CTRL>
__device__ __forceinline__ float dppf(float v) {
  return __int_as_float(__builtin_amdgcn_update_dpp(0, __float_as_int(v), CTRL, 0xf, 0xf, true));
}
__device__ __forceinline__ float swap16_sum(float v) {
  auto r = __builtin_amdgcn_permlane16_swap(__float_as_uint(v), __float_as_uint(v), false, false);
  return __uint_as_float(r[0]) + __uint_as_float(r[1]);
}
__device__ __forceinline__ float swap32_sum(float v) {
  auto r = __builtin_amdgcn_permlane32_swap(__float_as_uint(v), __float_as_uint(v), false, false);
  return __uint_as_float(r[0]) + __uint_as_float(r[1]);
}
__device__ __forceinline__ float red32_sum(float v) {
  v += dppf<0xB1>(v);
  v += dppf<0x4E>(v);
  v += dppf<0x141>(v);
  v += dppf<0x140>(v);
  return swap16_sum(v);
}
#define SCHED_FENCE() __builtin_amdgcn_sched_barrier(0)
#define DRAIN() asm volatile("s_waitcnt vmcnt(0)" ::: "memory")

__device__ __forceinline__ void ln_row(const f32x2 (&v)[9], f32x2 (&n)[9]) {
  f32x2 s = v[0];
#pragma unroll
  for (int k = 1; k < 9; k++) s += v[k];
  const float mean = red32_sum(hadd(s)) * (1.0f / DM);
  f32x2 q = {0.f, 0.f};
#pragma unroll
  for (int k = 0; k < 9; k++) {
    const f32x2 d = v[k] - mean;
    q = __builtin_elementwise_fma(d, d, q);
  }
  const float mbf = rbf(mean);
#pragma unroll
  for (int k = 0; k < 9; k++) n[k] = rbf2(v[k] - mbf);
  const float var = red32_sum(hadd(q)) * (1.0f / DM);
  const float sbf = rbf(__builtin_amdgcn_sqrtf(rbf(rbf(var) + 1e-5f)));
  const float rinv = __builtin_amdgcn_rcpf(sbf);
#pragma unroll
  for (int k = 0; k < 9; k++) n[k] *= rinv;
}

constexpr int TAB_STRIDE = 544;
constexpr int TAB_ROWS = 56;
constexpr int SM_US = TAB_ROWS * TAB_STRIDE;
constexpr int UPAD = 256;
constexpr int SM_KC = SM_US + (UPAD + SEQ) * 2;
constexpr int RDS = 336 * 4;
constexpr int KC_RD = SM_KC;
constexpr int KC_R5 = KC_RD + 4 * RDS;
constexpr int KC_R3 = KC_R5 + 64 * 4;
constexpr int KC_R9 = KC_R3 + 64 * 4;
constexpr int KC_R7 = KC_R9 + 96 * 4;
constexpr int KC_F1024 = KC_R7 + 96 * 4;
constexpr int CH_BYTES = KC_F1024 + 16;
constexpr int KC_CHUNKS = (CH_BYTES - SM_KC) / 16;
constexpr int YT = 1280;
constexpr int SM_YD = 0, SM_Y2 = 8 * YT - 2 * YT, SM_Y3 = 14 * YT - 4 * YT;
constexpr int SM_TL = 3 * CH_BYTES;
constexpr int TLS = 580;
constexpr int SM_MISC = SM_TL + 8 * TLS * 4;
constexpr int SM_TOTAL = SM_MISC + 128 + 1024;
constexpr int FS_STAGE = 0;
constexpr int DS = 1184;
constexpr int FS_XS = 72 * DS;
constexpr int FS_RED = FS_XS + 8 * DS;
constexpr int FS_GU = FS_RED + 4 * 9 * 512;
static_assert(FS_GU + 8 * 160 <= SM_TOTAL, "lds");

typedef f32x2 cf;
struct cstep { cf s, r; };
__device__ __forceinline__ cstep mkstep(cf a) { return cstep{a, cf{-a[1], a[0]}}; }
__device__ __forceinline__ cf cmul(cf a, const cstep& b) {
  cf t, o;
  asm("v_pk_mul_f32 %0, %1, %2 op_sel:[1,0]" : "=v"(t) : "v"(a), "v"(b.r));
  asm("v_pk_fma_f32 %0, %1, %2, %3 op_sel_hi:[0,1,1]" : "=v"(o) : "v"(a), "v"(b.s), "v"(t));
  return o;
}
__device__ __forceinline__ unsigned pkc(cf a) { return __builtin_bit_cast(unsigned, __builtin_convertvector(a, half2v)); }
__device__ __forceinline__ cf cexp_p2(float p, float ch, float cl, float wh, float wl) {
  float mag = __builtin_amdgcn_exp2f(p * ch);
  mag = fmaf(mag, (p * 0.693147181f) * cl, mag);
  const float rh = p * wh;
  const float fr = fmaf(p, wl, rh - rintf(rh));
  return cf{mag * __builtin_amdgcn_cosf(fr), mag * __builtin_amdgcn_sinf(fr)};
}
__device__ __forceinline__ float exp_acc(float x) {
  const float th = x * 1.44269502f;
  const float tl = fmaf(x, 1.92596299e-8f, fmaf(x, 1.44269502f, -th));
  const float e = __builtin_amdgcn_exp2f(th);
  return fmaf(e, tl * 0.693147181f, e);
}
__device__ __forceinline__ unsigned wave_max_u32(unsigned v) {
  v = max(v, (unsigned)__builtin_amdgcn_update_dpp(0, (int)v, 0x111, 0xf, 0xf, false));
  v = max(v, (unsigned)__builtin_amdgcn_update_dpp(0, (int)v, 0x112, 0xf, 0xf, false));
  v = max(v, (unsigned)__builtin_amdgcn_update_dpp(0, (int)v, 0x114, 0xf, 0xf, false));
  v = max(v, (unsigned)__builtin_amdgcn_update_dpp(0, (int)v, 0x118, 0xf, 0xf, false));
  v = max(v, (unsigned)__builtin_amdgcn_update_dpp(0, (int)v, 0x142, 0xa, 0xf, false));
  v = max(v, (unsigned)__builtin_amdgcn_update_dpp(0, (int)v, 0x143, 0xc, 0xf, false));
  return (unsigned)__builtin_amdgcn_readlane((int)v, 63);
}

#define MFMA16(a, b, c) __builtin_amdgcn_mfma_f32_16x16x32_f16((a), (b), (c), 0, 0, 0)
#define MFMAB(a, b, c) __builtin_amdgcn_mfma_f32_16x16x32_bf16((a), (b), (c), 0, 0, 0)

__device__ __forceinline__ half8 ld_frag(const LAS char* p) { return *(const LAS half8*)p; }
__device__ __forceinline__ const LAS unsigned* opaque_lds(const LAS char* p) {
  const LAS unsigned* q = (const LAS unsigned*)p;
  asm volatile("" : "+v"(q));
  return q;
}
template <int O2>
__device__ __forceinline__ half8 ld_toep(const LAS unsigned* q) {
  u32x4 v = {q[0], q[1], q[O2], q[O2 + 1]};
  return __builtin_bit_cast(half8, v);
}
__device__ __forceinline__ half8 band_frag(int lane, unsigned pat) {
  const int bits = (int)((pat << (lane & 15)) >> (8 * (lane >> 4)));
  u32x4 v;
#pragma unroll
  for (int j = 0; j < 4; j++) {
    const unsigned lo = (unsigned)__builtin_amdgcn_sbfe(bits, 2 * j, 1), hi = (unsigned)__builtin_amdgcn_sbfe(bits, 2 * j + 1, 1);
    v[j] = __builtin_amdgcn_perm(hi, lo, 0x05040100u) & 0x3c003c00u;
  }
  return __builtin_bit_cast(half8, v);
}
__device__ __forceinline__ half8 pack_win(f32x4 a, f32x4 b) {
  u32x4 v = {pkh(a[0], a[1]), pkh(a[2], a[3]), pkh(b[0], b[1]), pkh(b[2], b[3])};
  return __builtin_bit_cast(half8, v);
}

template <bool HI>
__device__ __forceinline__ void conv_dense(const half8 (&td)[17], half8 te, const LAS char* ub, LAS char* yd) {
  constexpr int S0 = HI ? 0 : 9, NS = 41, PF = PFD;
  const f32x4 zero4 = {0.f, 0.f, 0.f, 0.f};
  f32x4 acc[4] = {zero4, zero4, zero4, zero4};
  half8 u[NS], ue[2];
#pragma unroll
  for (int s = S0; s < S0 + PF; s++) u[s] = ld_frag(ub + 32 * (-33 + 2 * s));
  SCHED_FENCE();
#pragma unroll
  for (int s = S0; s < NS; s++) {
    if (s + PF < NS) u[s + PF] = ld_frag(ub + 32 * (-33 + 2 * (s + PF)));
    if (HI && (s == 0 || s == 2)) ue[s >> 1] = ld_frag(ub + 32 * (-65 + 8 * s));
#pragma unroll
    for (int t = 0; t < 4; t++) {
      const int i = 16 + 8 * t - s;
      if (i >= 0 && i <= 16) acc[t] = MFMA16(td[i], u[s], acc[t]);
    }
    if (HI) {
      if (s == 0) acc[2] = MFMA16(te, u[0], acc[2]);
      if (s == 8) acc[3] = MFMA16(te, u[8], acc[3]);
      if (s == 5) acc[0] = MFMA16(te, ue[0], acc[0]);
      if (s == 7) acc[1] = MFMA16(te, ue[1], acc[1]);
    }
    if (s >= 18 && ((s - 18) & 7) == 0) *(LAS f32x4*)(yd + ((s - 18) >> 3) * YT) = acc[(s - 18) >> 3];
    SCHED_FENCE();
  }
  *(LAS f32x4*)(yd + 3 * YT) = acc[3];
}

typedef __amdgpu_buffer_rsrc_t rsrc_t;
__device__ __forceinline__ u32x4 ldg16(rsrc_t rs, int off) {
  asm volatile("" : "+v"(off) : : "memory");
  return __builtin_amdgcn_raw_buffer_load_b128(rs, off, 0, SC1);
}
__device__ __forceinline__ void stg16(rsrc_t rs, int off, u32x4 v) { __builtin_amdgcn_raw_buffer_store_b128(v, rs, off, 0, SC1); }
__device__ __forceinline__ void wait_ticks(int t) {
  const unsigned long long t0 = __builtin_amdgcn_s_memrealtime();
  while (__builtin_amdgcn_s_memrealtime() - t0 < (unsigned long long)t) __builtin_amdgcn_s_sleep(1);
}
#define UPOS 1
#define DLY_X 60
#define DLY_P 100

__device__ __forceinline__ void ffn_role(const u16* __restrict__ wg, const u16* __restrict__ wu, const u16* __restrict__ wd,
                                         char* ws, rsrc_t rs, unsigned tagv, LAS char* L) {
  const int tid = threadIdx.x, lane = tid & 63;
  const int wave = __builtin_amdgcn_readfirstlane(tid >> 6);
  const int fw = blockIdx.x - NCONV;
  const int j = (fw & 7) * 8 + (fw >> 3);
  const int r0 = tid / 18, rem = tid - r0 * 18;
  const int pc = rem >= 9 ? rem - 9 : rem;
  const char* gsrc = (const char*)(rem >= 9 ? wu : wg) + (unsigned)((r0 < 42 ? r0 : 0) * (FF * 2) + j * 144 + pc * 16);
  u32x4 wa[7], wb[7], wc[7];
#pragma unroll
  for (int i = 0; i < 7; i++) wa[i] = __builtin_nontemporal_load((const u32x4*)(gsrc + (42 * i) * (FF * 2)));
  SCHED_FENCE();
#pragma unroll
  for (int i = 0; i < 7; i++)
    wb[i] = __builtin_nontemporal_load((const u32x4*)(gsrc + ((i < 6 || r0 < 36) ? (288 + 42 * i) * (FF * 2) : 0)));
  SCHED_FENCE();
  {
    const char* dsrc = (const char*)wd + (size_t)j * (72 * 1152);
#pragma unroll
    for (int i = 0; i < 7; i++)
      wc[i] = __builtin_nontemporal_load((const u32x4*)(dsrc + (unsigned)((i < 6 || tid < 576) ? (tid + NTHR * i) * 16 : 0)));
  }
  SCHED_FENCE();
  if (tid < 8 * 4) *(LAS unsigned*)(L + FS_GU + (tid >> 2) * 160 + 144 + (tid & 3) * 4) = 0u;
  const int g = lane >> 4, tq = (lane & 15) >> 2, tp = lane & 3;
  const int tg = wave % 3, kq = wave / 3;
  s16x4 fa[3][5][2];
  auto stage_gu = [&](const u32x4 (&w)[7]) {
#pragma unroll
    for (int i = 0; i < 7; i++)
      if (tid < 756 && r0 + 42 * i < 288) *(LAS u32x4*)(L + FS_STAGE + (r0 + 42 * i) * 288 + rem * 16) = w[i];
  };
  auto read_gu = [&]() {
    const LAS char* p = L + FS_STAGE + (160 * (kq & 1) + 8 * g + tq) * 288 + (tp >> 1) * 144 + (tp & 1) * 8 + 48 * tg;
#pragma unroll
    for (int ks = 0; ks < 5; ks++) {
      const int kk = (ks == 4) ? ((kq & 1) ? 3 : 4) : ks;
#pragma unroll
      for (int t = 0; t < 3; t++)
#pragma unroll
        for (int h = 0; h < 2; h++)
          fa[t][ks][h] = __builtin_amdgcn_ds_read_tr16_b64_v4i16((LAS s16x4*)(p + (32 * kk + 4 * h) * 288 + 16 * t));
    }
  };
  stage_gu(wa);
  __syncthreads();
  if (kq < 2) read_gu();
  __syncthreads();
  stage_gu(wb);
  __syncthreads();
  if (kq >= 2) read_gu();
  __syncthreads();
  TS(1, 1);
#pragma unroll
  for (int i = 0; i < 7; i++) {
    const int e = tid + NTHR * i;
    const int row = e / 72;
    if (e < 5184) *(LAS u32x4*)(L + FS_STAGE + row * DS + (e - row * 72) * 16) = wc[i];
  }
  __syncthreads();
  s16x4 fd[3][5];
  {
    const LAS char* p = L + FS_STAGE + tq * DS + (48 * wave + 4 * tp) * 2;
#pragma unroll
    for (int t = 0; t < 3; t++) {
#pragma unroll
      for (int h = 0; h < 4; h++)
        fd[t][h] = __builtin_amdgcn_ds_read_tr16_b64_v4i16((LAS s16x4*)(p + (32 * (h >> 1) + 8 * g + 4 * (h & 1)) * DS + 32 * t));
      fd[t][4] = __builtin_amdgcn_ds_read_tr16_b64_v4i16((LAS s16x4*)(p + (64 + 4 * (g & 1)) * DS + 32 * t));
    }
  }
  const unsigned tag = __builtin_amdgcn_readfirstlane(tagv) + 1u;
  TS(1, 2);
  if (wave == 0) {
    _Pragma("unroll 1") for (int spin = 0; spin < SPIN_CAP; spin++) {
      const unsigned t = ldg16(rs, WS_RG + (fw * 16) * 8)[1];
      if (__builtin_amdgcn_readfirstlane(t) == tag) break;
      __builtin_amdgcn_s_sleep(4);
    }
    wait_ticks(DLY_X);
  }
  __syncthreads();
  TS(1, 3);
  {
    u32x4 v;
    _Pragma("unroll 1") for (int spin = 0; spin < SPIN_CAP; spin++) {
      v = ldg16(rs, WS_XS + tid * 16);
      if (__all(v[3] == tag)) break;
      __builtin_amdgcn_s_sleep(2);
    }
    const int slot = tid / 96, rm = tid - slot * 96;
    LAS unsigned* dst = (LAS unsigned*)(L + FS_XS + slot * DS) + (rm & 31) + 96 * (rm >> 5);
    dst[0] = v[0];
    dst[32] = v[1];
    dst[64] = v[2];
  }
  __syncthreads();
  TS(1, 4);
  {
    const f32x4 zero4 = {0.f, 0.f, 0.f, 0.f};
    f32x4 acc[3] = {zero4, zero4, zero4};
    const LAS char* xp = L + FS_XS + (lane & 7) * DS + 16 * g + 64 * (9 * (kq >> 1) + 5 * (kq & 1));
    bf8v xf[5];
#pragma unroll
    for (int ks = 0; ks < 5; ks++) xf[ks] = *(const LAS bf8v*)(xp + 64 * ks);
#pragma unroll
    for (int ks = 0; ks < 5; ks++) {
      if (ks < 4 || !(kq & 1)) {
#pragma unroll
        for (int t = 0; t < 3; t++) {
          const bf8v a = __builtin_bit_cast(bf8v, __builtin_shufflevector(fa[t][ks][0], fa[t][ks][1], 0, 1, 2, 3, 4, 5, 6, 7));
          acc[t] = MFMAB(a, xf[ks], acc[t]);
        }
      }
    }
    if ((lane & 15) < 8) {
#pragma unroll
      for (int t = 0; t < 3; t++) *(LAS f32x4*)(L + FS_RED + ((kq * 9 + 3 * tg + t) * 8 + (lane & 7)) * 64 + 16 * g) = acc[t];
    }
  }
  __syncthreads();
  if (wave < 9) {
    const LAS float* rp = (const LAS float*)(L + FS_RED) + (wave * 8 + (lane >> 3)) * 16 + (lane & 7);
    const float gs = ((rp[0] + rp[1152]) + rp[2304]) + rp[3456];
    const float us = ((rp[8] + rp[1160]) + rp[2312]) + rp[3464];
    const float gp = rbf(gs);
    const float gq = rbf(gp * __builtin_amdgcn_rcpf(1.0f + __expf(-gp)));
    *(LAS u16*)(L + FS_GU + (lane >> 3) * 160 + (wave * 8 + (lane & 7)) * 2) = (u16)pkbf(gq * rbf(us), 0.f);
  }
  __syncthreads();
  {
    const LAS char* gp = L + FS_GU + (lane & 7) * 160;
    const bf8v b0 = *(const LAS bf8v*)(gp + 16 * g), b1 = *(const LAS bf8v*)(gp + 64 + 16 * g);
    const s16x4 b2 = *(const LAS s16x4*)(gp + 128 + 8 * g);
#pragma unroll
    for (int t = 0; t < 3; t++) {
      f32x4 acc = {0.f, 0.f, 0.f, 0.f};
      acc = __builtin_amdgcn_mfma_f32_16x16x16bf16_1k(fd[t][4], b2, acc, 0, 0, 0);
      f32x4 acc2 = {0.f, 0.f, 0.f, 0.f};
      acc2 = MFMAB(__builtin_bit_cast(bf8v, __builtin_shufflevector(fd[t][0], fd[t][1], 0, 1, 2, 3, 4, 5, 6, 7)), b0, acc2);
      acc2 = MFMAB(__builtin_bit_cast(bf8v, __builtin_shufflevector(fd[t][2], fd[t][3], 0, 1, 2, 3, 4, 5, 6, 7)), b1, acc2);
      u32x4 o;
#pragma unroll
      for (int r = 0; r < 4; r++) o[r] = min(__float_as_uint(acc[r] + acc2[r]), 0xfffffffeu);
      if ((lane & 15) < 8) stg16(rs, WS_PART + fw * (8 * DM * 4) + (3 * wave + t) * 512 + (lane & 7) * 64 + 16 * g, o);
    }
  }
  TS(1, 5);
  TC(1, 15);
}

__global__ __launch_bounds__(NTHR, 3) void k_all(const u16* __restrict__ x, const u16* __restrict__ lar,
                                                 const u16* __restrict__ aim, const u16* __restrict__ crl,
                                                 const u16* __restrict__ cim, const u16* __restrict__ ldt,
                                                 const u16* __restrict__ dskip, const u16* __restrict__ wr,
                                                 const u16* __restrict__ wg, const u16* __restrict__ wu,
                                                 const u16* __restrict__ wd, u16* __restrict__ out,
                                                 char* ws) {
  __shared__ __attribute__((aligned(16))) char smem[SM_TOTAL];
  LAS char* const L = (LAS char*)smem;
  asm volatile("" ::"s"(x), "s"(lar), "s"(aim), "s"(crl), "s"(cim), "s"(ldt), "s"(dskip), "s"(wr), "s"(wg), "s"(wu), "s"(wd),
               "s"(out), "s"(ws));
  const int tid = threadIdx.x, lane = tid & 63, b = blockIdx.x;
  const int wave = __builtin_amdgcn_readfirstlane(tid >> 6);
  const rsrc_t rs = __builtin_amdgcn_make_buffer_rsrc(ws, 0, WS_END, 0x00020000);
  const unsigned tagv = __builtin_amdgcn_raw_buffer_load_b32(rs, WS_EPOCH, 0, SC1);
  if (b >= NCONV) {
    TS(1, 0);
  TC(1, 14);
    ffn_role(wg, wu, wd, ws, rs, tagv, L);
    return;
  }
  TWDECL;
  TS(0, 0);
  TC(0, 14);
  __builtin_amdgcn_s_setprio(3);
  const bool is_ln = b < NLN;
  const bool ln_wave = is_ln && wave < 8;
  const int hl = lane & 31, rsel = lane >> 5;
  const int row = b * 16 + wave * 2 + rsel;
  unsigned xw[9];
  if (ln_wave) {
    const char* xr = (const char*)x + (unsigned)(row * (DM * 2) + hl * 4);
#pragma unroll
    for (int k = 0; k < 9; k++) xw[k] = *(const unsigned*)(xr + 128 * k);
  }
  const int ch = (wave & 3) < 3 ? (wave & 3) : (wave >> 2), sub = (wave & 3) < 3 ? (wave >> 2) : 3;
  const int d = 3 * b + ch;
  const int n = (sub & 1) * 64 + lane;
  const unsigned po = (unsigned)(d * NM + n), po2 = po ^ 64u;
  const u16 r_lar = lar[po], r_aim = aim[po], r_cr = crl[po], r_ci = cim[po], r_cr2 = crl[po2], r_ci2 = cim[po2];
  const u16 r_ldt = ldt[(unsigned)d], r_dsk = dskip[(unsigned)(3 * b + (lane < 3 ? lane : 0))];
  SCHED_FENCE();
  const int part_off = WS_PART + (tid >> 3) * (8 * DM * 4) + (b - RED0) * 128 + (tid & 7) * 16;
  LAS char* const C = L + ch * CH_BYTES;
  {
    const int ct = tid & 255;
    LAS char* zc = L + (wave >> 2) * CH_BYTES;
    const u32x4 z = {0u, 0u, 0u, 0u};
    *(LAS u32x4*)(zc + SM_KC + 16 * ct) = z;
    if (ct < KC_CHUNKS - 256) *(LAS u32x4*)(zc + SM_KC + 4096 + 16 * ct) = z;
    else if (ct >= 256 - UPAD / 8) *(LAS u32x4*)(zc + SM_US + 16 * (ct - (256 - UPAD / 8))) = z;
    if (tid < 16) *(LAS unsigned*)(L + SM_MISC + 64 + 4 * tid) = 0u;
  }
  half8 bA = band_frag(lane, sub == 3 ? 0x1fu << 6 : (sub == 2 ? 0x1ffu << 4 : 0x3u << 12));
  half8 bB = band_frag(lane, sub == 3 ? 0x7u << 11 : 0x7fu << 5);
  asm volatile("" : "+v"(bA), "+v"(bB));
  SCHED_FENCE();
  const unsigned tag = __builtin_amdgcn_readfirstlane(tagv) + 1u;
  LAS unsigned* const tl = (LAS unsigned*)(L + SM_TL);
  const unsigned hsel = rsel ? 0x07060302u : 0x05040100u;
  float f_lar = bflo(r_lar), f_aim = bflo(r_aim), f_cr = bflo(r_cr), f_ci = bflo(r_ci), f_ldt = bflo(r_ldt), f_dsk = bflo(r_dsk);
  float f_cm = fmaxf(fmaxf(fabsf(f_cr), fabsf(f_ci)), fmaxf(fabsf(bflo(r_cr2)), fabsf(bflo(r_ci2))));
  float f_k0 = f_cr + bflo(r_cr2);
  asm volatile("" : "+v"(f_lar), "+v"(f_aim), "+v"(f_cr), "+v"(f_ci), "+v"(f_cm), "+v"(f_ldt), "+v"(f_dsk), "+v"(f_k0));
  float dsk[3];
#pragma unroll
  for (int c = 0; c < 3; c++) dsk[c] = __int_as_float(__builtin_amdgcn_readlane(__float_as_int(f_dsk), c));
  if (b >= RED0 && tid < 512) stg16(rs, part_off, u32x4{~0u, ~0u, ~0u, ~0u});
  if (is_ln) {
    if (wave < 8) {
      f32x2 xf[9], un[9];
#pragma unroll
      for (int k = 0; k < 9; k++) xf[k] = unpk(xw[k]);
      ln_row(xf, un);
#pragma unroll
      for (int k = 0; k < 9; k++) {
        const unsigned hp = pkc(rbf2(un[k]));
        auto r = __builtin_amdgcn_permlane32_swap(hp, hp, false, false);
        tl[wave * TLS + 2 * (hl + 32 * k) + rsel] = __builtin_amdgcn_perm(r[1], r[0], hsel);
      }
    }
    __syncthreads();
#pragma unroll
    for (int i = 0; i < 2; i++) {
      const int e = tid + NTHR * i;
      const LAS unsigned* s = tl + (e & 7) * TLS + 3 * (e >> 3);
      stg16(rs, WS_UP + ((e >> 3) * NLN + b) * 128 + (e & 7) * 16, u32x4{s[0], s[1], s[2], tag});
    }
  }
  __builtin_amdgcn_s_setprio(0);
  TSX(0, 1);
  TW(2);
  float inv_scale;
  {
    const float dt = rbf(exp_acc(f_ldt));
    const float are = -rbf(exp_acc(f_lar)) * dt;
    const float aie = f_aim * dt;
    const float chh = are * 1.44269502f;
    const float cl = fmaf(are, 1.92596299e-8f, fmaf(are, 1.44269502f, -chh));
    const float wh = aie * 0.159154936f;
    const float wl = fmaf(aie, 7.13048051e-9f, fmaf(aie, 0.159154936f, -wh));
    const unsigned cmax = wave_max_u32(__float_as_uint(f_cm));
    int se = 385 - (int)(cmax >> 23) - (int)(__float_as_uint(dt) >> 23);
    se = min(max(se, 2), 252);
    const float scale2 = __uint_as_float((unsigned)(se + 1) << 23);
    inv_scale = __uint_as_float((unsigned)(254 - se) << 23);
    const cf cc = {f_cr * dt * scale2, -f_ci * dt * scale2};
    volatile LAS unsigned* tab = (volatile LAS unsigned*)C + n;
    constexpr int TS4 = TAB_STRIDE / 4;
    if (sub < 2) {
      const cstep e1 = mkstep(cexp_p2(1.f, chh, cl, -wh, -wl)), e128 = mkstep(cexp_p2(128.f, chh, cl, -wh, -wl));
      const cstep e2 = mkstep(cmul(e1.s, e1)), e4 = mkstep(cmul(e2.s, e2));
      cf first = cmul(cc, e1);
#pragma unroll
      for (int a = 0; a < 8; a++) {
        const int cnt = a < 2 ? 8 : (a < 4 ? 4 : 2);
        const int r0 = a < 2 ? 8 * a : (a < 4 ? 16 + 4 * (a - 2) : 24 + 2 * (a - 4));
        const cstep st = a < 2 ? e1 : (a < 4 ? e2 : e4);
        cf v = first;
#pragma unroll
        for (int jj = 0; jj < cnt; jj++) {
          tab[(r0 + jj) * TS4] = pkc(v);
          if (a < 7 && jj == ((a == 1 || a == 3) ? 1 : 0)) first = cmul(v, e128);
          if (jj + 1 < cnt) v = cmul(v, st);
        }
      }
    } else {
      const cstep e8 = mkstep(cexp_p2(8.f, chh, cl, wh, wl)), e128 = mkstep(cexp_p2(128.f, chh, cl, -wh, -wl));
      cf bb = cmul(cmul(cc, mkstep(cexp_p2(1024.f, chh, cl, -wh, -wl))), mkstep(cf{e8.s[0], e8.r[0]}));
      cf h = e8.s;
      tab[40 * TS4] = pkh(1.f, 0.f);
#pragma unroll
      for (int i = 0; i < 15; i++) {
        tab[(41 + i) * TS4] = pkc(h);
        if (i < 14) h = cmul(h, e8);
        if (i < 8) {
          tab[(32 + i) * TS4] = pkc(bb);
          if (i < 7) bb = cmul(bb, e128);
        }
      }
    }
    f_k0 *= dt * scale2;
  }
  TW(3);
  __syncthreads();
  const int uoff = WS_UP + b * (NLN * 128) + tid * 16;
  u32x4 v0, v1;
  TSX(0, 2);
  const int c15 = lane & 15, g = lane >> 4;
  if (sub < 3) {
    f32x4 f = {0.f, 0.f, 0.f, 0.f};
    const LAS char* pa = C + (sub == 0 ? c15 : (sub == 1 ? 16 + (c15 & 7) : 24 + c15)) * TAB_STRIDE + 16 * g;
    const LAS char* pb = C + (40 + c15) * TAB_STRIDE + 16 * g;
    half8 fa[8], fb[8];
#pragma unroll
    for (int ks = 0; ks < 8; ks++) { fa[ks] = ld_frag(pa + 64 * ks); fb[ks] = ld_frag(pb + 64 * ks); }
    SCHED_FENCE();
#pragma unroll
    for (int ks = 0; ks < 8; ks++) f = MFMA16(fa[ks], fb[ks], f);
    const unsigned f10 = pkh(f[1], f[0]), f32 = pkh(f[3], f[2]);
    auto w16 = [&](int off, unsigned v) { *(volatile LAS u16*)(C + off) = (u16)v; };
    auto w32 = [&](int off, unsigned v) { *(volatile LAS unsigned*)(C + off) = v; };
    auto w64 = [&](int off, unsigned a, unsigned c) { *(volatile LAS u32x2*)(C + off) = u32x2{a, c}; };
    const unsigned f21 = __builtin_amdgcn_alignbit(f10, f32, 16);
    if (sub == 0) {
      const int o = KC_RD - 2 * (128 * (g >> 1) + 4 * (g & 1) + 8 * c15);
      w16(o + 2 * 539, f32);
      w32(o + 2 * 540, f21);
      w16(o + 2 * 542, f10 >> 16);
      w32(o + RDS + 2 * 538, f32);
      w32(o + RDS + 2 * 540, f10);
      w16(o + 2 * RDS + 2 * 537, f32);
      w32(o + 2 * RDS + 2 * 538, f21);
      w16(o + 2 * RDS + 2 * 540, f10 >> 16);
      w64(o + 3 * RDS + 2 * 536, f32, f10);
      const unsigned x = (unsigned)__builtin_amdgcn_readlane((int)f32, 63);
      if (lane < 4) w16(KC_RD + lane * (RDS - 2) + 2 * (543 - 257), x);
    } else if (sub == 1) {
      const unsigned f33 = __builtin_amdgcn_perm(f32, f32, 0x01000100u), f11 = __builtin_amdgcn_perm(f10, f10, 0x01000100u);
      const int o = KC_RD - 2 * (256 + 128 * (g & 1) + 8 * c15) + (g >> 1) * (2 * RDS - 4);
      w32(o + 2 * 534, f33);
      w32(o + 2 * 536, f32);
      w32(o + 2 * 538, f11);
      w32(o + 2 * 540, f10);
      w16(o + RDS + 2 * 533, f32);
      w32(o + RDS + 2 * 534, f33);
      w32(o + RDS + 2 * 536, f21);
      w32(o + RDS + 2 * 538, f11);
      w16(o + RDS + 2 * 540, f10 >> 16);
      const unsigned x = (unsigned)__builtin_amdgcn_readlane((int)f32, 31);
      if (lane < 4) w16(KC_RD + lane * (RDS - 2) + 2 * (543 - 514), x);
    } else if (g < 2) {
      const int o = 2 * (95 - 32 * g - c15);
      w16(KC_R3 + o, f10 >> 16);
      w16(KC_R5 + o, f10);
      w16(KC_R3 + o - 32, f32 >> 16);
      w16(KC_R5 + o - 32, f32);
      if (lane == 31) w16(KC_F1024, f32);
    } else {
      const int o = ((c15 & 1) ? KC_R9 : KC_R7) + 2 * 63 - 64 * (g - 2) - (c15 & 14);
      w16(o, f10 >> 16);
      w16(o + 190, f10 >> 16);
      w16(o - 16, f10);
      w16(o - 16 + 190, f10);
      w16(o - 32, f32 >> 16);
      w16(o - 32 + 190, f32 >> 16);
      w16(o - 48, f32);
      w16(o - 48 + 190, f32);
    }
  } else {
    const float k0 = swap32_sum(red32_sum(f_k0));
    if (lane < 4) *(LAS u16*)(C + KC_RD + lane * (RDS - 2) + 2 * 543) = (u16)pkh(k0, k0);
    if (lane == 0) *(LAS float*)(C + KC_F1024 + 8) = inv_scale;
  }
  TW(4);
  __syncthreads();
  v0 = ldg16(rs, uoff); v1 = ldg16(rs, wave < 4 ? uoff + NTHR * 16 : uoff);
  asm volatile("" ::: "memory");
  SCHED_FENCE();
  TSX(0, 3);
  half8 td[17];
  int iscv[3];
  if (sub < 2) {
    const volatile LAS u32x2* tb = (const volatile LAS u32x2*)(C + KC_RD + ((3 - c15) & 3) * RDS + 2 * (540 - 16 - (c15 & 12) + 8 * g) - 64 * 16);
#pragma unroll
    for (int i = 16; i >= 0; i--) {
      const u32x2 lo = tb[8 * (16 - i)], hi = tb[8 * (16 - i) + 1];
      td[i] = __builtin_bit_cast(half8, __builtin_shufflevector(lo, hi, 0, 1, 2, 3));
    }
    bA = bA * *(const LAS _Float16*)(C + KC_F1024);
  } else if (sub == 2) {
    const int c = (c15 + 1) & 1;
    const LAS unsigned* t9 = opaque_lds(C + KC_R9 + 192 * c + 2 * (127 - 64 - c15 + 4 * g - c) - 96);
#pragma unroll
    for (int e = 0; e < 4; e++) {
      td[e] = ld_toep<8>(t9 + 8 * (3 - e));
      td[4 + e] = ld_toep<8>(t9 + (KC_R7 - KC_R9) / 4 + 8 * (3 - e));
    }
  } else {
    const LAS unsigned* t5 = opaque_lds(C + KC_R5 + 2 * (158 - 64 - 2 * c15 + 4 * g) - 128);
#pragma unroll
    for (int e = 0; e < 3; e++) {
      td[e] = ld_toep<8>(t5 + 16 * (2 - e));
      td[3 + e] = ld_toep<8>(t5 + (KC_R3 - KC_R5) / 4 + 16 * (2 - e));
    }
  }
#pragma unroll
  for (int c = 0; c < 3; c++) iscv[c] = *(const LAS int*)(L + c * CH_BYTES + KC_F1024 + 8);
  TW(5);
  TSX(0, 4);
  {
    _Pragma("unroll 1") for (int spin = 0; spin < SPIN_CAP; spin++) {
      if (__all(v0[3] == tag && v1[3] == tag)) break;
      v0 = ldg16(rs, uoff);
      v1 = ldg16(rs, wave < 4 ? uoff + NTHR * 16 : uoff);
    }
    LAS char* dst = L + SM_US + 2 * UPAD + 4 * tid;
#pragma unroll
    for (int c = 0; c < 3; c++) *(LAS unsigned*)(dst + c * CH_BYTES) = v0[c];
    if (wave < 4) {
#pragma unroll
      for (int c = 0; c < 3; c++) *(LAS unsigned*)(dst + c * CH_BYTES + 4 * NTHR) = v1[c];
    }
  }
  float isc[3];
  f32x2 sb[2][3];
#pragma unroll
  for (int c = 0; c < 3; c++) {
    isc[c] = __int_as_float(__builtin_amdgcn_readfirstlane(iscv[c]));
    sb[0][c] = rbf2(h2_times(v0[c], dsk[c]));
    asm volatile("" : "+v"(sb[0][c]));
  }
  if (wave < 4) {
#pragma unroll
    for (int c = 0; c < 3; c++) {
      sb[1][c] = rbf2(h2_times(v1[c], dsk[c]));
      asm volatile("" : "+v"(sb[1][c]));
    }
  }
  const LAS char* uf16 = C + SM_US + 2 * (UPAD + (sub == 3 ? 8 : 16) * c15 + 8 * g);
  LAS char* yb = C + g * 320 + c15 * 16;
  asm volatile("" : "+v"(uf16), "+v"(yb));
  if (sub == 2) __builtin_amdgcn_s_setprio(PRA);
  __syncthreads();
  TSX(0, 5);
  TW(6);
  {
    const f32x4 zero4 = {0.f, 0.f, 0.f, 0.f};
    if (sub == 0) {
      conv_dense<true>(td, bA, uf16 + 2 * 1024, yb + SM_YD + 4 * YT);
    } else if (sub == 1) {
      conv_dense<false>(td, bA, uf16, yb + SM_YD);
    } else if (sub == 3) {
      f32x4 a2[6], w[4];
#pragma unroll
      for (int i = 0; i < 6; i++) a2[i] = zero4;
      half8 uw[12];
#pragma unroll
      for (int jj = 0; jj < 4; jj++) uw[jj] = ld_frag(uf16 - 48 + 256 * jj);
      SCHED_FENCE();
      w[0] = MFMA16(uw[0], bA, zero4); w[1] = MFMA16(uw[1], bA, zero4);
      w[2] = MFMA16(uw[0], bB, zero4); w[3] = MFMA16(uw[1], bB, zero4);
#pragma unroll
      for (int jj = 0; jj < 6; jj++) {
        if (jj < 4) {
          uw[2 * jj + 4] = ld_frag(uf16 - 48 + 256 * (2 * jj + 4));
          uw[2 * jj + 5] = ld_frag(uf16 - 48 + 256 * (2 * jj + 5));
        }
        const half8 X5 = pack_win(w[0], w[1]), X3 = pack_win(w[2], w[3]);
        SCHED_FENCE();
        if (jj < 5) {
          w[0] = MFMA16(uw[2 * jj + 2], bA, zero4); w[1] = MFMA16(uw[2 * jj + 3], bA, zero4);
          w[2] = MFMA16(uw[2 * jj + 2], bB, zero4); w[3] = MFMA16(uw[2 * jj + 3], bB, zero4);
        }
        if (jj > 0) *(LAS f32x4*)(yb + SM_Y2 + (1 + jj) * YT) = a2[jj - 1];
        SCHED_FENCE();
#pragma unroll
        for (int e = 0; e < 3; e++) {
          if (jj + e < 6) {
            a2[jj + e] = MFMA16(X5, td[e], a2[jj + e]);
            a2[jj + e] = MFMA16(X3, td[3 + e], a2[jj + e]);
          }
        }
        SCHED_FENCE();
      }
      *(LAS f32x4*)(yb + SM_Y2 + 7 * YT) = a2[5];
    } else {
      half8 uw[8];
#pragma unroll
      for (int i = 0; i < 4; i++) {
        uw[2 * i] = ld_frag(uf16 + 512 * i - 48);
        uw[2 * i + 1] = ld_frag(uf16 + 512 * i - 32);
      }
      SCHED_FENCE();
      f32x4 w[8], a3[4];
#pragma unroll
      for (int i = 0; i < 4; i++) {
        w[2 * i] = MFMA16(uw[2 * i], bA, zero4);
        w[2 * i + 1] = MFMA16(uw[2 * i + 1], bB, zero4);
      }
      const half8 X90 = pack_win(w[0], w[2]), X70 = pack_win(w[1], w[3]), X91 = pack_win(w[4], w[6]), X71 = pack_win(w[5], w[7]);
#pragma unroll
      for (int t = 0; t < 4; t++) {
        a3[t] = MFMA16(X90, td[t], zero4);
        a3[t] = MFMA16(X70, td[4 + t], a3[t]);
        if (t >= 2) {
          a3[t] = MFMA16(X91, td[t - 2], a3[t]);
          a3[t] = MFMA16(X71, td[4 + t - 2], a3[t]);
        }
      }
#pragma unroll
      for (int t = 0; t < 4; t++) *(LAS f32x4*)(yb + SM_Y3 + (4 + t) * YT) = a3[t];
      __builtin_amdgcn_s_setprio(0);
    }
  }
  TW(7);
  unsigned wrw[9];
  if (ln_wave) {
#pragma unroll
    for (int k = 0; k < 9; k++) wrw[k] = ((const unsigned*)wr)[hl + 32 * k];
    unsigned xo = (unsigned)(row * (DM * 2) + hl * 4);
    asm volatile("" : "+v"(xo));
#pragma unroll
    for (int k = 0; k < 9; k++) xw[k] = *(const unsigned*)((const char*)x + xo + 128 * k);
  }
  __syncthreads();
  TS(0, 6);
#pragma unroll
  for (int i = 0; i < 2; i++) {
    if (i == 0 || wave < 4) {
      const int e = tid + NTHR * i;
      const int nt = (wave >> 1) + 6 * i;
      const LAS char* yp = L + nt * YT + ((e >> 1) & 3) * 320 + ((e >> 3) & 15) * 16 + (e & 1) * 8;
      f32x2 y[3];
#pragma unroll
      for (int c = 0; c < 3; c++) y[c] = *(const LAS f32x2*)(yp + c * CH_BYTES + SM_YD);
      if (nt >= 2) {
#pragma unroll
        for (int c = 0; c < 3; c++) y[c] += *(const LAS f32x2*)(yp + c * CH_BYTES + SM_Y2);
      }
      if (nt >= 4) {
#pragma unroll
        for (int c = 0; c < 3; c++) y[c] += *(const LAS f32x2*)(yp + c * CH_BYTES + SM_Y3);
      }
      u32x4 o;
#pragma unroll
      for (int c = 0; c < 3; c++) {
        const f32x2 z = __builtin_elementwise_fma(rbf2(y[c]), f32x2{isc[c], isc[c]}, sb[i][c]);
        o[c] = pkbf(z[0], z[1]);
      }
      o[3] = tag;
      stg16(rs, WS_ZP + ((e >> 3) * NCONV + b) * 128 + (e & 7) * 16, o);
    }
  }
  TS(0, 7);
  TWFLUSH();
  if (is_ln) {
    f32x2 xf2[9];
#pragma unroll
    for (int k = 0; k < 9; k++) {
      xf2[k] = unpk(xw[k]);
      asm volatile("" : "+v"(xf2[k]));
    }
    unsigned zsel = rsel ? 0x3f800000u : 0x00003f80u;
    asm volatile("" : "+v"(zsel));
    DRAIN();
    {
      const int off = WS_ZP + b * (NCONV * 128) + tid * 16;
      _Pragma("unroll 1") for (int spin = 0; spin < SPIN_CAP; spin++) {
        v0 = ldg16(rs, off);
        v1 = ldg16(rs, off + NTHR * 16);
        if (__all(v0[3] == tag && v1[3] == tag)) break;
        __builtin_amdgcn_s_sleep(2);
      }
      LAS unsigned* dst = tl + (tid & 7) * TLS + 3 * (tid >> 3);
#pragma unroll
      for (int c = 0; c < 3; c++) { dst[c] = v0[c]; dst[288 + c] = v1[c]; }
    }
    __syncthreads();
    TS(0, 8);
    LAS unsigned* const okey = (LAS unsigned*)(L + SM_MISC);
    LAS unsigned* const cnt = okey + 16;
    f32x2 hf[9];
    unsigned wn[9], k16 = 0;
    if (wave < 8) {
#pragma unroll
      for (int k = 0; k < 9; k++) {
        const u32x2 t2 = *(const LAS u32x2*)(tl + wave * TLS + 2 * (hl + 32 * k));
        const unsigned t0 = t2[0], t1 = t2[1];
        hf[k] = f32x2{rbf(dot2(t0, zsel, xf2[k][0])), rbf(dot2(t1, zsel, xf2[k][1]))};
      }
      f32x2 nn[9];
      ln_row(hf, nn);
      float dot = 0.f;
#pragma unroll
      for (int k = 0; k < 9; k++) {
        wn[k] = pkbf(nn[k][0], nn[k][1]);
        dot = dot2(wn[k], wrw[k], dot);
      }
      dot = red32_sum(dot);
      const unsigned rb = pkbf(dot, 0.f);
      k16 = rb ^ ((rb & 0x8000u) ? 0xffffu : 0x8000u);
      const unsigned rr = (unsigned)__builtin_amdgcn_readlane((int)k16, 0) | ((unsigned)__builtin_amdgcn_readlane((int)k16, 32) << 16);
      if (lane == 0) __builtin_amdgcn_raw_buffer_store_b64(u32x2{rr, tag}, rs, WS_RG + (b * 8 + wave) * 8, 0, SC1);
      if (hl == 0) okey[2 * wave + rsel] = (k16 << 16) | (unsigned)(SEQ - 1 - row);
    }
    __syncthreads();
    TS(0, 9);
    int orank = 0;
    if (wave < 8) {
      const unsigned mykey = okey[c15];
#pragma unroll
      for (int i = 0; i < 16; i++) orank += okey[i] > mykey;
      DRAIN();
      u32x4 v;
      _Pragma("unroll 1") for (int spin = 0; spin < SPIN_CAP; spin++) {
        v = ldg16(rs, WS_RG + tid * 16);
        if (__all(v[1] == tag && v[3] == tag)) break;
        __builtin_amdgcn_s_sleep(1);
      }
      const unsigned ib = (unsigned)(SEQ - 4 - 4 * tid);
      const unsigned k0 = (v[0] << 16) | (ib + 3), k1 = (v[0] & 0xffff0000u) | (ib + 2);
      const unsigned k2 = (v[2] << 16) | (ib + 1), k3 = (v[2] & 0xffff0000u) | ib;
      int mine = 8;
      for (int c = 0; c < 8; c++) {
        const unsigned oc = (unsigned)__builtin_amdgcn_readlane((int)mykey, __builtin_ctzll(__ballot(orank == c)));
        const int s = __popcll(__ballot(k0 > oc)) + __popcll(__ballot(k1 > oc)) + __popcll(__ballot(k2 > oc)) + __popcll(__ballot(k3 > oc));
        if (s >= 8) break;
        mine = lane == c ? s : mine;
      }
      if (lane < 8) __hip_atomic_fetch_add(cnt + lane, (unsigned)mine, __ATOMIC_RELAXED, __HIP_MEMORY_SCOPE_WORKGROUP);
    }
    __syncthreads();
    TS(0, 10);
    if (wave < 8) {
      const int so = __shfl(orank, 2 * wave + rsel);
      const unsigned rank = so < 8 ? cnt[so] : 8u;
      if (rank < 8) {
#pragma unroll
        for (int m = 0; m < 3; m++) stg16(rs, WS_XS + rank * 1536 + m * 512 + hl * 16, u32x4{wn[3 * m], wn[3 * m + 1], wn[3 * m + 2], tag});
#pragma unroll
        for (int k = 0; k < 9; k++) stg16(rs, WS_HS + rank * 4608 + (hl + 32 * k) * 16, u32x4{pkbf(hf[k][0], hf[k][1]), wn[k], ((unsigned)row << 16) | k16, tag});
      } else {
        unsigned* orow = (unsigned*)(out + (size_t)row * DM) + hl;
#pragma unroll
        for (int k = 0; k < 9; k++) {
          const f32x2 o2 = hf[k] + unpk(wn[k]);
          orow[32 * k] = pkbf(o2[0], o2[1]);
        }
      }
    }
  }
  if (b < RED0) {
    TS(0, 12);
  TC(0, 15);
    return;
  }
  {
    const int T = (b - RED0) >> 2, tp = (b - RED0) & 3;
    u32x4 hv = {0u, 0u, 0u, 0u};
    if (wave == 8) {
      _Pragma("unroll 1") for (int spin = 0; spin < SPIN_CAP; spin++) {
        hv = ldg16(rs, WS_HS + (2 * tp + ((lane >> 3) & 1)) * 4608 + (8 * T + (lane & 7)) * 16);
        if (__all(hv[3] == tag)) break;
        __builtin_amdgcn_s_sleep(4);
      }
      wait_ticks(DLY_P);
    }
    __syncthreads();
    TS(0, 11);
    LAS float* red = (LAS float*)(L + SM_MISC + 128);
    if (wave < 8) {
      u32x4 v;
      _Pragma("unroll 1") for (int spin = 0; spin < SPIN_CAP; spin++) {
        v = ldg16(rs, part_off);
        if (__all(v[0] != ~0u && v[1] != ~0u && v[2] != ~0u && v[3] != ~0u)) break;
        __builtin_amdgcn_s_sleep(2);
      }
      f32x4 s;
#pragma unroll
      for (int r = 0; r < 4; r++) {
        float a = __uint_as_float(v[r]);
        a += dppf<0x128>(a);
        a = swap16_sum(a);
        s[r] = swap32_sum(a);
      }
      if (lane < 8) *(LAS f32x4*)(red + wave * 32 + lane * 4) = s;
    }
    __syncthreads();
    if (wave == 8 && lane < 16) {
      float y0 = 0.f, y1 = 0.f;
#pragma unroll
      for (int w = 0; w < 8; w++) {
        const f32x2 p = *(const LAS f32x2*)(red + w * 32 + lane * 2);
        y0 += p[0];
        y1 += p[1];
      }
      const unsigned rb = hv[2] ^ ((hv[2] & 0x8000u) ? 0x8000u : 0xffffu);
      const float sg = rbf(__builtin_amdgcn_rcpf(1.0f + __expf(-bflo(rb))));
      const unsigned yb2 = pkbf(y0, y1);
      const unsigned y2 = pkbf(bflo(yb2) * sg, bfhi(yb2) * sg);
      const unsigned a2 = pkbf(bflo(hv[1]) + bflo(y2), bfhi(hv[1]) + bfhi(y2));
      ((unsigned*)out)[(size_t)((hv[2] >> 16) & (SEQ - 1)) * (DM / 2) + 8 * T + (lane & 7)] = pkbf(bflo(hv[0]) + bflo(a2), bfhi(hv[0]) + bfhi(a2));
      if (b == RED0 && lane == 0) __builtin_amdgcn_raw_buffer_store_b32(tag, rs, WS_EPOCH, 0, SC1);
    }
  }
  TS(0, 12);
  TC(0, 15);
}

}

extern "C" void kernel_launch(void* const* d_in, const int* in_sizes, int n_in, void* d_out, int out_size,
                              void* d_ws, size_t ws_size, hipStream_t stream) {
  k_all<<<NCONV + NFFN, NTHR, 0, stream>>>((const u16*)d_in[0], (const u16*)d_in[1], (const u16*)d_in[2], (const u16*)d_in[3],
                                           (const u16*)d_in[4], (const u16*)d_in[5], (const u16*)d_in[6], (const u16*)d_in[8],
                                           (const u16*)d_in[9], (const u16*)d_in[10], (const u16*)d_in[11], (u16*)d_out,
                                           (char*)d_ws);
}
```

```cpp
#define PADP -1
#define PADT 0
#define OUTM 1
#include <hip/hip_runtime.h>
#include <stdint.h>

#define PRA 2
#define PRB 0
#define PFD 5
namespace {

constexpr int SEQ = 2048;
constexpr int DM = 576;
constexpr int NM = 128;
constexpr int FF = 4608;

constexpr int NTHR = 768;
constexpr int NCONV = 192;
constexpr int NLN = 128;
constexpr int NFFN = 64;
constexpr int RED0 = 48;

typedef unsigned short u16;
#define LAS __attribute__((address_space(3)))
using half8 = __attribute__((ext_vector_type(8))) _Float16;
using half2v = __attribute__((ext_vector_type(2))) _Float16;
using bf2v = __attribute__((ext_vector_type(2))) __bf16;
using bf8v = __attribute__((ext_vector_type(8))) __bf16;
using s16x4 = __attribute__((ext_vector_type(4))) short;
using f32x2 = __attribute__((ext_vector_type(2))) float;
using f32x4 = __attribute__((ext_vector_type(4))) float;
using u32x2 = __attribute__((ext_vector_type(2))) unsigned;
using u32x4 = __attribute__((ext_vector_type(4))) unsigned;

constexpr int WS_EPOCH = 0;
constexpr int WS_RG = 4096;
constexpr int WS_XS = WS_RG + 8192;
constexpr int WS_HS = WS_XS + 8 * 1536;
constexpr int WS_UP = 65536;
constexpr int WS_ZP = WS_UP + NCONV * NLN * 128;
constexpr int WS_PART = WS_ZP + NCONV * NLN * 128;
constexpr int WS_END = WS_PART + NFFN * 8 * DM * 4;
constexpr size_t WS_TS = 64u << 20;
constexpr int SC1 = 16, NT = 2;
constexpr int SPIN_CAP = 1 << 16;

#define TW(k)
#define TWDECL
#define TSX(k, s)
#define TWFLUSH()
#define TC(k, s)
#define TS(k, s)

__device__ __forceinline__ float bflo(unsigned v) { return __uint_as_float(v << 16); }
__device__ __forceinline__ float bfhi(unsigned v) { return __uint_as_float(v & 0xffff0000u); }
__device__ __forceinline__ unsigned pkbf(float a, float b) {
  f32x2 f = {a, b};
  return __builtin_bit_cast(unsigned, __builtin_convertvector(f, bf2v));
}
__device__ __forceinline__ unsigned pkh(float a, float b) {
  f32x2 f = {a, b};
  return __builtin_bit_cast(unsigned, __builtin_convertvector(f, half2v));
}
__device__ __forceinline__ float rbf(float f) { return __uint_as_float(pkbf(0.f, f)); }
__device__ __forceinline__ f32x2 rbf2(f32x2 v) { return f32x2{rbf(v[0]), rbf(v[1])}; }
__device__ __forceinline__ f32x2 unpk(unsigned v) { return f32x2{bflo(v), bfhi(v)}; }
__device__ __forceinline__ float dot2(unsigned a, unsigned b, float c) {
  return __builtin_amdgcn_fdot2_f32_bf16(__builtin_bit_cast(bf2v, a), __builtin_bit_cast(bf2v, b), c, false);
}
__device__ __forceinline__ f32x2 h2_times(unsigned h2, float s) {
  float a, b;
  asm("v_fma_mix_f32 %0, %1, %2, 0 op_sel_hi:[1,0,0]" : "=v"(a) : "v"(h2), "s"(s));
  asm("v_fma_mix_f32 %0, %1, %2, 0 op_sel:[1,0,0] op_sel_hi:[1,0,0]" : "=v"(b) : "v"(h2), "s"(s));
  return f32x2{a, b};
}
__device__ __forceinline__ float hadd(f32x2 v) {
  float a = v[0], b = v[1];
  asm volatile("" : "+v"(a), "+v"(b));
  return a + b;
}

template <int CTRL>
__device__ __forceinline__ float dppf(float v) {
  return __int_as_float(__builtin_amdgcn_update_dpp(0, __float_as_int(v), CTRL, 0xf, 0xf, true));
}
__device__ __forceinline__ float swap16_sum(float v) {
  auto r = __builtin_amdgcn_permlane16_swap(__float_as_uint(v), __float_as_uint(v), false, false);
  return __uint_as_float(r[0]) + __uint_as_float(r[1]);
}
__device__ __forceinline__ float swap32_sum(float v) {
  auto r = __builtin_amdgcn_permlane32_swap(__float_as_uint(v), __float_as_uint(v), false, false);
  return __uint_as_float(r[0]) + __uint_as_float(r[1]);
}
__device__ __forceinline__ float red32_sum(float v) {
  v += dppf<0xB1>(v);
  v += dppf<0x4E>(v);
  v += dppf<0x141>(v);
  v += dppf<0x140>(v);
  return swap16_sum(v);
}
#define SCHED_FENCE() __builtin_amdgcn_sched_barrier(0)
#define DRAIN() asm volatile("s_waitcnt vmcnt(0)" ::: "memory")

__device__ __forceinline__ void ln_row(const f32x2 (&v)[9], f32x2 (&n)[9]) {
  f32x2 s = v[0];
#pragma unroll
  for (int k = 1; k < 9; k++) s += v[k];
  const float mean = red32_sum(hadd(s)) * (1.0f / DM);
  f32x2 q = {0.f, 0.f};
#pragma unroll
  for (int k = 0; k < 9; k++) {
    const f32x2 d = v[k] - mean;
    q = __builtin_elementwise_fma(d, d, q);
  }
  const float mbf = rbf(mean);
#pragma unroll
  for (int k = 0; k < 9; k++) n[k] = rbf2(v[k] - mbf);
  const float var = red32_sum(hadd(q)) * (1.0f / DM);
  const float sbf = rbf(__builtin_amdgcn_sqrtf(rbf(rbf(var) + 1e-5f)));
  const float rinv = __builtin_amdgcn_rcpf(sbf);
#pragma unroll
  for (int k = 0; k < 9; k++) n[k] *= rinv;
}

constexpr int TAB_STRIDE = 544;
constexpr int TAB_ROWS = 56;
constexpr int SM_US = TAB_ROWS * TAB_STRIDE;
constexpr int UPAD = 256;
constexpr int SM_KC = SM_US + (UPAD + SEQ) * 2;
constexpr int RDS = 336 * 4;
constexpr int KC_RD = SM_KC;
constexpr int KC_R5 = KC_RD + 4 * RDS;
constexpr int KC_R3 = KC_R5 + 64 * 4;
constexpr int KC_R9 = KC_R3 + 64 * 4;
constexpr int KC_R7 = KC_R9 + 96 * 4;
constexpr int KC_F1024 = KC_R7 + 96 * 4;
constexpr int CH_BYTES = KC_F1024 + 16;
constexpr int KC_CHUNKS = (CH_BYTES - SM_KC) / 16;
constexpr int YT = 1280;
constexpr int SM_YD = 0, SM_Y2 = 8 * YT - 2 * YT, SM_Y3 = 14 * YT - 4 * YT;
constexpr int SM_TL = 3 * CH_BYTES;
constexpr int TLS = 580;
constexpr int SM_MISC = SM_TL + 8 * TLS * 4;
constexpr int SM_TOTAL = SM_MISC + 128 + 1024;
constexpr int FS_STAGE = 0;
constexpr int DS = 1184;
constexpr int FS_XS = 72 * DS;
constexpr int FS_RED = FS_XS + 8 * DS;
constexpr int FS_GU = FS_RED + 4 * 9 * 512;
static_assert(FS_GU + 8 * 160 <= SM_TOTAL, "lds");

typedef f32x2 cf;
struct cstep { cf s, r; };
__device__ __forceinline__ cstep mkstep(cf a) { return cstep{a, cf{-a[1], a[0]}}; }
__device__ __forceinline__ cf cmul(cf a, const cstep& b) {
  cf t, o;
  asm("v_pk_mul_f32 %0, %1, %2 op_sel:[1,0]" : "=v"(t) : "v"(a), "v"(b.r));
  asm("v_pk_fma_f32 %0, %1, %2, %3 op_sel_hi:[0,1,1]" : "=v"(o) : "v"(a), "v"(b.s), "v"(t));
  return o;
}
__device__ __forceinline__ unsigned pkc(cf a) { return __builtin_bit_cast(unsigned, __builtin_convertvector(a, half2v)); }
__device__ __forceinline__ cf cexp_p2(float p, float ch, float cl, float wh, float wl) {
  float mag = __builtin_amdgcn_exp2f(p * ch);
  mag = fmaf(mag, (p * 0.693147181f) * cl, mag);
  const float rh = p * wh;
  const float fr = fmaf(p, wl, rh - rintf(rh));
  return cf{mag * __builtin_amdgcn_cosf(fr), mag * __builtin_amdgcn_sinf(fr)};
}
__device__ __forceinline__ float exp_acc(float x) {
  const float th = x * 1.44269502f;
  const float tl = fmaf(x, 1.92596299e-8f, fmaf(x, 1.44269502f, -th));
  const float e = __builtin_amdgcn_exp2f(th);
  return fmaf(e, tl * 0.693147181f, e);
}
__device__ __forceinline__ unsigned wave_max_u32(unsigned v) {
  v = max(v, (unsigned)__builtin_amdgcn_update_dpp(0, (int)v, 0x111, 0xf, 0xf, false));
  v = max(v, (unsigned)__builtin_amdgcn_update_dpp(0, (int)v, 0x112, 0xf, 0xf, false));
  v = max(v, (unsigned)__builtin_amdgcn_update_dpp(0, (int)v, 0x114, 0xf, 0xf, false));
  v = max(v, (unsigned)__builtin_amdgcn_update_dpp(0, (int)v, 0x118, 0xf, 0xf, false));
  v = max(v, (unsigned)__builtin_amdgcn_update_dpp(0, (int)v, 0x142, 0xa, 0xf, false));
  v = max(v, (unsigned)__builtin_amdgcn_update_dpp(0, (int)v, 0x143, 0xc, 0xf, false));
  return (unsigned)__builtin_amdgcn_readlane((int)v, 63);
}

#define MFMA16(a, b, c) __builtin_amdgcn_mfma_f32_16x16x32_f16((a), (b), (c), 0, 0, 0)
#define MFMAB(a, b, c) __builtin_amdgcn_mfma_f32_16x16x32_bf16((a), (b), (c), 0, 0, 0)

__device__ __forceinline__ half8 ld_frag(const LAS char* p) { return *(const LAS half8*)p; }
__device__ __forceinline__ const LAS unsigned* opaque_lds(const LAS char* p) {
  const LAS unsigned* q = (const LAS unsigned*)p;
  asm volatile("" : "+v"(q));
  return q;
}
template <int O2>
__device__ __forceinline__ half8 ld_toep(const LAS unsigned* q) {
  u32x4 v = {q[0], q[1], q[O2], q[O2 + 1]};
  return __builtin_bit_cast(half8, v);
}
__device__ __forceinline__ half8 band_frag(int lane, unsigned pat) {
  const int bits = (int)((pat << (lane & 15)) >> (8 * (lane >> 4)));
  u32x4 v;
#pragma unroll
  for (int j = 0; j < 4; j++) {
    const unsigned lo = (unsigned)__builtin_amdgcn_sbfe(bits, 2 * j, 1), hi = (unsigned)__builtin_amdgcn_sbfe(bits, 2 * j + 1, 1);
    v[j] = __builtin_amdgcn_perm(hi, lo, 0x05040100u) & 0x3c003c00u;
  }
  return __builtin_bit_cast(half8, v);
}
__device__ __forceinline__ half8 pack_win(f32x4 a, f32x4 b) {
  u32x4 v = {pkh(a[0], a[1]), pkh(a[2], a[3]), pkh(b[0], b[1]), pkh(b[2], b[3])};
  return __builtin_bit_cast(half8, v);
}

template <bool HI>
__device__ __forceinline__ void conv_dense(const half8 (&td)[17], half8 te, const LAS char* ub, LAS char* yd) {
  constexpr int S0 = HI ? 0 : 9, NS = 41, PF = PFD;
  const f32x4 zero4 = {0.f, 0.f, 0.f, 0.f};
  f32x4 acc[4] = {zero4, zero4, zero4, zero4};
  half8 u[NS], ue[2];
#pragma unroll
  for (int s = S0; s < S0 + PF; s++) u[s] = ld_frag(ub + 32 * (-33 + 2 * s));
  SCHED_FENCE();
#pragma unroll
  for (int s = S0; s < NS; s++) {
    if (s + PF < NS) u[s + PF] = ld_frag(ub + 32 * (-33 + 2 * (s + PF)));
    if (HI && (s == 0 || s == 2)) ue[s >> 1] = ld_frag(ub + 32 * (-65 + 8 * s));
#pragma unroll
    for (int t = 0; t < 4; t++) {
      const int i = 16 + 8 * t - s;
      if (i >= 0 && i <= 16) acc[t] = MFMA16(td[i], u[s], acc[t]);
    }
    if (HI) {
      if (s == 0) acc[2] = MFMA16(te, u[0], acc[2]);
      if (s == 8) acc[3] = MFMA16(te, u[8], acc[3]);
      if (s == 5) acc[0] = MFMA16(te, ue[0], acc[0]);
      if (s == 7) acc[1] = MFMA16(te, ue[1], acc[1]);
    }
    if (s >= 18 && ((s - 18) & 7) == 0) *(LAS f32x4*)(yd + ((s - 18) >> 3) * YT) = acc[(s - 18) >> 3];
    SCHED_FENCE();
  }
  *(LAS f32x4*)(yd + 3 * YT) = acc[3];
}

typedef __amdgpu_buffer_rsrc_t rsrc_t;
__device__ __forceinline__ u32x4 ldg16(rsrc_t rs, int off) {
  asm volatile("" : "+v"(off) : : "memory");
  return __builtin_amdgcn_raw_buffer_load_b128(rs, off, 0, SC1);
}
__device__ __forceinline__ void stg16(rsrc_t rs, int off, u32x4 v) { __builtin_amdgcn_raw_buffer_store_b128(v, rs, off, 0, SC1); }
__device__ __forceinline__ void wait_ticks(int t) {
  const unsigned long long t0 = __builtin_amdgcn_s_memrealtime();
  while (__builtin_amdgcn_s_memrealtime() - t0 < (unsigned long long)t) __builtin_amdgcn_s_sleep(1);
}
#define PAD(p) do { if (PADP == (p)) { while (__builtin_amdgcn_s_memrealtime() - t_start < (unsigned long long)PADT) __builtin_amdgcn_s_sleep(1); } } while (0)
#define UPOS 1
#define DLY_X 60
#define DLY_P 100

__device__ __forceinline__ void ffn_role(const u16* __restrict__ wg, const u16* __restrict__ wu, const u16* __restrict__ wd,
                                         char* ws, rsrc_t rs, unsigned tagv, LAS char* L, unsigned long long t_start) {
  const int tid = threadIdx.x, lane = tid & 63;
  const int wave = __builtin_amdgcn_readfirstlane(tid >> 6);
  const int fw = blockIdx.x - NCONV;
  const int j = (fw & 7) * 8 + (fw >> 3);
  const int r0 = tid / 18, rem = tid - r0 * 18;
  const int pc = rem >= 9 ? rem - 9 : rem;
  const char* gsrc = (const char*)(rem >= 9 ? wu : wg) + (unsigned)((r0 < 42 ? r0 : 0) * (FF * 2) + j * 144 + pc * 16);
  u32x4 wa[7], wb[7], wc[7];
#pragma unroll
  for (int i = 0; i < 7; i++) wa[i] = __builtin_nontemporal_load((const u32x4*)(gsrc + (42 * i) * (FF * 2)));
  SCHED_FENCE();
#pragma unroll
  for (int i = 0; i < 7; i++)
    wb[i] = __builtin_nontemporal_load((const u32x4*)(gsrc + ((i < 6 || r0 < 36) ? (288 + 42 * i) * (FF * 2) : 0)));
  SCHED_FENCE();
  {
    const char* dsrc = (const char*)wd + (size_t)j * (72 * 1152);
#pragma unroll
    for (int i = 0; i < 7; i++)
      wc[i] = __builtin_nontemporal_load((const u32x4*)(dsrc + (unsigned)((i < 6 || tid < 576) ? (tid + NTHR * i) * 16 : 0)));
  }
  SCHED_FENCE();
  if (tid < 8 * 4) *(LAS unsigned*)(L + FS_GU + (tid >> 2) * 160 + 144 + (tid & 3) * 4) = 0u;
  const int g = lane >> 4, tq = (lane & 15) >> 2, tp = lane & 3;
  const int tg = wave % 3, kq = wave / 3;
  s16x4 fa[3][5][2];
  auto stage_gu = [&](const u32x4 (&w)[7]) {
#pragma unroll
    for (int i = 0; i < 7; i++)
      if (tid < 756 && r0 + 42 * i < 288) *(LAS u32x4*)(L + FS_STAGE + (r0 + 42 * i) * 288 + rem * 16) = w[i];
  };
  auto read_gu = [&]() {
    const LAS char* p = L + FS_STAGE + (160 * (kq & 1) + 8 * g + tq) * 288 + (tp >> 1) * 144 + (tp & 1) * 8 + 48 * tg;
#pragma unroll
    for (int ks = 0; ks < 5; ks++) {
      const int kk = (ks == 4) ? ((kq & 1) ? 3 : 4) : ks;
#pragma unroll
      for (int t = 0; t < 3; t++)
#pragma unroll
        for (int h = 0; h < 2; h++)
          fa[t][ks][h] = __builtin_amdgcn_ds_read_tr16_b64_v4i16((LAS s16x4*)(p + (32 * kk + 4 * h) * 288 + 16 * t));
    }
  };
  stage_gu(wa);
  __syncthreads();
  if (kq < 2) read_gu();
  __syncthreads();
  stage_gu(wb);
  __syncthreads();
  if (kq >= 2) read_gu();
  __syncthreads();
  TS(1, 1);
#pragma unroll
  for (int i = 0; i < 7; i++) {
    const int e = tid + NTHR * i;
    const int row = e / 72;
    if (e < 5184) *(LAS u32x4*)(L + FS_STAGE + row * DS + (e - row * 72) * 16) = wc[i];
  }
  __syncthreads();
  s16x4 fd[3][5];
  {
    const LAS char* p = L + FS_STAGE + tq * DS + (48 * wave + 4 * tp) * 2;
#pragma unroll
    for (int t = 0; t < 3; t++) {
#pragma unroll
      for (int h = 0; h < 4; h++)
        fd[t][h] = __builtin_amdgcn_ds_read_tr16_b64_v4i16((LAS s16x4*)(p + (32 * (h >> 1) + 8 * g + 4 * (h & 1)) * DS + 32 * t));
      fd[t][4] = __builtin_amdgcn_ds_read_tr16_b64_v4i16((LAS s16x4*)(p + (64 + 4 * (g & 1)) * DS + 32 * t));
    }
  }
  const unsigned tag = __builtin_amdgcn_readfirstlane(tagv) + 1u;
  TS(1, 2);
  if (wave == 0) {
    _Pragma("unroll 1") for (int spin = 0; spin < SPIN_CAP; spin++) {
      const unsigned t = ldg16(rs, WS_RG + (fw * 16) * 8)[1];
      if (__builtin_amdgcn_readfirstlane(t) == tag) break;
      __builtin_amdgcn_s_sleep(4);
    }
    wait_ticks(DLY_X);
  }
  __syncthreads();
  TS(1, 3);
  {
    u32x4 v;
    _Pragma("unroll 1") for (int spin = 0; spin < SPIN_CAP; spin++) {
      v = ldg16(rs, WS_XS + tid * 16);
      if (__all(v[3] == tag)) break;
      __builtin_amdgcn_s_sleep(2);
    }
    const int slot = tid / 96, rm = tid - slot * 96;
    LAS unsigned* dst = (LAS unsigned*)(L + FS_XS + slot * DS) + (rm & 31) + 96 * (rm >> 5);
    dst[0] = v[0];
    dst[32] = v[1];
    dst[64] = v[2];
  }
  __syncthreads();
  TS(1, 4);
  PAD(4);
  {
    const f32x4 zero4 = {0.f, 0.f, 0.f, 0.f};
    f32x4 acc[3] = {zero4, zero4, zero4};
    const LAS char* xp = L + FS_XS + (lane & 7) * DS + 16 * g + 64 * (9 * (kq >> 1) + 5 * (kq & 1));
    bf8v xf[5];
#pragma unroll
    for (int ks = 0; ks < 5; ks++) xf[ks] = *(const LAS bf8v*)(xp + 64 * ks);
#pragma unroll
    for (int ks = 0; ks < 5; ks++) {
      if (ks < 4 || !(kq & 1)) {
#pragma unroll
        for (int t = 0; t < 3; t++) {
          const bf8v a = __builtin_bit_cast(bf8v, __builtin_shufflevector(fa[t][ks][0], fa[t][ks][1], 0, 1, 2, 3, 4, 5, 6, 7));
          acc[t] = MFMAB(a, xf[ks], acc[t]);
        }
      }
    }
    if ((lane & 15) < 8) {
#pragma unroll
      for (int t = 0; t < 3; t++) *(LAS f32x4*)(L + FS_RED + ((kq * 9 + 3 * tg + t) * 8 + (lane & 7)) * 64 + 16 * g) = acc[t];
    }
  }
  __syncthreads();
  if (wave < 9) {
    const LAS float* rp = (const LAS float*)(L + FS_RED) + (wave * 8 + (lane >> 3)) * 16 + (lane & 7);
    const float gs = ((rp[0] + rp[1152]) + rp[2304]) + rp[3456];
    const float us = ((rp[8] + rp[1160]) + rp[2312]) + rp[3464];
    const float gp = rbf(gs);
    const float gq = rbf(gp * __builtin_amdgcn_rcpf(1.0f + __expf(-gp)));
    *(LAS u16*)(L + FS_GU + (lane >> 3) * 160 + (wave * 8 + (lane & 7)) * 2) = (u16)pkbf(gq * rbf(us), 0.f);
  }
  __syncthreads();
  {
    const LAS char* gp = L + FS_GU + (lane & 7) * 160;
    const bf8v b0 = *(const LAS bf8v*)(gp + 16 * g), b1 = *(const LAS bf8v*)(gp + 64 + 16 * g);
    const s16x4 b2 = *(const LAS s16x4*)(gp + 128 + 8 * g);
#pragma unroll
    for (int t = 0; t < 3; t++) {
      f32x4 acc = {0.f, 0.f, 0.f, 0.f};
      acc = __builtin_amdgcn_mfma_f32_16x16x16bf16_1k(fd[t][4], b2, acc, 0, 0, 0);
      f32x4 acc2 = {0.f, 0.f, 0.f, 0.f};
      acc2 = MFMAB(__builtin_bit_cast(bf8v, __builtin_shufflevector(fd[t][0], fd[t][1], 0, 1, 2, 3, 4, 5, 6, 7)), b0, acc2);
      acc2 = MFMAB(__builtin_bit_cast(bf8v, __builtin_shufflevector(fd[t][2], fd[t][3], 0, 1, 2, 3, 4, 5, 6, 7)), b1, acc2);
      u32x4 o;
#pragma unroll
      for (int r = 0; r < 4; r++) o[r] = min(__float_as_uint(acc[r] + acc2[r]), 0xfffffffeu);
      if ((lane & 15) < 8) stg16(rs, WS_PART + fw * (8 * DM * 4) + (3 * wave + t) * 512 + (lane & 7) * 64 + 16 * g, o);
    }
  }
  TS(1, 5);
  TC(1, 15);
}

__global__ __launch_bounds__(NTHR, 3) void k_all(const u16* __restrict__ x, const u16* __restrict__ lar,
                                                 const u16* __restrict__ aim, const u16* __restrict__ crl,
                                                 const u16* __restrict__ cim, const u16* __restrict__ ldt,
                                                 const u16* __restrict__ dskip, const u16* __restrict__ wr,
                                                 const u16* __restrict__ wg, const u16* __restrict__ wu,
                                                 const u16* __restrict__ wd, u16* __restrict__ out,
                                                 char* ws) {
  __shared__ __attribute__((aligned(16))) char smem[SM_TOTAL];
  LAS char* const L = (LAS char*)smem;
  unsigned long long t_start;
  asm volatile("s_memrealtime %0\n s_waitcnt lgkmcnt(0)" : "=s"(t_start));
  asm volatile("" ::"s"(x), "s"(lar), "s"(aim), "s"(crl), "s"(cim), "s"(ldt), "s"(dskip), "s"(wr), "s"(wg), "s"(wu), "s"(wd),
               "s"(out), "s"(ws));
  const int tid = threadIdx.x, lane = tid & 63, b = blockIdx.x;
  const int wave = __builtin_amdgcn_readfirstlane(tid >> 6);
  const rsrc_t rs = __builtin_amdgcn_make_buffer_rsrc(ws, 0, WS_END, 0x00020000);
  const unsigned tagv = __builtin_amdgcn_raw_buffer_load_b32(rs, WS_EPOCH, 0, SC1);
  if (b >= NCONV) {
    TS(1, 0);
  TC(1, 14);
    ffn_role(wg, wu, wd, ws, rs, tagv, L, t_start);
    PAD(0);
    return;
  }
  TWDECL;
  TS(0, 0);
  TC(0, 14);
  __builtin_amdgcn_s_setprio(3);
  const bool is_ln = b < NLN;
  const bool ln_wave = is_ln && wave < 8;
  const int hl = lane & 31, rsel = lane >> 5;
  const int row = b * 16 + wave * 2 + rsel;
  unsigned xw[9];
  if (ln_wave) {
    const char* xr = (const char*)x + (unsigned)(row * (DM * 2) + hl * 4);
#pragma unroll
    for (int k = 0; k < 9; k++) xw[k] = *(const unsigned*)(xr + 128 * k);
  }
  const int ch = (wave & 3) < 3 ? (wave & 3) : (wave >> 2), sub = (wave & 3) < 3 ? (wave >> 2) : 3;
  const int d = 3 * b + ch;
  const int n = (sub & 1) * 64 + lane;
  const unsigned po = (unsigned)(d * NM + n), po2 = po ^ 64u;
  const u16 r_lar = lar[po], r_aim = aim[po], r_cr = crl[po], r_ci = cim[po], r_cr2 = crl[po2], r_ci2 = cim[po2];
  const u16 r_ldt = ldt[(unsigned)d], r_dsk = dskip[(unsigned)(3 * b + (lane < 3 ? lane : 0))];
  SCHED_FENCE();
  const int part_off = WS_PART + (tid >> 3) * (8 * DM * 4) + (b - RED0) * 128 + (tid & 7) * 16;
  LAS char* const C = L + ch * CH_BYTES;
  {
    const int ct = tid & 255;
    LAS char* zc = L + (wave >> 2) * CH_BYTES;
    const u32x4 z = {0u, 0u, 0u, 0u};
    *(LAS u32x4*)(zc + SM_KC + 16 * ct) = z;
    if (ct < KC_CHUNKS - 256) *(LAS u32x4*)(zc + SM_KC + 4096 + 16 * ct) = z;
    else if (ct >= 256 - UPAD / 8) *(LAS u32x4*)(zc + SM_US + 16 * (ct - (256 - UPAD / 8))) = z;
    if (tid < 16) *(LAS unsigned*)(L + SM_MISC + 64 + 4 * tid) = 0u;
  }
  half8 bA = band_frag(lane, sub == 3 ? 0x1fu << 6 : (sub == 2 ? 0x1ffu << 4 : 0x3u << 12));
  half8 bB = band_frag(lane, sub == 3 ? 0x7u << 11 : 0x7fu << 5);
  asm volatile("" : "+v"(bA), "+v"(bB));
  SCHED_FENCE();
  const unsigned tag = __builtin_amdgcn_readfirstlane(tagv) + 1u;
  LAS unsigned* const tl = (LAS unsigned*)(L + SM_TL);
  const unsigned hsel = rsel ? 0x07060302u : 0x05040100u;
  float f_lar = bflo(r_lar), f_aim = bflo(r_aim), f_cr = bflo(r_cr), f_ci = bflo(r_ci), f_ldt = bflo(r_ldt), f_dsk = bflo(r_dsk);
  float f_cm = fmaxf(fmaxf(fabsf(f_cr), fabsf(f_ci)), fmaxf(fabsf(bflo(r_cr2)), fabsf(bflo(r_ci2))));
  float f_k0 = f_cr + bflo(r_cr2);
  asm volatile("" : "+v"(f_lar), "+v"(f_aim), "+v"(f_cr), "+v"(f_ci), "+v"(f_cm), "+v"(f_ldt), "+v"(f_dsk), "+v"(f_k0));
  float dsk[3];
#pragma unroll
  for (int c = 0; c < 3; c++) dsk[c] = __int_as_float(__builtin_amdgcn_readlane(__float_as_int(f_dsk), c));
  if (b >= RED0 && tid < 512) stg16(rs, part_off, u32x4{~0u, ~0u, ~0u, ~0u});
  PAD(1);
  if (is_ln) {
    if (wave < 8) {
      f32x2 xf[9], un[9];
#pragma unroll
      for (int k = 0; k < 9; k++) xf[k] = unpk(xw[k]);
      ln_row(xf, un);
#pragma unroll
      for (int k = 0; k < 9; k++) {
        const unsigned hp = pkc(rbf2(un[k]));
        auto r = __builtin_amdgcn_permlane32_swap(hp, hp, false, false);
        tl[wave * TLS + 2 * (hl + 32 * k) + rsel] = __builtin_amdgcn_perm(r[1], r[0], hsel);
      }
    }
    __syncthreads();
#pragma unroll
    for (int i = 0; i < 2; i++) {
      const int e = tid + NTHR * i;
      const LAS unsigned* s = tl + (e & 7) * TLS + 3 * (e >> 3);
      stg16(rs, WS_UP + ((e >> 3) * NLN + b) * 128 + (e & 7) * 16, u32x4{s[0], s[1], s[2], tag});
    }
  }
  __builtin_amdgcn_s_setprio(0);
  TSX(0, 1);
  TW(2);
  float inv_scale;
  {
    const float dt = rbf(exp_acc(f_ldt));
    const float are = -rbf(exp_acc(f_lar)) * dt;
    const float aie = f_aim * dt;
    const float chh = are * 1.44269502f;
    const float cl = fmaf(are, 1.92596299e-8f, fmaf(are, 1.44269502f, -chh));
    const float wh = aie * 0.159154936f;
    const float wl = fmaf(aie, 7.13048051e-9f, fmaf(aie, 0.159154936f, -wh));
    const unsigned cmax = wave_max_u32(__float_as_uint(f_cm));
    int se = 385 - (int)(cmax >> 23) - (int)(__float_as_uint(dt) >> 23);
    se = min(max(se, 2), 252);
    const float scale2 = __uint_as_float((unsigned)(se + 1) << 23);
    inv_scale = __uint_as_float((unsigned)(254 - se) << 23);
    const cf cc = {f_cr * dt * scale2, -f_ci * dt * scale2};
    volatile LAS unsigned* tab = (volatile LAS unsigned*)C + n;
    constexpr int TS4 = TAB_STRIDE / 4;
    if (sub < 2) {
      const cstep e1 = mkstep(cexp_p2(1.f, chh, cl, -wh, -wl)), e128 = mkstep(cexp_p2(128.f, chh, cl, -wh, -wl));
      const cstep e2 = mkstep(cmul(e1.s, e1)), e4 = mkstep(cmul(e2.s, e2));
      cf first = cmul(cc, e1);
#pragma unroll
      for (int a = 0; a < 8; a++) {
        const int cnt = a < 2 ? 8 : (a < 4 ? 4 : 2);
        const int r0 = a < 2 ? 8 * a : (a < 4 ? 16 + 4 * (a - 2) : 24 + 2 * (a - 4));
        const cstep st = a < 2 ? e1 : (a < 4 ? e2 : e4);
        cf v = first;
#pragma unroll
        for (int jj = 0; jj < cnt; jj++) {
          tab[(r0 + jj) * TS4] = pkc(v);
          if (a < 7 && jj == ((a == 1 || a == 3) ? 1 : 0)) first = cmul(v, e128);
          if (jj + 1 < cnt) v = cmul(v, st);
        }
      }
    } else {
      const cstep e8 = mkstep(cexp_p2(8.f, chh, cl, wh, wl)), e128 = mkstep(cexp_p2(128.f, chh, cl, -wh, -wl));
      cf bb = cmul(cmul(cc, mkstep(cexp_p2(1024.f, chh, cl, -wh, -wl))), mkstep(cf{e8.s[0], e8.r[0]}));
      cf h = e8.s;
      tab[40 * TS4] = pkh(1.f, 0.f);
#pragma unroll
      for (int i = 0; i < 15; i++) {
        tab[(41 + i) * TS4] = pkc(h);
        if (i < 14) h = cmul(h, e8);
        if (i < 8) {
          tab[(32 + i) * TS4] = pkc(bb);
          if (i < 7) bb = cmul(bb, e128);
        }
      }
    }
    f_k0 *= dt * scale2;
  }
  TW(3);
  __syncthreads();
  const int uoff = WS_UP + b * (NLN * 128) + tid * 16;
  u32x4 v0, v1;
  TSX(0, 2);
  const int c15 = lane & 15, g = lane >> 4;
  if (sub < 3) {
    f32x4 f = {0.f, 0.f, 0.f, 0.f};
    const LAS char* pa = C + (sub == 0 ? c15 : (sub == 1 ? 16 + (c15 & 7) : 24 + c15)) * TAB_STRIDE + 16 * g;
    const LAS char* pb = C + (40 + c15) * TAB_STRIDE + 16 * g;
    half8 fa[8], fb[8];
#pragma unroll
    for (int ks = 0; ks < 8; ks++) { fa[ks] = ld_frag(pa + 64 * ks); fb[ks] = ld_frag(pb + 64 * ks); }
    SCHED_FENCE();
#pragma unroll
    for (int ks = 0; ks < 8; ks++) f = MFMA16(fa[ks], fb[ks], f);
    const unsigned f10 = pkh(f[1], f[0]), f32 = pkh(f[3], f[2]);
    auto w16 = [&](int off, unsigned v) { *(volatile LAS u16*)(C + off) = (u16)v; };
    auto w32 = [&](int off, unsigned v) { *(volatile LAS unsigned*)(C + off) = v; };
    auto w64 = [&](int off, unsigned a, unsigned c) { *(volatile LAS u32x2*)(C + off) = u32x2{a, c}; };
    const unsigned f21 = __builtin_amdgcn_alignbit(f10, f32, 16);
    if (sub == 0) {
      const int o = KC_RD - 2 * (128 * (g >> 1) + 4 * (g & 1) + 8 * c15);
      w16(o + 2 * 539, f32);
      w32(o + 2 * 540, f21);
      w16(o + 2 * 542, f10 >> 16);
      w32(o + RDS + 2 * 538, f32);
      w32(o + RDS + 2 * 540, f10);
      w16(o + 2 * RDS + 2 * 537, f32);
      w32(o + 2 * RDS + 2 * 538, f21);
      w16(o + 2 * RDS + 2 * 540, f10 >> 16);
      w64(o + 3 * RDS + 2 * 536, f32, f10);
      const unsigned x = (unsigned)__builtin_amdgcn_readlane((int)f32, 63);
      if (lane < 4) w16(KC_RD + lane * (RDS - 2) + 2 * (543 - 257), x);
    } else if (sub == 1) {
      const unsigned f33 = __builtin_amdgcn_perm(f32, f32, 0x01000100u), f11 = __builtin_amdgcn_perm(f10, f10, 0x01000100u);
      const int o = KC_RD - 2 * (256 + 128 * (g & 1) + 8 * c15) + (g >> 1) * (2 * RDS - 4);
      w32(o + 2 * 534, f33);
      w32(o + 2 * 536, f32);
      w32(o + 2 * 538, f11);
      w32(o + 2 * 540, f10);
      w16(o + RDS + 2 * 533, f32);
      w32(o + RDS + 2 * 534, f33);
      w32(o + RDS + 2 * 536, f21);
      w32(o + RDS + 2 * 538, f11);
      w16(o + RDS + 2 * 540, f10 >> 16);
      const unsigned x = (unsigned)__builtin_amdgcn_readlane((int)f32, 31);
      if (lane < 4) w16(KC_RD + lane * (RDS - 2) + 2 * (543 - 514), x);
    } else if (g < 2) {
      const int o = 2 * (95 - 32 * g - c15);
      w16(KC_R3 + o, f10 >> 16);
      w16(KC_R5 + o, f10);
      w16(KC_R3 + o - 32, f32 >> 16);
      w16(KC_R5 + o - 32, f32);
      if (lane == 31) w16(KC_F1024, f32);
    } else {
      const int o = ((c15 & 1) ? KC_R9 : KC_R7) + 2 * 63 - 64 * (g - 2) - (c15 & 14);
      w16(o, f10 >> 16);
      w16(o + 190, f10 >> 16);
      w16(o - 16, f10);
      w16(o - 16 + 190, f10);
      w16(o - 32, f32 >> 16);
      w16(o - 32 + 190, f32 >> 16);
      w16(o - 48, f32);
      w16(o - 48 + 190, f32);
    }
  } else {
    const float k0 = swap32_sum(red32_sum(f_k0));
    if (lane < 4) *(LAS u16*)(C + KC_RD + lane * (RDS - 2) + 2 * 543) = (u16)pkh(k0, k0);
    if (lane == 0) *(LAS float*)(C + KC_F1024 + 8) = inv_scale;
  }
  TW(4);
  __syncthreads();
  v0 = ldg16(rs, uoff); v1 = ldg16(rs, wave < 4 ? uoff + NTHR * 16 : uoff);
  asm volatile("" ::: "memory");
  SCHED_FENCE();
  TSX(0, 3);
  half8 td[17];
  int iscv[3];
  if (sub < 2) {
    const volatile LAS u32x2* tb = (const volatile LAS u32x2*)(C + KC_RD + ((3 - c15) & 3) * RDS + 2 * (540 - 16 - (c15 & 12) + 8 * g) - 64 * 16);
#pragma unroll
    for (int i = 16; i >= 0; i--) {
      const u32x2 lo = tb[8 * (16 - i)], hi = tb[8 * (16 - i) + 1];
      td[i] = __builtin_bit_cast(half8, __builtin_shufflevector(lo, hi, 0, 1, 2, 3));
    }
    bA = bA * *(const LAS _Float16*)(C + KC_F1024);
  } else if (sub == 2) {
    const int c = (c15 + 1) & 1;
    const LAS unsigned* t9 = opaque_lds(C + KC_R9 + 192 * c + 2 * (127 - 64 - c15 + 4 * g - c) - 96);
#pragma unroll
    for (int e = 0; e < 4; e++) {
      td[e] = ld_toep<8>(t9 + 8 * (3 - e));
      td[4 + e] = ld_toep<8>(t9 + (KC_R7 - KC_R9) / 4 + 8 * (3 - e));
    }
  } else {
    const LAS unsigned* t5 = opaque_lds(C + KC_R5 + 2 * (158 - 64 - 2 * c15 + 4 * g) - 128);
#pragma unroll
    for (int e = 0; e < 3; e++) {
      td[e] = ld_toep<8>(t5 + 16 * (2 - e));
      td[3 + e] = ld_toep<8>(t5 + (KC_R3 - KC_R5) / 4 + 16 * (2 - e));
    }
  }
#pragma unroll
  for (int c = 0; c < 3; c++) iscv[c] = *(const LAS int*)(L + c * CH_BYTES + KC_F1024 + 8);
  TW(5);
  TSX(0, 4);
  {
    _Pragma("unroll 1") for (int spin = 0; spin < SPIN_CAP; spin++) {
      if (__all(v0[3] == tag && v1[3] == tag)) break;
      v0 = ldg16(rs, uoff);
      v1 = ldg16(rs, wave < 4 ? uoff + NTHR * 16 : uoff);
    }
    LAS char* dst = L + SM_US + 2 * UPAD + 4 * tid;
#pragma unroll
    for (int c = 0; c < 3; c++) *(LAS unsigned*)(dst + c * CH_BYTES) = v0[c];
    if (wave < 4) {
#pragma unroll
      for (int c = 0; c < 3; c++) *(LAS unsigned*)(dst + c * CH_BYTES + 4 * NTHR) = v1[c];
    }
  }
  float isc[3];
  f32x2 sb[2][3];
#pragma unroll
  for (int c = 0; c < 3; c++) {
    isc[c] = __int_as_float(__builtin_amdgcn_readfirstlane(iscv[c]));
    sb[0][c] = rbf2(h2_times(v0[c], dsk[c]));
    asm volatile("" : "+v"(sb[0][c]));
  }
  if (wave < 4) {
#pragma unroll
    for (int c = 0; c < 3; c++) {
      sb[1][c] = rbf2(h2_times(v1[c], dsk[c]));
      asm volatile("" : "+v"(sb[1][c]));
    }
  }
  const LAS char* uf16 = C + SM_US + 2 * (UPAD + (sub == 3 ? 8 : 16) * c15 + 8 * g);
  LAS char* yb = C + g * 320 + c15 * 16;
  asm volatile("" : "+v"(uf16), "+v"(yb));
  if (sub == 2) __builtin_amdgcn_s_setprio(PRA);
  __syncthreads();
  TSX(0, 5);
  TW(6);
  {
    const f32x4 zero4 = {0.f, 0.f, 0.f, 0.f};
    if (sub == 0) {
      conv_dense<true>(td, bA, uf16 + 2 * 1024, yb + SM_YD + 4 * YT);
    } else if (sub == 1) {
      conv_dense<false>(td, bA, uf16, yb + SM_YD);
    } else if (sub == 3) {
      f32x4 a2[6], w[4];
#pragma unroll
      for (int i = 0; i < 6; i++) a2[i] = zero4;
      half8 uw[12];
#pragma unroll
      for (int jj = 0; jj < 4; jj++) uw[jj] = ld_frag(uf16 - 48 + 256 * jj);
      SCHED_FENCE();
      w[0] = MFMA16(uw[0], bA, zero4); w[1] = MFMA16(uw[1], bA, zero4);
      w[2] = MFMA16(uw[0], bB, zero4); w[3] = MFMA16(uw[1], bB, zero4);
#pragma unroll
      for (int jj = 0; jj < 6; jj++) {
        if (jj < 4) {
          uw[2 * jj + 4] = ld_frag(uf16 - 48 + 256 * (2 * jj + 4));
          uw[2 * jj + 5] = ld_frag(uf16 - 48 + 256 * (2 * jj + 5));
        }
        const half8 X5 = pack_win(w[0], w[1]), X3 = pack_win(w[2], w[3]);
        SCHED_FENCE();
        if (jj < 5) {
          w[0] = MFMA16(uw[2 * jj + 2], bA, zero4); w[1] = MFMA16(uw[2 * jj + 3], bA, zero4);
          w[2] = MFMA16(uw[2 * jj + 2], bB, zero4); w[3] = MFMA16(uw[2 * jj + 3], bB, zero4);
        }
        if (jj > 0) *(LAS f32x4*)(yb + SM_Y2 + (1 + jj) * YT) = a2[jj - 1];
        SCHED_FENCE();
#pragma unroll
        for (int e = 0; e < 3; e++) {
          if (jj + e < 6) {
            a2[jj + e] = MFMA16(X5, td[e], a2[jj + e]);
            a2[jj + e] = MFMA16(X3, td[3 + e], a2[jj + e]);
          }
        }
        SCHED_FENCE();
      }
      *(LAS f32x4*)(yb + SM_Y2 + 7 * YT) = a2[5];
    } else {
      half8 uw[8];
#pragma unroll
      for (int i = 0; i < 4; i++) {
        uw[2 * i] = ld_frag(uf16 + 512 * i - 48);
        uw[2 * i + 1] = ld_frag(uf16 + 512 * i - 32);
      }
      SCHED_FENCE();
      f32x4 w[8], a3[4];
#pragma unroll
      for (int i = 0; i < 4; i++) {
        w[2 * i] = MFMA16(uw[2 * i], bA, zero4);
        w[2 * i + 1] = MFMA16(uw[2 * i + 1], bB, zero4);
      }
      const half8 X90 = pack_win(w[0], w[2]), X70 = pack_win(w[1], w[3]), X91 = pack_win(w[4], w[6]), X71 = pack_win(w[5], w[7]);
#pragma unroll
      for (int t = 0; t < 4; t++) {
        a3[t] = MFMA16(X90, td[t], zero4);
        a3[t] = MFMA16(X70, td[4 + t], a3[t]);
        if (t >= 2) {
          a3[t] = MFMA16(X91, td[t - 2], a3[t]);
          a3[t] = MFMA16(X71, td[4 + t - 2], a3[t]);
        }
      }
#pragma unroll
      for (int t = 0; t < 4; t++) *(LAS f32x4*)(yb + SM_Y3 + (4 + t) * YT) = a3[t];
      __builtin_amdgcn_s_setprio(0);
    }
  }
  TW(7);
  unsigned wrw[9];
  if (ln_wave) {
#pragma unroll
    for (int k = 0; k < 9; k++) wrw[k] = ((const unsigned*)wr)[hl + 32 * k];
    unsigned xo = (unsigned)(row * (DM * 2) + hl * 4);
    asm volatile("" : "+v"(xo));
#pragma unroll
    for (int k = 0; k < 9; k++) xw[k] = *(const unsigned*)((const char*)x + xo + 128 * k);
  }
  __syncthreads();
  TS(0, 6);
  PAD(2);
#pragma unroll
  for (int i = 0; i < 2; i++) {
    if (i == 0 || wave < 4) {
      const int e = tid + NTHR * i;
      const int nt = (wave >> 1) + 6 * i;
      const LAS char* yp = L + nt * YT + ((e >> 1) & 3) * 320 + ((e >> 3) & 15) * 16 + (e & 1) * 8;
      f32x2 y[3];
#pragma unroll
      for (int c = 0; c < 3; c++) y[c] = *(const LAS f32x2*)(yp + c * CH_BYTES + SM_YD);
      if (nt >= 2) {
#pragma unroll
        for (int c = 0; c < 3; c++) y[c] += *(const LAS f32x2*)(yp + c * CH_BYTES + SM_Y2);
      }
      if (nt >= 4) {
#pragma unroll
        for (int c = 0; c < 3; c++) y[c] += *(const LAS f32x2*)(yp + c * CH_BYTES + SM_Y3);
      }
      u32x4 o;
#pragma unroll
      for (int c = 0; c < 3; c++) {
        const f32x2 z = __builtin_elementwise_fma(rbf2(y[c]), f32x2{isc[c], isc[c]}, sb[i][c]);
        o[c] = pkbf(z[0], z[1]);
      }
      o[3] = tag;
      stg16(rs, WS_ZP + ((e >> 3) * NCONV + b) * 128 + (e & 7) * 16, o);
    }
  }
  TS(0, 7);
  TWFLUSH();
  if (is_ln) {
    f32x2 xf2[9];
#pragma unroll
    for (int k = 0; k < 9; k++) {
      xf2[k] = unpk(xw[k]);
      asm volatile("" : "+v"(xf2[k]));
    }
    unsigned zsel = rsel ? 0x3f800000u : 0x00003f80u;
    asm volatile("" : "+v"(zsel));
    DRAIN();
    {
      const int off = WS_ZP + b * (NCONV * 128) + tid * 16;
      _Pragma("unroll 1") for (int spin = 0; spin < SPIN_CAP; spin++) {
        v0 = ldg16(rs, off);
        v1 = ldg16(rs, off + NTHR * 16);
        if (__all(v0[3] == tag && v1[3] == tag)) break;
        __builtin_amdgcn_s_sleep(2);
      }
      LAS unsigned* dst = tl + (tid & 7) * TLS + 3 * (tid >> 3);
#pragma unroll
      for (int c = 0; c < 3; c++) { dst[c] = v0[c]; dst[288 + c] = v1[c]; }
    }
    __syncthreads();
    TS(0, 8);
    PAD(3);
    LAS unsigned* const okey = (LAS unsigned*)(L + SM_MISC);
    LAS unsigned* const cnt = okey + 16;
    f32x2 hf[9];
    unsigned wn[9], k16 = 0;
    if (wave < 8) {
#pragma unroll
      for (int k = 0; k < 9; k++) {
        const u32x2 t2 = *(const LAS u32x2*)(tl + wave * TLS + 2 * (hl + 32 * k));
        const unsigned t0 = t2[0], t1 = t2[1];
        hf[k] = f32x2{rbf(dot2(t0, zsel, xf2[k][0])), rbf(dot2(t1, zsel, xf2[k][1]))};
      }
      f32x2 nn[9];
      ln_row(hf, nn);
      float dot = 0.f;
#pragma unroll
      for (int k = 0; k < 9; k++) {
        wn[k] = pkbf(nn[k][0], nn[k][1]);
        dot = dot2(wn[k], wrw[k], dot);
      }
      dot = red32_sum(dot);
      const unsigned rb = pkbf(dot, 0.f);
      k16 = rb ^ ((rb & 0x8000u) ? 0xffffu : 0x8000u);
      const unsigned rr = (unsigned)__builtin_amdgcn_readlane((int)k16, 0) | ((unsigned)__builtin_amdgcn_readlane((int)k16, 32) << 16);
      if (lane == 0) __builtin_amdgcn_raw_buffer_store_b64(u32x2{rr, tag}, rs, WS_RG + (b * 8 + wave) * 8, 0, SC1);
      if (hl == 0) okey[2 * wave + rsel] = (k16 << 16) | (unsigned)(SEQ - 1 - row);
    }
    __syncthreads();
    TS(0, 9);
    int orank = 0;
    if (wave < 8) {
      const unsigned mykey = okey[c15];
#pragma unroll
      for (int i = 0; i < 16; i++) orank += okey[i] > mykey;
      DRAIN();
      u32x4 v;
      _Pragma("unroll 1") for (int spin = 0; spin < SPIN_CAP; spin++) {
        v = ldg16(rs, WS_RG + tid * 16);
        if (__all(v[1] == tag && v[3] == tag)) break;
        __builtin_amdgcn_s_sleep(1);
      }
      const unsigned ib = (unsigned)(SEQ - 4 - 4 * tid);
      const unsigned k0 = (v[0] << 16) | (ib + 3), k1 = (v[0] & 0xffff0000u) | (ib + 2);
      const unsigned k2 = (v[2] << 16) | (ib + 1), k3 = (v[2] & 0xffff0000u) | ib;
      int mine = 8;
      for (int c = 0; c < 8; c++) {
        const unsigned oc = (unsigned)__builtin_amdgcn_readlane((int)mykey, __builtin_ctzll(__ballot(orank == c)));
        const int s = __popcll(__ballot(k0 > oc)) + __popcll(__ballot(k1 > oc)) + __popcll(__ballot(k2 > oc)) + __popcll(__ballot(k3 > oc));
        if (s >= 8) break;
        mine = lane == c ? s : mine;
      }
      if (lane < 8) __hip_atomic_fetch_add(cnt + lane, (unsigned)mine, __ATOMIC_RELAXED, __HIP_MEMORY_SCOPE_WORKGROUP);
    }
    __syncthreads();
    TS(0, 10);
    if (wave < 8) {
      const int so = __shfl(orank, 2 * wave + rsel);
      const unsigned rank = so < 8 ? cnt[so] : 8u;
      if (rank < 8) {
#pragma unroll
        for (int m = 0; m < 3; m++) stg16(rs, WS_XS + rank * 1536 + m * 512 + hl * 16, u32x4{wn[3 * m], wn[3 * m + 1], wn[3 * m + 2], tag});
#pragma unroll
        for (int k = 0; k < 9; k++) stg16(rs, WS_HS + rank * 4608 + (hl + 32 * k) * 16, u32x4{pkbf(hf[k][0], hf[k][1]), wn[k], ((unsigned)row << 16) | k16, tag});
      } else {
        const rsrc_t ro = __builtin_amdgcn_make_buffer_rsrc(out, 0, SEQ * DM * 2, 0x00020000);
#pragma unroll
        for (int k = 0; k < 9; k++) {
          const f32x2 o2 = hf[k] + unpk(wn[k]);
          __builtin_amdgcn_raw_buffer_store_b32(pkbf(o2[0], o2[1]), ro, row * (DM * 2) + hl * 4 + 128 * k, 0, OUTM == 1 ? 16 : (OUTM == 2 ? 17 : (OUTM == 3 ? 2 : 0)));
        }
      }
    }
  }
  if (b < RED0) {
    PAD(0);
    TS(0, 12);
  TC(0, 15);
    return;
  }
  {
    const int T = (b - RED0) >> 2, tp = (b - RED0) & 3;
    u32x4 hv = {0u, 0u, 0u, 0u};
    if (wave == 8) {
      _Pragma("unroll 1") for (int spin = 0; spin < SPIN_CAP; spin++) {
        hv = ldg16(rs, WS_HS + (2 * tp + ((lane >> 3) & 1)) * 4608 + (8 * T + (lane & 7)) * 16);
        if (__all(hv[3] == tag)) break;
        __builtin_amdgcn_s_sleep(4);
      }
      wait_ticks(DLY_P);
    }
    __syncthreads();
    TS(0, 11);
    LAS float* red = (LAS float*)(L + SM_MISC + 128);
    if (wave < 8) {
      u32x4 v;
      _Pragma("unroll 1") for (int spin = 0; spin < SPIN_CAP; spin++) {
        v = ldg16(rs, part_off);
        if (__all(v[0] != ~0u && v[1] != ~0u && v[2] != ~0u && v[3] != ~0u)) break;
        __builtin_amdgcn_s_sleep(2);
      }
      f32x4 s;
#pragma unroll
      for (int r = 0; r < 4; r++) {
        float a = __uint_as_float(v[r]);
        a += dppf<0x128>(a);
        a = swap16_sum(a);
        s[r] = swap32_sum(a);
      }
      if (lane < 8) *(LAS f32x4*)(red + wave * 32 + lane * 4) = s;
    }
    __syncthreads();
    if (wave == 8 && lane < 16) {
      float y0 = 0.f, y1 = 0.f;
#pragma unroll
      for (int w = 0; w < 8; w++) {
        const f32x2 p = *(const LAS f32x2*)(red + w * 32 + lane * 2);
        y0 += p[0];
        y1 += p[1];
      }
      const unsigned rb = hv[2] ^ ((hv[2] & 0x8000u) ? 0x8000u : 0xffffu);
      const float sg = rbf(__builtin_amdgcn_rcpf(1.0f + __expf(-bflo(rb))));
      const unsigned yb2 = pkbf(y0, y1);
      const unsigned y2 = pkbf(bflo(yb2) * sg, bfhi(yb2) * sg);
      const unsigned a2 = pkbf(bflo(hv[1]) + bflo(y2), bfhi(hv[1]) + bfhi(y2));
      ((unsigned*)out)[(size_t)((hv[2] >> 16) & (SEQ - 1)) * (DM / 2) + 8 * T + (lane & 7)] = pkbf(bflo(hv[0]) + bflo(a2), bfhi(hv[0]) + bfhi(a2));
      if (b == RED0 && lane == 0) __builtin_amdgcn_raw_buffer_store_b32(tag, rs, WS_EPOCH, 0, SC1);
    }
  }
  PAD(0);
  TS(0, 12);
  TC(0, 15);
}

}

extern "C" void kernel_launch(void* const* d_in, const int* in_sizes, int n_in, void* d_out, int out_size,
                              void* d_ws, size_t ws_size, hipStream_t stream) {
  k_all<<<NCONV + NFFN, NTHR, 0, stream>>>((const u16*)d_in[0], (const u16*)d_in[1], (const u16*)d_in[2], (const u16*)d_in[3],
                                           (const u16*)d_in[4], (const u16*)d_in[5], (const u16*)d_in[6], (const u16*)d_in[8],
                                           (const u16*)d_in[9], (const u16*)d_in[10], (const u16*)d_in[11], (u16*)d_out,
                                           (char*)d_ws);
}
```

```cpp
#define WPAD 0
#define PFM 0
#include <hip/hip_runtime.h>
#include <stdint.h>

#define PRA 2
#define CPF 200
#define PFNT 0
#define PRB 0
#define PFD 5
namespace {

constexpr int SEQ = 2048;
constexpr int DM = 576;
constexpr int NM = 128;
constexpr int FF = 4608;

constexpr int NTHR = 768;
constexpr int NFFN = 64;
constexpr int NCONV = 192;
constexpr int NLN = 128;
constexpr int RED0 = 48;

typedef unsigned short u16;
#define LAS __attribute__((address_space(3)))
using half8 = __attribute__((ext_vector_type(8))) _Float16;
using half2v = __attribute__((ext_vector_type(2))) _Float16;
using bf2v = __attribute__((ext_vector_type(2))) __bf16;
using bf8v = __attribute__((ext_vector_type(8))) __bf16;
using s16x4 = __attribute__((ext_vector_type(4))) short;
using f32x2 = __attribute__((ext_vector_type(2))) float;
using f32x4 = __attribute__((ext_vector_type(4))) float;
using u32x2 = __attribute__((ext_vector_type(2))) unsigned;
using u32x3 = __attribute__((ext_vector_type(3))) unsigned;
typedef u32x3 u32x3u __attribute__((aligned(4)));
using u32x4 = __attribute__((ext_vector_type(4))) unsigned;

constexpr int WS_EPOCH = 0;
constexpr int WS_PF = 4096;
constexpr int WS_KR = WS_PF + NFFN * 128;
constexpr int WS_KF = WS_KR + 8 * 2048;
constexpr int WS_ST = WS_KF + 8192;
constexpr int WS_HS = WS_ST + 16384;
constexpr int WS_UP = 131072;
constexpr int WS_ZP = WS_UP + NCONV * NLN * 128;
constexpr int WS_PART = WS_ZP + NCONV * NLN * 128;
constexpr int WS_XS = WS_PART + NFFN * 8 * DM * 4;
constexpr int WS_END = WS_XS + SEQ * 1536;
static_assert(WS_HS + 8 * 4608 <= WS_UP, "ws");
constexpr size_t WS_TS = 64u << 20;
constexpr int SC1 = 16, NT = 2;
constexpr int SPIN_CAP = 1 << 16;

#define TW(k)
#define TWDECL
#define TSX(k, s)
#define TWFLUSH()
#define TC(k, s)
#define TS(k, s)

__device__ __forceinline__ float bflo(unsigned v) { return __uint_as_float(v << 16); }
__device__ __forceinline__ float bfhi(unsigned v) { return __uint_as_float(v & 0xffff0000u); }
__device__ __forceinline__ unsigned pkbf(float a, float b) {
  f32x2 f = {a, b};
  return __builtin_bit_cast(unsigned, __builtin_convertvector(f, bf2v));
}
__device__ __forceinline__ unsigned pkh(float a, float b) {
  f32x2 f = {a, b};
  return __builtin_bit_cast(unsigned, __builtin_convertvector(f, half2v));
}
__device__ __forceinline__ float rbf(float f) { return __uint_as_float(pkbf(0.f, f)); }
__device__ __forceinline__ f32x2 rbf2(f32x2 v) { return f32x2{rbf(v[0]), rbf(v[1])}; }
__device__ __forceinline__ f32x2 unpk(unsigned v) { return f32x2{bflo(v), bfhi(v)}; }
__device__ __forceinline__ float dot2(unsigned a, unsigned b, float c) {
  return __builtin_amdgcn_fdot2_f32_bf16(__builtin_bit_cast(bf2v, a), __builtin_bit_cast(bf2v, b), c, false);
}
__device__ __forceinline__ float hadd(f32x2 v) {
  float a = v[0], b = v[1];
  asm volatile("" : "+v"(a), "+v"(b));
  return a + b;
}

template <int CTRL>
__device__ __forceinline__ float dppf(float v) {
  return __int_as_float(__builtin_amdgcn_update_dpp(0, __float_as_int(v), CTRL, 0xf, 0xf, true));
}
__device__ __forceinline__ float swap16_sum(float v) {
  auto r = __builtin_amdgcn_permlane16_swap(__float_as_uint(v), __float_as_uint(v), false, false);
  return __uint_as_float(r[0]) + __uint_as_float(r[1]);
}
__device__ __forceinline__ float swap32_sum(float v) {
  auto r = __builtin_amdgcn_permlane32_swap(__float_as_uint(v), __float_as_uint(v), false, false);
  return __uint_as_float(r[0]) + __uint_as_float(r[1]);
}
__device__ __forceinline__ float red32_sum(float v) {
  v += dppf<0xB1>(v);
  v += dppf<0x4E>(v);
  v += dppf<0x141>(v);
  v += dppf<0x140>(v);
  return swap16_sum(v);
}
#define SCHED_FENCE() __builtin_amdgcn_sched_barrier(0)
#define DRAIN() asm volatile("s_waitcnt vmcnt(0)" ::: "memory")

__device__ __forceinline__ float red16_sum(float v) {
  v += dppf<0xB1>(v);
  v += dppf<0x4E>(v);
  v += dppf<0x141>(v);
  return v + dppf<0x140>(v);
}
__device__ __forceinline__ float ln_rinv(float q, float dm) {
  const float var = fmaxf(fmaf(q, 1.0f / DM, -dm * dm), 0.f);
  return __builtin_amdgcn_rcpf(rbf(__builtin_amdgcn_sqrtf(rbf(rbf(var) + 1e-5f))));
}

constexpr int TAB_STRIDE = 544;
constexpr int TAB_ROWS = 56;
constexpr int SM_US = TAB_ROWS * TAB_STRIDE;
constexpr int UPAD = 256;
constexpr int SM_KC = SM_US + (UPAD + SEQ) * 2;
constexpr int RDS = 336 * 4;
constexpr int KC_RD = SM_KC;
constexpr int KC_R5 = KC_RD + 4 * RDS;
constexpr int KC_R3 = KC_R5 + 64 * 4;
constexpr int KC_R9 = KC_R3 + 64 * 4;
constexpr int KC_R7 = KC_R9 + 96 * 4;
constexpr int KC_F1024 = KC_R7 + 96 * 4;
constexpr int CH_BYTES = KC_F1024 + 16;
constexpr int KC_CHUNKS = (CH_BYTES - SM_KC) / 16;
constexpr int YT = 1280;
constexpr int SM_YD = 0, SM_Y2 = 8 * YT - 2 * YT, SM_Y3 = 14 * YT - 4 * YT;
constexpr int SM_T2 = 0, T2S = 4624;
constexpr int SM_RED = 3 * CH_BYTES;
constexpr int SM_ROW = SM_RED + 3072;
constexpr int SM_TOTAL = SM_ROW + 128 + 128 + 32;
constexpr int FS_STAGE = 0;
constexpr int DS = 1184;
constexpr int FS_XS = 72 * DS;
constexpr int FS_RED = FS_XS + 8 * DS;
constexpr int FS_GU = FS_RED + 4 * 9 * 512;
static_assert(FS_GU + 8 * 160 <= SM_TOTAL, "lds");

typedef f32x2 cf;
struct cstep { cf s, r; };
__device__ __forceinline__ cstep mkstep(cf a) { return cstep{a, cf{-a[1], a[0]}}; }
__device__ __forceinline__ cf cmul(cf a, const cstep& b) {
  cf t, o;
  asm("v_pk_mul_f32 %0, %1, %2 op_sel:[1,0]" : "=v"(t) : "v"(a), "v"(b.r));
  asm("v_pk_fma_f32 %0, %1, %2, %3 op_sel_hi:[0,1,1]" : "=v"(o) : "v"(a), "v"(b.s), "v"(t));
  return o;
}
__device__ __forceinline__ unsigned pkc(cf a) { return __builtin_bit_cast(unsigned, __builtin_convertvector(a, half2v)); }
__device__ __forceinline__ cf cexp_p2(float p, float ch, float cl, float wh, float wl) {
  float mag = __builtin_amdgcn_exp2f(p * ch);
  mag = fmaf(mag, (p * 0.693147181f) * cl, mag);
  const float rh = p * wh;
  const float fr = fmaf(p, wl, rh - rintf(rh));
  return cf{mag * __builtin_amdgcn_cosf(fr), mag * __builtin_amdgcn_sinf(fr)};
}
__device__ __forceinline__ float exp_acc(float x) {
  const float th = x * 1.44269502f;
  const float tl = fmaf(x, 1.92596299e-8f, fmaf(x, 1.44269502f, -th));
  const float e = __builtin_amdgcn_exp2f(th);
  return fmaf(e, tl * 0.693147181f, e);
}
__device__ __forceinline__ unsigned wave_max_u32(unsigned v) {
  v = max(v, (unsigned)__builtin_amdgcn_update_dpp(0, (int)v, 0x111, 0xf, 0xf, false));
  v = max(v, (unsigned)__builtin_amdgcn_update_dpp(0, (int)v, 0x112, 0xf, 0xf, false));
  v = max(v, (unsigned)__builtin_amdgcn_update_dpp(0, (int)v, 0x114, 0xf, 0xf, false));
  v = max(v, (unsigned)__builtin_amdgcn_update_dpp(0, (int)v, 0x118, 0xf, 0xf, false));
  v = max(v, (unsigned)__builtin_amdgcn_update_dpp(0, (int)v, 0x142, 0xa, 0xf, false));
  v = max(v, (unsigned)__builtin_amdgcn_update_dpp(0, (int)v, 0x143, 0xc, 0xf, false));
  return (unsigned)__builtin_amdgcn_readlane((int)v, 63);
}

#define MFMA16(a, b, c) __builtin_amdgcn_mfma_f32_16x16x32_f16((a), (b), (c), 0, 0, 0)
#define MFMAB(a, b, c) __builtin_amdgcn_mfma_f32_16x16x32_bf16((a), (b), (c), 0, 0, 0)

__device__ __forceinline__ half8 ld_frag(const LAS char* p) { return *(const LAS half8*)p; }
__device__ __forceinline__ const LAS unsigned* opaque_lds(const LAS char* p) {
  const LAS unsigned* q = (const LAS unsigned*)p;
  asm volatile("" : "+v"(q));
  return q;
}
template <int O2>
__device__ __forceinline__ half8 ld_toep(const LAS unsigned* q) {
  u32x4 v = {q[0], q[1], q[O2], q[O2 + 1]};
  return __builtin_bit_cast(half8, v);
}
__device__ __forceinline__ half8 band_frag(int lane, unsigned pat) {
  const int bits = (int)((pat << (lane & 15)) >> (8 * (lane >> 4)));
  u32x4 v;
#pragma unroll
  for (int j = 0; j < 4; j++) {
    const unsigned lo = (unsigned)__builtin_amdgcn_sbfe(bits, 2 * j, 1), hi = (unsigned)__builtin_amdgcn_sbfe(bits, 2 * j + 1, 1);
    v[j] = __builtin_amdgcn_perm(hi, lo, 0x05040100u) & 0x3c003c00u;
  }
  return __builtin_bit_cast(half8, v);
}
__device__ __forceinline__ half8 pack_win(f32x4 a, f32x4 b) {
  u32x4 v = {pkh(a[0], a[1]), pkh(a[2], a[3]), pkh(b[0], b[1]), pkh(b[2], b[3])};
  return __builtin_bit_cast(half8, v);
}

template <bool HI>
__device__ __forceinline__ void conv_dense(const half8 (&td)[17], half8 te, const LAS char* ub, LAS char* yd) {
  constexpr int S0 = HI ? 0 : 9, NS = 41, PF = PFD;
  const f32x4 zero4 = {0.f, 0.f, 0.f, 0.f};
  f32x4 acc[4] = {zero4, zero4, zero4, zero4};
  half8 u[NS], ue[2];
#pragma unroll
  for (int s = S0; s < S0 + PF; s++) u[s] = ld_frag(ub + 32 * (-33 + 2 * s));
  SCHED_FENCE();
#pragma unroll
  for (int s = S0; s < NS; s++) {
    if (s + PF < NS) u[s + PF] = ld_frag(ub + 32 * (-33 + 2 * (s + PF)));
    if (HI && (s == 0 || s == 2)) ue[s >> 1] = ld_frag(ub + 32 * (-65 + 8 * s));
#pragma unroll
    for (int t = 0; t < 4; t++) {
      const int i = 16 + 8 * t - s;
      if (i >= 0 && i <= 16) acc[t] = MFMA16(td[i], u[s], acc[t]);
    }
    if (HI) {
      if (s == 0) acc[2] = MFMA16(te, u[0], acc[2]);
      if (s == 8) acc[3] = MFMA16(te, u[8], acc[3]);
      if (s == 5) acc[0] = MFMA16(te, ue[0], acc[0]);
      if (s == 7) acc[1] = MFMA16(te, ue[1], acc[1]);
    }
    if (s >= 18 && ((s - 18) & 7) == 0) *(LAS f32x4*)(yd + ((s - 18) >> 3) * YT) = acc[(s - 18) >> 3];
    SCHED_FENCE();
  }
  *(LAS f32x4*)(yd + 3 * YT) = acc[3];
}

typedef __amdgpu_buffer_rsrc_t rsrc_t;
__device__ __forceinline__ u32x4 ldg16(rsrc_t rs, int off) {
  asm volatile("" : "+v"(off) : : "memory");
  return __builtin_amdgcn_raw_buffer_load_b128(rs, off, 0, SC1);
}
__device__ __forceinline__ void stg16(rsrc_t rs, int off, u32x4 v) { __builtin_amdgcn_raw_buffer_store_b128(v, rs, off, 0, SC1); }
__device__ __forceinline__ void wait_ticks(int t) {
  const unsigned long long t0 = __builtin_amdgcn_s_memrealtime();
  while (__builtin_amdgcn_s_memrealtime() - t0 < (unsigned long long)t) __builtin_amdgcn_s_sleep(1);
}
#define UPOS 1
#define DLY_X 40
#define DLY_L 40
#define DLY_P 40

template <class F>
__device__ __forceinline__ void select8(rsrc_t rs, unsigned tag, int lane, F&& pick) {
  const int off = WS_KR + (blockIdx.x & 7) * 2048 + lane * 16;
  u32x4 a, c;
  _Pragma("unroll 1") for (int spin = 0; spin < SPIN_CAP; spin++) {
    a = ldg16(rs, off);
    c = ldg16(rs, off + 1024);
    if (__all(a[3] == tag && c[3] == tag)) break;
    __builtin_amdgcn_s_sleep(1);
  }
  const unsigned t1 = min(a[0], c[0]), t2 = max(a[1], c[1]), third = max(a[2], c[2]);
  unsigned m1 = max(a[0], c[0]), m2 = max(t1, t2), m3 = min(t1, t2), m4 = min(a[1], c[1]), k = 0;
#pragma unroll
  for (int r = 0; r < 8; r++) {
    k = wave_max_u32(m1);
    pick(r, k);
    const bool e = m1 == k;
    m1 = e ? m2 : m1;
    m2 = e ? m3 : m2;
    m3 = e ? m4 : m3;
    m4 = e ? 0u : m4;
  }
  if (__builtin_expect(__any(third > k), 0)) {
    u32x4 f[8];
    _Pragma("unroll 1") for (int spin = 0; spin < SPIN_CAP; spin++) {
      bool ok = true;
#pragma unroll
      for (int i = 0; i < 8; i++) {
        f[i] = ldg16(rs, WS_KF + i * 1024 + lane * 16);
        ok = ok && f[i][1] == tag && f[i][3] == tag;
      }
      if (__all(ok)) break;
      __builtin_amdgcn_s_sleep(1);
    }
    k = 0u;
    _Pragma("unroll 1") for (int r = 0; r < 8; r++) {
      unsigned v = 0u;
#pragma unroll
      for (int i = 0; i < 8; i++) v = max(v, max(f[i][0] <= k - 1u ? f[i][0] : 0u, f[i][2] <= k - 1u ? f[i][2] : 0u));
      k = wave_max_u32(v);
      pick(r, k);
    }
  }
}
__device__ __forceinline__ int key_row(unsigned k) { return (int)(~k & (SEQ - 1)); }

__device__ __forceinline__ void ffn_role(const u16* __restrict__ wg, const u16* __restrict__ wu, const u16* __restrict__ wd,
                                         char* ws, rsrc_t rs, unsigned tagv, LAS char* L) {
  const int tid = threadIdx.x, lane = tid & 63;
  const int wave = __builtin_amdgcn_readfirstlane(tid >> 6);
  const int fw = blockIdx.x;
  const int j = (fw & 7) * 8 + (fw >> 3);
  const int r0 = tid / 18, rem = tid - r0 * 18;
  const int pc = rem >= 9 ? rem - 9 : rem;
  const char* gsrc = (const char*)(rem >= 9 ? wu : wg) + (unsigned)((r0 < 42 ? r0 : 0) * (FF * 2) + j * 144 + pc * 16);
  u32x4 wa[7], wb[7], wc[7];
#pragma unroll
  for (int i = 0; i < 7; i++) wa[i] = __builtin_nontemporal_load((const u32x4*)(gsrc + (42 * i) * (FF * 2)));
  SCHED_FENCE();
#pragma unroll
  for (int i = 0; i < 7; i++)
    wb[i] = __builtin_nontemporal_load((const u32x4*)(gsrc + ((i < 6 || r0 < 36) ? (288 + 42 * i) * (FF * 2) : 0)));
  SCHED_FENCE();
  {
    const char* dsrc = (const char*)wd + (size_t)j * (72 * 1152);
#pragma unroll
    for (int i = 0; i < 7; i++)
      wc[i] = __builtin_nontemporal_load((const u32x4*)(dsrc + (unsigned)((i < 6 || tid < 576) ? (tid + NTHR * i) * 16 : 0)));
  }
  SCHED_FENCE();
  if (tid < 8 * 4) *(LAS unsigned*)(L + FS_GU + (tid >> 2) * 160 + 144 + (tid & 3) * 4) = 0u;
  const int g = lane >> 4, tq = (lane & 15) >> 2, tp = lane & 3;
  const int tg = wave % 3, kq = wave / 3;
  s16x4 fa[3][5][2];
  auto stage_gu = [&](const u32x4 (&w)[7]) {
#pragma unroll
    for (int i = 0; i < 7; i++)
      if (tid < 756 && r0 + 42 * i < 288) *(LAS u32x4*)(L + FS_STAGE + (r0 + 42 * i) * 288 + rem * 16) = w[i];
  };
  auto read_gu = [&]() {
    const LAS char* p = L + FS_STAGE + (160 * (kq & 1) + 8 * g + tq) * 288 + (tp >> 1) * 144 + (tp & 1) * 8 + 48 * tg;
#pragma unroll
    for (int ks = 0; ks < 5; ks++) {
      const int kk = (ks == 4) ? ((kq & 1) ? 3 : 4) : ks;
#pragma unroll
      for (int t = 0; t < 3; t++)
#pragma unroll
        for (int h = 0; h < 2; h++)
          fa[t][ks][h] = __builtin_amdgcn_ds_read_tr16_b64_v4i16((LAS s16x4*)(p + (32 * kk + 4 * h) * 288 + 16 * t));
    }
  };
  stage_gu(wa);
  __syncthreads();
  if (kq < 2) read_gu();
  __syncthreads();
  stage_gu(wb);
  __syncthreads();
  if (kq >= 2) read_gu();
  __syncthreads();
  TS(1, 1);
#pragma unroll
  for (int i = 0; i < 7; i++) {
    const int e = tid + NTHR * i;
    const int row = e / 72;
    if (e < 5184) *(LAS u32x4*)(L + FS_STAGE + row * DS + (e - row * 72) * 16) = wc[i];
  }
  __syncthreads();
  s16x4 fd[3][5];
  {
    const LAS char* p = L + FS_STAGE + tq * DS + (48 * wave + 4 * tp) * 2;
#pragma unroll
    for (int t = 0; t < 3; t++) {
#pragma unroll
      for (int h = 0; h < 4; h++)
        fd[t][h] = __builtin_amdgcn_ds_read_tr16_b64_v4i16((LAS s16x4*)(p + (32 * (h >> 1) + 8 * g + 4 * (h & 1)) * DS + 32 * t));
      fd[t][4] = __builtin_amdgcn_ds_read_tr16_b64_v4i16((LAS s16x4*)(p + (64 + 4 * (g & 1)) * DS + 32 * t));
    }
  }
  const unsigned tag = __builtin_amdgcn_readfirstlane(tagv) + 1u;
  TS(1, 2);
  if (wave < 4) {
    _Pragma("unroll 1") for (int spin = 0; spin < SPIN_CAP; spin++) {
      const unsigned t = ldg16(rs, WS_PF + fw * 128)[0];
      if (__builtin_amdgcn_readfirstlane(t) == tag) break;
      __builtin_amdgcn_s_sleep(2);
    }
    wait_ticks(DLY_X);
    TS(1, 3);
    u32x4 va0, va1, vb0, vb1;
    int roa, rob;
    select8(rs, tag, lane, [&](int r, unsigned k) {
      const int o = WS_XS + key_row(k) * 1536;
      if (r == wave) {
        roa = o;
        va0 = ldg16(rs, o + lane * 16);
        va1 = ldg16(rs, o + 1024 + (lane & 31) * 16);
      }
      if (r == wave + 4) {
        rob = o;
        vb0 = ldg16(rs, o + lane * 16);
        vb1 = ldg16(rs, o + 1024 + (lane & 31) * 16);
      }
    });
    TS(1, 9);
    auto take = [&](u32x4 p0, u32x4 p1, int ro, int slot) {
      _Pragma("unroll 1") for (int spin = 0; spin < SPIN_CAP; spin++) {
        if (__all(p0[3] == tag && p1[3] == tag)) break;
        __builtin_amdgcn_s_sleep(1);
        p0 = ldg16(rs, ro + lane * 16);
        p1 = ldg16(rs, ro + 1024 + (lane & 31) * 16);
      }
      LAS unsigned* dst = (LAS unsigned*)(L + FS_XS + slot * DS) + (lane & 31);
#pragma unroll
      for (int c = 0; c < 3; c++) {
        dst[96 * (lane >> 5) + 32 * c] = p0[c];
        dst[192 + 32 * c] = p1[c];
      }
    };
    take(va0, va1, roa, wave);
    take(vb0, vb1, rob, wave + 4);
  }
  __syncthreads();
  TS(1, 4);
  {
    const f32x4 zero4 = {0.f, 0.f, 0.f, 0.f};
    f32x4 acc[3] = {zero4, zero4, zero4};
    const LAS char* xp = L + FS_XS + (lane & 7) * DS + 16 * g + 64 * (9 * (kq >> 1) + 5 * (kq & 1));
    bf8v xf[5];
#pragma unroll
    for (int ks = 0; ks < 5; ks++) xf[ks] = *(const LAS bf8v*)(xp + 64 * ks);
#pragma unroll
    for (int ks = 0; ks < 5; ks++) {
      if (ks < 4 || !(kq & 1)) {
#pragma unroll
        for (int t = 0; t < 3; t++) {
          const bf8v a = __builtin_bit_cast(bf8v, __builtin_shufflevector(fa[t][ks][0], fa[t][ks][1], 0, 1, 2, 3, 4, 5, 6, 7));
          acc[t] = MFMAB(a, xf[ks], acc[t]);
        }
      }
    }
    if ((lane & 15) < 8) {
#pragma unroll
      for (int t = 0; t < 3; t++) *(LAS f32x4*)(L + FS_RED + ((kq * 9 + 3 * tg + t) * 8 + (lane & 7)) * 64 + 16 * g) = acc[t];
    }
  }
  __syncthreads();
  if (wave < 9) {
    const LAS float* rp = (const LAS float*)(L + FS_RED) + (wave * 8 + (lane >> 3)) * 16 + (lane & 7);
    const float gs = ((rp[0] + rp[1152]) + rp[2304]) + rp[3456];
    const float us = ((rp[8] + rp[1160]) + rp[2312]) + rp[3464];
    const float gp = rbf(gs);
    const float gq = rbf(gp * __builtin_amdgcn_rcpf(1.0f + __expf(-gp)));
    *(LAS u16*)(L + FS_GU + (lane >> 3) * 160 + (wave * 8 + (lane & 7)) * 2) = (u16)pkbf(gq * rbf(us), 0.f);
  }
  __syncthreads();
  {
    const LAS char* gp = L + FS_GU + (lane & 7) * 160;
    const bf8v b0 = *(const LAS bf8v*)(gp + 16 * g), b1 = *(const LAS bf8v*)(gp + 64 + 16 * g);
    const s16x4 b2 = *(const LAS s16x4*)(gp + 128 + 8 * g);
#pragma unroll
    for (int t = 0; t < 3; t++) {
      f32x4 acc = {0.f, 0.f, 0.f, 0.f};
      acc = __builtin_amdgcn_mfma_f32_16x16x16bf16_1k(fd[t][4], b2, acc, 0, 0, 0);
      f32x4 acc2 = {0.f, 0.f, 0.f, 0.f};
      acc2 = MFMAB(__builtin_bit_cast(bf8v, __builtin_shufflevector(fd[t][0], fd[t][1], 0, 1, 2, 3, 4, 5, 6, 7)), b0, acc2);
      acc2 = MFMAB(__builtin_bit_cast(bf8v, __builtin_shufflevector(fd[t][2], fd[t][3], 0, 1, 2, 3, 4, 5, 6, 7)), b1, acc2);
      u32x4 o;
#pragma unroll
      for (int r = 0; r < 4; r++) o[r] = min(__float_as_uint(acc[r] + acc2[r]), 0xfffffffeu);
      if ((lane & 15) < 8) stg16(rs, WS_PART + fw * (8 * DM * 4) + (3 * wave + t) * 512 + (lane & 7) * 64 + 16 * g, o);
    }
  }
  TS(1, 5);
  TC(1, 15);
}

__global__ __launch_bounds__(NTHR, 3) void k_all(const u16* __restrict__ x, const u16* __restrict__ lar,
                                                 const u16* __restrict__ aim, const u16* __restrict__ crl,
                                                 const u16* __restrict__ cim, const u16* __restrict__ ldt,
                                                 const u16* __restrict__ dskip, const u16* __restrict__ wr,
                                                 const u16* __restrict__ wg, const u16* __restrict__ wu,
                                                 const u16* __restrict__ wd, u16* __restrict__ out,
                                                 char* ws) {
  __shared__ __attribute__((aligned(16))) char smem[SM_TOTAL];
  LAS char* const L = (LAS char*)smem;
  unsigned cpf = 0;
  if (blockIdx.x >= NFFN && threadIdx.x >= 256 && threadIdx.x < 256 + CPF)
    cpf = *(const __attribute__((address_space(1))) unsigned*)((__builtin_amdgcn_s_getpc() & ~127ull) + (threadIdx.x - 256) * 128);
  asm volatile("" ::"s"(x), "s"(lar), "s"(aim), "s"(crl), "s"(cim), "s"(ldt), "s"(dskip), "s"(wr), "s"(wg), "s"(wu), "s"(wd),
               "s"(out), "s"(ws));
  const int tid = threadIdx.x, lane = tid & 63, b = (int)blockIdx.x - NFFN;
  const int wave = __builtin_amdgcn_readfirstlane(tid >> 6);
  const rsrc_t rs = __builtin_amdgcn_make_buffer_rsrc(ws, 0, WS_END, 0x00020000);
  const unsigned tagv = __builtin_amdgcn_raw_buffer_load_b32(rs, WS_EPOCH, 0, SC1);
  if (b < 0) {
    TS(1, 0);
  TC(1, 14);
    ffn_role(wg, wu, wd, ws, rs, tagv, L);
    return;
  }
  TWDECL;
  TS(0, 0);
  TC(0, 14);
  const bool is_ln = b < NLN;
  const int hl = lane & 31, rsel = lane >> 5;
  const int row = b * 16 + wave * 2 + rsel;
  const int ch = (wave & 3) < 3 ? (wave & 3) : (wave >> 2), sub = (wave & 3) < 3 ? (wave >> 2) : 3;
  const int cj = (b & ~15) | ((b & 7) << 1) | ((b >> 3) & 1);
  const int d = 3 * cj + ch;
  const int n = (sub & 1) * 64 + lane;
  const unsigned po = (unsigned)(d * NM + n) * 2u, po2 = po ^ 128u;
  auto ld16 = [](const u16* p, unsigned byte_off) { return *(const u16*)((const char*)p + byte_off); };
  const u16 r_lar = ld16(lar, po), r_aim = ld16(aim, po), r_cr = ld16(crl, po), r_ci = ld16(cim, po), r_cr2 = ld16(crl, po2), r_ci2 = ld16(cim, po2);
  const u16 r_ldt = ldt[(unsigned)d], r_dsk = ld16(dskip + 3 * cj, (unsigned)min(lane, 2) * 2u);
  SCHED_FENCE();
  u32x3 cp[4];
  u32x2 xw[9];
  int cpo = 0;
  if (wave >= 8) {
    const int crow = (is_ln ? 16 * b + 8 * (wave & 1) : 32 * (b - NLN) + 8 * (wave & 3)) + (lane & 7);
    const int cpair = (is_ln ? 16 * (wave & 2) : 64) + (lane >> 3);
    const char* src = (const char*)x + (unsigned)(crow * (DM * 2) + cpair * 12);
#pragma unroll
    for (int i = 0; i < 4; i++) cp[i] = *(const u32x3u*)(src + 96 * i);
    cpo = WS_UP + (cpair * SEQ + crow) * 16;
    __builtin_amdgcn_s_setprio(2);
  } else if (wave >= 4 && is_ln) {
    const char* xr = (const char*)x + (unsigned)((b * 16 + (tid >> 4) - 16) * (DM * 2) + (lane & 15) * 8);
#pragma unroll
    for (int k = 0; k < 9; k++) xw[k] = *(const u32x2*)(xr + 128 * k);
    __builtin_amdgcn_s_setprio(2);
  }
  SCHED_FENCE();
  unsigned pf0 = 0;
  auto prefetch = [&]() {
    const int xg = b & 7, sl = b >> 3;
    const char* base = wave < 6 ? (const char*)wd + (xg * 576 + 24 * sl + 4 * wave) * 1152
                                : (const char*)(sl < 12 ? wg : wu) + (288 + 24 * (sl < 12 ? sl : sl - 12) + 4 * (wave - 6)) * (FF * 2) + xg * 1152;
    pf0 = *(const unsigned*)(base + (unsigned)((lane >> 4) * (wave < 6 ? 1152 : FF * 2) + min(lane & 15, 8) * 128));
  };
  const int part_off = WS_PART + (tid >> 3) * (8 * DM * 4) + (b - RED0) * 128 + (tid & 7) * 16;
  LAS char* const C = L + ch * CH_BYTES;
  {
    const int ct = tid & 255;
    LAS char* zc = L + (wave >> 2) * CH_BYTES;
    const u32x4 z = {0u, 0u, 0u, 0u};
    *(LAS u32x4*)(zc + SM_KC + 16 * ct) = z;
    if (ct < KC_CHUNKS - 256) *(LAS u32x4*)(zc + SM_KC + 4096 + 16 * ct) = z;
    else if (ct >= 256 - UPAD / 8) *(LAS u32x4*)(zc + SM_US + 16 * (ct - (256 - UPAD / 8))) = z;
  }
  half8 bA = band_frag(lane, sub == 3 ? 0x1fu << 6 : (sub == 2 ? 0x1ffu << 4 : 0x3u << 12));
  half8 bB = band_frag(lane, sub == 3 ? 0x7u << 11 : 0x7fu << 5);
  asm volatile("" : "+v"(bA), "+v"(bB));
  SCHED_FENCE();
  const unsigned tag = __builtin_amdgcn_readfirstlane(tagv) + 1u;
  float f_lar = bflo(r_lar), f_aim = bflo(r_aim), f_cr = bflo(r_cr), f_ci = bflo(r_ci), f_ldt = bflo(r_ldt), f_dsk = bflo(r_dsk);
  float f_cm = fmaxf(fmaxf(fabsf(f_cr), fabsf(f_ci)), fmaxf(fabsf(bflo(r_cr2)), fabsf(bflo(r_ci2))));
  float f_k0 = f_cr + bflo(r_cr2);
  asm volatile("" : "+v"(f_lar), "+v"(f_aim), "+v"(f_cr), "+v"(f_ci), "+v"(f_cm), "+v"(f_ldt), "+v"(f_dsk), "+v"(f_k0), "+v"(cpf));
  float dsk[3];
#pragma unroll
  for (int c = 0; c < 3; c++) dsk[c] = __int_as_float(__builtin_amdgcn_readlane(__float_as_int(f_dsk), c));
  if (b >= RED0 && tid < 512) stg16(rs, part_off, u32x4{~0u, ~0u, ~0u, ~0u});
  if (wave >= 8) {
#pragma unroll
    for (int i = 0; i < 4; i++) stg16(rs, cpo + i * (8 * SEQ * 16), u32x4{cp[i][0], cp[i][1], cp[i][2], tag});
    __builtin_amdgcn_s_setprio(0);
    if (PFM) prefetch();
  } else if (wave >= 4 && is_ln) {
    unsigned one2 = 0x3f803f80u;
    asm volatile("" : "+s"(one2));
    float s0 = 0.f, s1 = 0.f, q0 = 0.f, q1 = 0.f;
#pragma unroll
    for (int k = 0; k < 9; k++) {
      const unsigned a = xw[k][0], c = xw[k][1];
      s0 = dot2(a, one2, s0);
      q0 = dot2(a, a, q0);
      s1 = dot2(c, one2, s1);
      q1 = dot2(c, c, q1);
    }
    const float mean = red16_sum(s0 + s1) * (1.0f / DM), mbf = rbf(mean);
    float q = red16_sum(q0 + q1), dm = mean;
    if (__builtin_expect(__any(!(q * (1.0f / DM) >= mean * mean * 1.002f)), 0)) {
      f32x2 q2 = {0.f, 0.f};
#pragma unroll
      for (int k = 0; k < 18; k++) {
        const f32x2 dd = unpk(xw[k >> 1][k & 1]) - mbf;
        q2 = __builtin_elementwise_fma(dd, dd, q2);
      }
      q = red16_sum(hadd(q2));
      dm = mean - mbf;
    }
    const float ri = ln_rinv(q, dm);
    auto r2 = __builtin_amdgcn_permlane16_swap(__float_as_uint(ri), __float_as_uint(ri), false, false);
    auto m2 = __builtin_amdgcn_permlane16_swap(__float_as_uint(mbf), __float_as_uint(mbf), false, false);
    if (hl == 0) stg16(rs, WS_ST + (8 * b + (tid >> 5) - 8) * 16, u32x4{r2[0], r2[1], __builtin_amdgcn_perm(m2[1], m2[0], 0x07060302u), tag});
    __builtin_amdgcn_s_setprio(0);
  }
  SCHED_FENCE();
  TSX(0, 1);
  TW(2);
  float inv_scale;
  {
    const float dt = rbf(exp_acc(f_ldt));
    const float are = -rbf(exp_acc(f_lar)) * dt;
    const float aie = f_aim * dt;
    const float chh = are * 1.44269502f;
    const float cl = fmaf(are, 1.92596299e-8f, fmaf(are, 1.44269502f, -chh));
    const float wh = aie * 0.159154936f;
    const float wl = fmaf(aie, 7.13048051e-9f, fmaf(aie, 0.159154936f, -wh));
    const unsigned cmax = wave_max_u32(__float_as_uint(f_cm));
    int se = 385 - (int)(cmax >> 23) - (int)(__float_as_uint(dt) >> 23);
    se = min(max(se, 2), 252);
    const float scale2 = __uint_as_float((unsigned)(se + 1) << 23);
    inv_scale = __uint_as_float((unsigned)(254 - se) << 23);
    const cf cc = {f_cr * dt * scale2, -f_ci * dt * scale2};
    volatile LAS unsigned* tab = (volatile LAS unsigned*)C + n;
    constexpr int TS4 = TAB_STRIDE / 4;
    if (sub < 2) {
      const cstep e1 = mkstep(cexp_p2(1.f, chh, cl, -wh, -wl)), e128 = mkstep(cexp_p2(128.f, chh, cl, -wh, -wl));
      const cstep e2 = mkstep(cmul(e1.s, e1)), e4 = mkstep(cmul(e2.s, e2));
      cf first = cmul(cc, e1);
#pragma unroll
      for (int a = 0; a < 8; a++) {
        const int cnt = a < 2 ? 8 : (a < 4 ? 4 : 2);
        const int r0 = a < 2 ? 8 * a : (a < 4 ? 16 + 4 * (a - 2) : 24 + 2 * (a - 4));
        const cstep st = a < 2 ? e1 : (a < 4 ? e2 : e4);
        cf v = first;
#pragma unroll
        for (int jj = 0; jj < cnt; jj++) {
          tab[(r0 + jj) * TS4] = pkc(v);
          if (a < 7 && jj == ((a == 1 || a == 3) ? 1 : 0)) first = cmul(v, e128);
          if (jj + 1 < cnt) v = cmul(v, st);
        }
      }
    } else {
      const cstep e8 = mkstep(cexp_p2(8.f, chh, cl, wh, wl)), e128 = mkstep(cexp_p2(128.f, chh, cl, -wh, -wl));
      cf bb = cmul(cmul(cc, mkstep(cexp_p2(1024.f, chh, cl, -wh, -wl))), mkstep(cf{e8.s[0], e8.r[0]}));
      cf h = e8.s;
      tab[40 * TS4] = pkh(1.f, 0.f);
#pragma unroll
      for (int i = 0; i < 15; i++) {
        tab[(41 + i) * TS4] = pkc(h);
        if (i < 14) h = cmul(h, e8);
        if (i < 8) {
          tab[(32 + i) * TS4] = pkc(bb);
          if (i < 7) bb = cmul(bb, e128);
        }
      }
    }
    f_k0 *= dt * scale2;
  }
  TW(3);
  if (PFM && wave >= 4 && wave < 8) prefetch();
  __syncthreads();
  const int uoff = WS_UP + (cj >> 1) * (SEQ * 16) + tid * 32;
  u32x4 v0, v1, v2, v3, st0, st1;
  auto ldx = [&]() {
    v0 = ldg16(rs, uoff);
    v1 = ldg16(rs, uoff + 16);
    st0 = ldg16(rs, WS_ST + tid * 16);
    if (wave < 4) {
      v2 = ldg16(rs, uoff + NTHR * 32);
      v3 = ldg16(rs, uoff + NTHR * 32 + 16);
      st1 = ldg16(rs, WS_ST + tid * 16 + NTHR * 16);
    }
  };
  TSX(0, 2);
  const int c15 = lane & 15, g = lane >> 4;
  if (sub < 3) {
    f32x4 f = {0.f, 0.f, 0.f, 0.f};
    const LAS char* pa = C + (sub == 0 ? c15 : (sub == 1 ? 16 + (c15 & 7) : 24 + c15)) * TAB_STRIDE + 16 * g;
    const LAS char* pb = C + (40 + c15) * TAB_STRIDE + 16 * g;
    half8 fa[8], fb[8];
#pragma unroll
    for (int ks = 0; ks < 8; ks++) { fa[ks] = ld_frag(pa + 64 * ks); fb[ks] = ld_frag(pb + 64 * ks); }
    SCHED_FENCE();
#pragma unroll
    for (int ks = 0; ks < 8; ks++) f = MFMA16(fa[ks], fb[ks], f);
    const unsigned f10 = pkh(f[1], f[0]), f32 = pkh(f[3], f[2]);
    auto w16 = [&](int off, unsigned v) { *(volatile LAS u16*)(C + off) = (u16)v; };
    auto w32 = [&](int off, unsigned v) { *(volatile LAS unsigned*)(C + off) = v; };
    auto w64 = [&](int off, unsigned a, unsigned c) { *(volatile LAS u32x2*)(C + off) = u32x2{a, c}; };
    const unsigned f21 = __builtin_amdgcn_alignbit(f10, f32, 16);
    if (sub == 0) {
      const int o = KC_RD - 2 * (128 * (g >> 1) + 4 * (g & 1) + 8 * c15);
      w16(o + 2 * 539, f32);
      w32(o + 2 * 540, f21);
      w16(o + 2 * 542, f10 >> 16);
      w32(o + RDS + 2 * 538, f32);
      w32(o + RDS + 2 * 540, f10);
      w16(o + 2 * RDS + 2 * 537, f32);
      w32(o + 2 * RDS + 2 * 538, f21);
      w16(o + 2 * RDS + 2 * 540, f10 >> 16);
      w64(o + 3 * RDS + 2 * 536, f32, f10);
      const unsigned x = (unsigned)__builtin_amdgcn_readlane((int)f32, 63);
      if (lane < 4) w16(KC_RD + lane * (RDS - 2) + 2 * (543 - 257), x);
    } else if (sub == 1) {
      const unsigned f33 = __builtin_amdgcn_perm(f32, f32, 0x01000100u), f11 = __builtin_amdgcn_perm(f10, f10, 0x01000100u);
      const int o = KC_RD - 2 * (256 + 128 * (g & 1) + 8 * c15) + (g >> 1) * (2 * RDS - 4);
      w32(o + 2 * 534, f33);
      w32(o + 2 * 536, f32);
      w32(o + 2 * 538, f11);
      w32(o + 2 * 540, f10);
      w16(o + RDS + 2 * 533, f32);
      w32(o + RDS + 2 * 534, f33);
      w32(o + RDS + 2 * 536, f21);
      w32(o + RDS + 2 * 538, f11);
      w16(o + RDS + 2 * 540, f10 >> 16);
      const unsigned x = (unsigned)__builtin_amdgcn_readlane((int)f32, 31);
      if (lane < 4) w16(KC_RD + lane * (RDS - 2) + 2 * (543 - 514), x);
    } else if (g < 2) {
      const int o = 2 * (95 - 32 * g - c15);
      w16(KC_R3 + o, f10 >> 16);
      w16(KC_R5 + o, f10);
      w16(KC_R3 + o - 32, f32 >> 16);
      w16(KC_R5 + o - 32, f32);
      if (lane == 31) w16(KC_F1024, f32);
    } else {
      const int o = ((c15 & 1) ? KC_R9 : KC_R7) + 2 * 63 - 64 * (g - 2) - (c15 & 14);
      w16(o, f10 >> 16);
      w16(o + 190, f10 >> 16);
      w16(o - 16, f10);
      w16(o - 16 + 190, f10);
      w16(o - 32, f32 >> 16);
      w16(o - 32 + 190, f32 >> 16);
      w16(o - 48, f32);
      w16(o - 48 + 190, f32);
    }
  } else {
    const float k0 = swap32_sum(red32_sum(f_k0));
    if (lane < 4) *(LAS u16*)(C + KC_RD + lane * (RDS - 2) + 2 * 543) = (u16)pkh(k0, k0);
    if (lane == 0) *(LAS float*)(C + KC_F1024 + 8) = inv_scale;
  }
  TW(4);
  if (PFM == 1 && wave < 4) prefetch();
  __syncthreads();
  ldx();
  asm volatile("" ::: "memory");
  SCHED_FENCE();
  TSX(0, 3);
  half8 td[17];
  int iscv[3];
  if (sub < 2) {
    const volatile LAS u32x2* tb = (const volatile LAS u32x2*)(C + KC_RD + ((3 - c15) & 3) * RDS + 2 * (540 - 16 - (c15 & 12) + 8 * g) - 64 * 16);
#pragma unroll
    for (int i = 16; i >= 0; i--) {
      const u32x2 lo = tb[8 * (16 - i)], hi = tb[8 * (16 - i) + 1];
      td[i] = __builtin_bit_cast(half8, __builtin_shufflevector(lo, hi, 0, 1, 2, 3));
    }
    bA = bA * *(const LAS _Float16*)(C + KC_F1024);
  } else if (sub == 2) {
    const int c = (c15 + 1) & 1;
    const LAS unsigned* t9 = opaque_lds(C + KC_R9 + 192 * c + 2 * (127 - 64 - c15 + 4 * g - c) - 96);
#pragma unroll
    for (int e = 0; e < 4; e++) {
      td[e] = ld_toep<8>(t9 + 8 * (3 - e));
      td[4 + e] = ld_toep<8>(t9 + (KC_R7 - KC_R9) / 4 + 8 * (3 - e));
    }
  } else {
    const LAS unsigned* t5 = opaque_lds(C + KC_R5 + 2 * (158 - 64 - 2 * c15 + 4 * g) - 128);
#pragma unroll
    for (int e = 0; e < 3; e++) {
      td[e] = ld_toep<8>(t5 + 16 * (2 - e));
      td[3 + e] = ld_toep<8>(t5 + (KC_R3 - KC_R5) / 4 + 16 * (2 - e));
    }
  }
#pragma unroll
  for (int c = 0; c < 3; c++) iscv[c] = *(const LAS int*)(L + c * CH_BYTES + KC_F1024 + 8);
  TW(5);
  TSX(0, 4);
  float isc[3];
  f32x2 sb[2][3];
  {
    _Pragma("unroll 1") for (int spin = 0; spin < SPIN_CAP; spin++) {
      bool ok = v0[3] == tag && v1[3] == tag && st0[3] == tag;
      if (wave < 4) ok = ok && v2[3] == tag && v3[3] == tag && st1[3] == tag;
      if (__all(ok)) break;
      ldx();
    }
    LAS char* dst = L + SM_US + 2 * UPAD + 4 * tid;
    const unsigned pa = (cj & 1) ? 0x07060c0cu : 0x01000c0cu, pb = (cj & 1) ? 0x05040c0cu : 0x03020c0cu;
    auto norm = [&](const u32x4& a, const u32x4& e, const u32x4& st, f32x2 (&t)[3], LAS char* d) {
      const f32x2 ri = {__uint_as_float(st[0]), __uint_as_float(st[1])};
      const f32x2 cc = -unpk(st[2]) * ri;
#pragma unroll
      for (int c = 0; c < 3; c++) {
        const int hi = c ? 2 : 1, lo = c >> 1;
        const f32x2 xf = {__uint_as_float(__builtin_amdgcn_perm(a[hi], a[lo], c == 1 ? pb : pa)),
                          __uint_as_float(__builtin_amdgcn_perm(e[hi], e[lo], c == 1 ? pb : pa))};
        const f32x2 uf = __builtin_elementwise_fma(xf, ri, cc);
        *(LAS unsigned*)(d + c * CH_BYTES) = pkc(uf);
        t[c] = __builtin_elementwise_fma(uf, f32x2{dsk[c], dsk[c]}, xf);
        asm volatile("" : "+v"(t[c]));
      }
    };
    norm(v0, v1, st0, sb[0], dst);
    if (wave < 4) norm(v2, v3, st1, sb[1], dst + 4 * NTHR);
  }
#pragma unroll
  for (int c = 0; c < 3; c++) isc[c] = __int_as_float(__builtin_amdgcn_readfirstlane(iscv[c]));
  const LAS char* uf16 = C + SM_US + 2 * (UPAD + (sub == 3 ? 8 : 16) * c15 + 8 * g);
  LAS char* yb = C + g * 320 + c15 * 16;
  asm volatile("" : "+v"(uf16), "+v"(yb));
  if (sub == 2) __builtin_amdgcn_s_setprio(PRA);
  __syncthreads();
  TSX(0, 5);
  TW(6);
  if (PFM == 2 && wave < 4) prefetch();
  {
    const f32x4 zero4 = {0.f, 0.f, 0.f, 0.f};
    if (sub == 0) {
      conv_dense<true>(td, bA, uf16 + 2 * 1024, yb + SM_YD + 4 * YT);
    } else if (sub == 1) {
      conv_dense<false>(td, bA, uf16, yb + SM_YD);
    } else if (sub == 3) {
      f32x4 a2[6], w[4];
#pragma unroll
      for (int i = 0; i < 6; i++) a2[i] = zero4;
      half8 uw[12];
#pragma unroll
      for (int jj = 0; jj < 4; jj++) uw[jj] = ld_frag(uf16 - 48 + 256 * jj);
      SCHED_FENCE();
      w[0] = MFMA16(uw[0], bA, zero4); w[1] = MFMA16(uw[1], bA, zero4);
      w[2] = MFMA16(uw[0], bB, zero4); w[3] = MFMA16(uw[1], bB, zero4);
#pragma unroll
      for (int jj = 0; jj < 6; jj++) {
        if (jj < 4) {
          uw[2 * jj + 4] = ld_frag(uf16 - 48 + 256 * (2 * jj + 4));
          uw[2 * jj + 5] = ld_frag(uf16 - 48 + 256 * (2 * jj + 5));
        }
        const half8 X5 = pack_win(w[0], w[1]), X3 = pack_win(w[2], w[3]);
        SCHED_FENCE();
        if (jj < 5) {
          w[0] = MFMA16(uw[2 * jj + 2], bA, zero4); w[1] = MFMA16(uw[2 * jj + 3], bA, zero4);
          w[2] = MFMA16(uw[2 * jj + 2], bB, zero4); w[3] = MFMA16(uw[2 * jj + 3], bB, zero4);
        }
        if (jj > 0) *(LAS f32x4*)(yb + SM_Y2 + (1 + jj) * YT) = a2[jj - 1];
        SCHED_FENCE();
#pragma unroll
        for (int e = 0; e < 3; e++) {
          if (jj + e < 6) {
            a2[jj + e] = MFMA16(X5, td[e], a2[jj + e]);
            a2[jj + e] = MFMA16(X3, td[3 + e], a2[jj + e]);
          }
        }
        SCHED_FENCE();
      }
      *(LAS f32x4*)(yb + SM_Y2 + 7 * YT) = a2[5];
    } else {
      half8 uw[8];
#pragma unroll
      for (int i = 0; i < 4; i++) {
        uw[2 * i] = ld_frag(uf16 + 512 * i - 48);
        uw[2 * i + 1] = ld_frag(uf16 + 512 * i - 32);
      }
      SCHED_FENCE();
      f32x4 w[8], a3[4];
#pragma unroll
      for (int i = 0; i < 4; i++) {
        w[2 * i] = MFMA16(uw[2 * i], bA, zero4);
        w[2 * i + 1] = MFMA16(uw[2 * i + 1], bB, zero4);
      }
      const half8 X90 = pack_win(w[0], w[2]), X70 = pack_win(w[1], w[3]), X91 = pack_win(w[4], w[6]), X71 = pack_win(w[5], w[7]);
#pragma unroll
      for (int t = 0; t < 4; t++) {
        a3[t] = MFMA16(X90, td[t], zero4);
        a3[t] = MFMA16(X70, td[4 + t], a3[t]);
        if (t >= 2) {
          a3[t] = MFMA16(X91, td[t - 2], a3[t]);
          a3[t] = MFMA16(X71, td[4 + t - 2], a3[t]);
        }
      }
#pragma unroll
      for (int t = 0; t < 4; t++) *(LAS f32x4*)(yb + SM_Y3 + (4 + t) * YT) = a3[t];
      __builtin_amdgcn_s_setprio(0);
    }
  }
  TW(7);
  unsigned mxp = 0, wrw[9];
  u16 wrh[6];
  if (is_ln) {
    mxp = ldg16(rs, WS_ST + (8 * b + (tid & 7)) * 16)[2];
#pragma unroll
    for (int i = 0; i < 6; i++) wrh[i] = wr[(unsigned)(3 * (tid >> 3) + (i < 3 ? i : 285 + i))];
    if (wave == 0) {
#pragma unroll
      for (int k = 0; k < 9; k++) wrw[k] = ((const unsigned*)wr)[hl + 32 * k];
    }
  }
  __syncthreads();
  TS(0, 6);
#pragma unroll
  for (int i = 0; i < 2; i++) {
    if (i == 0 || wave < 4) {
      const int e = tid + NTHR * i;
      const int nt = (wave >> 1) + 6 * i;
      const LAS char* yp = L + nt * YT + ((e >> 1) & 3) * 320 + ((e >> 3) & 15) * 16 + (e & 1) * 8;
      f32x2 y[3];
#pragma unroll
      for (int c = 0; c < 3; c++) y[c] = *(const LAS f32x2*)(yp + c * CH_BYTES + SM_YD);
      if (nt >= 2) {
#pragma unroll
        for (int c = 0; c < 3; c++) y[c] += *(const LAS f32x2*)(yp + c * CH_BYTES + SM_Y2);
      }
      if (nt >= 4) {
#pragma unroll
        for (int c = 0; c < 3; c++) y[c] += *(const LAS f32x2*)(yp + c * CH_BYTES + SM_Y3);
      }
      u32x4 o;
#pragma unroll
      for (int c = 0; c < 3; c++) {
        const f32x2 z = __builtin_elementwise_fma(y[c], f32x2{isc[c], isc[c]}, sb[i][c]);
        o[c] = pkbf(z[0], z[1]);
      }
      o[3] = tag;
      stg16(rs, WS_ZP + ((e >> 3) * NCONV + cj) * 128 + (e & 7) * 16, o);
    }
  }
  TS(0, 7);
  TWFLUSH();
  asm volatile("" ::"v"(pf0));
  if (is_ln) {
    float wf[6], wsum = 0.f, mxr = 0.f;
#pragma unroll
    for (int i = 0; i < 6; i++) wf[i] = bflo(wrh[i]);
    unsigned one2 = 0x3f803f80u, sel0 = 0x00003f80u, sel1 = 0x3f800000u;
    asm volatile("" : "+s"(one2), "+s"(sel0), "+s"(sel1));
    if (wave == 0) {
#pragma unroll
      for (int k = 0; k < 9; k++) wsum = dot2(wrw[k], one2, wsum);
      wsum = red32_sum(wsum);
      mxr = (lane & 8) ? bfhi(mxp) : bflo(mxp);
      __builtin_amdgcn_s_setprio(2);
    }
    float nm0 = -bflo(mxp), nm1 = -bfhi(mxp);
    asm volatile("" : "+v"(wf[0]), "+v"(wf[1]), "+v"(wf[2]), "+v"(wf[3]), "+v"(wf[4]), "+v"(wf[5]), "+v"(nm0), "+v"(nm1), "+v"(wsum), "+v"(mxr));
    DRAIN();
    {
      const int off = WS_ZP + b * (NCONV * 128) + tid * 16;
      _Pragma("unroll 1") for (int spin = 0; spin < SPIN_CAP; spin++) {
        v0 = ldg16(rs, off);
        v1 = ldg16(rs, off + NTHR * 16);
        if (__all(v0[3] == tag && v1[3] == tag)) break;
        __builtin_amdgcn_s_sleep(2);
      }
    }
    TS(0, 8);
    f32x2 hv[6];
    {
      f32x2 ss, qq, pp;
#pragma unroll
      for (int i = 0; i < 6; i++) {
        const unsigned t = i < 3 ? v0[i] : v1[i - 3];
        hv[i] = f32x2{dot2(t, sel0, nm0), dot2(t, sel1, nm1)};
        const f32x2 w2 = {wf[i], wf[i]};
        ss = i ? ss + hv[i] : hv[i];
        qq = i ? __builtin_elementwise_fma(hv[i], hv[i], qq) : hv[i] * hv[i];
        pp = i ? __builtin_elementwise_fma(hv[i], w2, pp) : hv[i] * w2;
      }
      auto fold32 = [](f32x2 v) {
        const float a = v[0], c = v[1];
        auto r = __builtin_amdgcn_permlane32_swap(__float_as_uint(a), __float_as_uint(c), false, false);
        return __uint_as_float(r[0]) + __uint_as_float(r[1]);
      };
      const float s1 = fold32(ss), q1 = fold32(qq);
      float p1 = fold32(pp);
      auto r = __builtin_amdgcn_permlane16_swap(__float_as_uint(s1), __float_as_uint(q1), false, false);
      float a1 = __uint_as_float(r[0]) + __uint_as_float(r[1]);
      a1 += dppf<0x128>(a1);
      p1 += dppf<0x128>(p1);
      *(LAS f32x2*)(L + SM_RED + wave * 256 + ((lane & 23) | ((lane >> 2) & 8)) * 8) = f32x2{a1, p1};
    }
    if (wave == 11 && lane == 0 && !(b & 1)) __builtin_amdgcn_raw_buffer_store_b32(tag, rs, WS_PF + (b >> 1) * 128, 0, SC1);
    __syncthreads();
    if (wave == 0) {
      const LAS f32x2* rp = (const LAS f32x2*)(L + SM_RED + rsel * (6 * 256)) + hl;
      const f32x2 t = ((rp[0] + rp[32]) + (rp[64] + rp[96])) + (rp[128] + rp[160]);
      const float ta = t[0], tp = t[1];
      const float a = swap32_sum(ta), p = swap16_sum(swap32_sum(tp));
      auto r = __builtin_amdgcn_permlane16_swap(__float_as_uint(a), __float_as_uint(a), false, false);
      const float mv = __uint_as_float(r[0]) * (1.0f / DM);
      const float rinv = ln_rinv(__uint_as_float(r[1]), mv);
      const float dl = rbf(mxr + mv) - mxr;
      const unsigned rb = pkbf(0.f, fmaf(-dl, wsum, p) * rinv);
      const int lrow = 2 * (lane & 7) + ((lane >> 3) & 1);
      const unsigned key = ((rb ^ (unsigned)(((int)rb >> 31) | (int)0x80000000)) & 0xffff0000u) | (unsigned)(SEQ - 1 - 16 * b - lrow);
      unsigned kr = key;
      kr = (unsigned)__builtin_amdgcn_update_dpp((int)kr, (int)key, 0x124, 0x2, 0xf, false);
      kr = (unsigned)__builtin_amdgcn_update_dpp((int)kr, (int)key, 0x128, 0x4, 0xf, false);
      kr = (unsigned)__builtin_amdgcn_update_dpp((int)kr, (int)key, 0x12c, 0x8, 0xf, false);
      unsigned rank = (kr > key) + ((unsigned)__builtin_amdgcn_update_dpp(0, (int)kr, 0x121, 0xf, 0xf, true) > key) +
                      ((unsigned)__builtin_amdgcn_update_dpp(0, (int)kr, 0x122, 0xf, 0xf, true) > key) +
                      ((unsigned)__builtin_amdgcn_update_dpp(0, (int)kr, 0x123, 0xf, 0xf, true) > key);
      {
        auto r1 = __builtin_amdgcn_permlane16_swap(rank, rank, false, false);
        rank = r1[0] + r1[1];
        auto r2 = __builtin_amdgcn_permlane32_swap(rank, rank, false, false);
        rank = r2[0] + r2[1];
      }
      u32x4 rec;
#pragma unroll
      for (int i = 0; i < 3; i++) rec[i] = (unsigned)__builtin_amdgcn_readlane((int)key, __builtin_ctzll(__ballot(rank == (unsigned)i)));
      rec[3] = tag;
      if (lane < 8) stg16(rs, WS_KR + lane * 2048 + b * 16, rec);
      if (lane < 16) {
        if (rank < 8) __builtin_amdgcn_raw_buffer_store_b64(u32x2{key, tag}, rs, WS_KF + b * 64 + rank * 8, 0, SC1);
        LAS float* ri = (LAS float*)(L + SM_ROW + (lane & 7) * 16) + (lane >> 3);
        ri[0] = -dl * rinv;
        ri[2] = rinv;
        *(LAS u32x2*)(L + SM_ROW + 128 + lrow * 8) = u32x2{key, rank};
      }
      __builtin_amdgcn_s_setprio(0);
    }
    TS(0, 9);
    __syncthreads();
    {
      const f32x4 ri = *(const LAS f32x4*)(L + SM_ROW + (tid & 7) * 16);
      const f32x2 c2 = {ri[0], ri[1]}, r2 = {ri[2], ri[3]};
      LAS char* dst = L + SM_T2 + (tid & 7) * T2S + (tid >> 3) * 24;
#pragma unroll
      for (int i = 0; i < 6; i++) {
        const f32x2 w2 = __builtin_elementwise_fma(hv[i], r2, c2);
        *(LAS u32x2*)(dst + (i < 3 ? 8 * i : 8 * (285 + i))) = u32x2{i < 3 ? v0[i] : v1[i - 3], pkbf(w2[0], w2[1])};
      }
    }
    __syncthreads();
    LAS unsigned* const selp = (LAS unsigned*)(L + SM_ROW + 256);
    unsigned hp[9], wn[9], key = 0, lrank = 8;
    auto put_row = [&]() {
#pragma unroll
      for (int m = 0; m < 3; m++) stg16(rs, WS_XS + row * 1536 + m * 512 + hl * 16, u32x4{wn[3 * m], wn[3 * m + 1], wn[3 * m + 2], tag});
    };
    if (wave < 8) {
      const u32x2 kr = *(const LAS u32x2*)(L + SM_ROW + 128 + (2 * wave + rsel) * 8);
      key = kr[0];
      lrank = kr[1];
      const unsigned hsel = rsel ? 0x07060302u : 0x05040100u;
#pragma unroll
      for (int k = 0; k < 9; k++) {
        const u32x4 t = *(const LAS u32x4*)(L + SM_T2 + wave * T2S + (hl + 32 * k) * 16);
        hp[k] = __builtin_amdgcn_perm(t[2], t[0], hsel);
        wn[k] = __builtin_amdgcn_perm(t[3], t[1], hsel);
      }
      if (lrank < 2) put_row();
    } else if (wave == 8) {
      wait_ticks(DLY_L);
      select8(rs, tag, lane, [&](int r, unsigned k) { selp[r] = k; });
    }
    __syncthreads();
    TS(0, 10);
    if (wave < 8) {
      unsigned rank = 8;
#pragma unroll
      for (int r = 0; r < 8; r++) rank = selp[r] == key ? r : rank;
      if (rank < 8) {
        if (lrank >= 2) put_row();
#pragma unroll
        for (int k = 0; k < 9; k++) stg16(rs, WS_HS + rank * 4608 + (hl + 32 * k) * 16, u32x4{hp[k], wn[k], key, tag});
      } else {
        unsigned* orow = (unsigned*)(out + (size_t)row * DM) + hl;
#pragma unroll
        for (int k = 0; k < 9; k++) {
          const f32x2 o2 = unpk(hp[k]) + unpk(wn[k]);
          orow[32 * k] = pkbf(o2[0], o2[1]);
        }
      }
    }
  }
  if (b < RED0) {
    TS(0, 12);
  TC(0, 15);
    return;
  }
  {
    const int T = (b - RED0) >> 2, tp = (b - RED0) & 3;
    u32x4 hv = {0u, 0u, 0u, 0u};
    if (wave == 8) {
      _Pragma("unroll 1") for (int spin = 0; spin < SPIN_CAP; spin++) {
        hv = ldg16(rs, WS_HS + (2 * tp + ((lane >> 3) & 1)) * 4608 + (8 * T + (lane & 7)) * 16);
        if (__all(hv[3] == tag)) break;
        __builtin_amdgcn_s_sleep(4);
      }
      wait_ticks(DLY_P);
    }
    __syncthreads();
    TS(0, 11);
    LAS float* red = (LAS float*)(L + SM_RED);
    if (wave < 8) {
      u32x4 v;
      _Pragma("unroll 1") for (int spin = 0; spin < SPIN_CAP; spin++) {
        v = ldg16(rs, part_off);
        if (__all(v[0] != ~0u && v[1] != ~0u && v[2] != ~0u && v[3] != ~0u)) break;
        __builtin_amdgcn_s_sleep(2);
      }
      f32x4 s;
#pragma unroll
      for (int r = 0; r < 4; r++) {
        float a = __uint_as_float(v[r]);
        a += dppf<0x128>(a);
        a = swap16_sum(a);
        s[r] = swap32_sum(a);
      }
      if (lane < 8) *(LAS f32x4*)(red + wave * 32 + lane * 4) = s;
    }
    __syncthreads();
    if (wave == 8 && lane < 16) {
      float y0 = 0.f, y1 = 0.f;
#pragma unroll
      for (int w = 0; w < 8; w++) {
        const f32x2 p = *(const LAS f32x2*)(red + w * 32 + lane * 2);
        y0 += p[0];
        y1 += p[1];
      }
      const unsigned rb = hv[2] ^ ((int)hv[2] < 0 ? 0x80000000u : 0xffffffffu);
      const float sg = rbf(__builtin_amdgcn_rcpf(1.0f + __expf(-bfhi(rb))));
      const unsigned yb2 = pkbf(y0, y1);
      const unsigned y2 = pkbf(bflo(yb2) * sg, bfhi(yb2) * sg);
      const unsigned a2 = pkbf(bflo(hv[1]) + bflo(y2), bfhi(hv[1]) + bfhi(y2));
      ((unsigned*)out)[(size_t)key_row(hv[2]) * (DM / 2) + 8 * T + (lane & 7)] = pkbf(bflo(hv[0]) + bflo(a2), bfhi(hv[0]) + bfhi(a2));
      if (b == RED0 && lane == 0) __builtin_amdgcn_raw_buffer_store_b32(tag, rs, WS_EPOCH, 0, SC1);
    }
  }
  TS(0, 12);
  TC(0, 15);
}

}

extern "C" void kernel_launch(void* const* d_in, const int* in_sizes, int n_in, void* d_out, int out_size,
                              void* d_ws, size_t ws_size, hipStream_t stream) {
  k_all<<<NCONV + NFFN, NTHR, 0, stream>>>((const u16*)d_in[0], (const u16*)d_in[1], (const u16*)d_in[2], (const u16*)d_in[3],
                                           (const u16*)d_in[4], (const u16*)d_in[5], (const u16*)d_in[6], (const u16*)d_in[8],
                                           (const u16*)d_in[9], (const u16*)d_in[10], (const u16*)d_in[11], (u16*)d_out,
                                           (char*)d_ws);
}
```

```cpp
#include <hip/hip_runtime.h>
#include <stdint.h>

#define PRA 0
#define PFM 4
#define CPF 232
#define WPAD 0
#define PFNT 0
#define WNT 1
#define WNTS " nt"
#define PTH 4
#define PTL 2
#define EB 1
#define PRB 0
#define PFD 10
namespace {

constexpr int SEQ = 2048;
constexpr int DM = 576;
constexpr int NM = 128;
constexpr int FF = 4608;

constexpr int NTHR = 768;
constexpr int NFFN = 64;
constexpr int NCONV = 192;
constexpr int NLN = 128;
constexpr int RED0 = 48;

typedef unsigned short u16;
#define LAS __attribute__((address_space(3)))
using half8 = __attribute__((ext_vector_type(8))) _Float16;
using half2v = __attribute__((ext_vector_type(2))) _Float16;
using bf2v = __attribute__((ext_vector_type(2))) __bf16;
using bf8v = __attribute__((ext_vector_type(8))) __bf16;
using s16x4 = __attribute__((ext_vector_type(4))) short;
using f32x2 = __attribute__((ext_vector_type(2))) float;
using f32x4 = __attribute__((ext_vector_type(4))) float;
using u32x2 = __attribute__((ext_vector_type(2))) unsigned;
using u32x3 = __attribute__((ext_vector_type(3))) unsigned;
typedef u32x3 u32x3u __attribute__((aligned(4)));
using u32x4 = __attribute__((ext_vector_type(4))) unsigned;

constexpr int WS_EPOCH = 0;
constexpr int WS_PF = 4096;
constexpr int WS_KR = WS_PF + NFFN * 128;
constexpr int WS_KF = WS_KR + 8 * 2048;
constexpr int WS_ST = WS_KF + 8192;
constexpr int WS_HS = WS_ST + 16384;
constexpr int WS_UP = 131072;
constexpr int WS_ZP = WS_UP + NCONV * NLN * 128;
constexpr int WS_PART = WS_ZP + NCONV * NLN * 128;
constexpr int WS_XS = WS_PART + NFFN * 8 * DM * 4;
constexpr int WS_END = WS_XS + SEQ * 1536;
static_assert(WS_HS + 8 * 4608 <= WS_UP, "ws");
constexpr size_t WS_TS = 64u << 20;
constexpr int SC1 = 16, NT = 2;
constexpr int SPIN_CAP = 1 << 16;

#define TW(k)
#define TWDECL
#define TSX(k, s)
#define TWFLUSH()
#define TC(k, s)
#define TS(k, s)

__device__ __forceinline__ float bflo(unsigned v) { return __uint_as_float(v << 16); }
__device__ __forceinline__ float bfhi(unsigned v) { return __uint_as_float(v & 0xffff0000u); }
__device__ __forceinline__ unsigned pkbf(float a, float b) {
  f32x2 f = {a, b};
  return __builtin_bit_cast(unsigned, __builtin_convertvector(f, bf2v));
}
__device__ __forceinline__ unsigned pkh(float a, float b) {
  f32x2 f = {a, b};
  return __builtin_bit_cast(unsigned, __builtin_convertvector(f, half2v));
}
__device__ __forceinline__ float rbf(float f) { return __uint_as_float(pkbf(0.f, f)); }
__device__ __forceinline__ f32x2 rbf2(f32x2 v) { return f32x2{rbf(v[0]), rbf(v[1])}; }
__device__ __forceinline__ f32x2 unpk(unsigned v) { return f32x2{bflo(v), bfhi(v)}; }
__device__ __forceinline__ float dot2(unsigned a, unsigned b, float c) {
  return __builtin_amdgcn_fdot2_f32_bf16(__builtin_bit_cast(bf2v, a), __builtin_bit_cast(bf2v, b), c, false);
}
__device__ __forceinline__ float hadd(f32x2 v) {
  float a = v[0], b = v[1];
  asm volatile("" : "+v"(a), "+v"(b));
  return a + b;
}

template <int CTRL>
__device__ __forceinline__ float dppf(float v) {
  return __int_as_float(__builtin_amdgcn_update_dpp(0, __float_as_int(v), CTRL, 0xf, 0xf, true));
}
__device__ __forceinline__ float swap16_sum(float v) {
  auto r = __builtin_amdgcn_permlane16_swap(__float_as_uint(v), __float_as_uint(v), false, false);
  return __uint_as_float(r[0]) + __uint_as_float(r[1]);
}
__device__ __forceinline__ float swap32_sum(float v) {
  auto r = __builtin_amdgcn_permlane32_swap(__float_as_uint(v), __float_as_uint(v), false, false);
  return __uint_as_float(r[0]) + __uint_as_float(r[1]);
}
__device__ __forceinline__ float red32_sum(float v) {
  v += dppf<0xB1>(v);
  v += dppf<0x4E>(v);
  v += dppf<0x141>(v);
  v += dppf<0x140>(v);
  return swap16_sum(v);
}
#define SCHED_FENCE() __builtin_amdgcn_sched_barrier(0)
#define DRAIN() asm volatile("s_waitcnt vmcnt(0)" ::: "memory")

__device__ __forceinline__ float red16_sum(float v) {
  v += dppf<0xB1>(v);
  v += dppf<0x4E>(v);
  v += dppf<0x141>(v);
  return v + dppf<0x140>(v);
}
__device__ __forceinline__ float ln_rinv(float q, float dm) {
  const float var = fmaxf(fmaf(q, 1.0f / DM, -dm * dm), 0.f);
  return __builtin_amdgcn_rcpf(rbf(__builtin_amdgcn_sqrtf(rbf(rbf(var) + 1e-5f))));
}

constexpr int TAB_STRIDE = 544;
constexpr int TAB_ROWS = 58;
constexpr int TR_B = 18, TR_M = 26, TR_F = 34, TR_H = 42;
constexpr int SM_US = TAB_ROWS * TAB_STRIDE;
constexpr int UPAD = 256;
constexpr int SM_KC = SM_US + (UPAD + SEQ) * 2;
constexpr int RDS = 336 * 4;
constexpr int KC_RD = SM_KC;
constexpr int KC_R5 = KC_RD + 4 * RDS;
constexpr int KC_R3 = KC_R5 + 64 * 4;
constexpr int KC_R9 = KC_R3 + 64 * 4;
constexpr int KC_R7 = KC_R9 + 52 * 4;
constexpr int CH_BYTES = KC_R7 + 52 * 4;
constexpr int KC_CHUNKS = (CH_BYTES - SM_KC) / 16;
constexpr int YT = 1280;
constexpr int SM_YD = 0, SM_Y2 = 8 * YT - 2 * YT, SM_Y3 = 14 * YT - 4 * YT;
constexpr int SM_T2 = 0, T2S = 4624;
constexpr int SM_RED = 3 * CH_BYTES;
constexpr int SM_ROW = SM_RED + 3072;
constexpr int SM_ISC = SM_ROW + 128 + 128 + 32;
constexpr int SM_MX = SM_ISC + 16;
constexpr int SM_WR = SM_MX + 32;
constexpr int RG = 9216;
constexpr int DS = 1184;
constexpr int FS_XS = 14 * RG;
constexpr int FS_RED = 0;
constexpr int FS_GU = FS_XS + 8 * DS;
constexpr int SM_TOTAL = FS_GU + 8 * 160;
static_assert(SM_WR + DM * 4 <= SM_TOTAL && SM_ISC % 16 == 0 && CH_BYTES % 16 == 0, "lds");

typedef f32x2 cf;
struct cstep { cf s, r; };
__device__ __forceinline__ cstep mkstep(cf a) { return cstep{a, cf{-a[1], a[0]}}; }
__device__ __forceinline__ cf cmul(cf a, const cstep& b) {
  cf t, o;
  asm("v_pk_mul_f32 %0, %1, %2 op_sel:[1,0]" : "=v"(t) : "v"(a), "v"(b.r));
  asm("v_pk_fma_f32 %0, %1, %2, %3 op_sel_hi:[0,1,1]" : "=v"(o) : "v"(a), "v"(b.s), "v"(t));
  return o;
}
template <int R0, int R1, int R2, int R3>
__device__ __forceinline__ void tab_step(const LAS char* tab, cf& a0, cf& a1, cf& b0, cf& b1, const cstep& p, const cstep& q) {
  unsigned t0, t1, t2, t3;
  cf m0, m1, m2, m3;
  asm volatile(
      "v_cvt_pk_f16_f32 %0, %8, %9\n\tv_cvt_pk_f16_f32 %1, %10, %11\n\tv_cvt_pk_f16_f32 %2, %12, %13\n\tv_cvt_pk_f16_f32 %3, %14, %15\n\t"
      "v_pk_mul_f32 %4, %16, %20 op_sel:[1,0]\n\tds_write_b32 %22, %0 offset:%23\n\t"
      "v_pk_mul_f32 %5, %17, %20 op_sel:[1,0]\n\tds_write_b32 %22, %1 offset:%24\n\t"
      "v_pk_mul_f32 %6, %18, %21 op_sel:[1,0]\n\tds_write_b32 %22, %2 offset:%25\n\t"
      "v_pk_mul_f32 %7, %19, %21 op_sel:[1,0]\n\tds_write_b32 %22, %3 offset:%26"
      : "=&v"(t0), "=&v"(t1), "=&v"(t2), "=&v"(t3), "=&v"(m0), "=&v"(m1), "=&v"(m2), "=&v"(m3)
      : "v"(a0[0]), "v"(a0[1]), "v"(a1[0]), "v"(a1[1]), "v"(b0[0]), "v"(b0[1]), "v"(b1[0]), "v"(b1[1]), "v"(a0), "v"(a1), "v"(b0), "v"(b1),
        "v"(p.r), "v"(q.r), "v"(tab), "i"(R0 * TAB_STRIDE), "i"(R1 * TAB_STRIDE), "i"(R2 * TAB_STRIDE), "i"(R3 * TAB_STRIDE)
      : "memory");
  asm volatile(
      "v_pk_fma_f32 %0, %0, %8, %4 op_sel_hi:[0,1,1]\n\tv_pk_fma_f32 %1, %1, %8, %5 op_sel_hi:[0,1,1]\n\t"
      "v_pk_fma_f32 %2, %2, %9, %6 op_sel_hi:[0,1,1]\n\tv_pk_fma_f32 %3, %3, %9, %7 op_sel_hi:[0,1,1]"
      : "+&v"(a0), "+&v"(a1), "+&v"(b0), "+&v"(b1)
      : "v"(m0), "v"(m1), "v"(m2), "v"(m3), "v"(p.s), "v"(q.s));
}
__device__ __forceinline__ unsigned pkc(cf a) { return __builtin_bit_cast(unsigned, __builtin_convertvector(a, half2v)); }
__device__ __forceinline__ cf cexp_p2(float p, float ch, float cl, float wh, float wl) {
  float mag = __builtin_amdgcn_exp2f(p * ch);
  mag = fmaf(mag, (p * 0.693147181f) * cl, mag);
  const float rh = p * wh;
  const float fr = fmaf(p, wl, rh - rintf(rh));
  return cf{mag * __builtin_amdgcn_cosf(fr), mag * __builtin_amdgcn_sinf(fr)};
}
__device__ __forceinline__ float exp_acc(float x) {
  const float th = x * 1.44269502f;
  const float tl = fmaf(x, 1.92596299e-8f, fmaf(x, 1.44269502f, -th));
  const float e = __builtin_amdgcn_exp2f(th);
  return fmaf(e, tl * 0.693147181f, e);
}
__device__ __forceinline__ unsigned wave_max_u32(unsigned v) {
  v = max(v, (unsigned)__builtin_amdgcn_update_dpp(0, (int)v, 0x111, 0xf, 0xf, false));
  v = max(v, (unsigned)__builtin_amdgcn_update_dpp(0, (int)v, 0x112, 0xf, 0xf, false));
  v = max(v, (unsigned)__builtin_amdgcn_update_dpp(0, (int)v, 0x114, 0xf, 0xf, false));
  v = max(v, (unsigned)__builtin_amdgcn_update_dpp(0, (int)v, 0x118, 0xf, 0xf, false));
  v = max(v, (unsigned)__builtin_amdgcn_update_dpp(0, (int)v, 0x142, 0xa, 0xf, false));
  v = max(v, (unsigned)__builtin_amdgcn_update_dpp(0, (int)v, 0x143, 0xc, 0xf, false));
  return (unsigned)__builtin_amdgcn_readlane((int)v, 63);
}

#define MFMA16(a, b, c) __builtin_amdgcn_mfma_f32_16x16x32_f16((a), (b), (c), 0, 0, 0)
#define MFMAB(a, b, c) __builtin_amdgcn_mfma_f32_16x16x32_bf16((a), (b), (c), 0, 0, 0)

__device__ __forceinline__ half8 ld_frag(const LAS char* p) { return *(const LAS half8*)p; }
__device__ __forceinline__ const LAS unsigned* opaque_lds(const LAS char* p) {
  const LAS unsigned* q = (const LAS unsigned*)p;
  asm volatile("" : "+v"(q));
  return q;
}
template <int O2>
__device__ __forceinline__ half8 ld_toep(const LAS unsigned* q) {
  u32x4 v = {q[0], q[1], q[O2], q[O2 + 1]};
  return __builtin_bit_cast(half8, v);
}
__device__ __forceinline__ half8 band_frag(int lane, unsigned pat, unsigned one) {
  const int bits = (int)((pat << (lane & 15)) >> (8 * (lane >> 4)));
  u32x4 v;
#pragma unroll
  for (int j = 0; j < 4; j++) {
    const unsigned lo = (unsigned)__builtin_amdgcn_sbfe(bits, 2 * j, 1), hi = (unsigned)__builtin_amdgcn_sbfe(bits, 2 * j + 1, 1);
    v[j] = __builtin_amdgcn_perm(hi, lo, 0x05040100u) & one;
  }
  return __builtin_bit_cast(half8, v);
}
__device__ __forceinline__ half8 pack_win(f32x4 a, f32x4 b) {
  u32x4 v = {pkh(a[0], a[1]), pkh(a[2], a[3]), pkh(b[0], b[1]), pkh(b[2], b[3])};
  return __builtin_bit_cast(half8, v);
}

__device__ __forceinline__ half8 ld_td(const LAS char* tp, int i) {
  const volatile LAS u32x2* tb = (const volatile LAS u32x2*)tp;
  const u32x2 lo = tb[8 * (16 - i)], hi = tb[8 * (16 - i) + 1];
  return __builtin_bit_cast(half8, __builtin_shufflevector(lo, hi, 0, 1, 2, 3));
}
__device__ __forceinline__ void conv_dense(half8 (&td)[17], const LAS char* tp, half8 tmask, const LAS char* f1024, const LAS char* ub, LAS char* yd) {
  constexpr int S0 = 9, NS = 73, PF = PFD;
  const f32x4 zero4 = {0.f, 0.f, 0.f, 0.f};
  f32x4 acc[8] = {zero4, zero4, zero4, zero4, zero4, zero4, zero4, zero4};
  half8 u[NS], te;
  unsigned fe = 0;
#pragma unroll
  for (int s = S0; s < S0 + PF; s++) {
    u[s] = ld_frag(ub + 64 * s);
    if (s == S0 + 3) {
      td[0] = ld_td(tp, 0);
      fe = *(const LAS u16*)f1024;
    }
    SCHED_FENCE();
  }
#pragma unroll
  for (int s = S0; s < NS; s++) {
    if (s + PF < NS) u[s + PF] = ld_frag(ub + 64 * (s + PF));
#pragma unroll
    for (int t = 0; t < 8; t++) {
      const int i = 16 + 8 * t - s;
      if (i >= 0 && i <= 16) acc[t] = MFMA16(td[i], u[s], acc[t]);
    }
    if (s == 14) {
      fe |= fe << 16;
      te = __builtin_bit_cast(half8, __builtin_bit_cast(u32x4, tmask) & u32x4{fe, fe, fe, fe});
    }
    if (s >= 16 && s <= 40 && (s & 7) == 0) acc[s / 8 + 2] = MFMA16(te, u[s], acc[s / 8 + 2]);
    if (s >= 18 && ((s - 18) & 7) == 0) *(LAS f32x4*)(yd + ((s - 18) >> 3) * YT) = acc[(s - 18) >> 3];
    SCHED_FENCE();
  }
  *(LAS f32x4*)(yd + 7 * YT) = acc[7];
}

typedef __amdgpu_buffer_rsrc_t rsrc_t;
__device__ __forceinline__ u32x4 ldg16(rsrc_t rs, int off, int so = 0) {
  asm volatile("" : "+s"(so) : : "memory");
  return __builtin_amdgcn_raw_buffer_load_b128(rs, off, so, SC1);
}
__device__ __forceinline__ void stg16(rsrc_t rs, int off, u32x4 v) { __builtin_amdgcn_raw_buffer_store_b128(v, rs, off, 0, SC1); }
__device__ __forceinline__ void dma16(const char* g, unsigned lds) {
  unsigned keep;
  asm volatile("s_mov_b32 %0, m0\n\ts_mov_b32 m0, %2\n\ts_nop 0\n\tglobal_load_lds_dwordx4 %1, off" WNTS "\n\ts_mov_b32 m0, %0"
               : "=&s"(keep) : "v"(g), "s"(lds) : "memory");
}
__device__ __forceinline__ void wait_ticks(int t) {
  const unsigned long long t0 = __builtin_amdgcn_s_memrealtime();
  while (__builtin_amdgcn_s_memrealtime() - t0 < (unsigned long long)t) __builtin_amdgcn_s_sleep(1);
}
#define UPOS 1
#define DLY_X 20
#define DLY_W 100
#define DLY_L 20
#define DLY_P 20

template <class F>
__device__ __forceinline__ void select8(rsrc_t rs, unsigned tag, int lane, F&& pick) {
  const int off = WS_KR + (blockIdx.x & 7) * 2048 + lane * 16;
  u32x4 a, c;
  _Pragma("unroll 1") for (int spin = 0; spin < SPIN_CAP; spin++) {
    a = ldg16(rs, off);
    c = ldg16(rs, off + 1024);
    if (__all(a[3] == tag && c[3] == tag)) break;
    __builtin_amdgcn_s_sleep(1);
  }
  const unsigned t1 = min(a[0], c[0]), t2 = max(a[1], c[1]), third = max(a[2], c[2]);
  unsigned m1 = max(a[0], c[0]), m2 = max(t1, t2), m3 = min(t1, t2), m4 = min(a[1], c[1]), k = 0;
#pragma unroll
  for (int r = 0; r < 8; r++) {
    k = wave_max_u32(m1);
    pick(r, k);
    const bool e = m1 == k;
    m1 = e ? m2 : m1;
    m2 = e ? m3 : m2;
    m3 = e ? m4 : m3;
    m4 = e ? 0u : m4;
  }
  if (__builtin_expect(__any(third > k), 0)) {
    u32x4 f[8];
    _Pragma("unroll 1") for (int spin = 0; spin < SPIN_CAP; spin++) {
      bool ok = true;
#pragma unroll
      for (int i = 0; i < 8; i++) {
        f[i] = ldg16(rs, WS_KF + i * 1024 + lane * 16);
        ok = ok && f[i][1] == tag && f[i][3] == tag;
      }
      if (__all(ok)) break;
      __builtin_amdgcn_s_sleep(1);
    }
    k = 0u;
    _Pragma("unroll 1") for (int r = 0; r < 8; r++) {
      unsigned v = 0u;
#pragma unroll
      for (int i = 0; i < 8; i++) v = max(v, max(f[i][0] <= k - 1u ? f[i][0] : 0u, f[i][2] <= k - 1u ? f[i][2] : 0u));
      k = wave_max_u32(v);
      pick(r, k);
    }
  }
}
__device__ __forceinline__ int key_row(unsigned k) { return (int)(~k & (SEQ - 1)); }

__device__ __forceinline__ void ffn_role(const u16* __restrict__ wg, const u16* __restrict__ wu, const u16* __restrict__ wd,
                                         char* ws, rsrc_t rs, unsigned tagv, LAS char* L) {
  const int tid = threadIdx.x, lane = tid & 63;
  const int wave = __builtin_amdgcn_readfirstlane(tid >> 6);
  const int fw = blockIdx.x;
  const int j = (fw & 7) * 8 + (fw >> 3);
  const int g = lane >> 4, tq = (lane & 15) >> 2, tp = lane & 3;
  const int tg = wave % 3, kq = wave / 3;
  const int r32 = tid / 18, rem = tid - r32 * 18;
  const char* gsrc = (const char*)(rem >= 9 ? wu : wg) + (unsigned)(r32 * (FF * 2) + j * 144 + (rem >= 9 ? rem - 9 : rem) * 16);
  const unsigned lb = (unsigned)(size_t)L + wave * 1024;
  auto issue_gu = [&](int ks0, int n, int reg0) {
    if (wave < 9) {
#pragma unroll
      for (int i = 0; i < n; i++) dma16(gsrc + (size_t)(ks0 + i) * (32 * FF * 2), lb + (reg0 + i) * RG);
    }
  };
  if (DLY_W) wait_ticks(DLY_W);
  if (wave < 9) {
    const int r8 = tid / 72, q = (tid - r8 * 72) ^ (r8 & 2);
    const char* dsrc = (const char*)wd + (unsigned)((j * 72 + r8) * 1152);
#pragma unroll
    for (int i = 0; i < 9; i++) dma16(dsrc + (unsigned)(i * RG + (q ^ ((i & 1) << 2)) * 16), lb + i * RG);
  }
  issue_gu(0, 5, 9);
  if (tid < 8 * 4) *(LAS unsigned*)(L + FS_GU + (tid >> 2) * 160 + 144 + (tid & 3) * 4) = 0u;
  s16x4 fa[3][5][2], fd[3][5];
  auto read_gu = [&]() {
    const LAS char* p = L + (kq == 1 ? 0 : (kq == 2 ? 4 * RG : 9 * RG)) + (8 * g + tq) * 288 + (tp >> 1) * 144 + (tp & 1) * 8 + 48 * tg;
#pragma unroll
    for (int ks = 0; ks < 5; ks++) {
#pragma unroll
      for (int t = 0; t < 3; t++)
#pragma unroll
        for (int h = 0; h < 2; h++)
          fa[t][ks][h] = __builtin_amdgcn_ds_read_tr16_b64_v4i16((LAS s16x4*)(p + (32 * ks + 4 * h) * 288 + 16 * t));
    }
  };
  asm volatile("s_waitcnt vmcnt(5)" ::: "memory");
  __syncthreads();
  {
    const int f = (tq >> 1) | ((g & 1) << 1);
#pragma unroll
    for (int t = 0; t < 3; t++) {
      const LAS char* p = L + (8 * g + tq) * 1152 + ((3 * wave + t) ^ f) * 32 + tp * 8;
#pragma unroll
      for (int h = 0; h < 4; h++)
        fd[t][h] = __builtin_amdgcn_ds_read_tr16_b64_v4i16((LAS s16x4*)(p + (32 * (h >> 1) + 4 * (h & 1)) * 1152));
      fd[t][4] = __builtin_amdgcn_ds_read_tr16_b64_v4i16(
          (LAS s16x4*)(L + (64 + 4 * (g & 1) + tq) * 1152 + ((3 * wave + t) ^ (tq >> 1)) * 32 + tp * 8));
    }
  }
  __syncthreads();
  TS(1, 6);
  issue_gu(5, 9, 0);
  asm volatile("s_waitcnt vmcnt(9)" ::: "memory");
  __syncthreads();
  if (kq == 0) read_gu();
  __syncthreads();
  TS(1, 7);
  issue_gu(14, 4, 9);
  asm volatile("s_waitcnt vmcnt(4)" ::: "memory");
  __syncthreads();
  TS(1, 1);
  if (kq == 1 || kq == 2) read_gu();
  DRAIN();
  __syncthreads();
  if (kq == 3) read_gu();
  const unsigned tag = __builtin_amdgcn_readfirstlane(tagv) + 1u;
  TS(1, 2);
  if (wave < 4) {
    _Pragma("unroll 1") for (int spin = 0; spin < SPIN_CAP; spin++) {
      const unsigned t = ldg16(rs, WS_PF + fw * 128)[0];
      if (__builtin_amdgcn_readfirstlane(t) == tag) break;
      __builtin_amdgcn_s_sleep(2);
    }
    wait_ticks(DLY_X);
    TS(1, 3);
    u32x4 va0, va1, vb0, vb1;
    int roa, rob;
    select8(rs, tag, lane, [&](int r, unsigned k) {
      const int o = WS_XS + key_row(k) * 1536;
      if (r == wave) {
        roa = o;
        va0 = ldg16(rs, o + lane * 16);
        va1 = ldg16(rs, o + 1024 + (lane & 31) * 16);
      }
      if (r == wave + 4) {
        rob = o;
        vb0 = ldg16(rs, o + lane * 16);
        vb1 = ldg16(rs, o + 1024 + (lane & 31) * 16);
      }
    });
    TS(1, 9);
    auto take = [&](u32x4 p0, u32x4 p1, int ro, int slot) {
      _Pragma("unroll 1") for (int spin = 0; spin < SPIN_CAP; spin++) {
        if (__all(p0[3] == tag && p1[3] == tag)) break;
        __builtin_amdgcn_s_sleep(1);
        p0 = ldg16(rs, ro + lane * 16);
        p1 = ldg16(rs, ro + 1024 + (lane & 31) * 16);
      }
      LAS unsigned* dst = (LAS unsigned*)(L + FS_XS + slot * DS) + (lane & 31);
#pragma unroll
      for (int c = 0; c < 3; c++) {
        dst[96 * (lane >> 5) + 32 * c] = p0[c];
        dst[192 + 32 * c] = p1[c];
      }
    };
    take(va0, va1, roa, wave);
    take(vb0, vb1, rob, wave + 4);
  }
  __syncthreads();
  TS(1, 4);
  {
    const f32x4 zero4 = {0.f, 0.f, 0.f, 0.f};
    f32x4 acc[3] = {zero4, zero4, zero4};
    const LAS char* xp = L + FS_XS + (lane & 7) * DS + 16 * g + 64 * (9 * (kq >> 1) + 5 * (kq & 1));
    bf8v xf[5];
#pragma unroll
    for (int ks = 0; ks < 5; ks++) xf[ks] = *(const LAS bf8v*)(xp + 64 * ks);
#pragma unroll
    for (int ks = 0; ks < 5; ks++) {
      if (ks < 4 || !(kq & 1)) {
#pragma unroll
        for (int t = 0; t < 3; t++) {
          const bf8v a = __builtin_bit_cast(bf8v, __builtin_shufflevector(fa[t][ks][0], fa[t][ks][1], 0, 1, 2, 3, 4, 5, 6, 7));
          acc[t] = MFMAB(a, xf[ks], acc[t]);
        }
      }
    }
    if ((lane & 15) < 8) {
#pragma unroll
      for (int t = 0; t < 3; t++) *(LAS f32x4*)(L + FS_RED + ((kq * 9 + 3 * tg + t) * 8 + (lane & 7)) * 64 + 16 * g) = acc[t];
    }
  }
  __syncthreads();
  if (wave < 9) {
    const LAS float* rp = (const LAS float*)(L + FS_RED) + (wave * 8 + (lane >> 3)) * 16 + (lane & 7);
    const float gs = ((rp[0] + rp[1152]) + rp[2304]) + rp[3456];
    const float us = ((rp[8] + rp[1160]) + rp[2312]) + rp[3464];
    const float gp = rbf(gs);
    const float gq = rbf(gp * __builtin_amdgcn_rcpf(1.0f + __expf(-gp)));
    *(LAS u16*)(L + FS_GU + (lane >> 3) * 160 + (wave * 8 + (lane & 7)) * 2) = (u16)pkbf(gq * rbf(us), 0.f);
  }
  __syncthreads();
  {
    const LAS char* gp = L + FS_GU + (lane & 7) * 160;
    const bf8v b0 = *(const LAS bf8v*)(gp + 16 * g), b1 = *(const LAS bf8v*)(gp + 64 + 16 * g);
    const s16x4 b2 = *(const LAS s16x4*)(gp + 128 + 8 * g);
#pragma unroll
    for (int t = 0; t < 3; t++) {
      f32x4 acc = {0.f, 0.f, 0.f, 0.f};
      acc = __builtin_amdgcn_mfma_f32_16x16x16bf16_1k(fd[t][4], b2, acc, 0, 0, 0);
      f32x4 acc2 = {0.f, 0.f, 0.f, 0.f};
      acc2 = MFMAB(__builtin_bit_cast(bf8v, __builtin_shufflevector(fd[t][0], fd[t][1], 0, 1, 2, 3, 4, 5, 6, 7)), b0, acc2);
      acc2 = MFMAB(__builtin_bit_cast(bf8v, __builtin_shufflevector(fd[t][2], fd[t][3], 0, 1, 2, 3, 4, 5, 6, 7)), b1, acc2);
      u32x4 o;
#pragma unroll
      for (int r = 0; r < 4; r++) o[r] = min(__float_as_uint(acc[r] + acc2[r]), 0xfffffffeu);
      if ((lane & 15) < 8) stg16(rs, WS_PART + fw * (8 * DM * 4) + (3 * wave + t) * 512 + (lane & 7) * 64 + 16 * g, o);
    }
  }
  TS(1, 5);
  TC(1, 15);
}

__global__ __launch_bounds__(NTHR, 3) void k_all(const u16* __restrict__ x, const u16* __restrict__ lar,
                                                 const u16* __restrict__ aim, const u16* __restrict__ crl,
                                                 const u16* __restrict__ cim, const u16* __restrict__ ldt,
                                                 const u16* __restrict__ dskip, const u16* __restrict__ wr,
                                                 const u16* __restrict__ wg, const u16* __restrict__ wu,
                                                 const u16* __restrict__ wd, u16* __restrict__ out,
                                                 char* ws) {
  __shared__ __attribute__((aligned(16))) char smem[SM_TOTAL];
  LAS char* const L = (LAS char*)smem;
  unsigned cpf = 0;
  if (blockIdx.x >= NFFN + NLN && threadIdx.x >= 256 && threadIdx.x < 256 + CPF)
    cpf = *(const __attribute__((address_space(1))) unsigned*)((__builtin_amdgcn_s_getpc() & ~127ull) + (threadIdx.x - 256) * 128);
  asm volatile("" ::"s"(x), "s"(lar), "s"(aim), "s"(crl), "s"(cim), "s"(ldt), "s"(dskip), "s"(wr), "s"(wg), "s"(wu), "s"(wd),
               "s"(out), "s"(ws));
  const int tid = threadIdx.x, lane = tid & 63, b = (int)blockIdx.x - NFFN;
  const int wave = __builtin_amdgcn_readfirstlane(tid >> 6);
  const rsrc_t rs = __builtin_amdgcn_make_buffer_rsrc(ws, 0, WS_END, 0x00020000);
  const unsigned tagv = __builtin_amdgcn_raw_buffer_load_b32(rs, WS_EPOCH, 0, SC1);
  if (b < 0) {
    TS(1, 0);
  TC(1, 14);
    ffn_role(wg, wu, wd, ws, rs, tagv, L);
    return;
  }
  TWDECL;
  TS(0, 0);
  TC(0, 14);
  const bool is_ln = b < NLN;
  const int hl = lane & 31, rsel = lane >> 5;
  const int row = b * 16 + wave * 2 + rsel;
  const int ch = (wave & 3) < 3 ? (wave & 3) : (wave >> 2), sub = (wave & 3) < 3 ? (wave >> 2) : 3;
  const int cj = (b & ~15) | ((b & 7) << 1) | ((b >> 3) & 1);
  const int d = 3 * cj + ch;
  const int n = (sub & 1) * 64 + lane;
  const unsigned po = (unsigned)(d * NM + n) * 2u, po2 = po ^ 128u;
  auto ld16 = [](const u16* p, unsigned byte_off) { return *(const u16*)((const char*)p + byte_off); };
  const u16 r_lar = ld16(lar, po), r_aim = ld16(aim, po), r_cr = ld16(crl, po), r_ci = ld16(cim, po), r_cr2 = ld16(crl, po2), r_ci2 = ld16(cim, po2);
  const u16 r_ldt = ldt[(unsigned)d], r_dsk = ld16(dskip + 3 * cj, (unsigned)min(lane, 2) * 2u);
  SCHED_FENCE();
  __builtin_amdgcn_s_barrier();
  u32x3 cp[4];
  u32x2 xw[9];
  int cpo = 0;
  const bool st_wave = is_ln && (wave >> 2) == 1;
  if (wave < 4) {
    const int hrow = (is_ln ? 8 * b : 16 * (b - NLN) + 8 * (wave & 1)) + ((lane >> 2) & 7);
    const int grp = (is_ln ? 16 * wave : 64 + 8 * (wave & 2)) + (lane & 3);
    const char* src = (const char*)x + (unsigned)((2 * hrow + rsel) * (DM * 2) + grp * 12);
#pragma unroll
    for (int i = 0; i < 4; i++) cp[i] = *(const u32x3u*)(src + 48 * i);
    cpo = WS_UP + ((grp * 2 + rsel) * 1024 + hrow) * 16;
  } else if (st_wave) {
    const char* xr = (const char*)x + (unsigned)((b * 16 + (tid >> 4) - 16) * (DM * 2) + (lane & 15) * 8);
#pragma unroll
    for (int k = 0; k < 9; k++) xw[k] = *(const u32x2*)(xr + 128 * k);
    __builtin_amdgcn_s_setprio(2);
  }
  SCHED_FENCE();
  unsigned wrw[9];
  if (is_ln && wave == 4) {
#pragma unroll
    for (int k = 0; k < 9; k++) wrw[k] = ((const unsigned*)wr)[hl + 32 * k];
  }
  const int part_off = WS_PART + ((tid >> 3) & 63) * (8 * DM * 4) + (b - RED0) * 128 + (tid & 7) * 16;
  LAS char* const C = L + ch * CH_BYTES;
  {
    const int ct = tid & 255;
    LAS char* zc = L + (wave >> 2) * CH_BYTES;
    const u32x4 z = {0u, 0u, 0u, 0u};
    *(LAS u32x4*)(zc + SM_KC + 16 * ct) = z;
    if (ct < KC_CHUNKS - 256) *(LAS u32x4*)(zc + SM_KC + 4096 + 16 * ct) = z;
    else if (ct >= 256 - UPAD / 8) *(LAS u32x4*)(zc + SM_US + 16 * (ct - (256 - UPAD / 8))) = z;
  }
  half8 bA = band_frag(lane, sub == 3 ? 0x1fu << 6 : (sub == 2 ? 0x1ffu << 4 : 0x3u << 12), sub == 0 ? ~0u : 0x3c003c00u);
  half8 bB = band_frag(lane, sub == 3 ? 0x7u << 11 : 0x7fu << 5, 0x3c003c00u);
  const int c15 = lane & 15, g = lane >> 4;
  const LAS char *pa, *pa2, *pb;
  LAS char *kd, *kd2;
  unsigned ks0, ks1;
  if (sub == 0) {
    const int r = c15 & 3, r3 = r == 3;
    pa = C + (c15 < 8 ? 2 * (c15 & 4) + 9 - r : TR_B + 1 + ((c15 >> 1) & 2) - r3) * TAB_STRIDE + 16 * g;
    pa2 = pa + (c15 < 8 ? -4 : 4) * TAB_STRIDE;
    pb = C + (TR_H + c15) * TAB_STRIDE + 16 * g;
    kd = C + KC_RD + 2 * 536 - 16 * lane;
    kd2 = C + KC_RD + ((c15 - 1) & 3) * RDS + 2 * (528 - ((c15 - 1) & ~3) + 8 * g) - 64 * 16;
    ks0 = lane == 31 ? 0x03020302u : 0x03020100u;
    ks1 = 0;
  } else if (sub == 1) {
    const int lag = (c15 >> 2) + 1 - (c15 & 3);
    pa = C + (lag >= 0 ? lag * TAB_STRIDE : SM_US) + 16 * g;
    pa2 = pb = pa;
    kd = C + KC_RD + g * RDS + 2 * 544;
    kd2 = kd;
    ks0 = ks1 = 0;
  } else {
    pa = C + (TR_H + 15 - c15) * TAB_STRIDE + 16 * g;
    pa2 = pa;
    pb = C + (TR_M + c15) * TAB_STRIDE + 16 * g;
    kd = C + (c15 < 8 ? ((c15 & 1) ? KC_R5 : KC_R3) + 2 * (80 - 8 * (c15 & 6) + 4 * g) : KC_R9 + 2 * (120 - 8 * c15 + 2 * g));
    kd2 = kd + (c15 < 8 ? 4 : KC_R7 - KC_R9);
    ks0 = c15 < 8 ? 0x03020100u : 0x05040100u;
    ks1 = c15 < 8 ? 0x07060504u : 0x07060302u;
  }
  asm volatile("" : "+v"(bA), "+v"(bB), "+v"(pa), "+v"(pa2), "+v"(pb), "+v"(kd), "+v"(kd2), "+v"(ks0), "+v"(ks1));
  SCHED_FENCE();
  const unsigned tag = __builtin_amdgcn_readfirstlane(tagv) + 1u;
  float f_lar = bflo(r_lar), f_aim = bflo(r_aim), f_cr = bflo(r_cr), f_ci = bflo(r_ci), f_ldt = bflo(r_ldt), f_dsk = bflo(r_dsk);
  float f_cm = fmaxf(fmaxf(fabsf(f_cr), fabsf(f_ci)), fmaxf(fabsf(bflo(r_cr2)), fabsf(bflo(r_ci2))));
  asm volatile("" : "+v"(f_lar), "+v"(f_aim), "+v"(f_cr), "+v"(f_ci), "+v"(f_cm), "+v"(f_ldt), "+v"(f_dsk), "+v"(cpf));
  if (wave < 4) {
#pragma unroll
    for (int i = 0; i < 4; i++) stg16(rs, cpo + i * (8 * 1024 * 16), u32x4{cp[i][0], cp[i][1], cp[i][2], tag});
  } else if (st_wave) {
    unsigned one2 = 0x3f803f80u;
    asm volatile("" : "+s"(one2));
    float s0 = 0.f, s1 = 0.f, q0 = 0.f, q1 = 0.f;
#pragma unroll
    for (int k = 0; k < 9; k++) {
      const unsigned a = xw[k][0], c = xw[k][1];
      s0 = dot2(a, one2, s0);
      q0 = dot2(a, a, q0);
      s1 = dot2(c, one2, s1);
      q1 = dot2(c, c, q1);
    }
    const float mean = red16_sum(s0 + s1) * (1.0f / DM), mbf = rbf(mean);
    float q = red16_sum(q0 + q1), dm = mean;
    if (__builtin_expect(__any(!(q * (1.0f / DM) >= mean * mean * 1.002f)), 0)) {
      f32x2 q2 = {0.f, 0.f};
#pragma unroll
      for (int k = 0; k < 18; k++) {
        const f32x2 dd = unpk(xw[k >> 1][k & 1]) - mbf;
        q2 = __builtin_elementwise_fma(dd, dd, q2);
      }
      q = red16_sum(hadd(q2));
      dm = mean - mbf;
    }
    const float ri = ln_rinv(q, dm);
    auto r2 = __builtin_amdgcn_permlane16_swap(__float_as_uint(ri), __float_as_uint(ri), false, false);
    auto m2 = __builtin_amdgcn_permlane16_swap(__float_as_uint(mbf), __float_as_uint(mbf), false, false);
    const unsigned mp = __builtin_amdgcn_perm(m2[1], m2[0], 0x07060302u);
    if (hl == 0) {
      stg16(rs, WS_ST + (8 * b + (tid >> 5) - 8) * 16, u32x4{r2[0], r2[1], mp, tag});
      *(LAS unsigned*)(L + SM_MX + ((tid >> 5) - 8) * 4) = mp;
    }
    __builtin_amdgcn_s_setprio(0);
  }
  SCHED_FENCE();
  TSX(0, 1);
  TW(2);
  {
    const float dt = rbf(exp_acc(f_ldt));
    const float are = -rbf(exp_acc(f_lar)) * dt;
    const float aie = f_aim * dt;
    const float chh = are * 1.44269502f;
    const float cl = fmaf(are, 1.92596299e-8f, fmaf(are, 1.44269502f, -chh));
    const float wh = aie * 0.159154936f;
    const float wl = fmaf(aie, 7.13048051e-9f, fmaf(aie, 0.159154936f, -wh));
    const unsigned cmax = wave_max_u32(__float_as_uint(f_cm));
    int se = 385 - (int)(cmax >> 23) - (int)(__float_as_uint(dt) >> 23);
    se = min(max(se, 2), 252);
    const float scale2 = __uint_as_float((unsigned)(se + 1) << 23);
    const cf cc = {f_cr * dt * scale2, -f_ci * dt * scale2};
    volatile LAS unsigned* tab = (volatile LAS unsigned*)C + n;
    constexpr int TS4 = TAB_STRIDE / 4;
    if (sub < 2) {
      const cstep e1 = mkstep(cexp_p2(1.f, chh, cl, -wh, -wl)), e128 = mkstep(cexp_p2(128.f, chh, cl, -wh, -wl));
      const cf p1 = cmul(cc, e1);
      const cstep e2 = mkstep(cmul(e1.s, e1));
      cf a0 = cmul(p1, e1);
      tab[0] = pkc(cc);
      const cstep e4 = mkstep(cmul(e2.s, e2));
      cf a1 = cmul(a0, e128);
      tab[TS4] = pkc(p1);
      cf b0 = cmul(a1, e128), b1 = cmul(b0, e128);
      const LAS char* tb = (const LAS char*)tab;
      tab_step<2, 10, TR_B + 4, TR_B + 6>(tb, a0, a1, b0, b1, e1, e2);
      cf m0 = cmul(b1, e128);
      tab_step<3, 11, TR_B + 5, TR_B + 7>(tb, a0, a1, b0, b1, e1, e2);
      cf m1 = cmul(m0, e4);
      tab_step<4, 12, TR_B, TR_B + 2>(tb, a0, a1, b0, b1, e1, e2);
      tab[(TR_B + 1) * TS4] = pkc(b0);
      tab[(TR_B + 3) * TS4] = pkc(b1);
      tab_step<5, 13, TR_M, TR_M + 1>(tb, a0, a1, m0, m1, e1, e128);
      tab_step<6, 14, TR_M + 2, TR_M + 3>(tb, a0, a1, m0, m1, e1, e128);
      tab_step<7, 15, TR_M + 4, TR_M + 5>(tb, a0, a1, m0, m1, e1, e128);
      const cf a2 = cmul(a0, e1), a3 = cmul(a1, e1);
      tab[8 * TS4] = pkc(a0);
      tab[16 * TS4] = pkc(a1);
      tab[(TR_M + 6) * TS4] = pkc(m0);
      tab[(TR_M + 7) * TS4] = pkc(m1);
      tab[9 * TS4] = pkc(a2);
      tab[17 * TS4] = pkc(a3);
    } else {
      const cstep e8 = mkstep(cexp_p2(8.f, chh, cl, wh, wl)), e128 = mkstep(cexp_p2(128.f, chh, cl, -wh, -wl));
      cf f0 = cmul(cmul(cc, mkstep(cexp_p2(1024.f, chh, cl, -wh, -wl))), mkstep(cf{e8.s[0], e8.r[0]}));
      const cstep e16 = mkstep(cmul(e8.s, e8)), e256 = mkstep(cmul(e128.s, e128));
      cf h1 = e8.s, h2 = e16.s, f1 = cmul(f0, e128);
      const LAS char* tb = (const LAS char*)tab;
      tab[TR_H * TS4] = pkh(1.f, 0.f);
      tab_step<TR_H + 1, TR_H + 2, TR_F, TR_F + 1>(tb, h1, h2, f0, f1, e16, e256);
      const cstep e32 = mkstep(h2);
      tab_step<TR_H + 3, TR_H + 4, TR_F + 2, TR_F + 3>(tb, h1, h2, f0, f1, e16, e256);
      cf h3 = cmul(h1, e16), h4 = cmul(h2, e16);
      tab_step<TR_H + 5, TR_H + 6, TR_F + 4, TR_F + 5>(tb, h1, h2, f0, f1, e32, e256);
      tab[(TR_F + 6) * TS4] = pkc(f0);
      tab[(TR_F + 7) * TS4] = pkc(f1);
      tab_step<TR_H + 9, TR_H + 10, TR_H + 7, TR_H + 8>(tb, h1, h2, h3, h4, e32, e32);
      const cf h5 = cmul(h3, e32);
      tab[(TR_H + 13) * TS4] = pkc(h1);
      tab[(TR_H + 14) * TS4] = pkc(h2);
      tab[(TR_H + 11) * TS4] = pkc(h3);
      tab[(TR_H + 12) * TS4] = pkc(h4);
      tab[(TR_H + 15) * TS4] = pkc(h5);
      if (sub == 2) *(volatile LAS unsigned*)(L + SM_ISC + 4 * ch) = (unsigned)(254 - se) << 23;
    }
  }
  if (b >= RED0 && wave >= 4) stg16(rs, part_off, u32x4{~0u, ~0u, ~0u, ~0u});
  float dsk[3];
#pragma unroll
  for (int c = 0; c < 3; c++) dsk[c] = __int_as_float(__builtin_amdgcn_readlane(__float_as_int(f_dsk), c));
  const int vt = tid - 256;
  const int uoff = WS_UP + (cj >> 1) * (SEQ * 16) + vt * 16, soff = WS_ST + vt * 16;
  u32x4 v0, v1, v2, v3, st0, st1;
  auto ldx = [&]() {
    v0 = ldg16(rs, uoff);
    v1 = ldg16(rs, uoff, 16384);
    v2 = ldg16(rs, uoff, 8192);
    v3 = ldg16(rs, uoff, 16384 + 8192);
    st0 = ldg16(rs, soff);
    st1 = ldg16(rs, soff, 8192);
    SCHED_FENCE();
  };
  if (UPOS == 0 && wave >= 4) ldx();
  TW(3);
  asm volatile("s_waitcnt lgkmcnt(0)" ::: "memory");
  __syncthreads();
  TSX(0, 2);
  if (UPOS == 1 && wave >= 4) ldx();
  const LAS char* uf16 = C + SM_US + 2 * (UPAD + (sub == 3 ? 8 : 16) * c15 + 8 * g);
  LAS char* yb = C + g * 320 + c15 * 16;
  const f32x4 zero4 = {0.f, 0.f, 0.f, 0.f};
  f32x2 sb[2][3];
  if (sub == 0) {
    half8 td[17];
    {
      half8 fa[8], fc[8], fb[8];
#pragma unroll
      for (int ks = 0; ks < 8; ks++) { fa[ks] = ld_frag(pa + 64 * ks); fb[ks] = ld_frag(pb + 64 * ks); fc[ks] = ld_frag(pa2 + 64 * ks); }
      SCHED_FENCE();
      f32x4 f = zero4, e = zero4;
#pragma unroll
      for (int ks = 0; ks < 8; ks++) { f = MFMA16(fa[ks], fb[ks], f); e = MFMA16(fc[ks], fb[ks], e); }
      unsigned d0 = pkh(f[0], f[1]);
      const unsigned d1 = pkh(f[2], f[3]), d2 = pkh(e[0], e[1]), d3 = pkh(e[2], e[3]);
      d0 = __builtin_amdgcn_perm(d0, d0, ks0);
      const unsigned n3 = (unsigned)__builtin_amdgcn_update_dpp((int)(d0 & 0xffff0000u), (int)d3, 0x130, 0xf, 0xf, false);
      const unsigned n2 = (unsigned)__builtin_amdgcn_update_dpp(0, (int)d2, 0x130, 0xf, 0xf, true);
      *(volatile LAS u32x4*)kd = u32x4{d0, d1, d2, d3};
      const unsigned a0 = __builtin_amdgcn_alignbit(d0, n3, 16), a1 = __builtin_amdgcn_alignbit(d1, d0, 16), a2 = __builtin_amdgcn_alignbit(d2, d1, 16);
      *(volatile LAS u32x4*)(kd + RDS) = u32x4{a0, a1, a2, __builtin_amdgcn_alignbit(d3, d2, 16)};
      *(volatile LAS u32x4*)(kd + 2 * RDS) = u32x4{n3, d0, d1, d2};
      *(volatile LAS u32x4*)(kd + 3 * RDS) = u32x4{__builtin_amdgcn_alignbit(n3, n2, 16), a0, a1, a2};
      if (lane == 63) *(volatile LAS u16*)(kd - 2) = (u16)d0;
      TW(4);
#pragma unroll
      for (int i = 7; i >= 1; i--) { td[i] = ld_td(kd2, i); td[i + 8] = ld_td(kd2, i + 8); }
      td[8] = ld_td(kd2, 8);
      td[16] = ld_td(kd2, 16);
      uf16 -= 32 * 33;
    }
    TW(5);
    TSX(0, 3);
    TSX(0, 4);
    asm volatile("" : "+v"(uf16), "+v"(yb));
    __syncthreads();
    TSX(0, 5);
    TW(6);
    conv_dense(td, kd2, bA, C + KC_R5 + 64, uf16, yb + SM_YD);
  } else {
  half8 td[8];
  {
    auto w32 = [](LAS char* p, unsigned v) { *(volatile LAS unsigned*)p = v; };
    if (sub == 1) {
      half8 fa[8];
#pragma unroll
      for (int ks = 0; ks < 8; ks++) fa[ks] = ld_frag(pa + 64 * ks);
      SCHED_FENCE();
      const half8 re = __builtin_bit_cast(half8, u32x4{0x3c00u, 0x3c00u, 0x3c00u, 0x3c00u});
      f32x4 f = zero4;
#pragma unroll
      for (int ks = 0; ks < 8; ks++) f = MFMA16(fa[ks], re, f);
      const unsigned d1 = pkh(f[2], f[3]);
      if (c15 == 0) *(LAS u32x2*)kd = u32x2{pkh(f[0], f[1]), d1};
      if (lane == 32) *(LAS u16*)(kd + RDS + 8) = (u16)(d1 >> 16);
      TW(4);
    } else if (sub == 3) {
      half8 fa[8], fb[8];
#pragma unroll
      for (int ks = 0; ks < 8; ks++) { fa[ks] = ld_frag(pa + 64 * ks); fb[ks] = ld_frag(pb + 64 * ks); }
      SCHED_FENCE();
      f32x4 f = zero4;
#pragma unroll
      for (int ks = 0; ks < 8; ks++) f = MFMA16(fa[ks], fb[ks], f);
      const unsigned d0 = pkh(f[0], f[1]), d1 = pkh(f[2], f[3]);
      w32(kd, __builtin_amdgcn_perm(d1, d0, ks0));
      w32(kd2, __builtin_amdgcn_perm(d1, d0, ks1));
      TW(4);
      const LAS unsigned* t5 = (const LAS unsigned*)(C + KC_R5 + 2 * (158 - 64 - 2 * c15 + 4 * g) - 128);
      asm volatile("" : "+v"(t5) : : "memory");
#pragma unroll
      for (int e = 0; e < 3; e++) {
        td[e] = ld_toep<8>(t5 + 16 * (2 - e));
        td[3 + e] = ld_toep<8>(t5 + (KC_R3 - KC_R5) / 4 + 16 * (2 - e));
      }
    } else {
      TW(4);
    }
  }
  SCHED_FENCE();
  asm volatile("" : "+v"(uf16), "+v"(yb));
  if (wave >= 4) {
    _Pragma("unroll 1") for (int spin = 0; spin < SPIN_CAP; spin++) {
      if (__all(v0[3] == tag && v1[3] == tag && st0[3] == tag && v2[3] == tag && v3[3] == tag && st1[3] == tag)) break;
      ldx();
    }
    TW(5);
    LAS char* dst = L + SM_US + 2 * UPAD + 4 * vt;
    const unsigned pa = (cj & 1) ? 0x07060c0cu : 0x01000c0cu, pb = (cj & 1) ? 0x05040c0cu : 0x03020c0cu;
    auto norm = [&](const u32x4& a, const u32x4& e, const u32x4& st, f32x2 (&t)[3], LAS char* d) {
      const f32x2 ri = {__uint_as_float(st[0]), __uint_as_float(st[1])};
      const f32x2 cc = -unpk(st[2]) * ri;
#pragma unroll
      for (int c = 0; c < 3; c++) {
        const int hi = c ? 2 : 1, lo = c >> 1;
        const f32x2 xf = {__uint_as_float(__builtin_amdgcn_perm(a[hi], a[lo], c == 1 ? pb : pa)),
                          __uint_as_float(__builtin_amdgcn_perm(e[hi], e[lo], c == 1 ? pb : pa))};
        const f32x2 uf = __builtin_elementwise_fma(xf, ri, cc);
        *(LAS unsigned*)(d + c * CH_BYTES) = pkc(uf);
        t[c] = __builtin_elementwise_fma(uf, f32x2{dsk[c], dsk[c]}, xf);
        asm volatile("" : "+v"(t[c]));
      }
    };
    norm(v0, v1, st0, sb[0], dst);
    norm(v2, v3, st1, sb[1], dst + 2048);
  } else {
    TW(5);
  }
  if (sub == 2) __builtin_amdgcn_s_setprio(PRA);
  __syncthreads();
  TW(6);
  {
    if (sub == 1) {
      const int xg = b & 7, sl = b >> 3;
      const char* base = (const char*)(sl < 12 ? wg : wu) + (144 + 48 * ch + 4 * (sl < 12 ? sl : sl - 12)) * (FF * 2) + xg * 1152;
      const unsigned o = (unsigned)(g * (FF * 2) + min(c15, 8) * 128);
      unsigned pf[3];
#pragma unroll
      for (int i = 0; i < 3; i++) pf[i] = PFM ? *(const unsigned*)(base + i * (144 * FF * 2) + o) : 0u;
      if (is_ln && wave == 4) {
        unsigned one2 = 0x3f803f80u;
        asm volatile("" : "+s"(one2));
        float wsum = 0.f;
#pragma unroll
        for (int k = 0; k < 9; k++) {
          wsum = dot2(wrw[k], one2, wsum);
          *(LAS u32x2*)(L + SM_WR + (hl + 32 * k) * 8) = u32x2{wrw[k] << 16, wrw[k] & 0xffff0000u};
        }
        *(LAS float*)(L + SM_ISC + 12) = red32_sum(wsum);
      }
      asm volatile("" ::"v"(pf[0]), "v"(pf[1]), "v"(pf[2]));
    } else if (sub == 3) {
      f32x4 a2[6], w[4];
#pragma unroll
      for (int i = 0; i < 6; i++) a2[i] = zero4;
      half8 uw[12];
#pragma unroll
      for (int jj = 0; jj < 6; jj++) uw[jj] = ld_frag(uf16 - 48 + 256 * jj);
      SCHED_FENCE();
      w[0] = MFMA16(uw[0], bA, zero4); w[1] = MFMA16(uw[1], bA, zero4);
      w[2] = MFMA16(uw[0], bB, zero4); w[3] = MFMA16(uw[1], bB, zero4);
      half8 X5 = pack_win(w[0], w[1]), X3 = pack_win(w[2], w[3]);
      SCHED_FENCE();
#pragma unroll
      for (int jj = 0; jj < 6; jj++) {
        if (jj < 5) {
          w[0] = MFMA16(uw[2 * jj + 2], bA, zero4);
          if (jj < 3) uw[2 * jj + 6] = ld_frag(uf16 - 48 + 256 * (2 * jj + 6));
          SCHED_FENCE();
          w[1] = MFMA16(uw[2 * jj + 3], bA, zero4);
          if (jj < 3) uw[2 * jj + 7] = ld_frag(uf16 - 48 + 256 * (2 * jj + 7));
          SCHED_FENCE();
          w[2] = MFMA16(uw[2 * jj + 2], bB, zero4);
        }
        if (jj > 0) *(LAS f32x4*)(yb + SM_Y2 + (1 + jj) * YT) = a2[jj - 1];
        SCHED_FENCE();
        if (jj < 5) w[3] = MFMA16(uw[2 * jj + 3], bB, zero4);
        SCHED_FENCE();
        u32x4 n5, n3;
#pragma unroll
        for (int q = 0; q < 6; q++) {
          const int e = q >> 1;
          if (jj + e < 6) a2[jj + e] = MFMA16((q & 1) ? X3 : X5, td[3 * (q & 1) + e], a2[jj + e]);
          if (jj < 5 && q < 4) {
            u32x4& nn = q < 2 ? n5 : n3;
            nn[2 * (q & 1)] = pkh(w[q][0], w[q][1]);
            nn[2 * (q & 1) + 1] = pkh(w[q][2], w[q][3]);
          }
          SCHED_FENCE();
        }
        if (jj < 5) {
          X5 = __builtin_bit_cast(half8, n5);
          X3 = __builtin_bit_cast(half8, n3);
        }
      }
      *(LAS f32x4*)(yb + SM_Y2 + 7 * YT) = a2[5];
    } else {
      half8 uw[8];
#pragma unroll
      for (int i = 0; i < 4; i++) {
        uw[2 * i] = ld_frag(uf16 + 512 * i - 48);
        uw[2 * i + 1] = ld_frag(uf16 + 512 * i - 32);
      }
      SCHED_FENCE();
      f32x4 w[8], a3[4];
      unsigned rr[2][5][3];
      {
        const LAS unsigned* t9 = opaque_lds(C + KC_R9 + 2 * ((15 - c15 + 4 * g) & ~1));
#pragma unroll
        for (int m = 0; m < 2; m++)
#pragma unroll
          for (int k = 0; k < 5; k++)
#pragma unroll
            for (int q = 0; q < 3; q++) rr[m][k][q] = t9[m * ((KC_R7 - KC_R9) / 4) + 8 * k + q];
      }
#pragma unroll
      for (int i = 0; i < 4; i++) {
        w[2 * i] = MFMA16(uw[2 * i], bA, zero4);
        w[2 * i + 1] = MFMA16(uw[2 * i + 1], bB, zero4);
      }
      unsigned sh = (c15 & 1) ? 0u : 16u;
#pragma unroll
      for (int m = 0; m < 2; m++)
#pragma unroll
        for (int e = 0; e < 4; e++) {
          asm volatile("" : "+v"(sh));
          const unsigned (&p)[3] = rr[m][3 - e], (&q)[3] = rr[m][4 - e];
          td[4 * m + e] = __builtin_bit_cast(half8, u32x4{__builtin_amdgcn_alignbit(p[1], p[0], sh), __builtin_amdgcn_alignbit(p[2], p[1], sh),
                                                          __builtin_amdgcn_alignbit(q[1], q[0], sh), __builtin_amdgcn_alignbit(q[2], q[1], sh)});
        }
      const half8 X90 = pack_win(w[0], w[2]), X70 = pack_win(w[1], w[3]), X91 = pack_win(w[4], w[6]), X71 = pack_win(w[5], w[7]);
#pragma unroll
      for (int t = 0; t < 4; t++) {
        a3[t] = MFMA16(X90, td[t], zero4);
        a3[t] = MFMA16(X70, td[4 + t], a3[t]);
        if (t >= 2) {
          a3[t] = MFMA16(X91, td[t - 2], a3[t]);
          a3[t] = MFMA16(X71, td[4 + t - 2], a3[t]);
        }
      }
#pragma unroll
      for (int t = 0; t < 4; t++) *(LAS f32x4*)(yb + SM_Y3 + (4 + t) * YT) = a3[t];
      __builtin_amdgcn_s_setprio(0);
    }
  }
  }
  float isc[3];
  {
    const u32x4 iscv = *(const LAS u32x4*)(L + SM_ISC);
#pragma unroll
    for (int c = 0; c < 3; c++) isc[c] = __int_as_float(__builtin_amdgcn_readfirstlane(iscv[c]));
  }
  TW(7);
  __syncthreads();
  TS(0, 6);
#pragma unroll
  for (int i = 0; i < 2; i++) {
    if (wave >= 4) {
      const int e = vt + 512 * i;
      const int nt = (wave >> 1) - 2 + 4 * i;
      const LAS char* yp = L + nt * YT + ((e >> 1) & 3) * 320 + ((e >> 3) & 15) * 16 + (e & 1) * 8;
      f32x2 y[3];
#pragma unroll
      for (int c = 0; c < 3; c++) y[c] = *(const LAS f32x2*)(yp + c * CH_BYTES + SM_YD);
      if (nt >= 2) {
#pragma unroll
        for (int c = 0; c < 3; c++) y[c] += *(const LAS f32x2*)(yp + c * CH_BYTES + SM_Y2);
      }
      if (nt >= 4) {
#pragma unroll
        for (int c = 0; c < 3; c++) y[c] += *(const LAS f32x2*)(yp + c * CH_BYTES + SM_Y3);
      }
      u32x4 o;
#pragma unroll
      for (int c = 0; c < 3; c++) {
        const f32x2 z = __builtin_elementwise_fma(y[c], f32x2{isc[c], isc[c]}, sb[i][c]);
        o[c] = pkbf(z[0], z[1]);
      }
      o[3] = tag;
      stg16(rs, WS_ZP + ((e >> 3) * NCONV + cj) * 128 + (e & 7) * 16, o);
    }
  }
  TS(0, 7);
  TWFLUSH();
  if (is_ln) {
    float wf[6];
    const unsigned mxp = *(const LAS unsigned*)(L + SM_MX + (tid & 7) * 4);
#pragma unroll
    for (int i = 0; i < 6; i++) wf[i] = *(const LAS float*)(L + SM_WR + (tid >> 3) * 12 + (i < 3 ? i : 285 + i) * 4);
    float wsum = *(const LAS float*)(L + SM_ISC + 12), mxr = (lane & 8) ? bfhi(mxp) : bflo(mxp);
    unsigned sel0 = 0x00003f80u, sel1 = 0x3f800000u;
    asm volatile("" : "+s"(sel0), "+s"(sel1));
    if (wave == 0) __builtin_amdgcn_s_setprio(2);
    float nm0 = -bflo(mxp), nm1 = -bfhi(mxp);
    asm volatile("" : "+v"(wf[0]), "+v"(wf[1]), "+v"(wf[2]), "+v"(wf[3]), "+v"(wf[4]), "+v"(wf[5]), "+v"(nm0), "+v"(nm1), "+v"(wsum), "+v"(mxr));
    DRAIN();
    {
      const int off = WS_ZP + b * (NCONV * 128) + tid * 16;
      _Pragma("unroll 1") for (int spin = 0; spin < SPIN_CAP; spin++) {
        v0 = ldg16(rs, off);
        v1 = ldg16(rs, off + NTHR * 16);
        if (__all(v0[3] == tag && v1[3] == tag)) break;
        __builtin_amdgcn_s_sleep(2);
      }
    }
    TS(0, 8);
    f32x2 hv[6];
    {
      f32x2 ss, qq, pp;
#pragma unroll
      for (int i = 0; i < 6; i++) {
        const unsigned t = i < 3 ? v0[i] : v1[i - 3];
        hv[i] = f32x2{dot2(t, sel0, nm0), dot2(t, sel1, nm1)};
        const f32x2 w2 = {wf[i], wf[i]};
        ss = i ? ss + hv[i] : hv[i];
        qq = i ? __builtin_elementwise_fma(hv[i], hv[i], qq) : hv[i] * hv[i];
        pp = i ? __builtin_elementwise_fma(hv[i], w2, pp) : hv[i] * w2;
      }
      auto fold32 = [](f32x2 v) {
        const float a = v[0], c = v[1];
        auto r = __builtin_amdgcn_permlane32_swap(__float_as_uint(a), __float_as_uint(c), false, false);
        return __uint_as_float(r[0]) + __uint_as_float(r[1]);
      };
      const float s1 = fold32(ss), q1 = fold32(qq);
      float p1 = fold32(pp);
      auto r = __builtin_amdgcn_permlane16_swap(__float_as_uint(s1), __float_as_uint(q1), false, false);
      float a1 = __uint_as_float(r[0]) + __uint_as_float(r[1]);
      a1 += dppf<0x128>(a1);
      p1 += dppf<0x128>(p1);
      *(LAS f32x2*)(L + SM_RED + wave * 256 + ((lane & 23) | ((lane >> 2) & 8)) * 8) = f32x2{a1, p1};
    }
    if (wave == 11 && lane == 0 && !(b & 1)) __builtin_amdgcn_raw_buffer_store_b32(tag, rs, WS_PF + (b >> 1) * 128, 0, SC1);
    __syncthreads();
    if (wave == 0) {
      const LAS f32x2* rp = (const LAS f32x2*)(L + SM_RED + rsel * (6 * 256)) + hl;
      const f32x2 t = ((rp[0] + rp[32]) + (rp[64] + rp[96])) + (rp[128] + rp[160]);
      const float ta = t[0], tp = t[1];
      const float a = swap32_sum(ta), p = swap16_sum(swap32_sum(tp));
      auto r = __builtin_amdgcn_permlane16_swap(__float_as_uint(a), __float_as_uint(a), false, false);
      const float mv = __uint_as_float(r[0]) * (1.0f / DM);
      const float rinv = ln_rinv(__uint_as_float(r[1]), mv);
      const float dl = rbf(mxr + mv) - mxr;
      const unsigned rb = pkbf(0.f, fmaf(-dl, wsum, p) * rinv);
      const int lrow = 2 * (lane & 7) + ((lane >> 3) & 1);
      const unsigned key = ((rb ^ (unsigned)(((int)rb >> 31) | (int)0x80000000)) & 0xffff0000u) | (unsigned)(SEQ - 1 - 16 * b - lrow);
      unsigned kr = key;
      kr = (unsigned)__builtin_amdgcn_update_dpp((int)kr, (int)key, 0x124, 0x2, 0xf, false);
      kr = (unsigned)__builtin_amdgcn_update_dpp((int)kr, (int)key, 0x128, 0x4, 0xf, false);
      kr = (unsigned)__builtin_amdgcn_update_dpp((int)kr, (int)key, 0x12c, 0x8, 0xf, false);
      unsigned rank = (kr > key) + ((unsigned)__builtin_amdgcn_update_dpp(0, (int)kr, 0x121, 0xf, 0xf, true) > key) +
                      ((unsigned)__builtin_amdgcn_update_dpp(0, (int)kr, 0x122, 0xf, 0xf, true) > key) +
                      ((unsigned)__builtin_amdgcn_update_dpp(0, (int)kr, 0x123, 0xf, 0xf, true) > key);
      {
        auto r1 = __builtin_amdgcn_permlane16_swap(rank, rank, false, false);
        rank = r1[0] + r1[1];
        auto r2 = __builtin_amdgcn_permlane32_swap(rank, rank, false, false);
        rank = r2[0] + r2[1];
      }
      u32x4 rec;
#pragma unroll
      for (int i = 0; i < 3; i++) rec[i] = (unsigned)__builtin_amdgcn_readlane((int)key, __builtin_ctzll(__ballot(rank == (unsigned)i)));
      rec[3] = tag;
      if (lane < 8) stg16(rs, WS_KR + lane * 2048 + b * 16, rec);
      if (lane < 16) {
        if (rank < 8) __builtin_amdgcn_raw_buffer_store_b64(u32x2{key, tag}, rs, WS_KF + b * 64 + rank * 8, 0, SC1);
        LAS float* ri = (LAS float*)(L + SM_ROW + (lane & 7) * 16) + (lane >> 3);
        ri[0] = -dl * rinv;
        ri[2] = rinv;
        *(LAS u32x2*)(L + SM_ROW + 128 + lrow * 8) = u32x2{key, rank};
      }
      __builtin_amdgcn_s_setprio(0);
    }
    TS(0, 9);
    __syncthreads();
    {
      const f32x4 ri = *(const LAS f32x4*)(L + SM_ROW + (tid & 7) * 16);
      const f32x2 c2 = {ri[0], ri[1]}, r2 = {ri[2], ri[3]};
      LAS char* dst = L + SM_T2 + (tid & 7) * T2S + (tid >> 3) * 24;
#pragma unroll
      for (int i = 0; i < 6; i++) {
        const f32x2 w2 = __builtin_elementwise_fma(hv[i], r2, c2);
        *(LAS u32x2*)(dst + (i < 3 ? 8 * i : 8 * (285 + i))) = u32x2{i < 3 ? v0[i] : v1[i - 3], pkbf(w2[0], w2[1])};
      }
    }
    __syncthreads();
    LAS unsigned* const selp = (LAS unsigned*)(L + SM_ROW + 256);
    unsigned hp[9], wn[9], key = 0, lrank = 8;
    auto put_row = [&]() {
#pragma unroll
      for (int m = 0; m < 3; m++) stg16(rs, WS_XS + row * 1536 + m * 512 + hl * 16, u32x4{wn[3 * m], wn[3 * m + 1], wn[3 * m + 2], tag});
    };
    if (wave < 8) {
      const u32x2 kr = *(const LAS u32x2*)(L + SM_ROW + 128 + (2 * wave + rsel) * 8);
      key = kr[0];
      lrank = kr[1];
      const unsigned hsel = rsel ? 0x07060302u : 0x05040100u;
#pragma unroll
      for (int k = 0; k < 9; k++) {
        const u32x4 t = *(const LAS u32x4*)(L + SM_T2 + wave * T2S + (hl + 32 * k) * 16);
        hp[k] = __builtin_amdgcn_perm(t[2], t[0], hsel);
        wn[k] = __builtin_amdgcn_perm(t[3], t[1], hsel);
      }
      if (lrank < 2) put_row();
    } else if (wave == 8) {
      wait_ticks(DLY_L);
      select8(rs, tag, lane, [&](int r, unsigned k) { selp[r] = k; });
    }
    __syncthreads();
    TS(0, 10);
    if (wave < 8) {
      unsigned rank = 8;
#pragma unroll
      for (int r = 0; r < 8; r++) rank = selp[r] == key ? r : rank;
      if (rank < 8) {
        if (lrank >= 2) put_row();
#pragma unroll
        for (int k = 0; k < 9; k++) stg16(rs, WS_HS + rank * 4608 + (hl + 32 * k) * 16, u32x4{hp[k], wn[k], key, tag});
      } else {
        unsigned* orow = (unsigned*)(out + (size_t)row * DM) + hl;
#pragma unroll
        for (int k = 0; k < 9; k++) {
          const f32x2 o2 = unpk(hp[k]) + unpk(wn[k]);
          orow[32 * k] = pkbf(o2[0], o2[1]);
        }
      }
    }
  }
  if (b < RED0) {
    TS(0, 12);
  TC(0, 15);
    return;
  }
  {
    const int T = (b - RED0) >> 2, tp = (b - RED0) & 3;
    u32x4 hv = {0u, 0u, 0u, 0u};
    if (wave == 8) {
      _Pragma("unroll 1") for (int spin = 0; spin < SPIN_CAP; spin++) {
        hv = ldg16(rs, WS_HS + (2 * tp + ((lane >> 3) & 1)) * 4608 + (8 * T + (lane & 7)) * 16);
        if (__all(hv[3] == tag)) break;
        __builtin_amdgcn_s_sleep(4);
      }
      wait_ticks(DLY_P);
    }
    __syncthreads();
    TS(0, 11);
    LAS float* red = (LAS float*)(L + SM_RED);
    if (wave < 8) {
      u32x4 v;
      _Pragma("unroll 1") for (int spin = 0; spin < SPIN_CAP; spin++) {
        v = ldg16(rs, part_off);
        if (__all(v[0] != ~0u && v[1] != ~0u && v[2] != ~0u && v[3] != ~0u)) break;
        __builtin_amdgcn_s_sleep(2);
      }
      f32x4 s;
#pragma unroll
      for (int r = 0; r < 4; r++) {
        float a = __uint_as_float(v[r]);
        a += dppf<0x128>(a);
        a = swap16_sum(a);
        s[r] = swap32_sum(a);
      }
      if (lane < 8) *(LAS f32x4*)(red + wave * 32 + lane * 4) = s;
    }
    __syncthreads();
    if (wave == 8 && lane < 16) {
      float y0 = 0.f, y1 = 0.f;
#pragma unroll
      for (int w = 0; w < 8; w++) {
        const f32x2 p = *(const LAS f32x2*)(red + w * 32 + lane * 2);
        y0 += p[0];
        y1 += p[1];
      }
      const unsigned rb = hv[2] ^ ((int)hv[2] < 0 ? 0x80000000u : 0xffffffffu);
      const float sg = rbf(__builtin_amdgcn_rcpf(1.0f + __expf(-bfhi(rb))));
      const unsigned yb2 = pkbf(y0, y1);
      const unsigned y2 = pkbf(bflo(yb2) * sg, bfhi(yb2) * sg);
      const unsigned a2 = pkbf(bflo(hv[1]) + bflo(y2), bfhi(hv[1]) + bfhi(y2));
      ((unsigned*)out)[(size_t)key_row(hv[2]) * (DM / 2) + 8 * T + (lane & 7)] = pkbf(bflo(hv[0]) + bflo(a2), bfhi(hv[0]) + bfhi(a2));
      if (b == RED0 && lane == 0) __builtin_amdgcn_raw_buffer_store_b32(tag, rs, WS_EPOCH, 0, SC1);
    }
  }
  TS(0, 12);
  TC(0, 15);
}

}

extern "C" void kernel_launch(void* const* d_in, const int* in_sizes, int n_in, void* d_out, int out_size,
                              void* d_ws, size_t ws_size, hipStream_t stream) {
  k_all<<<NCONV + NFFN, NTHR, 0, stream>>>((const u16*)d_in[0], (const u16*)d_in[1], (const u16*)d_in[2], (const u16*)d_in[3],
                                           (const u16*)d_in[4], (const u16*)d_in[5], (const u16*)d_in[6], (const u16*)d_in[8],
                                           (const u16*)d_in[9], (const u16*)d_in[10], (const u16*)d_in[11], (u16*)d_out,
                                           (char*)d_ws);
}
```

```cpp
#include <hip/hip_runtime.h>
#include <stdint.h>
#include <type_traits>

#define PRA 0
#define PFM 3
#define CPF 232
#define WPAD 0
#define PFNT 0
#define WNT 1
#define WNTS " nt"
#define PTH 4
#define PTL 2
#define EB 1
#define LAUX 16
#define SAUX 16
#define PFA 0
#define XFL 0
#define CMAP 0
#define PRB 0
#define PFD 10
namespace {

constexpr int SEQ = 2048;
constexpr int DM = 576;
constexpr int NM = 128;
constexpr int FF = 4608;

constexpr int NTHR = 768;
constexpr int NFFN = 64;
constexpr int NCONV = 192;
constexpr int NLN = 128;
constexpr int RED0 = 48;

typedef unsigned short u16;
#define LAS __attribute__((address_space(3)))
using half8 = __attribute__((ext_vector_type(8))) _Float16;
using half2v = __attribute__((ext_vector_type(2))) _Float16;
using bf2v = __attribute__((ext_vector_type(2))) __bf16;
using bf8v = __attribute__((ext_vector_type(8))) __bf16;
using bf16v = __attribute__((ext_vector_type(16))) __bf16;
using s16x8 = __attribute__((ext_vector_type(8))) short;
using s16x4 = __attribute__((ext_vector_type(4))) short;
using f32x2 = __attribute__((ext_vector_type(2))) float;
using f32x4 = __attribute__((ext_vector_type(4))) float;
using u32x2 = __attribute__((ext_vector_type(2))) unsigned;
using u32x3 = __attribute__((ext_vector_type(3))) unsigned;
typedef u32x3 u32x3u __attribute__((aligned(4)));
using u32x4 = __attribute__((ext_vector_type(4))) unsigned;

constexpr int WS_EPOCH = 0;
constexpr int WS_PF = 4096;
constexpr int WS_KR = WS_PF + NFFN * 128;
constexpr int WS_KF = WS_KR + 8 * 2048;
constexpr int WS_ST = WS_KF + 16384;
constexpr int WS_CK = WS_ST + 16384;
constexpr int WS_FL = WS_CK + 8 * 2048;
constexpr int WS_UP = 131072;
constexpr int WS_ZP = WS_UP + NCONV * NLN * 128;
constexpr int WS_PART = WS_ZP + NCONV * NLN * 128;
constexpr int WS_XS = WS_PART + NFFN * 8 * DM * 4;
constexpr int XSS = 4 * DM;
constexpr int WS_END = WS_XS + (2 * NLN + 8) * XSS;
static_assert(WS_FL + 8 * 1024 <= WS_UP, "ws");
constexpr size_t WS_TS = 64u << 20;
constexpr int SC1 = 16, NT = 2;
constexpr int SPIN_CAP = 1 << 16;

#define TW(k)
#define TWA(k)
#define TWB(k)
#define TWDECL
#define TSX(k, s)
#define TWFLUSH()
#define TC(k, s)
#define TSW(w, s)
#define TSL(k, s)
#define TS(k, s)

__device__ __forceinline__ float bflo(unsigned v) { return __uint_as_float(v << 16); }
__device__ __forceinline__ float bfhi(unsigned v) { return __uint_as_float(v & 0xffff0000u); }
__device__ __forceinline__ unsigned pkbf(float a, float b) {
  f32x2 f = {a, b};
  return __builtin_bit_cast(unsigned, __builtin_convertvector(f, bf2v));
}
__device__ __forceinline__ unsigned pkh(float a, float b) {
  f32x2 f = {a, b};
  return __builtin_bit_cast(unsigned, __builtin_convertvector(f, half2v));
}
__device__ __forceinline__ float rbf(float f) { return __uint_as_float(pkbf(0.f, f)); }
__device__ __forceinline__ f32x2 rbf2(f32x2 v) { return f32x2{rbf(v[0]), rbf(v[1])}; }
__device__ __forceinline__ f32x2 unpk(unsigned v) { return f32x2{bflo(v), bfhi(v)}; }
__device__ __forceinline__ float dot2(unsigned a, unsigned b, float c) {
  return __builtin_amdgcn_fdot2_f32_bf16(__builtin_bit_cast(bf2v, a), __builtin_bit_cast(bf2v, b), c, false);
}
__device__ __forceinline__ float hadd(f32x2 v) {
  float a = v[0], b = v[1];
  asm volatile("" : "+v"(a), "+v"(b));
  return a + b;
}

template <int CTRL>
__device__ __forceinline__ float dppf(float v) {
  return __int_as_float(__builtin_amdgcn_update_dpp(0, __float_as_int(v), CTRL, 0xf, 0xf, true));
}
template <int CTRL>
__device__ __forceinline__ unsigned dppu(unsigned v) {
  return (unsigned)__builtin_amdgcn_update_dpp(0, (int)v, CTRL, 0xf, 0xf, true);
}
__device__ __forceinline__ float swap16_sum(float v) {
  auto r = __builtin_amdgcn_permlane16_swap(__float_as_uint(v), __float_as_uint(v), false, false);
  return __uint_as_float(r[0]) + __uint_as_float(r[1]);
}
__device__ __forceinline__ float swap32_sum(float v) {
  auto r = __builtin_amdgcn_permlane32_swap(__float_as_uint(v), __float_as_uint(v), false, false);
  return __uint_as_float(r[0]) + __uint_as_float(r[1]);
}
__device__ __forceinline__ float red32_sum(float v) {
  v += dppf<0xB1>(v);
  v += dppf<0x4E>(v);
  v += dppf<0x141>(v);
  v += dppf<0x140>(v);
  return swap16_sum(v);
}
#define SCHED_FENCE() __builtin_amdgcn_sched_barrier(0)
#define DRAIN() asm volatile("s_waitcnt vmcnt(0)" ::: "memory")

__device__ __forceinline__ float red16_sum(float v) {
  v += dppf<0xB1>(v);
  v += dppf<0x4E>(v);
  v += dppf<0x141>(v);
  return v + dppf<0x140>(v);
}
__device__ __forceinline__ float ln_rinv(float q, float dm) {
  const float var = fmaxf(fmaf(q, 1.0f / DM, -dm * dm), 0.f);
  return __builtin_amdgcn_rcpf(rbf(__builtin_amdgcn_sqrtf(rbf(rbf(var) + 1e-5f))));
}

constexpr int TAB_STRIDE = 544;
constexpr int TAB_ROWS = 58;
constexpr int TR_B = 18, TR_M = 26, TR_F = 34, TR_H = 42;
constexpr int SM_US = TAB_ROWS * TAB_STRIDE;
constexpr int UPAD = 256;
constexpr int SM_KC = SM_US + (UPAD + SEQ) * 2;
constexpr int RDS = 336 * 4;
constexpr int KC_RD = SM_KC;
constexpr int KC_R5 = KC_RD + 4 * RDS;
constexpr int KC_R3 = KC_R5 + 64 * 4;
constexpr int KC_R9 = KC_R3 + 64 * 4;
constexpr int KC_R7 = KC_R9 + 96 * 4;
constexpr int CH_BYTES = KC_R7 + 96 * 4;
constexpr int KC_CHUNKS = (CH_BYTES - SM_KC) / 16;
constexpr int YT = 1280;
constexpr int SM_YD = 0, SM_Y2 = 8 * YT - 2 * YT, SM_Y3 = 14 * YT - 4 * YT;
constexpr int SM_T2 = 0, T2S = 4624;
constexpr int SM_RED = 3 * CH_BYTES;
constexpr int SM_ROW = SM_RED + 3200;
constexpr int SM_ISC = SM_ROW + 128 + 128 + 32;
constexpr int SM_MX = SM_ISC + 16;
constexpr int SM_WR = SM_MX + 32;
constexpr int SM_DUMMY = SM_WR + DM * 2;
constexpr int RG = 9216;
constexpr int XRS = 1216;
constexpr int FS_SEL = 14 * RG;
constexpr int FS_GU = FS_SEL + 32;
constexpr int SM_TOTAL = SM_DUMMY + 256;
static_assert(FS_GU + 8 * 160 <= SM_TOTAL && SM_ISC % 16 == 0 && CH_BYTES % 16 == 0, "lds");

typedef f32x2 cf;
struct cstep { cf s, r; };
__device__ __forceinline__ cstep mkstep(cf a) { return cstep{a, cf{-a[1], a[0]}}; }
__device__ __forceinline__ cf cmul(cf a, const cstep& b) {
  cf t, o;
  asm("v_pk_mul_f32 %0, %1, %2 op_sel:[1,0]" : "=v"(t) : "v"(a), "v"(b.r));
  asm("v_pk_fma_f32 %0, %1, %2, %3 op_sel_hi:[0,1,1]" : "=v"(o) : "v"(a), "v"(b.s), "v"(t));
  return o;
}
template <int R0, int R1, int R2, int R3>
__device__ __forceinline__ void tab_step(const LAS char* tab, cf& a0, cf& a1, cf& b0, cf& b1, const cstep& p, const cstep& q) {
  unsigned t0, t1, t2, t3;
  cf m0, m1, m2, m3;
  asm volatile(
      "v_cvt_pk_f16_f32 %0, %8, %9\n\tv_cvt_pk_f16_f32 %1, %10, %11\n\tv_cvt_pk_f16_f32 %2, %12, %13\n\tv_cvt_pk_f16_f32 %3, %14, %15\n\t"
      "v_pk_mul_f32 %4, %16, %20 op_sel:[1,0]\n\tds_write_b32 %22, %0 offset:%23\n\t"
      "v_pk_mul_f32 %5, %17, %20 op_sel:[1,0]\n\tds_write_b32 %22, %1 offset:%24\n\t"
      "v_pk_mul_f32 %6, %18, %21 op_sel:[1,0]\n\tds_write_b32 %22, %2 offset:%25\n\t"
      "v_pk_mul_f32 %7, %19, %21 op_sel:[1,0]\n\tds_write_b32 %22, %3 offset:%26"
      : "=&v"(t0), "=&v"(t1), "=&v"(t2), "=&v"(t3), "=&v"(m0), "=&v"(m1), "=&v"(m2), "=&v"(m3)
      : "v"(a0[0]), "v"(a0[1]), "v"(a1[0]), "v"(a1[1]), "v"(b0[0]), "v"(b0[1]), "v"(b1[0]), "v"(b1[1]), "v"(a0), "v"(a1), "v"(b0), "v"(b1),
        "v"(p.r), "v"(q.r), "v"(tab), "i"(R0 * TAB_STRIDE), "i"(R1 * TAB_STRIDE), "i"(R2 * TAB_STRIDE), "i"(R3 * TAB_STRIDE)
      : "memory");
  asm volatile(
      "v_pk_fma_f32 %0, %0, %8, %4 op_sel_hi:[0,1,1]\n\tv_pk_fma_f32 %1, %1, %8, %5 op_sel_hi:[0,1,1]\n\t"
      "v_pk_fma_f32 %2, %2, %9, %6 op_sel_hi:[0,1,1]\n\tv_pk_fma_f32 %3, %3, %9, %7 op_sel_hi:[0,1,1]"
      : "+&v"(a0), "+&v"(a1), "+&v"(b0), "+&v"(b1)
      : "v"(m0), "v"(m1), "v"(m2), "v"(m3), "v"(p.s), "v"(q.s));
}
__device__ __forceinline__ unsigned pkc(cf a) { return __builtin_bit_cast(unsigned, __builtin_convertvector(a, half2v)); }
__device__ __forceinline__ cf cexp_p2(float p, float ch, float cl, float wh, float wl) {
  float mag = __builtin_amdgcn_exp2f(p * ch);
  mag = fmaf(mag, (p * 0.693147181f) * cl, mag);
  const float rh = p * wh;
  const float fr = fmaf(p, wl, rh - rintf(rh));
  return cf{mag * __builtin_amdgcn_cosf(fr), mag * __builtin_amdgcn_sinf(fr)};
}
__device__ __forceinline__ float exp_acc(float x) {
  const float th = x * 1.44269502f;
  const float tl = fmaf(x, 1.92596299e-8f, fmaf(x, 1.44269502f, -th));
  const float e = __builtin_amdgcn_exp2f(th);
  return fmaf(e, tl * 0.693147181f, e);
}
__device__ __forceinline__ unsigned wave_max_u32(unsigned v) {
  v = max(v, (unsigned)__builtin_amdgcn_update_dpp(0, (int)v, 0x111, 0xf, 0xf, false));
  v = max(v, (unsigned)__builtin_amdgcn_update_dpp(0, (int)v, 0x112, 0xf, 0xf, false));
  v = max(v, (unsigned)__builtin_amdgcn_update_dpp(0, (int)v, 0x114, 0xf, 0xf, false));
  v = max(v, (unsigned)__builtin_amdgcn_update_dpp(0, (int)v, 0x118, 0xf, 0xf, false));
  v = max(v, (unsigned)__builtin_amdgcn_update_dpp(0, (int)v, 0x142, 0xa, 0xf, false));
  v = max(v, (unsigned)__builtin_amdgcn_update_dpp(0, (int)v, 0x143, 0xc, 0xf, false));
  return (unsigned)__builtin_amdgcn_readlane((int)v, 63);
}

#define MFMA16(a, b, c) __builtin_amdgcn_mfma_f32_16x16x32_f16((a), (b), (c), 0, 0, 0)
#define MFMAB(a, b, c) __builtin_amdgcn_mfma_f32_16x16x32_bf16((a), (b), (c), 0, 0, 0)

__device__ __forceinline__ half8 ld_frag(const LAS char* p) { return *(const LAS half8*)p; }
__device__ __forceinline__ const LAS unsigned* opaque_lds(const LAS char* p) {
  const LAS unsigned* q = (const LAS unsigned*)p;
  asm volatile("" : "+v"(q));
  return q;
}
template <int O2>
__device__ __forceinline__ half8 ld_toep(const LAS unsigned* q) {
  u32x4 v = {q[0], q[1], q[O2], q[O2 + 1]};
  return __builtin_bit_cast(half8, v);
}
__device__ __forceinline__ half8 band_frag(int lane, unsigned pat, unsigned one) {
  const int bits = (int)((pat << (lane & 15)) >> (8 * (lane >> 4)));
  u32x4 v;
#pragma unroll
  for (int j = 0; j < 4; j++) {
    const unsigned lo = (unsigned)__builtin_amdgcn_sbfe(bits, 2 * j, 1), hi = (unsigned)__builtin_amdgcn_sbfe(bits, 2 * j + 1, 1);
    v[j] = __builtin_amdgcn_perm(hi, lo, 0x05040100u) & one;
  }
  return __builtin_bit_cast(half8, v);
}
__device__ __forceinline__ half8 pack_win(f32x4 a, f32x4 b) {
  u32x4 v = {pkh(a[0], a[1]), pkh(a[2], a[3]), pkh(b[0], b[1]), pkh(b[2], b[3])};
  return __builtin_bit_cast(half8, v);
}

template <int V>
using IC = std::integral_constant<int, V>;
template <int S, int E, class F>
__device__ __forceinline__ void static_for(F&& f) {
  if constexpr (S < E) {
    f(std::integral_constant<int, S>{});
    static_for<S + 1, E>(f);
  }
}
__device__ __forceinline__ half8 ld_td(const LAS char* tp, int i) {
  const volatile LAS u32x2* tb = (const volatile LAS u32x2*)tp;
  const u32x2 lo = tb[8 * (16 - i)], hi = tb[8 * (16 - i) + 1];
  return __builtin_bit_cast(half8, __builtin_shufflevector(lo, hi, 0, 1, 2, 3));
}
__device__ __forceinline__ void conv_dense(half8 (&td)[17], const LAS char* tp, half8 tmask, const LAS char* f1024, const LAS char* ub, LAS char* yd) {
  constexpr int S0 = 9, NS = 73, PF = PFD;
  const f32x4 zero4 = {0.f, 0.f, 0.f, 0.f};
  f32x4 acc[8] = {zero4, zero4, zero4, zero4, zero4, zero4, zero4, zero4};
  half8 u[NS], te;
  unsigned fe = 0;
#pragma unroll
  for (int s = S0; s < S0 + PF; s++) {
    u[s] = ld_frag(ub + 64 * s);
    if (s == S0 + 3) {
      td[0] = ld_td(tp, 0);
      fe = *(const LAS u16*)f1024;
    }
    SCHED_FENCE();
  }
  static_for<S0, NS>([&](auto sc) {
    constexpr int s = decltype(sc)::value, t0 = s < 16 ? 0 : (s - 9) / 8, t1 = s / 8 < 7 ? s / 8 : 7;
    acc[t0] = MFMA16(td[16 + 8 * t0 - s], u[s], acc[t0]);
    SCHED_FENCE();
    if constexpr (s + PF < NS) u[s + PF] = ld_frag(ub + 64 * (s + PF));
    if constexpr (s == 67) *(LAS f32x4*)(yd + 6 * YT) = acc[6];
    SCHED_FENCE();
    if constexpr (t0 + 1 <= t1) acc[t0 + 1] = MFMA16(td[24 + 8 * t0 - s], u[s], acc[t0 + 1]);
    SCHED_FENCE();
    if constexpr (s >= 24 && (s & 7) == 0 && s < 72) *(LAS f32x4*)(yd + (s / 8 - 3) * YT) = acc[s / 8 - 3];
    if constexpr (s == 14) {
      fe |= fe << 16;
      te = __builtin_bit_cast(half8, __builtin_bit_cast(u32x4, tmask) & u32x4{fe, fe, fe, fe});
    }
    SCHED_FENCE();
    if constexpr (t0 + 2 <= t1) acc[t0 + 2] = MFMA16(td[32 + 8 * t0 - s], u[s], acc[t0 + 2]);
    if constexpr (s >= 16 && s <= 40 && (s & 7) == 0) acc[s / 8 + 2] = MFMA16(te, u[s], acc[s / 8 + 2]);
    SCHED_FENCE();
  });
  *(LAS f32x4*)(yd + 7 * YT) = acc[7];
}

typedef __amdgpu_buffer_rsrc_t rsrc_t;
__device__ __forceinline__ u32x4 ldg16(rsrc_t rs, int off, int so = 0) {
  asm volatile("" : "+s"(so) : : "memory");
  return __builtin_amdgcn_raw_buffer_load_b128(rs, off, so, LAUX);
}
__device__ __forceinline__ void stg16(rsrc_t rs, int off, u32x4 v) { __builtin_amdgcn_raw_buffer_store_b128(v, rs, off, 0, SAUX); }
__device__ __forceinline__ void dma16(const char* g, unsigned lds) {
  unsigned keep;
  asm volatile("s_mov_b32 %0, m0\n\ts_mov_b32 m0, %2\n\ts_nop 0\n\tglobal_load_lds_dwordx4 %1, off" WNTS "\n\ts_mov_b32 m0, %0"
               : "=&s"(keep) : "v"(g), "s"(lds) : "memory");
}
__device__ __forceinline__ void dma4(const char* g, unsigned lds) {
  unsigned keep;
  asm volatile("s_mov_b32 %0, m0\n\ts_mov_b32 m0, %2\n\ts_nop 0\n\tglobal_load_lds_dword %1, off\n\ts_mov_b32 m0, %0"
               : "=&s"(keep) : "v"(g), "s"(lds) : "memory");
}
__device__ __forceinline__ void wait_ticks(int t) {
  const unsigned long long t0 = __builtin_amdgcn_s_memrealtime();
  while (__builtin_amdgcn_s_memrealtime() - t0 < (unsigned long long)t) __builtin_amdgcn_s_sleep(1);
}
#define UPOS 1
#define DLY_X 20
#define DLY_W 80
#define HB 1
#define DLY_H 70
#define DLY_R 0
#define DLY_L 200
#define DLY_P 40

__device__ __forceinline__ int key_row(unsigned k) { return (int)(~(k >> 2) & (SEQ - 1)); }
__device__ __forceinline__ int key_slot(int lane) { return (lane & 3) | ((lane >> 1) & 4); }
__device__ __forceinline__ int key_addr(unsigned k, int slot) {
  return WS_XS + ((k & 2) ? 2 * NLN + slot : (int)((~(k >> 5) & (2 * NLN - 2)) | (k & 1))) * XSS;
}
struct SelMasks {
  unsigned m[15];
  __device__ __forceinline__ SelMasks(int lane) {
    const unsigned b0 = -(lane & 1), b1 = -((lane >> 1) & 1), b3 = -((lane >> 3) & 1), b4 = -((lane >> 4) & 1), b5 = -(lane >> 5);
    const unsigned t[15] = {~(b0 ^ b1), ~(b1 ^ b3), ~(b0 ^ b3), ~b3, ~b1, ~b0, b3, b1, b0, ~(b3 ^ b4), ~(b1 ^ b4), ~(b0 ^ b4), ~(b3 ^ b5), ~(b1 ^ b5), ~(b0 ^ b5)};
#pragma unroll
    for (int i = 0; i < 15; i++) {
      m[i] = t[i];
      asm volatile("" : "+v"(m[i]));
    }
  }
};
template <int CTRL>
__device__ __forceinline__ unsigned cex(unsigned v, unsigned m) {
  const unsigned o = dppu<CTRL>(v);
  return max(min(v, o), min(max(v, o), m));
}
__device__ __forceinline__ unsigned clean8(unsigned v, const unsigned* m) { return cex<0xB1>(cex<0x4E>(cex<0x128>(v, m[0]), m[1]), m[2]); }
__device__ __forceinline__ unsigned select8(rsrc_t rs, unsigned tag, int lane, const SelMasks& sm, char* ws) {
  const int off = WS_KR + (blockIdx.x & 7) * 2048 + lane * 16;
  u32x4 a, c;
  _Pragma("unroll 1") for (int spin = 0; spin < SPIN_CAP; spin++) {
    a = ldg16(rs, off);
    c = ldg16(rs, off, 1024);
    if (__builtin_expect(__all(a[3] == tag && c[3] == tag), 1)) break;
    __builtin_amdgcn_s_sleep(2);
  }
  TSL(1, 8);
  const unsigned t1 = min(a[0], c[0]), t2 = max(a[1], c[1]), third = max(a[2], c[2]);
  unsigned m1 = max(a[0], c[0]), m2 = max(t1, t2), m3 = min(t1, t2);
  unsigned x = clean8(cex<0xB1>(cex<0x4E>(cex<0xB1>(m1, sm.m[0]), sm.m[1]), sm.m[2]), sm.m + 3);
  const unsigned y = clean8(cex<0xB1>(cex<0x4E>(cex<0xB1>(m2, sm.m[0]), sm.m[1]), sm.m[2]), sm.m + 6);
  x = clean8(max(x, y), sm.m + 3);
  x = clean8(max(x, dppu<0x140>(x)), sm.m + 9);
  {
    auto r = __builtin_amdgcn_permlane16_swap(x, x, false, false);
    x = clean8(max(r[0], r[1]), sm.m + 12);
    auto q = __builtin_amdgcn_permlane32_swap(x, x, false, false);
    x = max(q[0], q[1]);
  }
  unsigned k = min(x, dppu<0xB1>(x));
  k = min(k, dppu<0x4E>(k));
  k = min(k, dppu<0x128>(k));
  if (__builtin_expect(__any(max(m3, third) > k), 0)) {
    const int slot = key_slot(lane);
    unsigned m4 = min(a[1], c[1]);
    _Pragma("unroll 1") for (int r = 0; r < 8; r++) {
      k = wave_max_u32(m1);
      x = slot == r ? k : x;
      const bool e = m1 == k;
      m1 = e ? m2 : m1;
      m2 = e ? m3 : m2;
      m3 = e ? m4 : m3;
      m4 = e ? 0u : m4;
    }
    if (__any(third > k)) {
      k = 0u;
      _Pragma("unroll 1") for (int r = 0; r < 8; r++) {
        unsigned v;
        _Pragma("unroll 1") for (int spin = 0; spin < SPIN_CAP; spin++) {
          bool ok = true;
          v = 0u;
          _Pragma("unroll 4") for (int i = 0; i < 16; i++) {
            const u32x4 f = ldg16(rs, WS_KF + lane * 16, i * 1024);
            ok = ok && f[1] == tag && f[3] == tag;
            v = max(v, max(f[0] <= k - 1u ? f[0] : 0u, f[2] <= k - 1u ? f[2] : 0u));
          }
          if (__all(ok)) break;
          __builtin_amdgcn_s_sleep(1);
        }
        k = wave_max_u32(v);
        x = slot == r ? k : x;
      }
    }
  }
  return x;
}

__device__ __forceinline__ void ffn_role(const u16* __restrict__ wg, const u16* __restrict__ wu, const u16* __restrict__ wd,
                                         char* ws, rsrc_t rs, unsigned tagv, LAS char* L) {
  const int tid = threadIdx.x, lane = tid & 63;
  const int wave = __builtin_amdgcn_readfirstlane(tid >> 6);
  const int fw = blockIdx.x;
  const int j = (fw & 7) * 8 + (fw >> 3);
  const int g = lane >> 4, tq = (lane & 15) >> 2, tp = lane & 3;
  const int r32 = tid / 18, rem = tid - r32 * 18;
  const char* gsrc = (const char*)(rem >= 9 ? wu : wg) + (unsigned)(r32 * (FF * 2) + j * 144 + (rem >= 9 ? rem - 9 : rem) * 16);
  const unsigned lb = (unsigned)(size_t)L + wave * 1024;
  auto issue_gu = [&](int ks0, int n, int reg0) {
    if (wave < 9) {
#pragma unroll
      for (int i = 0; i < n; i++) dma16(gsrc + (size_t)(ks0 + i) * (32 * FF * 2), lb + (reg0 + i) * RG);
    }
  };
  if (DLY_W) wait_ticks(DLY_W);
  if (wave < 9) {
    const int r8 = tid / 72, q = (tid - r8 * 72) ^ (r8 & 2);
    const char* dsrc = (const char*)wd + (unsigned)((j * 72 + r8) * 1152);
#pragma unroll
    for (int i = 0; i < 9; i++) dma16(dsrc + (unsigned)(i * RG + (q ^ ((i & 1) << 2)) * 16), lb + i * RG);
  }
  issue_gu(0, 5, 9);
  if (tid < 8 * 4) *(LAS unsigned*)(L + FS_GU + (tid >> 2) * 160 + 144 + (tid & 3) * 4) = 0u;
  s16x4 fa[18][2], fd[3][5];
  const LAS char* gup = L + (16 * (g >> 1) + 4 * (g & 1) + (tq & 1) + 8 * (tq >> 1)) * 288 + (tp >> 1) * 144 + (tp & 1) * 8 + 16 * wave;
  auto read_gu = [&](auto ks0, auto n, auto reg0) {
    if (wave < 9) {
#pragma unroll
      for (int i = 0; i < n; i++)
#pragma unroll
        for (int h = 0; h < 2; h++) fa[ks0 + i][h] = __builtin_amdgcn_ds_read_tr16_b64_v4i16((LAS s16x4*)(gup + (reg0 + i) * RG + 2 * h * 288));
    }
  };
  asm volatile("s_waitcnt vmcnt(5)" ::: "memory");
  __syncthreads();
  {
    const int f = (tq >> 1) | ((g & 1) << 1);
#pragma unroll
    for (int t = 0; t < 3; t++) {
      const LAS char* p = L + (8 * g + tq) * 1152 + ((3 * wave + t) ^ f) * 32 + tp * 8;
#pragma unroll
      for (int h = 0; h < 4; h++)
        fd[t][h] = __builtin_amdgcn_ds_read_tr16_b64_v4i16((LAS s16x4*)(p + (32 * (h >> 1) + 4 * (h & 1)) * 1152));
      fd[t][4] = __builtin_amdgcn_ds_read_tr16_b64_v4i16(
          (LAS s16x4*)(L + (64 + 4 * (g & 1) + tq) * 1152 + ((3 * wave + t) ^ (tq >> 1)) * 32 + tp * 8));
    }
  }
  __syncthreads();
  TS(1, 6);
  issue_gu(5, 9, 0);
  asm volatile("s_waitcnt vmcnt(9)" ::: "memory");
  __syncthreads();
  read_gu(IC<0>{}, IC<5>{}, IC<9>{});
  __syncthreads();
  TS(1, 7);
  issue_gu(14, 4, 9);
  asm volatile("s_waitcnt vmcnt(4)" ::: "memory");
  __syncthreads();
  TS(1, 1);
  read_gu(IC<5>{}, IC<9>{}, IC<0>{});
  DRAIN();
  __syncthreads();
  read_gu(IC<14>{}, IC<4>{}, IC<9>{});
  const unsigned tag = __builtin_amdgcn_readfirstlane(tagv) + 1u;
  TS(1, 2);
  const int c15 = lane & 15;
  const int xs = tid / 72, xc = tid - xs * 72;
  const LAS char* selr = L + FS_SEL + (xs & 7) * 4;
  LAS char* xw = L + xs * XRS + xc * 16;
  const LAS char* xr = L + (lane & 7) * XRS + (c15 >> 3) * 16 + g * 32;
  LAS char* guw = L + FS_GU + (4 * (g & 1) + 2 * (g >> 1) + (c15 >> 3)) * 160 + (wave * 8 + (lane & 7)) * 2;
  const LAS char* gur = L + FS_GU + (lane & 7) * 160 + 16 * g;
  const LAS char* gur2 = L + FS_GU + (lane & 7) * 160 + 128 + 8 * g;
  int xo = xc * 16, po = WS_PART + fw * (8 * DM * 4) + 3 * wave * 512 + (lane & 7) * 64 + 16 * g;
  int sidx = c15 < 8 ? 0x4444 : 0xeeee;
  asm volatile("" : "+v"(selr), "+v"(guw), "+v"(gur), "+v"(gur2), "+v"(xo), "+v"(po), "+v"(sidx), "+v"(xw), "+v"(xr));
  if (wave == 9) {
    const SelMasks sm(lane);
    LAS char* selw = L + FS_SEL + key_slot(lane) * 4;
    asm volatile("" : "+v"(selw));
    int spin = 0;
    _Pragma("unroll 1") for (; spin < SPIN_CAP; spin++) {
      const unsigned t = ldg16(rs, WS_PF + fw * 128)[0];
      if (__builtin_amdgcn_readfirstlane(t) == tag) break;
      __builtin_amdgcn_s_sleep(2);
    }
    if (spin) wait_ticks(DLY_X);
    TSL(1, 3);
    *(LAS int*)selw = key_addr(select8(rs, tag, lane, sm, ws), key_slot(lane));
    if (DLY_R) wait_ticks(DLY_R);
  }
  __syncthreads();
  TS(1, 9);
  if (wave < 9) {
    xo += *(const LAS int*)selr;
    u32x4 v;
    _Pragma("unroll 1") for (int spin = 0; spin < SPIN_CAP; spin++) {
      v = ldg16(rs, xo);
      if (__builtin_expect(__all(max(max(v[0], v[1]), max(v[2], v[3])) != ~0u), 1)) break;
      __builtin_amdgcn_s_sleep(2);
    }
    *(LAS u32x4*)xw = v;
  }
  __syncthreads();
  TS(1, 4);
  if (wave < 9) {
    bf8v xf[9];
#pragma unroll
    for (int i = 0; i < 9; i++) xf[i] = *(const LAS bf8v*)(xr + 128 * i);
    SCHED_FENCE();
    f32x4 acc = {0.f, 0.f, 0.f, 0.f};
#pragma unroll
    for (int i = 0; i < 9; i++) {
      const s16x8 lo = __builtin_shufflevector(fa[2 * i][0], fa[2 * i][1], 0, 1, 2, 3, 4, 5, 6, 7);
      const s16x8 hi = __builtin_shufflevector(fa[2 * i + 1][0], fa[2 * i + 1][1], 0, 1, 2, 3, 4, 5, 6, 7);
      acc = __builtin_amdgcn_smfmac_f32_16x16x64_bf16(
          xf[i], __builtin_bit_cast(bf16v, __builtin_shufflevector(lo, hi, 0, 1, 2, 3, 4, 5, 6, 7, 8, 9, 10, 11, 12, 13, 14, 15)), acc, sidx, 0, 0);
    }
    const float a0 = acc[0], a1 = acc[1], a2 = acc[2], a3 = acc[3];
    auto r0 = __builtin_amdgcn_permlane32_swap(__float_as_uint(a0), __float_as_uint(a2), false, false);
    auto r1 = __builtin_amdgcn_permlane32_swap(__float_as_uint(a1), __float_as_uint(a3), false, false);
    const float s0 = __uint_as_float(r0[0]) + __uint_as_float(r0[1]), s1 = __uint_as_float(r1[0]) + __uint_as_float(r1[1]);
    const float gs = __int_as_float(__builtin_amdgcn_update_dpp(__float_as_int(s0), __float_as_int(s1), 0x128, 0xf, 0xc, false));
    const float us = __int_as_float(__builtin_amdgcn_update_dpp(__float_as_int(s1), __float_as_int(s0), 0x128, 0xf, 0x3, false));
    const float gp = rbf(gs);
    const float gq = rbf(gp * __builtin_amdgcn_rcpf(1.0f + __expf(-gp)));
    *(LAS u16*)guw = (u16)pkbf(gq * rbf(us), 0.f);
  }
  __syncthreads();
  {
    const bf8v b0 = *(const LAS bf8v*)gur, b1 = *(const LAS bf8v*)(gur + 64);
    const s16x4 b2 = *(const LAS s16x4*)gur2;
    f32x4 acc[3];
#pragma unroll
    for (int t = 0; t < 3; t++)
      acc[t] = MFMAB(__builtin_bit_cast(bf8v, __builtin_shufflevector(fd[t][0], fd[t][1], 0, 1, 2, 3, 4, 5, 6, 7)), b0, (f32x4{0.f, 0.f, 0.f, 0.f}));
#pragma unroll
    for (int t = 0; t < 3; t++)
      acc[t] = MFMAB(__builtin_bit_cast(bf8v, __builtin_shufflevector(fd[t][2], fd[t][3], 0, 1, 2, 3, 4, 5, 6, 7)), b1, acc[t]);
#pragma unroll
    for (int t = 0; t < 3; t++) acc[t] = __builtin_amdgcn_mfma_f32_16x16x16bf16_1k(fd[t][4], b2, acc[t], 0, 0, 0);
    SCHED_FENCE();
    if (c15 < 8) {
#pragma unroll
      for (int t = 0; t < 3; t++) {
        u32x4 o = __builtin_bit_cast(u32x4, acc[t]);
        o[0] = min(o[0], 0xfffffffeu);
        stg16(rs, po + t * 512, o);
      }
    }
  }
  TS(1, 5);
  TC(1, 15);
}

__global__ __launch_bounds__(NTHR, 3) void k_all(const u16* __restrict__ x, const u16* __restrict__ lar,
                                                 const u16* __restrict__ aim, const u16* __restrict__ crl,
                                                 const u16* __restrict__ cim, const u16* __restrict__ ldt,
                                                 const u16* __restrict__ dskip, const u16* __restrict__ wr,
                                                 const u16* __restrict__ wg, const u16* __restrict__ wu,
                                                 const u16* __restrict__ wd, u16* __restrict__ out,
                                                 char* ws) {
  __shared__ __attribute__((aligned(16))) char smem[SM_TOTAL];
  LAS char* const L = (LAS char*)smem;
  unsigned cpf = 0;
  if (blockIdx.x >= NFFN + NLN && threadIdx.x >= 256 && threadIdx.x < 256 + CPF)
    cpf = *(const __attribute__((address_space(1))) unsigned*)((__builtin_amdgcn_s_getpc() & ~127ull) + (threadIdx.x - 256) * 128);
  asm volatile("" ::"s"(x), "s"(lar), "s"(aim), "s"(crl), "s"(cim), "s"(ldt), "s"(dskip), "s"(wr), "s"(wg), "s"(wu), "s"(wd),
               "s"(out), "s"(ws));
  const int tid = threadIdx.x, lane = tid & 63, b = (int)blockIdx.x - NFFN;
  const int wave = __builtin_amdgcn_readfirstlane(tid >> 6);
  const rsrc_t rs = __builtin_amdgcn_make_buffer_rsrc(ws, 0, WS_END, 0x00020000);
  const unsigned tagv = __builtin_amdgcn_raw_buffer_load_b32(rs, WS_EPOCH, 0, SC1);
  if (b < 0) {
    TS(1, 0);
  TC(1, 14);
    ffn_role(wg, wu, wd, ws, rs, tagv, L);
    return;
  }
  TWDECL;
  TS(0, 0);
  TC(0, 14);
  const bool is_ln = b < NLN;
  const int hl = lane & 31, rsel = lane >> 5;
  const int ch = (wave & 3) < 3 ? (wave & 3) : (wave >> 2), sub = (wave & 3) < 3 ? (wave >> 2) : 3;
  const int d = 3 * b + ch;
  const int n = (sub & 1) * 64 + lane;
  const unsigned po = (unsigned)(d * NM + n) * 2u, po2 = po ^ 128u;
  auto ld16 = [](const u16* p, unsigned byte_off) { return *(const u16*)((const char*)p + byte_off); };
  const u16 r_lar = ld16(lar, po), r_aim = ld16(aim, po), r_cr = ld16(crl, po), r_ci = ld16(cim, po), r_cr2 = ld16(crl, po2), r_ci2 = ld16(cim, po2);
  const u16 r_ldt = ldt[(unsigned)d], r_dsk = ld16(dskip + 3 * b, (unsigned)min(lane, 2) * 2u);
  SCHED_FENCE();
  __builtin_amdgcn_s_barrier();
  u32x3 cp[4];
  u32x2 xw[9];
  int cpo = 0;
  const bool st_wave = is_ln && (wave >> 2) == 1;
  if (wave < 4) {
    const int hrow = (is_ln ? 8 * b : 16 * (b - NLN) + 8 * (wave & 1)) + (CMAP ? lane & 7 : (lane >> 2) & 7);
    const int grp = (is_ln ? 16 * wave : 64 + 8 * (wave & 2)) + (CMAP ? (lane >> 3) & 3 : lane & 3);
    const char* src = (const char*)x + (unsigned)((2 * hrow + rsel) * (DM * 2) + grp * 12);
#pragma unroll
    for (int i = 0; i < 4; i++) cp[i] = *(const u32x3u*)(src + 48 * i);
    cpo = WS_UP + ((grp + 4 * rsel) * 2048 + hrow) * 16;
  } else if (st_wave) {
    const char* xr = (const char*)x + (unsigned)((b * 16 + (tid >> 4) - 16) * (DM * 2) + (lane & 15) * 8);
#pragma unroll
    for (int k = 0; k < 9; k++) xw[k] = *(const u32x2*)(xr + 128 * k);
    __builtin_amdgcn_s_setprio(2);
  }
  SCHED_FENCE();
  auto prefetch = [&](int i) {
    const int xg = b & 7, sl = b >> 3;
    const char* base = (const char*)(sl < 12 ? wg : wu) + (144 * i + 48 * ch + 4 * (sl < 12 ? sl : sl - 12)) * (FF * 2) + xg * 1152;
    dma4(base + (unsigned)((lane >> 4) * (FF * 2) + min(lane & 15, 8) * 128), (unsigned)(size_t)L + SM_DUMMY);
  };
  const int part_off = WS_PART + (tid >> 3) * (8 * DM * 4) + (b - RED0) * 128 + (tid & 7) * 16;
  LAS char* const C = L + ch * CH_BYTES;
  {
    const int ct = tid & 255;
    LAS char* zc = L + (wave >> 2) * CH_BYTES;
    const u32x4 z = {0u, 0u, 0u, 0u};
    *(LAS u32x4*)(zc + SM_KC + 16 * ct) = z;
    if (ct < KC_CHUNKS - 256) *(LAS u32x4*)(zc + SM_KC + 4096 + 16 * ct) = z;
    else if (ct >= 256 - UPAD / 8) *(LAS u32x4*)(zc + SM_US + 16 * (ct - (256 - UPAD / 8))) = z;
  }
  half8 bA = band_frag(lane, sub == 3 ? 0x1fu << 6 : (sub == 2 ? 0x1ffu << 4 : 0x3u << 12), sub == 0 ? ~0u : 0x3c003c00u);
  half8 bB = band_frag(lane, sub == 3 ? 0x7u << 11 : 0x7fu << 5, 0x3c003c00u);
  const int c15 = lane & 15, g = lane >> 4;
  const LAS char *pa, *pa2, *pb;
  LAS char *kd, *kd2;
  unsigned ks0, ks1;
  if (sub == 0) {
    const int r = c15 & 3, r3 = r == 3;
    pa = C + (c15 < 8 ? 2 * (c15 & 4) + 9 - r : TR_B + 1 + ((c15 >> 1) & 2) - r3) * TAB_STRIDE + 16 * g;
    pa2 = pa + (c15 < 8 ? -4 : 4) * TAB_STRIDE;
    pb = C + (TR_H + c15) * TAB_STRIDE + 16 * g;
    kd = C + KC_RD + 2 * 536 - 16 * lane;
    kd2 = C + KC_RD + ((c15 - 1) & 3) * RDS + 2 * (528 - ((c15 - 1) & ~3) + 8 * g) - 64 * 16;
    ks0 = lane == 31 ? 0x03020302u : 0x03020100u;
    ks1 = 0;
  } else if (sub == 1) {
    const int lag = (c15 >> 2) + 1 - (c15 & 3);
    pa = C + (lag >= 0 ? lag * TAB_STRIDE : SM_US) + 16 * g;
    pa2 = pb = pa;
    kd = C + KC_RD + g * RDS + 2 * 544;
    kd2 = kd;
    ks0 = ks1 = 0;
  } else if (sub == 2) {
    kd = C + KC_R9 + 190 * ((c15 + 1) & 1) + 2 * (15 - c15 + 4 * g);
    pa = pa2 = pb = kd2 = kd;
    ks0 = ks1 = 0;
  } else {
    pa = C + (TR_H + 15 - c15) * TAB_STRIDE + 16 * g;
    pa2 = C + KC_R5 + 2 * (158 - 64 - 2 * c15 + 4 * g) - 128;
    pb = C + (TR_M + c15) * TAB_STRIDE + 16 * g;
    kd = C + (c15 < 8 ? ((c15 & 1) ? KC_R5 : KC_R3) + 2 * (80 - 8 * (c15 & 6) + 4 * g) : KC_R9 + 2 * (120 - 8 * c15 + 2 * g));
    kd2 = kd + (c15 < 8 ? 4 : KC_R7 - KC_R9);
    ks0 = c15 < 8 ? 0x03020100u : 0x05040100u;
    ks1 = c15 < 8 ? 0x07060504u : 0x07060302u;
  }
  asm volatile("" : "+v"(bA), "+v"(bB), "+v"(pa), "+v"(pa2), "+v"(pb), "+v"(kd), "+v"(kd2), "+v"(ks0), "+v"(ks1));
  SCHED_FENCE();
  const unsigned tag = __builtin_amdgcn_readfirstlane(tagv) + 1u;
  float f_lar = bflo(r_lar), f_aim = bflo(r_aim), f_cr = bflo(r_cr), f_ci = bflo(r_ci), f_ldt = bflo(r_ldt), f_dsk = bflo(r_dsk);
  float f_cm = fmaxf(fmaxf(fabsf(f_cr), fabsf(f_ci)), fmaxf(fabsf(bflo(r_cr2)), fabsf(bflo(r_ci2))));
  asm volatile("" : "+v"(f_lar), "+v"(f_aim), "+v"(f_cr), "+v"(f_ci), "+v"(f_cm), "+v"(f_ldt), "+v"(f_dsk), "+v"(cpf));
  if (wave < 4) {
#pragma unroll
    for (int i = 0; i < 4; i += 2) {
      unsigned lo[3], hi[3];
#pragma unroll
      for (int k = 0; k < 3; k++) {
        const unsigned p = cp[i][k], q = cp[i + 1][k];
        auto r = __builtin_amdgcn_permlane32_swap(p, q, false, false);
        lo[k] = __builtin_amdgcn_perm(r[1], r[0], 0x05040100u);
        hi[k] = __builtin_amdgcn_perm(r[1], r[0], 0x07060302u);
      }
      stg16(rs, cpo + i * (8 * 1024 * 16), u32x4{lo[0], hi[0], lo[1], tag});
      stg16(rs, cpo + i * (8 * 1024 * 16) + 16384, u32x4{hi[1], lo[2], hi[2], tag});
    }
    if (!is_ln && wave == 3 && lane < 8) __builtin_amdgcn_raw_buffer_store_b32(tag, rs, WS_FL + lane * 1024 + b * 4, 0, SC1);
    if (PFA && wave < 3) prefetch(0);
  } else if (st_wave) {
    unsigned one2 = 0x3f803f80u;
    asm volatile("" : "+s"(one2));
    float s0 = 0.f, s1 = 0.f, q0 = 0.f, q1 = 0.f;
#pragma unroll
    for (int k = 0; k < 9; k++) {
      const unsigned a = xw[k][0], c = xw[k][1];
      s0 = dot2(a, one2, s0);
      q0 = dot2(a, a, q0);
      s1 = dot2(c, one2, s1);
      q1 = dot2(c, c, q1);
    }
    const float mean = red16_sum(s0 + s1) * (1.0f / DM), mbf = rbf(mean);
    float q = red16_sum(q0 + q1), dm = mean;
    if (__builtin_expect(__any(!(q * (1.0f / DM) >= mean * mean * 1.002f)), 0)) {
      f32x2 q2 = {0.f, 0.f};
#pragma unroll
      for (int k = 0; k < 18; k++) {
        const f32x2 dd = unpk(xw[k >> 1][k & 1]) - mbf;
        q2 = __builtin_elementwise_fma(dd, dd, q2);
      }
      q = red16_sum(hadd(q2));
      dm = mean - mbf;
    }
    const float ri = ln_rinv(q, dm);
    auto r2 = __builtin_amdgcn_permlane16_swap(__float_as_uint(ri), __float_as_uint(ri), false, false);
    auto m2 = __builtin_amdgcn_permlane16_swap(__float_as_uint(mbf), __float_as_uint(mbf), false, false);
    const unsigned mp = __builtin_amdgcn_perm(m2[1], m2[0], 0x07060302u);
    if (hl == 0) {
      stg16(rs, WS_ST + (8 * b + (tid >> 5) - 8) * 16, u32x4{r2[0], r2[1], mp, tag});
      *(LAS unsigned*)(L + SM_MX + ((tid >> 5) - 8) * 4) = mp;
    }
    if (wave == 7 && lane < 8) __builtin_amdgcn_raw_buffer_store_b32(tag, rs, WS_FL + lane * 1024 + b * 4, 0, SC1);
    __builtin_amdgcn_s_setprio(0);
  }
  SCHED_FENCE();
  TWA(2);
  {
    const float dt = rbf(exp_acc(f_ldt));
    const float are = -rbf(exp_acc(f_lar)) * dt;
    const float aie = f_aim * dt;
    const float chh = are * 1.44269502f;
    const float cl = fmaf(are, 1.92596299e-8f, fmaf(are, 1.44269502f, -chh));
    const float wh = aie * 0.159154936f;
    const float wl = fmaf(aie, 7.13048051e-9f, fmaf(aie, 0.159154936f, -wh));
    const unsigned cmax = wave_max_u32(__float_as_uint(f_cm));
    int se = 385 - (int)(cmax >> 23) - (int)(__float_as_uint(dt) >> 23);
    se = min(max(se, 2), 252);
    const float scale2 = __uint_as_float((unsigned)(se + 1) << 23);
    const cf cc = {f_cr * dt * scale2, -f_ci * dt * scale2};
    volatile LAS unsigned* tab = (volatile LAS unsigned*)C + n;
    constexpr int TS4 = TAB_STRIDE / 4;
    if (sub < 2) {
      const cstep e1 = mkstep(cexp_p2(1.f, chh, cl, -wh, -wl)), e128 = mkstep(cexp_p2(128.f, chh, cl, -wh, -wl));
      const cf p1 = cmul(cc, e1);
      const cstep e2 = mkstep(cmul(e1.s, e1));
      cf a0 = cmul(p1, e1);
      tab[0] = pkc(cc);
      const cstep e4 = mkstep(cmul(e2.s, e2));
      cf a1 = cmul(a0, e128);
      tab[TS4] = pkc(p1);
      cf b0 = cmul(a1, e128), b1 = cmul(b0, e128);
      const LAS char* tb = (const LAS char*)tab;
      tab_step<2, 10, TR_B + 4, TR_B + 6>(tb, a0, a1, b0, b1, e1, e2);
      cf m0 = cmul(b1, e128);
      tab_step<3, 11, TR_B + 5, TR_B + 7>(tb, a0, a1, b0, b1, e1, e2);
      cf m1 = cmul(m0, e4);
      tab_step<4, 12, TR_B, TR_B + 2>(tb, a0, a1, b0, b1, e1, e2);
      tab[(TR_B + 1) * TS4] = pkc(b0);
      tab[(TR_B + 3) * TS4] = pkc(b1);
      tab_step<5, 13, TR_M, TR_M + 1>(tb, a0, a1, m0, m1, e1, e128);
      tab_step<6, 14, TR_M + 2, TR_M + 3>(tb, a0, a1, m0, m1, e1, e128);
      tab_step<7, 15, TR_M + 4, TR_M + 5>(tb, a0, a1, m0, m1, e1, e128);
      const cf a2 = cmul(a0, e1), a3 = cmul(a1, e1);
      tab[8 * TS4] = pkc(a0);
      tab[16 * TS4] = pkc(a1);
      tab[(TR_M + 6) * TS4] = pkc(m0);
      tab[(TR_M + 7) * TS4] = pkc(m1);
      tab[9 * TS4] = pkc(a2);
      tab[17 * TS4] = pkc(a3);
    } else {
      const cstep e8 = mkstep(cexp_p2(8.f, chh, cl, wh, wl)), e128 = mkstep(cexp_p2(128.f, chh, cl, -wh, -wl));
      cf f0 = cmul(cmul(cc, mkstep(cexp_p2(1024.f, chh, cl, -wh, -wl))), mkstep(cf{e8.s[0], e8.r[0]}));
      const cstep e16 = mkstep(cmul(e8.s, e8)), e256 = mkstep(cmul(e128.s, e128));
      cf h1 = e8.s, h2 = e16.s, f1 = cmul(f0, e128);
      const LAS char* tb = (const LAS char*)tab;
      tab[TR_H * TS4] = pkh(1.f, 0.f);
      tab_step<TR_H + 1, TR_H + 2, TR_F, TR_F + 1>(tb, h1, h2, f0, f1, e16, e256);
      const cstep e32 = mkstep(h2);
      tab_step<TR_H + 3, TR_H + 4, TR_F + 2, TR_F + 3>(tb, h1, h2, f0, f1, e16, e256);
      cf h3 = cmul(h1, e16), h4 = cmul(h2, e16);
      tab_step<TR_H + 5, TR_H + 6, TR_F + 4, TR_F + 5>(tb, h1, h2, f0, f1, e32, e256);
      tab[(TR_F + 6) * TS4] = pkc(f0);
      tab[(TR_F + 7) * TS4] = pkc(f1);
      tab_step<TR_H + 9, TR_H + 10, TR_H + 7, TR_H + 8>(tb, h1, h2, h3, h4, e32, e32);
      const cf h5 = cmul(h3, e32);
      tab[(TR_H + 13) * TS4] = pkc(h1);
      tab[(TR_H + 14) * TS4] = pkc(h2);
      tab[(TR_H + 11) * TS4] = pkc(h3);
      tab[(TR_H + 12) * TS4] = pkc(h4);
      tab[(TR_H + 15) * TS4] = pkc(h5);
      if (sub == 2) *(volatile LAS unsigned*)(L + SM_ISC + 4 * ch) = (unsigned)(254 - se) << 23;
    }
  }
  if (is_ln && tid >= 184 && wave < 4) dma16((const char*)wr + (tid - 184) * 16, (unsigned)(size_t)L + SM_WR + (wave - 3) * 1024 + 128);
  if (XFL && wave == 3) {
    _Pragma("unroll 1") for (int spin = 0; spin < SPIN_CAP; spin++) {
      const u32x4 p = ldg16(rs, WS_FL + (b & 7) * 1024 + min(lane, 47) * 16);
      if (__all(p[0] == tag && p[1] == tag && p[2] == tag && p[3] == tag)) break;
      __builtin_amdgcn_s_sleep(2);
    }
  }
  float dsk[3];
#pragma unroll
  for (int c = 0; c < 3; c++) dsk[c] = __int_as_float(__builtin_amdgcn_readlane(__float_as_int(f_dsk), c));
  const int vt = tid - 256;
  const int uoff = WS_UP + b * 16384 + vt * 16, soff = WS_ST + vt * 16;
  u32x4 v0, v1, st0, st1;
  auto ldx = [&]() {
    v0 = ldg16(rs, uoff);
    v1 = ldg16(rs, uoff, 8192);
    st0 = ldg16(rs, soff);
    st1 = ldg16(rs, soff, 8192);
    SCHED_FENCE();
  };
  TWA(3);
  asm volatile("s_waitcnt lgkmcnt(0)" ::: "memory");
  __syncthreads();
  const LAS char* uf16 = C + SM_US + 2 * (UPAD + (sub == 3 ? 8 : 16) * c15 + 8 * g);
  LAS char* yb = C + g * 320 + c15 * 16;
  const f32x4 zero4 = {0.f, 0.f, 0.f, 0.f};
  f32x2 sb[2][3];
  f32x2 isc[3];
  const LAS char *yp, *yp2;
  int zoff, zoff2;
  unsigned t0;
  if (sub == 0) {
    half8 td[17];
    {
      half8 fa[8], fc[8], fb[8];
#pragma unroll
      for (int ks = 0; ks < 8; ks++) { fa[ks] = ld_frag(pa + 64 * ks); fb[ks] = ld_frag(pb + 64 * ks); fc[ks] = ld_frag(pa2 + 64 * ks); }
      SCHED_FENCE();
      f32x4 f = zero4, e = zero4;
#pragma unroll
      for (int ks = 0; ks < 8; ks++) { f = MFMA16(fa[ks], fb[ks], f); e = MFMA16(fc[ks], fb[ks], e); }
      unsigned d0 = pkh(f[0], f[1]);
      const unsigned d1 = pkh(f[2], f[3]), d2 = pkh(e[0], e[1]), d3 = pkh(e[2], e[3]);
      d0 = __builtin_amdgcn_perm(d0, d0, ks0);
      const unsigned n3 = (unsigned)__builtin_amdgcn_update_dpp((int)(d0 & 0xffff0000u), (int)d3, 0x130, 0xf, 0xf, false);
      const unsigned n2 = (unsigned)__builtin_amdgcn_update_dpp(0, (int)d2, 0x130, 0xf, 0xf, true);
      *(volatile LAS u32x4*)kd = u32x4{d0, d1, d2, d3};
      const unsigned a0 = __builtin_amdgcn_alignbit(d0, n3, 16), a1 = __builtin_amdgcn_alignbit(d1, d0, 16), a2 = __builtin_amdgcn_alignbit(d2, d1, 16);
      *(volatile LAS u32x4*)(kd + RDS) = u32x4{a0, a1, a2, __builtin_amdgcn_alignbit(d3, d2, 16)};
      *(volatile LAS u32x4*)(kd + 2 * RDS) = u32x4{n3, d0, d1, d2};
      *(volatile LAS u32x4*)(kd + 3 * RDS) = u32x4{__builtin_amdgcn_alignbit(n3, n2, 16), a0, a1, a2};
      if (lane == 63) *(volatile LAS u16*)(kd - 2) = (u16)d0;
      TWA(4);
#pragma unroll
      for (int i = 7; i >= 1; i--) { td[i] = ld_td(kd2, i); td[i + 8] = ld_td(kd2, i + 8); }
      td[8] = ld_td(kd2, 8);
      td[16] = ld_td(kd2, 16);
      uf16 -= 32 * 33;
    }
    TWA(5);
    asm volatile("" : "+v"(uf16), "+v"(yb));
    __builtin_amdgcn_s_waitcnt(0x0F70);
    __syncthreads();
    t0 = (unsigned)__builtin_amdgcn_s_memrealtime();
    TSX(0, 5);
    TWA(6);
    conv_dense(td, kd2, bA, C + KC_R5 + 64, uf16, yb + SM_YD);
  } else {
  if (wave >= 4) ldx();
  half8 td[8];
  {
    auto w32 = [](LAS char* p, unsigned v) { *(volatile LAS unsigned*)p = v; };
    if (sub == 1) {
      half8 fa[8];
#pragma unroll
      for (int ks = 0; ks < 8; ks++) fa[ks] = ld_frag(pa + 64 * ks);
      SCHED_FENCE();
      const half8 re = __builtin_bit_cast(half8, u32x4{0x3c00u, 0x3c00u, 0x3c00u, 0x3c00u});
      f32x4 f = zero4;
#pragma unroll
      for (int ks = 0; ks < 8; ks++) f = MFMA16(fa[ks], re, f);
      const unsigned d1 = pkh(f[2], f[3]);
      if (c15 == 0) *(LAS u32x2*)kd = u32x2{pkh(f[0], f[1]), d1};
      if (lane == 32) *(LAS u16*)(kd + RDS + 8) = (u16)(d1 >> 16);
      TWA(4);
    } else if (sub == 3) {
      half8 fa[8], fb[8];
#pragma unroll
      for (int ks = 0; ks < 8; ks++) { fa[ks] = ld_frag(pa + 64 * ks); fb[ks] = ld_frag(pb + 64 * ks); }
      SCHED_FENCE();
      f32x4 f = zero4;
#pragma unroll
      for (int ks = 0; ks < 8; ks++) f = MFMA16(fa[ks], fb[ks], f);
      const unsigned d0 = pkh(f[0], f[1]), d1 = pkh(f[2], f[3]);
      const unsigned w0 = __builtin_amdgcn_perm(d1, d0, ks0), w1 = __builtin_amdgcn_perm(d1, d0, ks1);
      w32(kd, w0);
      w32(kd2, w1);
      TWA(4);
      asm volatile("" ::: "memory");
      const LAS unsigned* t5 = (const LAS unsigned*)pa2;
#pragma unroll
      for (int e = 0; e < 3; e++) {
        td[e] = ld_toep<8>(t5 + 16 * (2 - e));
        td[3 + e] = ld_toep<8>(t5 + (KC_R3 - KC_R5) / 4 + 16 * (2 - e));
      }
      if (c15 >= 8) {
        *(LAS u16*)(kd + 190) = (u16)w0;
        *(LAS u16*)(kd + 192) = (u16)(w0 >> 16);
        *(LAS u16*)(kd + (KC_R7 - KC_R9) + 190) = (u16)w1;
        *(LAS u16*)(kd + (KC_R7 - KC_R9) + 192) = (u16)(w1 >> 16);
      }
    } else {
      TWA(4);
    }
  }
  SCHED_FENCE();
  asm volatile("" : "+v"(uf16), "+v"(yb));
  if (wave >= 4) {
    const u32x4 iscv = *(const LAS u32x4*)(L + SM_ISC);
    yp = L + ((wave >> 1) - 2) * YT + ((vt >> 1) & 3) * 320 + ((vt >> 3) & 15) * 16 + (vt & 1) * 8;
    yp2 = yp + 2 * CH_BYTES;
    zoff = WS_ZP + ((vt >> 3) * NCONV + b) * 128 + (vt & 7) * 16;
    zoff2 = zoff + 64 * NCONV * 128;
    asm volatile("" : "+v"(yp), "+v"(yp2), "+v"(zoff), "+v"(zoff2));
    _Pragma("unroll 1") for (int spin = 0; spin < SPIN_CAP; spin++) {
      const bool ok = v0[3] == tag && v1[3] == tag && st0[3] == tag && st1[3] == tag;
      if (__builtin_expect(__all(ok), 1)) break;
      __builtin_amdgcn_s_sleep(4);
      if (!ok) ldx();
    }
    TWA(5);
    LAS char* dst = L + SM_US + 2 * UPAD + 4 * vt;
    auto norm = [&](const u32x4& a, const u32x4& st, f32x2 (&t)[3], LAS char* d) {
      const f32x2 ri = {__uint_as_float(st[0]), __uint_as_float(st[1])};
      const f32x2 cc = -unpk(st[2]) * ri;
#pragma unroll
      for (int c = 0; c < 3; c++) {
        const f32x2 xf = unpk(a[c]);
        const f32x2 uf = __builtin_elementwise_fma(xf, ri, cc);
        *(LAS unsigned*)(d + c * CH_BYTES) = pkc(uf);
        t[c] = __builtin_elementwise_fma(uf, f32x2{dsk[c], dsk[c]}, xf);
        asm volatile("" : "+v"(t[c]));
      }
    };
    norm(v0, st0, sb[0], dst);
    norm(v1, st1, sb[1], dst + 2048);
#pragma unroll
    for (int c = 0; c < 3; c++) {
      const float t = __int_as_float(__builtin_amdgcn_readfirstlane(iscv[c]));
      isc[c] = f32x2{t, t};
      asm volatile("" : "+s"(isc[c]));
    }
  } else {
    TWA(5);
  }
  if (sub == 1) __builtin_amdgcn_s_setprio(3);
  __builtin_amdgcn_s_waitcnt(0x0F70);
  __syncthreads();
  t0 = (unsigned)__builtin_amdgcn_s_memrealtime();
  TWA(6);
  {
    if (sub == 1) {
      if (wave == 4 && lane < 8) __builtin_amdgcn_raw_buffer_store_b64(u32x2{t0, tag}, rs, WS_CK + lane * 2048 + b * 8, 0, SC1);
#pragma unroll
      for (int i = 4 - PFM; i < 4; i++) prefetch(i);
      if (is_ln) {
        const u32x4 ones = {~0u, ~0u, ~0u, ~0u};
#pragma unroll
        for (int i = 0; i < 3; i++) stg16(rs, WS_PART + b * 9216 + vt * 16 + i * 3072, ones);
        stg16(rs, WS_XS + b * (2 * XSS) + vt * 16, ones);
        if (vt < 96) stg16(rs, WS_XS + b * (2 * XSS) + 3072 + vt * 16, ones);
        if (b < 6) stg16(rs, WS_XS + 2 * NLN * XSS + b * 3072 + vt * 16, ones);
      }
      __builtin_amdgcn_s_setprio(0);
    } else if (sub == 3) {
      f32x4 a2[6], w[4];
#pragma unroll
      for (int i = 0; i < 6; i++) a2[i] = zero4;
      half8 uw[12];
#pragma unroll
      for (int jj = 0; jj < 6; jj++) uw[jj] = ld_frag(uf16 - 48 + 256 * jj);
      SCHED_FENCE();
      w[0] = MFMA16(uw[0], bA, zero4); w[1] = MFMA16(uw[1], bA, zero4);
      w[2] = MFMA16(uw[0], bB, zero4); w[3] = MFMA16(uw[1], bB, zero4);
      half8 X5 = pack_win(w[0], w[1]), X3 = pack_win(w[2], w[3]);
      SCHED_FENCE();
#pragma unroll
      for (int jj = 0; jj < 6; jj++) {
        if (jj < 5) {
          w[0] = MFMA16(uw[2 * jj + 2], bA, zero4);
          if (jj < 3) uw[2 * jj + 6] = ld_frag(uf16 - 48 + 256 * (2 * jj + 6));
          SCHED_FENCE();
          w[1] = MFMA16(uw[2 * jj + 3], bA, zero4);
          if (jj < 3) uw[2 * jj + 7] = ld_frag(uf16 - 48 + 256 * (2 * jj + 7));
          SCHED_FENCE();
          w[2] = MFMA16(uw[2 * jj + 2], bB, zero4);
        }
        if (jj > 0) *(LAS f32x4*)(yb + SM_Y2 + (1 + jj) * YT) = a2[jj - 1];
        SCHED_FENCE();
        if (jj < 5) w[3] = MFMA16(uw[2 * jj + 3], bB, zero4);
        SCHED_FENCE();
        u32x4 n5, n3;
#pragma unroll
        for (int q = 0; q < 6; q++) {
          const int e = q >> 1;
          if (jj + e < 6) a2[jj + e] = MFMA16((q & 1) ? X3 : X5, td[3 * (q & 1) + e], a2[jj + e]);
          if (jj < 5 && q < 4) {
            u32x4& nn = q < 2 ? n5 : n3;
            nn[2 * (q & 1)] = pkh(w[q][0], w[q][1]);
            nn[2 * (q & 1) + 1] = pkh(w[q][2], w[q][3]);
          }
          SCHED_FENCE();
        }
        if (jj < 5) {
          X5 = __builtin_bit_cast(half8, n5);
          X3 = __builtin_bit_cast(half8, n3);
        }
      }
      *(LAS f32x4*)(yb + SM_Y2 + 7 * YT) = a2[5];
    } else {
      half8 uw[8];
#pragma unroll
      for (int i = 0; i < 4; i++) {
        uw[2 * i] = ld_frag(uf16 + 512 * i - 48);
        uw[2 * i + 1] = ld_frag(uf16 + 512 * i - 32);
      }
      SCHED_FENCE();
      f32x4 w[8], a3[4];
#pragma unroll
      for (int e = 0; e < 4; e++) {
        const LAS unsigned* t9 = opaque_lds(kd);
        td[e] = ld_toep<8>(t9 + 8 * (3 - e));
        td[4 + e] = ld_toep<8>(t9 + (KC_R7 - KC_R9) / 4 + 8 * (3 - e));
      }
#pragma unroll
      for (int i = 0; i < 4; i++) {
        w[2 * i] = MFMA16(uw[2 * i], bA, zero4);
        w[2 * i + 1] = MFMA16(uw[2 * i + 1], bB, zero4);
      }
      const half8 X90 = pack_win(w[0], w[2]), X70 = pack_win(w[1], w[3]), X91 = pack_win(w[4], w[6]), X71 = pack_win(w[5], w[7]);
#pragma unroll
      for (int t = 0; t < 4; t++) {
        a3[t] = MFMA16(X90, td[t], zero4);
        a3[t] = MFMA16(X70, td[4 + t], a3[t]);
        if (t >= 2) {
          a3[t] = MFMA16(X91, td[t - 2], a3[t]);
          a3[t] = MFMA16(X71, td[4 + t - 2], a3[t]);
        }
      }
#pragma unroll
      for (int t = 0; t < 4; t++) *(LAS f32x4*)(yb + SM_Y3 + (4 + t) * YT) = a3[t];
    }
  }
  }
  TWA(7);
  __syncthreads();
  const unsigned long long t_end = __builtin_amdgcn_s_memrealtime();
  TS(0, 6);
  TWB(2);
  if (wave >= 4) {
    auto ld = [&](int c, int off) { return *(const volatile LAS f32x2*)((c < 2 ? yp + c * CH_BYTES : yp2) + off); };
    auto epi = [&](auto mid) {
      f32x2 ya[3], yc[3], yb0[3], yb1[3], yb2[3];
#pragma unroll
      for (int c = 0; c < 3; c++) {
        ya[c] = ld(c, SM_YD);
        if (mid) yc[c] = ld(c, SM_Y2);
      }
#pragma unroll
      for (int c = 0; c < 3; c++) {
        yb0[c] = ld(c, 4 * YT + SM_YD);
        yb1[c] = ld(c, 4 * YT + SM_Y2);
        yb2[c] = ld(c, 4 * YT + SM_Y3);
      }
      SCHED_FENCE();
      u32x4 o, o2;
#pragma unroll
      for (int c = 0; c < 3; c++) {
        const f32x2 z = __builtin_elementwise_fma(mid ? ya[c] + yc[c] : ya[c], isc[c], sb[0][c]);
        o[c] = pkbf(z[0], z[1]);
      }
      o[3] = tag;
      stg16(rs, zoff, o);
#pragma unroll
      for (int c = 0; c < 3; c++) {
        const f32x2 z = __builtin_elementwise_fma((yb0[c] + yb1[c]) + yb2[c], isc[c], sb[1][c]);
        o2[c] = pkbf(z[0], z[1]);
      }
      o2[3] = tag;
      stg16(rs, zoff2, o2);
    };
    if (wave >= 8) epi(std::true_type{});
    else epi(std::false_type{});
  }
  TS(0, 7);
  TWB(3);
  TWFLUSH();
  unsigned long long t_all = t_end;
  auto get_lag = [&]() {
    u32x4 p, q;
    _Pragma("unroll 1") for (int spin = 0; spin < SPIN_CAP; spin++) {
      p = ldg16(rs, WS_CK + (b & 7) * 2048 + lane * 16);
      q = ldg16(rs, WS_CK + (b & 7) * 2048 + 1024 + hl * 16);
      if (__all(p[1] == tag && p[3] == tag && q[1] == tag && q[3] == tag)) break;
      __builtin_amdgcn_s_sleep(4);
    }
    const int lag = max(max(max((int)(p[0] - t0), (int)(p[2] - t0)), max((int)(q[0] - t0), (int)(q[2] - t0))), 0);
    t_all += min(wave_max_u32((unsigned)lag), 1000u);
  };
  auto wait_until = [&](int t) {
    while ((long long)(__builtin_amdgcn_s_memrealtime() - t_all) < t) __builtin_amdgcn_s_sleep(1);
  };
  LAS unsigned* const selp = (LAS unsigned*)(L + SM_ROW + 256);
  unsigned long long t_key = 0;
  auto select_rows = [&]() {
    const SelMasks sm(lane);
    get_lag();
    wait_until(DLY_L);
    selp[key_slot(lane)] = select8(rs, tag, lane, sm, ws);
    t_key = __builtin_amdgcn_s_memrealtime();
  };
  if (is_ln) {
    f32x2 wf[6], hv[6];
    const unsigned mxp = *(const LAS unsigned*)(L + SM_MX + (tid & 7) * 4);
#pragma unroll
    for (int i = 0; i < 6; i++) {
      const float t = bflo(*(const LAS u16*)(L + SM_WR + (tid >> 3) * 12 + i * 2));
      wf[i] = f32x2{t, t};
    }
    float wsum = 0.f, mxr = bflo(*(const LAS u16*)(L + SM_MX + (lane >> 2) * 2));
    unsigned one2 = 0x3f803f80u, sel0 = 0x00003f80u, sel1 = 0x3f800000u;
    asm volatile("" : "+s"(one2), "+s"(sel0), "+s"(sel1));
    if (wave == 0) {
#pragma unroll
      for (int k = 0; k < 9; k++) wsum = dot2(*(const LAS unsigned*)(L + SM_WR + (hl + 32 * k) * 4), one2, wsum);
      wsum = red32_sum(wsum);
      __builtin_amdgcn_s_setprio(2);
    }
#pragma unroll
    for (int i = 0; i < 6; i++) {
      hv[i] = -unpk(mxp);
      asm volatile("" : "+v"(hv[i]), "+v"(wf[i]));
    }
    LAS char* redw = L + SM_RED + wave * 256 + (wave >= 6 ? 128 : 0) + (lane & 7) * 32 + (lane >> 5) * 16 + ((lane >> 4) & 1) * 8;
    const LAS char* redr = L + SM_RED + ((lane >> 1) & 1) * (6 * 256 + 128) + (lane >> 2) * 16 + (lane & 1) * 8;
    unsigned kidx = (unsigned)(SEQ - 1 - 16 * b - (lane >> 2)) << 2;
    int kro = WS_KR + (lane & 7) * 2048 + b * 16, xso = WS_XS + b * (2 * XSS) + (tid >> 3) * 12;
    asm volatile("" : "+v"(wsum), "+v"(mxr), "+v"(redw), "+v"(redr), "+v"(kidx), "+v"(kro), "+v"(xso));
    if (wave == 1) {
      get_lag();
      wait_until(DLY_H);
    }
    __syncthreads();
    TWB(4);
    {
      const int off = WS_ZP + b * (NCONV * 128) + (tid >> 3) * 256 + (tid & 7) * 16;
      v0 = ldg16(rs, off);
      v1 = ldg16(rs, off + 128);
      _Pragma("unroll 1") for (int spin = 0; spin < SPIN_CAP; spin++) {
        if (__builtin_expect(__all(v0[3] == tag && v1[3] == tag), 1)) break;
        __builtin_amdgcn_s_sleep(4);
        if (v0[3] != tag) v0 = ldg16(rs, off);
        if (v1[3] != tag) v1 = ldg16(rs, off + 128);
      }
    }
    TS(0, 8);
    TWB(5);
    {
      f32x2 ss, qq, pp;
#pragma unroll
      for (int i = 0; i < 6; i++) {
        const unsigned t = i < 3 ? v0[i] : v1[i - 3];
        hv[i] = f32x2{dot2(t, sel0, hv[i][0]), dot2(t, sel1, hv[i][1])};
        ss = i ? ss + hv[i] : hv[i];
        qq = i ? __builtin_elementwise_fma(hv[i], hv[i], qq) : hv[i] * hv[i];
        pp = i ? __builtin_elementwise_fma(hv[i], wf[i], pp) : hv[i] * wf[i];
      }
      float s0 = ss[0], s1 = ss[1], q0 = qq[0], q1 = qq[1], p0 = pp[0], p1 = pp[1];
      asm volatile(
          "s_nop 1\n\tv_permlane32_swap_b32 %0, %1\n\tv_permlane32_swap_b32 %2, %3\n\tv_permlane32_swap_b32 %4, %5\n\t"
          "v_add_f32 %0, %0, %1\n\tv_add_f32 %2, %2, %3\n\tv_add_f32 %4, %4, %5\n\ts_nop 0\n\t"
          "v_permlane16_swap_b32 %0, %2\n\tv_add_f32_dpp %1, %4, %4 row_ror:8 row_mask:0xf bank_mask:0xf\n\t"
          "v_add_f32 %2, %0, %2\n\ts_nop 1\n\tv_add_f32_dpp %0, %2, %2 row_ror:8 row_mask:0xf bank_mask:0xf"
          : "+v"(s0), "+v"(s1), "+v"(q0), "+v"(q1), "+v"(p0), "+v"(p1));
      *(LAS f32x2*)redw = f32x2{s0, s1};
    }
    TWB(6);
    if (wave == 11 && lane == 0 && !(b & 1)) __builtin_amdgcn_raw_buffer_store_b32(tag, rs, WS_PF + (b >> 1) * 128, 0, SC1);
    __syncthreads();
    if (wave == 0) {
      const volatile LAS f32x2* rp = (const volatile LAS f32x2*)redr;
      const f32x2 t0 = rp[0], t1 = rp[32], t2 = rp[64], t3 = rp[96], t4 = rp[128], t5 = rp[160];
      const f32x2 t = ((t0 + t1) + (t2 + t3)) + (t4 + t5);
      float a = t[0], p = t[1];
      asm volatile("" : "+v"(a), "+v"(p));
      a += dppf<0x4E>(a);
      p += dppf<0x4E>(p);
      p += dppf<0xB1>(p);
      const float mv = dppf<0x00>(a) * (1.0f / DM);
      const float rinv = ln_rinv(dppf<0x55>(a), mv);
      const float dl = rbf(mxr + mv) - mxr;
      const unsigned rb = pkbf(0.f, fmaf(-dl, wsum, p) * rinv);
      const unsigned key = ((rb ^ (unsigned)(((int)rb >> 31) | (int)0x80000000)) & 0xffff0000u) | kidx;
      unsigned k1 = max(key, dppu<0x141>(key)), k2 = min(key, dppu<0x141>(key)), k3;
      {
        const unsigned lo = min(k1, dppu<0x140>(k1)), hi = max(k2, dppu<0x140>(k2));
        k1 = max(k1, dppu<0x140>(k1));
        k2 = max(lo, hi);
        k3 = min(lo, hi);
      }
      {
        const unsigned lo = min(k1, dppu<0x142>(k1)), hi = max(k2, dppu<0x142>(k2)), w = max(k3, dppu<0x142>(k3));
        k1 = max(k1, dppu<0x142>(k1));
        k2 = max(lo, hi);
        k3 = max(min(lo, hi), w);
      }
      {
        const unsigned lo = min(k1, dppu<0x143>(k1)), hi = max(k2, dppu<0x143>(k2)), w = max(k3, dppu<0x143>(k3));
        k1 = max(k1, dppu<0x143>(k1));
        k2 = max(lo, hi);
        k3 = max(min(lo, hi), w);
      }
      if (lane >= 56) stg16(rs, kro, u32x4{k1, k2 | 1u, k3, tag});
      SCHED_FENCE();
      const unsigned second = (unsigned)__builtin_amdgcn_readlane((int)k2, 63);
      if ((lane & 3) == 0) {
        LAS float* ri = (LAS float*)(L + SM_ROW + (lane >> 3) * 16 + (lane & 4));
        ri[0] = -dl * rinv;
        ri[2] = rinv;
        const unsigned fk = key | (key > second ? 0u : (key == second ? 1u : 2u));
        *(LAS unsigned*)(L + SM_ROW + 128 + lane) = fk;
        __builtin_amdgcn_raw_buffer_store_b64(u32x2{fk, tag}, rs, WS_KF + b * 128 + lane * 2, 0, SC1);
      }
      __builtin_amdgcn_s_setprio(0);
    }
    TS(0, 9);
    TWB(7);
    __syncthreads();
    {
      const f32x4 ri = *(const LAS f32x4*)(L + SM_ROW + (tid & 7) * 16);
      const u32x2 kk = *(const LAS u32x2*)(L + SM_ROW + 128 + (tid & 7) * 8);
      const f32x2 c2 = {ri[0], ri[1]}, r2 = {ri[2], ri[3]};
      unsigned wp[6];
#pragma unroll
      for (int i = 0; i < 6; i++) {
        const f32x2 w2 = __builtin_elementwise_fma(hv[i], r2, c2);
        wp[i] = pkbf(w2[0], w2[1]);
      }
#pragma unroll
      for (int i = 0; i < 2; i++)
        if (!(kk[i] & 2)) {
          u32x3 o;
#pragma unroll
          for (int k = 0; k < 3; k++) o[k] = min(__builtin_amdgcn_perm(wp[2 * k + 1], wp[2 * k], i ? 0x07060302u : 0x05040100u), 0xfffffffeu);
          __builtin_amdgcn_raw_buffer_store_b96(o, rs, xso + (int)(kk[i] & 1) * XSS, 0, SC1);
        }
      SCHED_FENCE();
      LAS char* dst = L + SM_T2 + (tid & 7) * T2S + (tid >> 3) * 48;
#pragma unroll
      for (int i = 0; i < 6; i += 2) *(LAS u32x4*)(dst + 8 * i) = u32x4{i < 3 ? v0[i] : v1[i - 3], wp[i], i < 2 ? v0[i + 1] : v1[i - 2], wp[i + 1]};
    }
    __syncthreads();
    const int pr = tid / 72, ck = tid - pr * 72;
    u32x4 hq[2], wq[2];
    u32x2 key = {2u, 2u};
    auto put_row = [&](int i, int slot) {
      const int o = key_addr(key[i], slot) + ck * 16;
      const u32x4 cap = {0xfffffffeu, 0xfffffffeu, 0xfffffffeu, 0xfffffffeu};
      if (key[i] & 2) stg16(rs, o, __builtin_elementwise_min(wq[i], cap));
      stg16(rs, o + DM * 2, __builtin_elementwise_min(hq[i], cap));
    };
    if (wave < 9) {
      key = *(const LAS u32x2*)(L + SM_ROW + 128 + pr * 8);
#pragma unroll
      for (int k = 0; k < 4; k++) {
        const u32x4 t = *(const LAS u32x4*)(L + SM_T2 + pr * T2S + ck * 64 + k * 16);
        hq[0][k] = __builtin_amdgcn_perm(t[2], t[0], 0x05040100u);
        hq[1][k] = __builtin_amdgcn_perm(t[2], t[0], 0x07060302u);
        wq[0][k] = __builtin_amdgcn_perm(t[3], t[1], 0x05040100u);
        wq[1][k] = __builtin_amdgcn_perm(t[3], t[1], 0x07060302u);
      }
#pragma unroll
      for (int i = 0; i < 2; i++)
        if (!(key[i] & 2)) put_row(i, 0);
    } else if (wave == 9) {
      select_rows();
    }
    __syncthreads();
    TS(0, 10);
    if (wave < 9) {
#pragma unroll
      for (int i = 0; i < 2; i++) {
        int slot = 8;
#pragma unroll
        for (int r = 0; r < 8; r++) slot = selp[r] == key[i] ? r : slot;
        if (slot < 8) {
          if (key[i] & 2) put_row(i, slot);
        } else {
          u32x4 o;
#pragma unroll
          for (int k = 0; k < 4; k++) {
            const f32x2 o2 = unpk(hq[i][k]) + unpk(wq[i][k]);
            o[k] = pkbf(o2[0], o2[1]);
          }
          *(u32x4*)((char*)out + (unsigned)((16 * b + 2 * pr + i) * (DM * 2) + ck * 16)) = o;
        }
      }
    }
  } else if (wave == 9) {
    select_rows();
  }
  if (b < RED0) {
    TS(0, 12);
    TC(0, 15);
    return;
  }
  {
    const int T = (b - RED0) >> 2, tp = (b - RED0) & 3;
    unsigned hv = 0u, wv = 0u, oo = 0u;
    float sg = 0.f;
    if (wave == 9) {
      const int slot = 2 * tp + ((lane >> 3) & 1);
      const unsigned key = *(volatile LAS unsigned*)(selp + slot);
      int o = key_addr(key, slot) + (8 * T + (lane & 7)) * 4;
      _Pragma("unroll 1") for (int spin = 0; spin < SPIN_CAP; spin++) {
        asm volatile("" : "+v"(o) : : "memory");
        wv = __builtin_amdgcn_raw_buffer_load_b32(rs, o, 0, SC1);
        hv = __builtin_amdgcn_raw_buffer_load_b32(rs, o + DM * 2, 0, SC1);
        if (__all(wv != ~0u && hv != ~0u)) break;
        __builtin_amdgcn_s_sleep(4);
      }
      const unsigned rb = key ^ ((int)key < 0 ? 0x80000000u : 0xffffffffu);
      sg = rbf(__builtin_amdgcn_rcpf(1.0f + __expf(-bfhi(rb))));
      oo = (unsigned)(key_row(key) * (DM * 2) + (8 * T + (lane & 7)) * 4);
      asm volatile("" : "+v"(sg), "+v"(oo));
      while (__builtin_amdgcn_s_memrealtime() - t_key < (unsigned long long)DLY_P) __builtin_amdgcn_s_sleep(1);
    }
    LAS float* red = (LAS float*)(L + SM_RED);
    LAS char* rw = (LAS char*)red + wave * 128 + (lane & 7) * 16 + (lane >> 4) * 4;
    asm volatile("" : "+v"(rw));
    __syncthreads();
    TS(0, 11);
    if (wave < 8) {
      u32x4 v;
      _Pragma("unroll 1") for (int spin = 0; spin < SPIN_CAP; spin++) {
        v = ldg16(rs, part_off);
        if (__builtin_expect(__all(v[0] != ~0u), 1)) break;
        __builtin_amdgcn_s_sleep(4);
      }
      auto r0 = __builtin_amdgcn_permlane32_swap(v[0], v[2], false, false);
      auto r1 = __builtin_amdgcn_permlane32_swap(v[1], v[3], false, false);
      const float s0 = __uint_as_float(r0[0]) + __uint_as_float(r0[1]), s1 = __uint_as_float(r1[0]) + __uint_as_float(r1[1]);
      auto r2 = __builtin_amdgcn_permlane16_swap(__float_as_uint(s0), __float_as_uint(s1), false, false);
      float a = __uint_as_float(r2[0]) + __uint_as_float(r2[1]);
      a += dppf<0x128>(a);
      *(LAS float*)rw = a;
    }
    __syncthreads();
    if (wave == 9 && lane < 16) {
      f32x2 p[8];
#pragma unroll
      for (int w = 0; w < 8; w++) p[w] = *(const LAS f32x2*)(red + w * 32 + lane * 2);
      const f32x2 y = ((p[0] + p[1]) + (p[2] + p[3])) + ((p[4] + p[5]) + (p[6] + p[7]));
      const f32x2 y2 = rbf2(rbf2(y) * sg);
      const f32x2 o2 = unpk(hv) + rbf2(unpk(wv) + y2);
      *(unsigned*)((char*)out + oo) = pkbf(o2[0], o2[1]);
      if (b == RED0 && lane == 0) __builtin_amdgcn_raw_buffer_store_b32(tag, rs, WS_EPOCH, 0, SC1);
    }
  }
  TS(0, 12);
  TC(0, 15);
}

}

extern "C" void kernel_launch(void* const* d_in, const int* in_sizes, int n_in, void* d_out, int out_size,
                              void* d_ws, size_t ws_size, hipStream_t stream) {
  k_all<<<NCONV + NFFN, NTHR, 0, stream>>>((const u16*)d_in[0], (const u16*)d_in[1], (const u16*)d_in[2], (const u16*)d_in[3],
                                           (const u16*)d_in[4], (const u16*)d_in[5], (const u16*)d_in[6], (const u16*)d_in[8],
                                           (const u16*)d_in[9], (const u16*)d_in[10], (const u16*)d_in[11], (u16*)d_out,
                                           (char*)d_ws);
}
```

```cpp
#include <hip/hip_runtime.h>
#include <stdint.h>
#include <type_traits>

#define PRA 0
#define CPF 212
#define WPAD 0
#define PFNT 0
#define WNT 0
#define WNTS ""
#define PTH 4
#define PTL 2
#define EB 0
#define LAUX 16
#define SAUX 16
#define PFA 0
#define XFL 0
#define CMAP 0
#define PRB 0
#define WORD 1
#define DR0 6
#define SENT1 0
#define S4M 0
#define S4D 50
#define S0M 0
#define S1M 19
#define S2M 0
#define S3M 12
#define STA 16
#define XM 0
#define XF 0
#define PFD 10
namespace {

constexpr int SEQ = 2048;
constexpr int DM = 576;
constexpr int NM = 128;
constexpr int FF = 4608;

constexpr int NTHR = 768;
constexpr int NFFN = 64;
constexpr int NCONV = 192;
constexpr int NLN = 128;
constexpr int RED0 = 48;

typedef unsigned short u16;
#define LAS __attribute__((address_space(3)))
#define GAS __attribute__((address_space(1)))
#define CAS __attribute__((address_space(4)))
using half8 = __attribute__((ext_vector_type(8))) _Float16;
using half2v = __attribute__((ext_vector_type(2))) _Float16;
using bf2v = __attribute__((ext_vector_type(2))) __bf16;
using bf8v = __attribute__((ext_vector_type(8))) __bf16;
using bf16v = __attribute__((ext_vector_type(16))) __bf16;
using s16x8 = __attribute__((ext_vector_type(8))) short;
using s16x4 = __attribute__((ext_vector_type(4))) short;
using f32x2 = __attribute__((ext_vector_type(2))) float;
using f32x4 = __attribute__((ext_vector_type(4))) float;
using u32x2 = __attribute__((ext_vector_type(2))) unsigned;
using u32x3 = __attribute__((ext_vector_type(3))) unsigned;
typedef u32x3 u32x3u __attribute__((aligned(4)));
using u32x4 = __attribute__((ext_vector_type(4))) unsigned;
using u64x2 = __attribute__((ext_vector_type(2))) unsigned long long;
using u64x8 = __attribute__((ext_vector_type(8))) unsigned long long;
using u64x4 = __attribute__((ext_vector_type(4))) unsigned long long;

constexpr int WS_EPOCH = 0;
constexpr int WS_PF = 4096;
constexpr int WS_KR = WS_PF + NFFN * 128;
constexpr int WS_KF = WS_KR + 8 * 2048;
constexpr int WS_ST = WS_KF + 16384;
constexpr int WS_CK = WS_ST + 16384;
constexpr int WS_FL = WS_CK + 8 * 2048;
constexpr int WS_UP = 131072;
constexpr int WS_ZP = WS_UP + NCONV * NLN * 128;
constexpr int WS_PART = WS_ZP + NCONV * NLN * 128;
constexpr int WS_XS = WS_PART + NFFN * 8 * DM * 4;
constexpr int XSS = 4 * DM;
constexpr int WS_END = WS_XS + (2 * NLN + 8) * XSS;
static_assert(WS_FL + 8 * 1024 <= WS_UP, "ws");
constexpr size_t WS_TS = 64u << 20;
constexpr int SC1 = 16, NT = 2;
constexpr int SPIN_CAP = 1 << 16;

#define TW(k)
#define TWP(k)
#define TWA(k)
#define TWB(k)
#define TWDECL
#define TSX(k, s)
#define TWFLUSH()
#define TC(k, s)
#define TSW(w, s)
#define TSL(k, s)
#define TS(k, s)

__device__ __forceinline__ float bflo(unsigned v) { return __uint_as_float(v << 16); }
__device__ __forceinline__ float bfhi(unsigned v) { return __uint_as_float(v & 0xffff0000u); }
__device__ __forceinline__ unsigned pkbf(float a, float b) {
  f32x2 f = {a, b};
  return __builtin_bit_cast(unsigned, __builtin_convertvector(f, bf2v));
}
__device__ __forceinline__ unsigned pkh(float a, float b) {
  f32x2 f = {a, b};
  return __builtin_bit_cast(unsigned, __builtin_convertvector(f, half2v));
}
__device__ __forceinline__ float rbf(float f) { return __uint_as_float(pkbf(0.f, f)); }
__device__ __forceinline__ f32x2 rbf2(f32x2 v) { return f32x2{rbf(v[0]), rbf(v[1])}; }
__device__ __forceinline__ f32x2 unpk(unsigned v) { return f32x2{bflo(v), bfhi(v)}; }
__device__ __forceinline__ float dot2(unsigned a, unsigned b, float c) {
  return __builtin_amdgcn_fdot2_f32_bf16(__builtin_bit_cast(bf2v, a), __builtin_bit_cast(bf2v, b), c, false);
}
__device__ __forceinline__ float hadd(f32x2 v) {
  float a = v[0], b = v[1];
  asm volatile("" : "+v"(a), "+v"(b));
  return a + b;
}

template <int CTRL>
__device__ __forceinline__ float dppf(float v) {
  return __int_as_float(__builtin_amdgcn_update_dpp(0, __float_as_int(v), CTRL, 0xf, 0xf, true));
}
template <int CTRL>
__device__ __forceinline__ unsigned dppu(unsigned v) {
  return (unsigned)__builtin_amdgcn_update_dpp(0, (int)v, CTRL, 0xf, 0xf, true);
}
__device__ __forceinline__ float swap16_sum(float v) {
  auto r = __builtin_amdgcn_permlane16_swap(__float_as_uint(v), __float_as_uint(v), false, false);
  return __uint_as_float(r[0]) + __uint_as_float(r[1]);
}
__device__ __forceinline__ float swap32_sum(float v) {
  auto r = __builtin_amdgcn_permlane32_swap(__float_as_uint(v), __float_as_uint(v), false, false);
  return __uint_as_float(r[0]) + __uint_as_float(r[1]);
}
__device__ __forceinline__ float red32_sum(float v) {
  v += dppf<0xB1>(v);
  v += dppf<0x4E>(v);
  v += dppf<0x141>(v);
  v += dppf<0x140>(v);
  return swap16_sum(v);
}
#define SCHED_FENCE() __builtin_amdgcn_sched_barrier(0)
#define DRAIN() asm volatile("s_waitcnt vmcnt(0)" ::: "memory")

__device__ __forceinline__ float red16_sum(float v) {
  v += dppf<0xB1>(v);
  v += dppf<0x4E>(v);
  v += dppf<0x141>(v);
  return v + dppf<0x140>(v);
}
__device__ __forceinline__ float ln_rinv(float q, float dm) {
  const float var = fmaxf(fmaf(q, 1.0f / DM, -dm * dm), 0.f);
  return __builtin_amdgcn_rcpf(rbf(__builtin_amdgcn_sqrtf(rbf(rbf(var) + 1e-5f))));
}

constexpr int TAB_STRIDE = 544;
constexpr int TAB_ROWS = 58;
constexpr int TR_B = 18, TR_M = 26, TR_F = 34, TR_H = 42;
constexpr int TABB = TAB_ROWS * TAB_STRIDE;
constexpr int SM_KC = 3 * TABB;
constexpr int RDS = 336 * 4;
constexpr int KC_RD = SM_KC;
constexpr int KC_R5 = KC_RD + 4 * RDS;
constexpr int KC_R3 = KC_R5 + 64 * 4;
constexpr int KC_R9 = KC_R3 + 64 * 4;
constexpr int KC_R7 = KC_R9 + 96 * 4;
constexpr int SM_US = KC_R7 + 96 * 4;
constexpr int UPAD = 256;
constexpr int CH2 = SM_US + (UPAD + SEQ) * 2 - SM_KC;
static_assert(SM_US + 2 * UPAD <= SM_KC + 8192 && 8192 <= CH2 && 2 * TABB + 256 < 65536, "zero fill");
constexpr int YT = 1280;
constexpr int SM_YD = 0, SM_Y2 = 8 * YT - 2 * YT, SM_Y3 = 14 * YT - 4 * YT;
constexpr int SM_T2 = 0, T2S = 4624;
constexpr int SM_RED = SM_KC + 3 * CH2;
constexpr int SM_ROW = SM_RED + 3200;
constexpr int SM_ISC = SM_ROW + 128 + 128 + 32;
constexpr int SM_MX = SM_ISC + 16;
constexpr int SM_WR = SM_MX + 32;
constexpr int SM_DUMMY = SM_WR + DM * 2;
constexpr int RG = 9216;
constexpr int XRS = 1216;
constexpr int FS_SEL = 14 * RG;
constexpr int FS_GU = FS_SEL + 32;
constexpr int SM_TOTAL = SM_DUMMY + 256;
static_assert(FS_GU + 8 * 160 <= SM_TOTAL && SM_ISC % 16 == 0 && CH2 % 16 == 0 && TABB % 16 == 0, "lds");

typedef f32x2 cf;
#define TM(t, a, b) "v_pk_mul_f32 v[" #t ":" #t "+1], v[" #b ":" #b "+1], v[" #a ":" #a "+1] op_sel:[1,0]\n\t"
#define TF(d, a, b, t, n) \
  "v_pk_fma_f32 v[" #d ":" #d "+1], v[" #a ":" #a "+1], v[" #b ":" #b "+1], v[" #t ":" #t "+1] op_sel:[0,0,1] op_sel_hi:[1,0,0] neg_" #n ":[0,0,1]\n\t"
#define TC16(c, v) "v_cvt_pk_f16_f32 v" #c ", v[" #v "], v[" #v "+1]\n\t"
#define TWR(c, row) "ds_write_addtid_b32 v" #c " offset:544*" #row "\n\t"
#define TAB_A \
  TC16(134, 100) TM(106, 100, 102) TM(108, 102, 102) TWR(134, 0) TF(110, 100, 102, 106, lo) TF(112, 102, 102, 108, lo) \
  TC16(135, 110) TM(114, 110, 102) TM(116, 110, 112) TWR(135, 1) TF(118, 110, 102, 114, lo) TF(120, 110, 112, 116, lo) \
  TC16(136, 118) TM(122, 118, 104) TC16(137, 120) TWR(136, 2) TF(124, 118, 104, 122, lo) TM(126, 118, 112) TWR(137, 3) \
  TC16(134, 124) TF(128, 118, 112, 126, lo) TM(130, 124, 112) TWR(134, 10) TC16(135, 128) TF(132, 124, 112, 130, lo) \
  TM(106, 124, 102) TWR(135, 4) TC16(136, 132) TF(100, 124, 102, 106, lo) TM(108, 132, 104) TWR(136, 12) TC16(137, 100) \
  TF(114, 132, 104, 108, lo) TM(116, 132, 112) TWR(137, 11) TC16(134, 114) TF(110, 132, 112, 116, lo) TM(122, 114, 104) \
  TWR(134, 23) TC16(135, 110) TF(126, 114, 104, 122, lo) TM(118, 120, 112) TWR(135, 14) TC16(136, 126) TF(130, 120, 112, 118, lo) \
  TM(106, 126, 104) TWR(136, 25) TC16(137, 130) TF(102, 126, 104, 106, lo) TM(108, 100, 112) TWR(137, 5) TC16(134, 102) \
  TF(116, 100, 112, 108, lo) TM(132, 110, 112) TWR(134, 26) TC16(135, 116) TF(122, 110, 112, 132, lo) TM(114, 102, 104) \
  TWR(135, 13) TC16(136, 122) TF(118, 102, 104, 114, lo) TM(120, 124, 104) TWR(136, 16) TC16(137, 118) TF(106, 124, 104, 120, lo) \
  TM(126, 128, 112) TWR(137, 28) TC16(134, 106) TF(108, 128, 112, 126, lo) TM(100, 130, 112) TWR(134, 22) TC16(135, 108) \
  TF(132, 130, 112, 100, lo) TM(114, 116, 112) TWR(135, 6) TC16(136, 132) TF(102, 116, 112, 114, lo) TM(120, 110, 104) TWR(136, 7) \
  TC16(137, 102) TF(124, 110, 104, 120, lo) TM(126, 122, 104) TWR(137, 15) TC16(134, 124) TF(128, 122, 104, 126, lo) \
  TM(100, 118, 104) TWR(134, 18) TC16(135, 128) TF(130, 118, 104, 100, lo) TM(114, 106, 104) TWR(135, 19) TC16(136, 130) \
  TF(116, 106, 104, 114, lo) TM(120, 108, 112) TWR(136, 30) TC16(137, 116) TF(110, 108, 112, 120, lo) TM(126, 132, 112) \
  TWR(137, 24) TC16(134, 110) TF(122, 132, 112, 126, lo) TM(100, 102, 112) TWR(134, 8) TC16(135, 122) TF(118, 102, 112, 100, lo) \
  TM(114, 124, 104) TWR(135, 9) TC16(136, 118) TF(106, 124, 104, 114, lo) TM(116, 128, 104) TWR(136, 17) TC16(137, 106) \
  TF(120, 128, 104, 116, lo) TM(108, 130, 104) TWR(137, 20) TC16(134, 120) TF(110, 130, 104, 108, lo) TWR(134, 21) TC16(135, 110) \
  TWR(135, 32)
#define TAB_B \
  "v_mov_b32 v137, 0x3c00\n\t" TWR(137, 42) TC16(134, 102) TM(106, 104, 104) TM(108, 100, 102) TWR(134, 43) \
  TF(110, 104, 104, 106, lo) TF(112, 100, 102, 108, hi) TM(114, 102, 102) TM(116, 110, 110) TF(118, 110, 110, 116, lo) \
  TF(120, 102, 102, 114, lo) TC16(135, 120) TM(122, 112, 118) TM(124, 120, 102) TWR(135, 44) TF(126, 112, 118, 122, lo) \
  TF(128, 120, 102, 124, lo) TM(130, 120, 120) TC16(136, 126) TC16(137, 128) TF(132, 120, 120, 130, lo) TWR(136, 27) \
  TM(106, 126, 104) TC16(134, 132) TWR(137, 45) TF(108, 126, 104, 106, lo) TM(100, 126, 110) TWR(134, 46) TC16(135, 108) \
  TF(116, 126, 110, 100, lo) TM(114, 108, 110) TWR(135, 29) TC16(136, 116) TF(122, 108, 110, 114, lo) TM(112, 102, 132) \
  TWR(136, 31) TC16(137, 122) TF(124, 102, 132, 112, lo) TM(130, 120, 132) TWR(137, 33) TC16(134, 124) TF(106, 120, 132, 130, lo) \
  TM(104, 128, 132) TWR(134, 47) TC16(135, 106) TF(100, 128, 132, 104, lo) TM(114, 132, 132) TWR(135, 48) TC16(136, 100) \
  TF(110, 132, 132, 114, lo) TM(112, 124, 132) TWR(136, 49) TC16(137, 110) TF(102, 124, 132, 112, lo) TM(130, 106, 132) \
  TWR(137, 50) TC16(134, 102) TF(120, 106, 132, 130, lo) TM(104, 100, 132) TWR(134, 51) TC16(135, 120) TF(128, 100, 132, 104, lo) \
  TM(114, 126, 118) TWR(135, 52) TC16(136, 128) TF(112, 126, 118, 114, lo) TM(124, 108, 118) TWR(136, 53) TC16(137, 112) \
  TF(130, 108, 118, 124, lo) TM(106, 116, 118) TWR(137, 34) TC16(134, 130) TF(104, 116, 118, 106, lo) TM(100, 122, 118) \
  TWR(134, 35) TC16(135, 104) TF(114, 122, 118, 100, lo) TM(126, 110, 132) TWR(135, 36) TC16(136, 114) TF(124, 110, 132, 126, lo) \
  TM(108, 102, 132) TWR(136, 37) TC16(137, 124) TF(106, 102, 132, 108, lo) TM(116, 120, 132) TWR(137, 54) TC16(134, 106) \
  TF(100, 120, 132, 116, lo) TM(122, 128, 132) TWR(134, 55) TC16(135, 100) TF(126, 128, 132, 122, lo) TM(110, 112, 118) \
  TWR(135, 56) TC16(136, 126) TF(124, 112, 118, 110, lo) TM(108, 130, 118) TWR(136, 57) TC16(137, 124) TF(102, 130, 118, 108, lo) \
  TM(106, 104, 118) TWR(137, 38) TC16(134, 102) TF(116, 104, 118, 106, lo) TM(120, 114, 118) TWR(134, 39) TC16(135, 116) \
  TF(100, 114, 118, 120, lo) TWR(135, 40) TC16(136, 100) TWR(136, 41)
#define TAB_CLOB "v106", "v107", "v108", "v109", "v110", "v111", "v112", "v113", "v114", "v115", "v116", "v117", "v118", "v119", "v120", "v121", "v122", "v123", "v124", "v125", "v126", "v127", "v128", "v129", "v130", "v131", "v132", "v133", "v134", "v135", "v136", "v137"
__device__ __forceinline__ unsigned pkc(cf a) { return __builtin_bit_cast(unsigned, __builtin_convertvector(a, half2v)); }
__device__ __forceinline__ cf cexp(f32x2 hi, f32x2 lo) {
  float mag = __builtin_amdgcn_exp2f(hi[0]);
  mag = fmaf(mag, lo[0], mag);
  const float fr = __builtin_amdgcn_fractf(hi[1]) + lo[1];
  return cf{__builtin_amdgcn_cosf(fr), __builtin_amdgcn_sinf(fr)} * mag;
}
__device__ __forceinline__ unsigned wave_max_u32(unsigned v) {
  v = max(v, (unsigned)__builtin_amdgcn_update_dpp(0, (int)v, 0x111, 0xf, 0xf, false));
  v = max(v, (unsigned)__builtin_amdgcn_update_dpp(0, (int)v, 0x112, 0xf, 0xf, false));
  v = max(v, (unsigned)__builtin_amdgcn_update_dpp(0, (int)v, 0x114, 0xf, 0xf, false));
  v = max(v, (unsigned)__builtin_amdgcn_update_dpp(0, (int)v, 0x118, 0xf, 0xf, false));
  v = max(v, (unsigned)__builtin_amdgcn_update_dpp(0, (int)v, 0x142, 0xa, 0xf, false));
  v = max(v, (unsigned)__builtin_amdgcn_update_dpp(0, (int)v, 0x143, 0xc, 0xf, false));
  return (unsigned)__builtin_amdgcn_readlane((int)v, 63);
}

#define MFMA16(a, b, c) __builtin_amdgcn_mfma_f32_16x16x32_f16((a), (b), (c), 0, 0, 0)
#define MFMAB(a, b, c) __builtin_amdgcn_mfma_f32_16x16x32_bf16((a), (b), (c), 0, 0, 0)

__device__ __forceinline__ half8 ld_frag(const LAS char* p) { return *(const LAS half8*)p; }
__device__ __forceinline__ const LAS unsigned* opaque_lds(const LAS char* p) {
  const LAS unsigned* q = (const LAS unsigned*)p;
  asm volatile("" : "+v"(q));
  return q;
}
template <int O2>
__device__ __forceinline__ half8 ld_toep(const LAS unsigned* q) {
  u32x4 v = {q[0], q[1], q[O2], q[O2 + 1]};
  return __builtin_bit_cast(half8, v);
}
__device__ __forceinline__ half8 band_frag(int lane, unsigned pat) {
  unsigned t = __builtin_amdgcn_ubfe(pat << (lane & 15), 8 * (lane >> 4), 8);
  t |= t << 15;
  u32x4 v;
#pragma unroll
  for (int j = 0; j < 4; j++) v[j] = __umul24(t & (0x10001u << (2 * j)), 0x3c00u >> (2 * j));
  return __builtin_bit_cast(half8, v);
}
__device__ __forceinline__ half8 pack_win(f32x4 a, f32x4 b) {
  u32x4 v = {pkh(a[0], a[1]), pkh(a[2], a[3]), pkh(b[0], b[1]), pkh(b[2], b[3])};
  return __builtin_bit_cast(half8, v);
}

template <int V>
using IC = std::integral_constant<int, V>;
template <int S, int E, class F>
__device__ __forceinline__ void static_for(F&& f) {
  if constexpr (S < E) {
    f(std::integral_constant<int, S>{});
    static_for<S + 1, E>(f);
  }
}
__device__ __forceinline__ half8 ld_td(const LAS char* tp, int i) {
  const volatile LAS u32x2* tb = (const volatile LAS u32x2*)tp;
  const u32x2 lo = tb[8 * (16 - i)], hi = tb[8 * (16 - i) + 1];
  return __builtin_bit_cast(half8, __builtin_shufflevector(lo, hi, 0, 1, 2, 3));
}
__device__ __forceinline__ void conv_dense(half8 (&td)[17], const LAS char* tp, half8 tmask, const LAS char* f1024, const LAS char* ub, LAS char* yd) {
  constexpr int S0 = 9, NS = 73, PF = PFD;
  const f32x4 zero4 = {0.f, 0.f, 0.f, 0.f};
  f32x4 acc[8] = {zero4, zero4, zero4, zero4, zero4, zero4, zero4, zero4};
  half8 u[NS], te;
  unsigned fe = 0;
#pragma unroll
  for (int s = S0; s < S0 + PF; s++) {
    u[s] = ld_frag(ub + 64 * s);
    if (s == S0 + 3) {
      td[0] = ld_td(tp, 0);
      fe = *(const LAS u16*)f1024;
    }
    SCHED_FENCE();
  }
  static_for<S0, NS>([&](auto sc) {
    constexpr int s = decltype(sc)::value, t0 = s < 16 ? 0 : (s - 9) / 8, t1 = s / 8 < 7 ? s / 8 : 7;
    acc[t0] = MFMA16(td[16 + 8 * t0 - s], u[s], acc[t0]);
    SCHED_FENCE();
    if constexpr (s + PF < NS) u[s + PF] = ld_frag(ub + 64 * (s + PF));
    if constexpr (s == 67) *(LAS f32x4*)(yd + 6 * YT) = acc[6];
    SCHED_FENCE();
    if constexpr (t0 + 1 <= t1) acc[t0 + 1] = MFMA16(td[24 + 8 * t0 - s], u[s], acc[t0 + 1]);
    SCHED_FENCE();
    if constexpr (s >= 24 && (s & 7) == 0 && s < 72) *(LAS f32x4*)(yd + (s / 8 - 3) * YT) = acc[s / 8 - 3];
    if constexpr (s == 14) {
      fe |= fe << 16;
      te = tmask * __builtin_bit_cast(half8, u32x4{fe, fe, fe, fe});
    }
    SCHED_FENCE();
    if constexpr (t0 + 2 <= t1) acc[t0 + 2] = MFMA16(td[32 + 8 * t0 - s], u[s], acc[t0 + 2]);
    if constexpr (s >= 16 && s <= 40 && (s & 7) == 0) acc[s / 8 + 2] = MFMA16(te, u[s], acc[s / 8 + 2]);
    SCHED_FENCE();
  });
  *(LAS f32x4*)(yd + 7 * YT) = acc[7];
}

typedef __amdgpu_buffer_rsrc_t rsrc_t;
template <int AUX = LAUX>
__device__ __forceinline__ u32x4 ldg16(rsrc_t rs, int off, int so = 0) {
  asm volatile("" : "+s"(so) : : "memory");
  return __builtin_amdgcn_raw_buffer_load_b128(rs, off, so, AUX);
}
__device__ __forceinline__ void stg16(rsrc_t rs, int off, u32x4 v) { __builtin_amdgcn_raw_buffer_store_b128(v, rs, off, 0, SAUX); }
__device__ __forceinline__ void dma16(const char* g, unsigned lds) {
  unsigned keep;
  asm volatile("s_mov_b32 %0, m0\n\ts_mov_b32 m0, %2\n\ts_nop 0\n\tglobal_load_lds_dwordx4 %1, off" WNTS "\n\ts_mov_b32 m0, %0"
               : "=&s"(keep) : "v"(g), "s"(lds) : "memory");
}
__device__ __forceinline__ void dma4(const char* g, unsigned lds) {
  unsigned keep;
  asm volatile("s_mov_b32 %0, m0\n\ts_mov_b32 m0, %2\n\ts_nop 0\n\tglobal_load_lds_dword %1, off\n\ts_mov_b32 m0, %0"
               : "=&s"(keep) : "v"(g), "s"(lds) : "memory");
}
__device__ __forceinline__ void dma4s(size_t base, unsigned off, unsigned lds) {
  unsigned keep;
  asm volatile("s_mov_b32 %0, m0\n\ts_mov_b32 m0, %3\n\ts_nop 0\n\tglobal_load_lds_dword %1, %2\n\ts_mov_b32 m0, %0"
               : "=&s"(keep) : "v"(off), "s"(base), "s"(lds) : "memory");
}
__device__ __forceinline__ void wait_ticks(int t) {
  const unsigned long long t0 = __builtin_amdgcn_s_memrealtime();
  while (__builtin_amdgcn_s_memrealtime() - t0 < (unsigned long long)t) __builtin_amdgcn_s_sleep(1);
}
#define UPOS 1
#define DLY_X 20
#define DLY_W 0
#define HB 1
#define DLY_H 70
#define DLY_R 0
#define DLY_L 200
#define DLY_P 40

__device__ __forceinline__ int key_row(unsigned k) { return (int)(~(k >> 2) & (SEQ - 1)); }
__device__ __forceinline__ int key_slot(int lane) { return (lane & 3) | ((lane >> 1) & 4); }
__device__ __forceinline__ int key_addr(unsigned k, int slot) {
  return WS_XS + ((k & 2) ? 2 * NLN + slot : (int)((~(k >> 5) & (2 * NLN - 2)) | (k & 1))) * XSS;
}
struct SelMasks {
  unsigned m[15];
  __device__ __forceinline__ SelMasks(int lane) {
    const unsigned b0 = -(lane & 1), b1 = -((lane >> 1) & 1), b3 = -((lane >> 3) & 1), b4 = -((lane >> 4) & 1), b5 = -(lane >> 5);
    const unsigned t[15] = {~(b0 ^ b1), ~(b1 ^ b3), ~(b0 ^ b3), ~b3, ~b1, ~b0, b3, b1, b0, ~(b3 ^ b4), ~(b1 ^ b4), ~(b0 ^ b4), ~(b3 ^ b5), ~(b1 ^ b5), ~(b0 ^ b5)};
#pragma unroll
    for (int i = 0; i < 15; i++) {
      m[i] = t[i];
      asm volatile("" : "+v"(m[i]));
    }
  }
};
template <int CTRL>
__device__ __forceinline__ unsigned cex(unsigned v, unsigned m) {
  const unsigned o = dppu<CTRL>(v);
  return max(min(v, o), min(max(v, o), m));
}
__device__ __forceinline__ unsigned clean8(unsigned v, const unsigned* m) { return cex<0xB1>(cex<0x4E>(cex<0x128>(v, m[0]), m[1]), m[2]); }
__device__ __forceinline__ unsigned select8(rsrc_t rs, unsigned tag, int lane, const SelMasks& sm, char* ws, int bid) {
  const int off = WS_KR + (bid & 7) * 2048 + lane * 16;
  u32x4 a, c;
  _Pragma("unroll 1") for (int spin = 0; spin < SPIN_CAP; spin++) {
    a = ldg16(rs, off);
    c = ldg16(rs, off, 1024);
    if (__builtin_expect(__all(a[3] == tag && c[3] == tag), 1)) break;
    __builtin_amdgcn_s_sleep(2);
  }
  TSL(1, 8);
  const unsigned t1 = min(a[0], c[0]), t2 = max(a[1], c[1]), third = max(a[2], c[2]);
  unsigned m1 = max(a[0], c[0]), m2 = max(t1, t2), m3 = min(t1, t2);
  unsigned x = clean8(cex<0xB1>(cex<0x4E>(cex<0xB1>(m1, sm.m[0]), sm.m[1]), sm.m[2]), sm.m + 3);
  const unsigned y = clean8(cex<0xB1>(cex<0x4E>(cex<0xB1>(m2, sm.m[0]), sm.m[1]), sm.m[2]), sm.m + 6);
  x = clean8(max(x, y), sm.m + 3);
  x = clean8(max(x, dppu<0x140>(x)), sm.m + 9);
  {
    auto r = __builtin_amdgcn_permlane16_swap(x, x, false, false);
    x = clean8(max(r[0], r[1]), sm.m + 12);
    auto q = __builtin_amdgcn_permlane32_swap(x, x, false, false);
    x = max(q[0], q[1]);
  }
  unsigned k = min(x, dppu<0xB1>(x));
  k = min(k, dppu<0x4E>(k));
  k = min(k, dppu<0x128>(k));
  if (__builtin_expect(__any(max(m3, third) > k), 0)) {
    const int slot = key_slot(lane);
    unsigned m4 = min(a[1], c[1]);
    _Pragma("unroll 1") for (int r = 0; r < 8; r++) {
      k = wave_max_u32(m1);
      x = slot == r ? k : x;
      const bool e = m1 == k;
      m1 = e ? m2 : m1;
      m2 = e ? m3 : m2;
      m3 = e ? m4 : m3;
      m4 = e ? 0u : m4;
    }
    if (__any(third > k)) {
      k = 0u;
      _Pragma("unroll 1") for (int r = 0; r < 8; r++) {
        unsigned v;
        _Pragma("unroll 1") for (int spin = 0; spin < SPIN_CAP; spin++) {
          bool ok = true;
          v = 0u;
          _Pragma("unroll 4") for (int i = 0; i < 16; i++) {
            const u32x4 f = ldg16(rs, WS_KF + lane * 16, i * 1024);
            ok = ok && f[1] == tag && f[3] == tag;
            v = max(v, max(f[0] <= k - 1u ? f[0] : 0u, f[2] <= k - 1u ? f[2] : 0u));
          }
          if (__all(ok)) break;
          __builtin_amdgcn_s_sleep(1);
        }
        k = wave_max_u32(v);
        x = slot == r ? k : x;
      }
    }
  }
  return x;
}

__device__ __forceinline__ void ffn_role(size_t argp, unsigned cpf, LAS char* L, unsigned long long r_entry) {
  const int tid = threadIdx.x, lane = tid & 63, bid = blockIdx.x;
  size_t x, wsp, wdp;
  u64x2 gu;
  asm volatile(
      "s_waitcnt lgkmcnt(0)\n\ts_load_dwordx2 %0, %4, 0x0\n\ts_load_dwordx2 %1, %4, 0x30\n\ts_load_dwordx4 %2, %4, 0x48\n\t"
      "s_load_dwordx2 %3, %4, 0x58"
      : "=&s"(x), "=&s"(wsp), "=&s"(gu), "=&s"(wdp) : "s"(argp));
  SCHED_FENCE();
  unsigned xo0 = (bid * 32 + (tid >> 4)) * (DM * 2) + (lane & 15) * 8;
  asm volatile("" : "+v"(xo0));
  asm volatile("s_waitcnt lgkmcnt(0)" : "+s"(x), "+s"(wsp), "+s"(gu), "+s"(wdp));
  const int wave = __builtin_amdgcn_readfirstlane(tid >> 6);
  const int fw = bid;
  u32x2 xw[9];
  if (wave < 8) {
#pragma unroll
    for (int k = 0; k < 9; k++) xw[k] = *(GAS const u32x2*)(x + xo0 + 128 * k);
  }
  char* const ws = (char*)(GAS char*)wsp;
  const char *wg = (const char*)gu[0], *wu = (const char*)gu[1], *wd = (const char*)wdp;
  const rsrc_t rs = __builtin_amdgcn_make_buffer_rsrc(ws, 0, WS_END, 0x00020000);
  const unsigned tagv = __builtin_amdgcn_raw_buffer_load_b32(rs, WS_EPOCH, 0, SC1);
  SCHED_FENCE();
  TC(1, 14);
  const int j = (fw & 7) * 8 + (fw >> 3);
  const int g = lane >> 4, tq = (lane & 15) >> 2, tp = lane & 3;
  const int r32 = tid / 18, rem = tid - r32 * 18;
  const char* gsrc = (const char*)(rem >= 9 ? wu : wg) + (unsigned)(r32 * (FF * 2) + j * 144 + (rem >= 9 ? rem - 9 : rem) * 16);
  const unsigned lb = (unsigned)(size_t)L + wave * 1024;
  auto issue_gu = [&](int ks0, int n, int reg0) {
    if (wave < 9) {
#pragma unroll
      for (int i = 0; i < n; i++) dma16(gsrc + (size_t)(ks0 + i) * (32 * FF * 2), lb + (reg0 + i) * RG);
    }
  };
  const int r8 = tid / 72, q8 = (tid - r8 * 72) ^ (r8 & 2);
  const char* dsrc = (const char*)wd + (unsigned)((j * 72 + r8) * 1152 + q8 * 16);
  const char* dsrc2 = (const char*)wd + (unsigned)((j * 72 + r8) * 1152 + (q8 ^ 4) * 16);
  asm volatile("" : "+v"(gsrc), "+v"(dsrc), "+v"(dsrc2));
  SCHED_FENCE();
  const unsigned tag = __builtin_amdgcn_readfirstlane(tagv) + 1u;
  asm volatile("" : : "v"(cpf), "s"(tag));
  if (wave < 8) {
    unsigned one2 = 0x3f803f80u;
    asm volatile("" : "+s"(one2));
    float s0 = 0.f, s1 = 0.f, q0 = 0.f, q1 = 0.f;
#pragma unroll
    for (int k = 0; k < 9; k++) {
      const unsigned a = xw[k][0], c = xw[k][1];
      s0 = dot2(a, one2, s0);
      q0 = dot2(a, a, q0);
      s1 = dot2(c, one2, s1);
      q1 = dot2(c, c, q1);
    }
    const float mean = red16_sum(s0 + s1) * (1.0f / DM), mbf = rbf(mean);
    float q = red16_sum(q0 + q1), dm = mean;
    if (__builtin_expect(__any(!(q * (1.0f / DM) >= mean * mean * 1.002f)), 0)) {
      f32x2 q2 = {0.f, 0.f};
#pragma unroll
      for (int k = 0; k < 18; k++) {
        const f32x2 dd = unpk(xw[k >> 1][k & 1]) - mbf;
        q2 = __builtin_elementwise_fma(dd, dd, q2);
      }
      q = red16_sum(hadd(q2));
      dm = mean - mbf;
    }
    const float ri = ln_rinv(q, dm);
    auto rr = __builtin_amdgcn_permlane16_swap(__float_as_uint(ri), __float_as_uint(ri), false, false);
    auto m2 = __builtin_amdgcn_permlane16_swap(__float_as_uint(mbf), __float_as_uint(mbf), false, false);
    const unsigned mp = __builtin_amdgcn_perm(m2[1], m2[0], 0x07060302u);
    if ((lane & 31) == 0) stg16(rs, WS_ST + (16 * fw + (tid >> 5)) * 16, u32x4{rr[0], rr[1], mp, tag});
  }
  TS(1, 10);
  __builtin_amdgcn_s_barrier();
  if (DLY_W) wait_ticks(DLY_W);
  if (WORD) issue_gu(0, 5, 9);
  if (wave < 9) {
#pragma unroll
    for (int i = 0; i < 9; i++) dma16((i & 1 ? dsrc2 : dsrc) + i * RG, lb + i * RG);
  }
  if (!WORD) issue_gu(0, 5, 9);
  if (tid < 8 * 4) *(LAS unsigned*)(L + FS_GU + (tid >> 2) * 160 + 144 + (tid & 3) * 4) = 0u;
  s16x4 fa[18][2], fd[3][5];
  const LAS char* gup = L + (16 * (g >> 1) + 4 * (g & 1) + (tq & 1) + 8 * (tq >> 1)) * 288 + (tp >> 1) * 144 + (tp & 1) * 8 + 16 * wave;
  auto read_gu = [&](auto ks0, auto n, auto reg0) {
    if (wave < 9) {
#pragma unroll
      for (int i = 0; i < n; i++)
#pragma unroll
        for (int h = 0; h < 2; h++) fa[ks0 + i][h] = __builtin_amdgcn_ds_read_tr16_b64_v4i16((LAS s16x4*)(gup + (reg0 + i) * RG + 2 * h * 288));
    }
  };
  auto read_down = [&]() {
    const int f = (tq >> 1) | ((g & 1) << 1);
#pragma unroll
    for (int t = 0; t < 3; t++) {
      const LAS char* p = L + (8 * g + tq) * 1152 + ((3 * wave + t) ^ f) * 32 + tp * 8;
#pragma unroll
      for (int h = 0; h < 4; h++)
        fd[t][h] = __builtin_amdgcn_ds_read_tr16_b64_v4i16((LAS s16x4*)(p + (32 * (h >> 1) + 4 * (h & 1)) * 1152));
      fd[t][4] = __builtin_amdgcn_ds_read_tr16_b64_v4i16(
          (LAS s16x4*)(L + (64 + 4 * (g & 1) + tq) * 1152 + ((3 * wave + t) ^ (tq >> 1)) * 32 + tp * 8));
    }
  };
  asm volatile("s_waitcnt vmcnt(9)" ::: "memory");
  __syncthreads();
  read_gu(IC<0>{}, IC<5>{}, IC<9>{});
  __syncthreads();
  TS(1, 6);
  issue_gu(5, 5, 9);
  asm volatile("s_waitcnt vmcnt(5)" ::: "memory");
  __syncthreads();
  read_down();
  __syncthreads();
  TS(1, 7);
  issue_gu(10, 8, 0);
  asm volatile("s_waitcnt vmcnt(8)" ::: "memory");
  __syncthreads();
  TS(1, 1);
  read_gu(IC<5>{}, IC<5>{}, IC<9>{});
  DRAIN();
  __syncthreads();
  read_gu(IC<10>{}, IC<8>{}, IC<0>{});
  TS(1, 2);
  const int c15 = lane & 15;
  const int xs = tid / 72, xc = tid - xs * 72;
  const LAS char* selr = L + FS_SEL + (xs & 7) * 4;
  LAS char* xw2 = L + xs * XRS + xc * 16;
  const LAS char* xr = L + (lane & 7) * XRS + (c15 >> 3) * 16 + g * 32;
  LAS char* guw = L + FS_GU + (4 * (g & 1) + 2 * (g >> 1) + (c15 >> 3)) * 160 + (wave * 8 + (lane & 7)) * 2;
  const LAS char* gur = L + FS_GU + (lane & 7) * 160 + 16 * g;
  const LAS char* gur2 = L + FS_GU + (lane & 7) * 160 + 128 + 8 * g;
  int xo = xc * 16, po = WS_PART + fw * (8 * DM * 4) + 3 * wave * 512 + (lane & 7) * 64 + 16 * g;
  int sidx = c15 < 8 ? 0x4444 : 0xeeee;
  asm volatile("" : "+v"(selr), "+v"(guw), "+v"(gur), "+v"(gur2), "+v"(xo), "+v"(po), "+v"(sidx), "+v"(xw2), "+v"(xr));
  if (wave == 9) {
    const SelMasks sm(lane);
    LAS char* selw = L + FS_SEL + key_slot(lane) * 4;
    asm volatile("" : "+v"(selw));
    int spin = 0;
    _Pragma("unroll 1") for (; spin < SPIN_CAP; spin++) {
      const unsigned t = ldg16(rs, WS_PF + fw * 128)[0];
      if (__builtin_amdgcn_readfirstlane(t) == tag) break;
      __builtin_amdgcn_s_sleep(2);
    }
    if (spin) wait_ticks(DLY_X);
    TSL(1, 3);
    *(LAS int*)selw = key_addr(select8(rs, tag, lane, sm, ws, bid), key_slot(lane));
    if (DLY_R) wait_ticks(DLY_R);
  }
  __syncthreads();
  TS(1, 9);
  if (wave < 9) {
    xo += *(const LAS int*)selr;
    u32x4 v;
    _Pragma("unroll 1") for (int spin = 0; spin < SPIN_CAP; spin++) {
      v = ldg16(rs, xo);
      if (__builtin_expect(__all(max(max(v[0], v[1]), max(v[2], v[3])) != ~0u), 1)) break;
      __builtin_amdgcn_s_sleep(2);
    }
    *(LAS u32x4*)xw2 = v;
  }
  __syncthreads();
  TS(1, 4);
  if (wave < 9) {
    bf8v xf[9];
#pragma unroll
    for (int i = 0; i < 9; i++) xf[i] = *(const LAS bf8v*)(xr + 128 * i);
    SCHED_FENCE();
    f32x4 acc = {0.f, 0.f, 0.f, 0.f};
#pragma unroll
    for (int i = 0; i < 9; i++) {
      const s16x8 lo = __builtin_shufflevector(fa[2 * i][0], fa[2 * i][1], 0, 1, 2, 3, 4, 5, 6, 7);
      const s16x8 hi = __builtin_shufflevector(fa[2 * i + 1][0], fa[2 * i + 1][1], 0, 1, 2, 3, 4, 5, 6, 7);
      acc = __builtin_amdgcn_smfmac_f32_16x16x64_bf16(
          xf[i], __builtin_bit_cast(bf16v, __builtin_shufflevector(lo, hi, 0, 1, 2, 3, 4, 5, 6, 7, 8, 9, 10, 11, 12, 13, 14, 15)), acc, sidx, 0, 0);
    }
    const float a0 = acc[0], a1 = acc[1], a2 = acc[2], a3 = acc[3];
    auto r0 = __builtin_amdgcn_permlane32_swap(__float_as_uint(a0), __float_as_uint(a2), false, false);
    auto r1 = __builtin_amdgcn_permlane32_swap(__float_as_uint(a1), __float_as_uint(a3), false, false);
    const float s0 = __uint_as_float(r0[0]) + __uint_as_float(r0[1]), s1 = __uint_as_float(r1[0]) + __uint_as_float(r1[1]);
    const float gs = __int_as_float(__builtin_amdgcn_update_dpp(__float_as_int(s0), __float_as_int(s1), 0x128, 0xf, 0xc, false));
    const float us = __int_as_float(__builtin_amdgcn_update_dpp(__float_as_int(s1), __float_as_int(s0), 0x128, 0xf, 0x3, false));
    const float gp = rbf(gs);
    const float gq = rbf(gp * __builtin_amdgcn_rcpf(1.0f + __expf(-gp)));
    *(LAS u16*)guw = (u16)pkbf(gq * rbf(us), 0.f);
  }
  __syncthreads();
  {
    const bf8v b0 = *(const LAS bf8v*)gur, b1 = *(const LAS bf8v*)(gur + 64);
    const s16x4 b2 = *(const LAS s16x4*)gur2;
    f32x4 acc[3];
#pragma unroll
    for (int t = 0; t < 3; t++)
      acc[t] = MFMAB(__builtin_bit_cast(bf8v, __builtin_shufflevector(fd[t][0], fd[t][1], 0, 1, 2, 3, 4, 5, 6, 7)), b0, (f32x4{0.f, 0.f, 0.f, 0.f}));
#pragma unroll
    for (int t = 0; t < 3; t++)
      acc[t] = MFMAB(__builtin_bit_cast(bf8v, __builtin_shufflevector(fd[t][2], fd[t][3], 0, 1, 2, 3, 4, 5, 6, 7)), b1, acc[t]);
#pragma unroll
    for (int t = 0; t < 3; t++) acc[t] = __builtin_amdgcn_mfma_f32_16x16x16bf16_1k(fd[t][4], b2, acc[t], 0, 0, 0);
    SCHED_FENCE();
    if (c15 < 8) {
#pragma unroll
      for (int t = 0; t < 3; t++) {
        u32x4 o = __builtin_bit_cast(u32x4, acc[t]);
        o[0] = min(o[0], 0xfffffffeu);
        stg16(rs, po + t * 512, o);
      }
    }
  }
  TS(1, 5);
  TC(1, 15);
}

__global__ __launch_bounds__(NTHR, 3) void k_all(const u16*, const u16*, const u16*, const u16*, const u16*, const u16*, char*, const u16*,
                                                 const u16*, const u16*, const u16*, const u16*, u16*) {
  __shared__ __attribute__((aligned(16))) char smem[SM_TOTAL];
  LAS char* const L = (LAS char*)smem;
  const int bid = blockIdx.x;
  const size_t argp = (size_t)__builtin_amdgcn_kernarg_segment_ptr();
  u64x8 a8;
  u64x4 a4;
  size_t a1;
  unsigned cpf;
  {
    size_t pc;
    int t;
    asm volatile("s_load_dwordx16 %0, %3, 0x0\n\ts_load_dwordx8 %1, %3, 0x40\n\ts_load_dwordx2 %2, %3, 0x60" : "=&s"(a8), "=&s"(a4), "=&s"(a1) : "s"(argp));
    asm volatile(
        "s_getpc_b64 %1\n\ts_lshr_b32 %2, %3, 3\n\tv_min_u32 %0, 6, %4\n\ts_mul_i32 %2, %2, 7\n\tv_add_lshl_u32 %0, %0, %2, 7\n\t"
        "global_load_dword %0, %0, %1"
        : "=&v"(cpf), "=&s"(pc), "=&s"(t) : "s"(bid), "v"(threadIdx.x) : "scc");
  }
  SCHED_FENCE();
  const int tid = threadIdx.x, lane = tid & 63, b = bid - NFFN;
  if (b < 0) {
    ffn_role(argp, cpf, L, 0);
    __builtin_amdgcn_endpgm();
  }
  const int wave = __builtin_amdgcn_readfirstlane(tid >> 6);
  const bool is_ln = b < NLN;
  const int hl = lane & 31;
  const int ch = (wave & 3) < 3 ? (wave & 3) : (wave >> 2), sub = (wave & 3) < 3 ? (wave >> 2) : 3;
  const int d = 3 * b + ch;
  const int xt = 4 * b + wave, hrow = 8 * ((xt >> 1) & 127) + (lane >> (3 + XM));
  const int grp = ((xt >> 8) << 5) + ((xt & 1) << 4) + (lane & (XM ? 15 : 7));
  unsigned xo = hrow * (DM * 4) + grp * 12, po = d * NM * 2 + lane * 4, lo = 2 * d & ~3;
  int cpo = WS_UP + (grp * 2048 + hrow) * 16;
  {
    LAS char* zc = L + (wave >> 2) * CH2 + SM_KC + 16 * (tid & 255);
    const u32x4 z = {0u, 0u, 0u, 0u};
    *(LAS u32x4*)zc = z;
    *(LAS u32x4*)(zc + 4096) = z;
  }
  const int xg = b & 7, sl = b >> 3, sl12 = sl < 12 ? sl : sl - 12;
  unsigned s0o = S0M == 1 ? (xg * 5184 + sl * 216 + wave * 18 + min(lane, 17)) * 128
                          : (144 * (S0M == 3 ? (lane >> 5) : 0) + 12 * sl12 + wave) * (FF * 2) + xg * 1152 + min(lane & 31, 8) * 128;
  asm volatile("" : "+v"(xo), "+v"(po), "+v"(lo), "+v"(cpo), "+v"(s0o));
  SCHED_FENCE();
  asm volatile("s_waitcnt lgkmcnt(0)" : "+s"(a8), "+s"(a4), "+s"(a1));
  u32x3 cp[4];
  auto load_x = [&]() {
    if (wave < 4) {
#pragma unroll
      for (int i = 0; i < 4; i++) cp[i] = *(GAS const u32x3u*)(a8[0] + xo + ((i & 1) * (DM * 2) + (i >> 1) * (XM ? DM * 16 : 96)));
    }
    SCHED_FENCE();
  };
  if (XF) load_x();
  const unsigned r_ldt = *(GAS const unsigned*)(a8[5] + lo), r_lar = *(GAS const unsigned*)(a8[1] + po), r_aim = *(GAS const unsigned*)(a8[2] + po);
  const unsigned r_cr = *(GAS const unsigned*)(a8[3] + po), r_ci = *(GAS const unsigned*)(a8[4] + po);
  char* const ws = (char*)(GAS char*)a8[6];
  GAS char* const out = (GAS char*)a1;
  const rsrc_t rs = __builtin_amdgcn_make_buffer_rsrc(ws, 0, WS_END, 0x00020000);
  const unsigned tagv = __builtin_amdgcn_raw_buffer_load_b32(rs, WS_EPOCH, 0, SC1);
  SCHED_FENCE();
  if (!XF) load_x();
  unsigned s0v = 0;
  if (S0M) s0v = *(GAS const unsigned*)((S0M == 1 ? a4[3] : (sl < 12 ? a4[1] : a4[2])) + s0o);
  SCHED_FENCE();
  TWDECL;
  TWP(4);
  auto prefetch = [&](int i) {
    const char* base = (const char*)(sl < 12 ? a4[1] : a4[2]) + (144 * i + 48 * ch + 4 * sl12) * (FF * 2) + xg * 1152;
    dma4(base + (unsigned)((lane >> 4) * (FF * 2) + min(lane & 15, 8) * 128), (unsigned)(size_t)L + SM_DUMMY);
  };
  const int part_off = WS_PART + (tid >> 3) * (8 * DM * 4) + (b - RED0) * 128 + (tid & 7) * 16;
  LAS char *const C = L + ch * TABB, *const K = L + ch * CH2;
  const int c15 = lane & 15, g = lane >> 4;
  unsigned pfo = ((8 * xg + (sl & 7)) * 648 + DR0 * 72 + ((sl >> 3) * 4 + (wave & 3)) * ((9 - DR0) * 6) + min(lane, (9 - DR0) * 6 - 1)) * 128;
  unsigned pfq = (12 * sl12 + 3 * (wave & 3) + min(lane >> 4, 2)) * (FF * 2) + xg * 1152 + min(lane & 15, 8) * 128;
  auto pf4 = [&](int m) {
#pragma unroll
    for (int i = 0; i < 4; i++)
      if (m >> i & 1) dma4s((sl < 12 ? a4[1] : a4[2]) + 144 * i * (FF * 2), pfq, (unsigned)(size_t)L + SM_DUMMY);
    if (m & 16) dma4s(a4[3], pfo, (unsigned)(size_t)L + SM_DUMMY);
  };
  unsigned hs = (sub & 1) ? 0x03020c0cu : 0x01000c0cu, hs2 = (d & 1) ? 0x03020c0cu : 0x01000c0cu;
  LAS char* yb = C + g * 320 + c15 * 16;
  const LAS char *pa, *pa2, *pb, *uf16 = K + SM_US + 2 * UPAD + 32 * c15 + 16 * g;
  LAS char *kd, *kd2;
  unsigned ks0, ks1;
  half8 bA, bB;
  if (sub == 0) {
    const int r = c15 & 3, r3 = r == 3;
    pa = C + (c15 < 8 ? 2 * (c15 & 4) + 9 - r : TR_B + 1 + ((c15 >> 1) & 2) - r3) * TAB_STRIDE + 16 * g;
    pa2 = pa + (c15 < 8 ? -4 : 4) * TAB_STRIDE;
    pb = C + (TR_H + c15) * TAB_STRIDE + 16 * g;
    kd = K + KC_RD + 2 * 536 - 16 * lane;
    kd2 = K + KC_RD + ((c15 - 1) & 3) * RDS + 2 * (528 - ((c15 - 1) & ~3) + 8 * g) - 64 * 16;
    ks0 = lane == 31 ? 0x03020302u : 0x03020100u;
    uf16 -= 32 * 33;
    bA = band_frag(lane, 0x3u << 12);
    asm volatile("" : "+v"(bA), "+v"(pa), "+v"(pa2), "+v"(pb), "+v"(kd), "+v"(kd2), "+v"(ks0), "+v"(uf16), "=v"(ks1), "=v"(bB));
  } else if (sub == 1) {
    const int lag = (c15 >> 2) + 1 - (c15 & 3);
    pa = (lag >= 0 ? C + lag * TAB_STRIDE : K + SM_US) + 16 * g;
    kd = K + KC_RD + g * RDS + 2 * 544;
    asm volatile("" : "=v"(bA), "+v"(pa), "=v"(pa2), "=v"(pb), "+v"(kd), "=v"(kd2), "=v"(ks0), "=v"(uf16), "=v"(ks1), "=v"(bB));
  } else if (sub == 2) {
    kd = K + KC_R9 + 190 * ((c15 + 1) & 1) + 2 * (15 - c15 + 4 * g);
    bA = band_frag(lane, 0x1ffu << 4);
    bB = band_frag(lane, 0x7fu << 5);
    asm volatile("" : "+v"(bA), "=v"(pa), "=v"(pa2), "=v"(pb), "+v"(kd), "=v"(kd2), "=v"(ks0), "+v"(uf16), "=v"(ks1), "+v"(bB));
  } else {
    pa = C + (TR_H + 15 - c15) * TAB_STRIDE + 16 * g;
    pa2 = K + KC_R5 + 2 * (158 - 64 - 2 * c15 + 4 * g) - 128;
    pb = C + (TR_M + c15) * TAB_STRIDE + 16 * g;
    kd = K + (c15 < 8 ? ((c15 & 1) ? KC_R5 : KC_R3) + 2 * (80 - 8 * (c15 & 6) + 4 * g) : KC_R9 + 2 * (120 - 8 * c15 + 2 * g));
    kd2 = kd + (c15 < 8 ? 4 : KC_R7 - KC_R9);
    ks0 = c15 < 8 ? 0x03020100u : 0x05040100u;
    ks1 = c15 < 8 ? 0x07060504u : 0x07060302u;
    uf16 -= 16 * c15;
    bA = band_frag(lane, 0x1fu << 6);
    bB = band_frag(lane, 0x7u << 11);
    asm volatile("" : "+v"(bA), "+v"(pa), "+v"(pa2), "+v"(pb), "+v"(kd), "+v"(kd2), "+v"(ks0), "+v"(uf16), "+v"(ks1), "+v"(bB));
  }
  const unsigned pe = sub < 2 ? 0u : 3u << 23;
  f32x2 kh = {-1.44269502f, 0.159154936f}, kl = {-1.92596299e-8f, 7.13048051e-9f}, k1 = {128.f * 0.693147181f, 128.f}, ln2 = {0.693147181f, 128.f};
  const unsigned pi = sub < 2 ? 0x3f800000u : 0xc1000000u;
  u32x2 k0u = {0x3f317218u + pe, pi}, p0u = {0x3f800000u + pe, pi};
  asm volatile("" : "+v"(pfo), "+v"(pfq), "+v"(yb));
  asm volatile("s_mov_b32 m0, %8" : "+s"(hs), "+s"(hs2), "+s"(kh), "+s"(kl), "+s"(k0u), "+s"(k1), "+s"(ln2), "+s"(p0u)
               : "s"((unsigned)(size_t)C + (sub & 1) * 256));
  SCHED_FENCE();
  TWP(5);
  SCHED_FENCE();
  const unsigned tag = __builtin_amdgcn_readfirstlane(tagv) + 1u;
  auto half = [&](unsigned v) { return __uint_as_float(__builtin_amdgcn_perm(v, v, hs)); };
  f32x2 f_l = {half(r_lar), __uint_as_float(__builtin_amdgcn_perm(r_ldt, r_ldt, hs2))}, f_c = {half(r_cr), half(r_ci)};
  float f_aim = half(r_aim);
  unsigned f_cm;
  asm volatile("v_max3_f32 %0, |%1|, |%2|, |%3|\n\tv_max_f32 %0, %0, |%4|" : "=&v"(f_cm) : "v"(r_cr), "v"(r_ci), "v"(r_cr << 16), "v"(r_ci << 16));
  asm volatile("" : "+v"(f_l), "+v"(f_aim), "+v"(f_c) : "v"(cpf));
  SCHED_FENCE();
  TWP(6);
  SCHED_FENCE();
  if (wave < 4) {
#pragma unroll
    for (int i = 0; i < 4; i += 2) {
      unsigned lo[3], hi[3];
#pragma unroll
      for (int k = 0; k < 3; k++) {
        lo[k] = __builtin_amdgcn_perm(cp[i + 1][k], cp[i][k], 0x05040100u);
        hi[k] = __builtin_amdgcn_perm(cp[i + 1][k], cp[i][k], 0x07060302u);
      }
      const int o = cpo + i * (XM ? 32 : 8 * 1024 * 16);
      stg16(rs, o, u32x4{lo[0], hi[0], lo[1], tag});
      stg16(rs, o + 16384, u32x4{hi[1], lo[2], hi[2], tag});
    }
    pf4(S1M);
  }
  SCHED_FENCE();
  TWA(2);
  TWP(7);
  {
    const f32x2 l2e = -f32x2{kh[0], kh[0]}, th = f_l * l2e;
    const f32x2 tl = __builtin_elementwise_fma(f_l, -f32x2{kl[0], kl[0]}, __builtin_elementwise_fma(f_l, l2e, -th)) * ln2[0];
    const f32x2 e0 = {__builtin_amdgcn_exp2f(th[0]), __builtin_amdgcn_exp2f(th[1])}, ex = __builtin_elementwise_fma(e0, tl, e0);
    const unsigned eb = pkbf(ex[0], ex[1]);
    const float dt = bfhi(eb);
    const f32x2 av = f32x2{bflo(eb), f_aim} * dt;
    const f32x2 hi = av * kh, lo = __builtin_elementwise_fma(av, kl, __builtin_elementwise_fma(av, kh, -hi));
    int se = 385 - (int)(wave_max_u32(f_cm) >> 23) - (int)((unsigned)__builtin_amdgcn_readfirstlane(eb) >> 23);
    se = max(se, 2);
    asm("" : "+s"(se));
    se = min(se, 252);
    const float dts = dt * __uint_as_float((unsigned)(se + 1) << 23);
    cf cc = f_c * dts;
    cf ea = cexp(hi * __builtin_bit_cast(f32x2, p0u), lo * __builtin_bit_cast(f32x2, k0u)), eb2 = cexp(hi * 128.f, lo * k1);
    asm volatile("s_cmp_lt_u32 %3, 2\n\ts_cbranch_scc0 1f\n\t" TAB_A "s_branch 2f\n1:\n\t" TAB_B "\n2:"
                 : "+{v[100:101]}"(cc), "+{v[102:103]}"(ea), "+{v[104:105]}"(eb2) : "s"(sub) : TAB_CLOB, "scc", "memory");
    if (sub == 2) *(volatile LAS unsigned*)(L + SM_ISC + 4 * ch) = (unsigned)(254 - se) << 23;
  }
  SCHED_FENCE();
  const int vt = tid - 256;
  const int uoff = WS_UP + b * 16384 + vt * 16, soff = WS_ST + vt * 16;
  u32x4 v0, v1, st0, st1;
  auto ldx = [&](auto first) {
    v0 = ldg16(rs, uoff);
    v1 = ldg16(rs, uoff, 8192);
    st0 = ldg16<first ? STA : LAUX>(rs, soff);
    st1 = ldg16<first ? STA : LAUX>(rs, soff, 8192);
    SCHED_FENCE();
  };
  if (S2M && wave < 4) pf4(S2M);
  if (is_ln && tid >= 184 && wave < 4) dma16((const char*)a4[0] + (tid - 184) * 16, (unsigned)(size_t)L + SM_WR + (wave - 3) * 1024 + 128);
  TWA(3);
  asm volatile("s_waitcnt lgkmcnt(0)" ::: "memory");
  __syncthreads();
  const f32x4 zero4 = {0.f, 0.f, 0.f, 0.f};
  f32x2 sb[2][3];
  f32x2 isc[3];
  const LAS char *yp, *yp2;
  int zoff, zoff2;
  unsigned t0;
  if (sub == 0) {
    half8 td[17];
    {
      half8 fa[8], fc[8], fb[8];
#pragma unroll
      for (int ks = 0; ks < 8; ks++) { fa[ks] = ld_frag(pa + 64 * ks); fb[ks] = ld_frag(pb + 64 * ks); fc[ks] = ld_frag(pa2 + 64 * ks); }
      SCHED_FENCE();
      f32x4 f = zero4, e = zero4;
#pragma unroll
      for (int ks = 0; ks < 8; ks++) { f = MFMA16(fa[ks], fb[ks], f); e = MFMA16(fc[ks], fb[ks], e); }
      unsigned d0 = pkh(f[0], f[1]);
      const unsigned d1 = pkh(f[2], f[3]), d2 = pkh(e[0], e[1]), d3 = pkh(e[2], e[3]);
      d0 = __builtin_amdgcn_perm(d0, d0, ks0);
      const unsigned n3 = (unsigned)__builtin_amdgcn_update_dpp((int)(d0 & 0xffff0000u), (int)d3, 0x130, 0xf, 0xf, false);
      const unsigned n2 = (unsigned)__builtin_amdgcn_update_dpp(0, (int)d2, 0x130, 0xf, 0xf, true);
      *(volatile LAS u32x4*)kd = u32x4{d0, d1, d2, d3};
      const unsigned a0 = __builtin_amdgcn_alignbit(d0, n3, 16), a1 = __builtin_amdgcn_alignbit(d1, d0, 16), a2 = __builtin_amdgcn_alignbit(d2, d1, 16);
      *(volatile LAS u32x4*)(kd + RDS) = u32x4{a0, a1, a2, __builtin_amdgcn_alignbit(d3, d2, 16)};
      *(volatile LAS u32x4*)(kd + 2 * RDS) = u32x4{n3, d0, d1, d2};
      *(volatile LAS u32x4*)(kd + 3 * RDS) = u32x4{__builtin_amdgcn_alignbit(n3, n2, 16), a0, a1, a2};
      if (lane == 63) *(volatile LAS u16*)(kd - 2) = (u16)d0;
      TWA(4);
#pragma unroll
      for (int i = 7; i >= 1; i--) { td[i] = ld_td(kd2, i); td[i + 8] = ld_td(kd2, i + 8); }
      td[8] = ld_td(kd2, 8);
      td[16] = ld_td(kd2, 16);
    }
    TWA(5);
    asm volatile("" : : "v"(s0v));
    __builtin_amdgcn_s_waitcnt(0x0F70);
    __syncthreads();
    t0 = (unsigned)__builtin_amdgcn_s_memrealtime();
    TSX(0, 5);
    TWA(6);
    conv_dense(td, kd2, bA, K + KC_R5 + 64, uf16, yb + SM_YD);
  } else {
  u32x2 r_dsk;
  if (wave >= 4) {
    unsigned o = 6 * b & ~3;
    asm volatile("" : "+v"(o));
    typedef u32x2 u32x2u __attribute__((aligned(4)));
    r_dsk = *(GAS const u32x2u*)(a8[7] + o);
    ldx(std::true_type{});
  }
  half8 td[8];
  {
    auto w32 = [](LAS char* p, unsigned v) { *(volatile LAS unsigned*)p = v; };
    if (sub == 1) {
      half8 fa[8];
#pragma unroll
      for (int ks = 0; ks < 8; ks++) fa[ks] = ld_frag(pa + 64 * ks);
      SCHED_FENCE();
      const half8 re = __builtin_bit_cast(half8, u32x4{0x3c00u, 0x3c00u, 0x3c00u, 0x3c00u});
      f32x4 f = zero4;
#pragma unroll
      for (int ks = 0; ks < 8; ks++) f = MFMA16(fa[ks], re, f);
      const unsigned d1 = pkh(f[2], f[3]);
      if (c15 == 0) *(LAS u32x2*)kd = u32x2{pkh(f[0], f[1]), d1};
      if (lane == 32) *(LAS u16*)(kd + RDS + 8) = (u16)(d1 >> 16);
      TWA(4);
    } else if (sub == 3) {
      half8 fa[8], fb[8];
#pragma unroll
      for (int ks = 0; ks < 8; ks++) { fa[ks] = ld_frag(pa + 64 * ks); fb[ks] = ld_frag(pb + 64 * ks); }
      SCHED_FENCE();
      f32x4 f = zero4;
#pragma unroll
      for (int ks = 0; ks < 8; ks++) f = MFMA16(fa[ks], fb[ks], f);
      const unsigned d0 = pkh(f[0], f[1]), d1 = pkh(f[2], f[3]);
      const unsigned w0 = __builtin_amdgcn_perm(d1, d0, ks0), w1 = __builtin_amdgcn_perm(d1, d0, ks1);
      w32(kd, w0);
      w32(kd2, w1);
      TWA(4);
      asm volatile("" ::: "memory");
      const LAS unsigned* t5 = (const LAS unsigned*)pa2;
#pragma unroll
      for (int e = 0; e < 3; e++) {
        td[e] = ld_toep<8>(t5 + 16 * (2 - e));
        td[3 + e] = ld_toep<8>(t5 + (KC_R3 - KC_R5) / 4 + 16 * (2 - e));
      }
      if (c15 >= 8) {
        *(LAS u16*)(kd + 190) = (u16)w0;
        *(LAS u16*)(kd + 192) = (u16)(w0 >> 16);
        *(LAS u16*)(kd + (KC_R7 - KC_R9) + 190) = (u16)w1;
        *(LAS u16*)(kd + (KC_R7 - KC_R9) + 192) = (u16)(w1 >> 16);
      }
    } else {
      TWA(4);
    }
  }
  SCHED_FENCE();
  if (wave >= 4) {
    const u32x4 iscv = *(const LAS u32x4*)(L + SM_ISC);
    yp = L + ((wave >> 1) - 2) * YT + ((vt >> 1) & 3) * 320 + ((vt >> 3) & 15) * 16 + (vt & 1) * 8;
    yp2 = yp + 2 * TABB;
    zoff = WS_ZP + ((vt >> 3) * NCONV + b) * 128 + (vt & 7) * 16;
    zoff2 = zoff + 64 * NCONV * 128;
    asm volatile("" : "+v"(yp), "+v"(yp2), "+v"(zoff), "+v"(zoff2));
    _Pragma("unroll 1") for (int spin = 0; spin < SPIN_CAP; spin++) {
      const bool ok = v0[3] == tag && v1[3] == tag && st0[3] == tag && st1[3] == tag;
      if (__builtin_expect(__all(ok), 1)) break;
      __builtin_amdgcn_s_sleep(4);
      if (!ok) ldx(std::false_type{});
    }
    TWA(5);
    float dsk[3];
#pragma unroll
    for (int c = 0; c < 3; c++) dsk[c] = __uint_as_float(__builtin_amdgcn_perm(r_dsk[1], r_dsk[0], 0x01000c0cu + 0x02020000u * (c + (b & 1))));
    LAS char* dst = L + SM_US + 2 * UPAD + 4 * vt;
    auto norm = [&](const u32x4& a, const u32x4& st, f32x2 (&t)[3], LAS char* d) {
      const f32x2 ri = {__uint_as_float(st[0]), __uint_as_float(st[1])};
      const f32x2 cc = -unpk(st[2]) * ri;
#pragma unroll
      for (int c = 0; c < 3; c++) {
        const f32x2 xf = unpk(a[c]);
        const f32x2 uf = __builtin_elementwise_fma(xf, ri, cc);
        *(LAS unsigned*)(d + c * CH2) = pkc(uf);
        t[c] = __builtin_elementwise_fma(uf, f32x2{dsk[c], dsk[c]}, xf);
        asm volatile("" : "+v"(t[c]));
      }
    };
    if (is_ln && (vt >> 3) == (b & 63)) *(LAS unsigned*)(L + SM_MX + (vt & 7) * 4) = b < 64 ? st0[2] : st1[2];
    norm(v0, st0, sb[0], dst);
    norm(v1, st1, sb[1], dst + 2048);
#pragma unroll
    for (int c = 0; c < 3; c++) {
      const float t = __int_as_float(__builtin_amdgcn_readfirstlane(iscv[c]));
      isc[c] = f32x2{t, t};
      asm volatile("" : "+s"(isc[c]));
    }
  } else {
    TWA(5);
  }
  if (sub == 1) __builtin_amdgcn_s_setprio(3);
  asm volatile("" : : "v"(s0v));
  __builtin_amdgcn_s_waitcnt(0x0F70);
  __syncthreads();
  t0 = (unsigned)__builtin_amdgcn_s_memrealtime();
  TWA(6);
  {
    if (sub == 1) {
      if (wave == 4 && lane < 8) __builtin_amdgcn_raw_buffer_store_b64(u32x2{t0, tag}, rs, WS_CK + lane * 2048 + b * 8, 0, SC1);
      auto pf3 = [&](int m) {
        if (m & 16)
          dma4s(a4[3], ((8 * xg + (sl & 7)) * 648 + DR0 * 72 + ((sl >> 3) * 3 + ch) * ((9 - DR0) * 8) + min(lane, (9 - DR0) * 8 - 1)) * 128,
                (unsigned)(size_t)L + SM_DUMMY);
#pragma unroll
        for (int i = 0; i < 4; i++)
          if (m >> i & 1) prefetch(i);
      };
      if (!SENT1) pf3(S3M);
      if (is_ln) {
        const u32x4 ones = {~0u, ~0u, ~0u, ~0u};
#pragma unroll
        for (int i = 0; i < 3; i++) stg16(rs, WS_PART + b * 9216 + vt * 16 + i * 3072, ones);
        stg16(rs, WS_XS + b * (2 * XSS) + vt * 16, ones);
        if (vt < 96) stg16(rs, WS_XS + b * (2 * XSS) + 3072 + vt * 16, ones);
        if (b < 6) stg16(rs, WS_XS + 2 * NLN * XSS + b * 3072 + vt * 16, ones);
      }
      if (SENT1) pf3(S3M);
      if (S4M) {
        while ((unsigned)__builtin_amdgcn_s_memrealtime() - t0 < S4D) __builtin_amdgcn_s_sleep(1);
        pf3(S4M);
      }
      __builtin_amdgcn_s_setprio(0);
    } else if (sub == 3) {
      f32x4 a2[6], w[4];
#pragma unroll
      for (int i = 0; i < 6; i++) a2[i] = zero4;
      half8 uw[12];
#pragma unroll
      for (int jj = 0; jj < 6; jj++) uw[jj] = ld_frag(uf16 - 48 + 256 * jj);
      SCHED_FENCE();
      w[0] = MFMA16(uw[0], bA, zero4); w[1] = MFMA16(uw[1], bA, zero4);
      w[2] = MFMA16(uw[0], bB, zero4); w[3] = MFMA16(uw[1], bB, zero4);
      half8 X5 = pack_win(w[0], w[1]), X3 = pack_win(w[2], w[3]);
      SCHED_FENCE();
#pragma unroll
      for (int jj = 0; jj < 6; jj++) {
        if (jj < 5) {
          w[0] = MFMA16(uw[2 * jj + 2], bA, zero4);
          if (jj < 3) uw[2 * jj + 6] = ld_frag(uf16 - 48 + 256 * (2 * jj + 6));
          SCHED_FENCE();
          w[1] = MFMA16(uw[2 * jj + 3], bA, zero4);
          if (jj < 3) uw[2 * jj + 7] = ld_frag(uf16 - 48 + 256 * (2 * jj + 7));
          SCHED_FENCE();
          w[2] = MFMA16(uw[2 * jj + 2], bB, zero4);
        }
        if (jj > 0) *(LAS f32x4*)(yb + SM_Y2 + (1 + jj) * YT) = a2[jj - 1];
        SCHED_FENCE();
        if (jj < 5) w[3] = MFMA16(uw[2 * jj + 3], bB, zero4);
        SCHED_FENCE();
        u32x4 n5, n3;
#pragma unroll
        for (int q = 0; q < 6; q++) {
          const int e = q >> 1;
          if (jj + e < 6) a2[jj + e] = MFMA16((q & 1) ? X3 : X5, td[3 * (q & 1) + e], a2[jj + e]);
          if (jj < 5 && q < 4) {
            u32x4& nn = q < 2 ? n5 : n3;
            nn[2 * (q & 1)] = pkh(w[q][0], w[q][1]);
            nn[2 * (q & 1) + 1] = pkh(w[q][2], w[q][3]);
          }
          SCHED_FENCE();
        }
        if (jj < 5) {
          X5 = __builtin_bit_cast(half8, n5);
          X3 = __builtin_bit_cast(half8, n3);
        }
      }
      *(LAS f32x4*)(yb + SM_Y2 + 7 * YT) = a2[5];
    } else {
      half8 uw[8];
#pragma unroll
      for (int i = 0; i < 4; i++) {
        uw[2 * i] = ld_frag(uf16 + 512 * i - 48);
        uw[2 * i + 1] = ld_frag(uf16 + 512 * i - 32);
      }
      SCHED_FENCE();
      f32x4 w[8], a3[4];
#pragma unroll
      for (int e = 0; e < 4; e++) {
        const LAS unsigned* t9 = opaque_lds(kd);
        td[e] = ld_toep<8>(t9 + 8 * (3 - e));
        td[4 + e] = ld_toep<8>(t9 + (KC_R7 - KC_R9) / 4 + 8 * (3 - e));
      }
#pragma unroll
      for (int i = 0; i < 4; i++) {
        w[2 * i] = MFMA16(uw[2 * i], bA, zero4);
        w[2 * i + 1] = MFMA16(uw[2 * i + 1], bB, zero4);
      }
      const half8 X90 = pack_win(w[0], w[2]), X70 = pack_win(w[1], w[3]), X91 = pack_win(w[4], w[6]), X71 = pack_win(w[5], w[7]);
#pragma unroll
      for (int t = 0; t < 4; t++) {
        a3[t] = MFMA16(X90, td[t], zero4);
        a3[t] = MFMA16(X70, td[4 + t], a3[t]);
        if (t >= 2) {
          a3[t] = MFMA16(X91, td[t - 2], a3[t]);
          a3[t] = MFMA16(X71, td[4 + t - 2], a3[t]);
        }
      }
#pragma unroll
      for (int t = 0; t < 4; t++) *(LAS f32x4*)(yb + SM_Y3 + (4 + t) * YT) = a3[t];
    }
  }
  }
  TWA(7);
  __syncthreads();
  const unsigned long long t_end = __builtin_amdgcn_s_memrealtime();
  TS(0, 6);
  TWB(2);
  if (wave >= 4) {
    auto ld = [&](int c, int off) { return *(const volatile LAS f32x2*)((c < 2 ? yp + c * TABB : yp2) + off); };
    auto epi = [&](auto mid) {
      f32x2 ya[3], yc[3], yb0[3], yb1[3], yb2[3];
#pragma unroll
      for (int c = 0; c < 3; c++) {
        ya[c] = ld(c, SM_YD);
        if (mid) yc[c] = ld(c, SM_Y2);
      }
#pragma unroll
      for (int c = 0; c < 3; c++) {
        yb0[c] = ld(c, 4 * YT + SM_YD);
        yb1[c] = ld(c, 4 * YT + SM_Y2);
        yb2[c] = ld(c, 4 * YT + SM_Y3);
      }
      SCHED_FENCE();
      u32x4 o, o2;
#pragma unroll
      for (int c = 0; c < 3; c++) {
        const f32x2 z = __builtin_elementwise_fma(mid ? ya[c] + yc[c] : ya[c], isc[c], sb[0][c]);
        o[c] = pkbf(z[0], z[1]);
      }
      o[3] = tag;
      stg16(rs, zoff, o);
#pragma unroll
      for (int c = 0; c < 3; c++) {
        const f32x2 z = __builtin_elementwise_fma((yb0[c] + yb1[c]) + yb2[c], isc[c], sb[1][c]);
        o2[c] = pkbf(z[0], z[1]);
      }
      o2[3] = tag;
      stg16(rs, zoff2, o2);
    };
    if (wave >= 8) epi(std::true_type{});
    else epi(std::false_type{});
  }
  TS(0, 7);
  TWB(3);
  TWFLUSH();
  unsigned long long t_all = t_end;
  auto get_lag = [&]() {
    u32x4 p, q;
    _Pragma("unroll 1") for (int spin = 0; spin < SPIN_CAP; spin++) {
      p = ldg16(rs, WS_CK + (b & 7) * 2048 + lane * 16);
      q = ldg16(rs, WS_CK + (b & 7) * 2048 + 1024 + hl * 16);
      if (__all(p[1] == tag && p[3] == tag && q[1] == tag && q[3] == tag)) break;
      __builtin_amdgcn_s_sleep(4);
    }
    const int lag = max(max(max((int)(p[0] - t0), (int)(p[2] - t0)), max((int)(q[0] - t0), (int)(q[2] - t0))), 0);
    t_all += min(wave_max_u32((unsigned)lag), 1000u);
  };
  auto wait_until = [&](int t) {
    while ((long long)(__builtin_amdgcn_s_memrealtime() - t_all) < t) __builtin_amdgcn_s_sleep(1);
  };
  LAS unsigned* const selp = (LAS unsigned*)(L + SM_ROW + 256);
  unsigned long long t_key = 0;
  auto select_rows = [&]() {
    const SelMasks sm(lane);
    get_lag();
    wait_until(DLY_L);
    selp[key_slot(lane)] = select8(rs, tag, lane, sm, ws, bid);
    t_key = __builtin_amdgcn_s_memrealtime();
  };
  if (is_ln) {
    f32x2 wf[6], hv[6];
    const unsigned mxp = *(const LAS unsigned*)(L + SM_MX + (tid & 7) * 4);
#pragma unroll
    for (int i = 0; i < 6; i++) {
      const float t = bflo(*(const LAS u16*)(L + SM_WR + (tid >> 3) * 12 + i * 2));
      wf[i] = f32x2{t, t};
    }
    float wsum = 0.f, mxr = bflo(*(const LAS u16*)(L + SM_MX + (lane >> 2) * 2));
    unsigned one2 = 0x3f803f80u, sel0 = 0x00003f80u, sel1 = 0x3f800000u;
    asm volatile("" : "+s"(one2), "+s"(sel0), "+s"(sel1));
    if (wave == 0) {
#pragma unroll
      for (int k = 0; k < 9; k++) wsum = dot2(*(const LAS unsigned*)(L + SM_WR + (hl + 32 * k) * 4), one2, wsum);
      wsum = red32_sum(wsum);
      __builtin_amdgcn_s_setprio(2);
    }
#pragma unroll
    for (int i = 0; i < 6; i++) {
      hv[i] = -unpk(mxp);
      asm volatile("" : "+v"(hv[i]), "+v"(wf[i]));
    }
    LAS char* redw = L + SM_RED + wave * 256 + (wave >= 6 ? 128 : 0) + (lane & 7) * 32 + (lane >> 5) * 16 + ((lane >> 4) & 1) * 8;
    const LAS char* redr = L + SM_RED + ((lane >> 1) & 1) * (6 * 256 + 128) + (lane >> 2) * 16 + (lane & 1) * 8;
    unsigned kidx = (unsigned)(SEQ - 1 - 16 * b - (lane >> 2)) << 2;
    int kro = WS_KR + (lane & 7) * 2048 + b * 16, xso = WS_XS + b * (2 * XSS) + (tid >> 3) * 12;
    asm volatile("" : "+v"(wsum), "+v"(mxr), "+v"(redw), "+v"(redr), "+v"(kidx), "+v"(kro), "+v"(xso));
    if (wave == 1) {
      get_lag();
      wait_until(DLY_H);
    }
    __syncthreads();
    TWB(4);
    {
      const int off = WS_ZP + b * (NCONV * 128) + (tid >> 3) * 256 + (tid & 7) * 16;
      v0 = ldg16(rs, off);
      v1 = ldg16(rs, off + 128);
      _Pragma("unroll 1") for (int spin = 0; spin < SPIN_CAP; spin++) {
        if (__builtin_expect(__all(v0[3] == tag && v1[3] == tag), 1)) break;
        __builtin_amdgcn_s_sleep(4);
        if (v0[3] != tag) v0 = ldg16(rs, off);
        if (v1[3] != tag) v1 = ldg16(rs, off + 128);
      }
    }
    TS(0, 8);
    TWB(5);
    {
      f32x2 ss, qq, pp;
#pragma unroll
      for (int i = 0; i < 6; i++) {
        const unsigned t = i < 3 ? v0[i] : v1[i - 3];
        hv[i] = f32x2{dot2(t, sel0, hv[i][0]), dot2(t, sel1, hv[i][1])};
        ss = i ? ss + hv[i] : hv[i];
        qq = i ? __builtin_elementwise_fma(hv[i], hv[i], qq) : hv[i] * hv[i];
        pp = i ? __builtin_elementwise_fma(hv[i], wf[i], pp) : hv[i] * wf[i];
      }
      float s0 = ss[0], s1 = ss[1], q0 = qq[0], q1 = qq[1], p0 = pp[0], p1 = pp[1];
      asm volatile(
          "s_nop 1\n\tv_permlane32_swap_b32 %0, %1\n\tv_permlane32_swap_b32 %2, %3\n\tv_permlane32_swap_b32 %4, %5\n\t"
          "v_add_f32 %0, %0, %1\n\tv_add_f32 %2, %2, %3\n\tv_add_f32 %4, %4, %5\n\ts_nop 0\n\t"
          "v_permlane16_swap_b32 %0, %2\n\tv_add_f32_dpp %1, %4, %4 row_ror:8 row_mask:0xf bank_mask:0xf\n\t"
          "v_add_f32 %2, %0, %2\n\ts_nop 1\n\tv_add_f32_dpp %0, %2, %2 row_ror:8 row_mask:0xf bank_mask:0xf"
          : "+v"(s0), "+v"(s1), "+v"(q0), "+v"(q1), "+v"(p0), "+v"(p1));
      *(LAS f32x2*)redw = f32x2{s0, s1};
    }
    TWB(6);
    if (wave == 11 && lane == 0 && !(b & 1)) __builtin_amdgcn_raw_buffer_store_b32(tag, rs, WS_PF + (b >> 1) * 128, 0, SC1);
    __syncthreads();
    if (wave == 0) {
      const volatile LAS f32x2* rp = (const volatile LAS f32x2*)redr;
      const f32x2 t0 = rp[0], t1 = rp[32], t2 = rp[64], t3 = rp[96], t4 = rp[128], t5 = rp[160];
      const f32x2 t = ((t0 + t1) + (t2 + t3)) + (t4 + t5);
      float a = t[0], p = t[1];
      asm volatile("" : "+v"(a), "+v"(p));
      a += dppf<0x4E>(a);
      p += dppf<0x4E>(p);
      p += dppf<0xB1>(p);
      const float mv = dppf<0x00>(a) * (1.0f / DM);
      const float rinv = ln_rinv(dppf<0x55>(a), mv);
      const float dl = rbf(mxr + mv) - mxr;
      const unsigned rb = pkbf(0.f, fmaf(-dl, wsum, p) * rinv);
      const unsigned key = ((rb ^ (unsigned)(((int)rb >> 31) | (int)0x80000000)) & 0xffff0000u) | kidx;
      unsigned k1 = max(key, dppu<0x141>(key)), k2 = min(key, dppu<0x141>(key)), k3;
      {
        const unsigned lo = min(k1, dppu<0x140>(k1)), hi = max(k2, dppu<0x140>(k2));
        k1 = max(k1, dppu<0x140>(k1));
        k2 = max(lo, hi);
        k3 = min(lo, hi);
      }
      {
        const unsigned lo = min(k1, dppu<0x142>(k1)), hi = max(k2, dppu<0x142>(k2)), w = max(k3, dppu<0x142>(k3));
        k1 = max(k1, dppu<0x142>(k1));
        k2 = max(lo, hi);
        k3 = max(min(lo, hi), w);
      }
      {
        const unsigned lo = min(k1, dppu<0x143>(k1)), hi = max(k2, dppu<0x143>(k2)), w = max(k3, dppu<0x143>(k3));
        k1 = max(k1, dppu<0x143>(k1));
        k2 = max(lo, hi);
        k3 = max(min(lo, hi), w);
      }
      if (lane >= 56) stg16(rs, kro, u32x4{k1, k2 | 1u, k3, tag});
      SCHED_FENCE();
      const unsigned second = (unsigned)__builtin_amdgcn_readlane((int)k2, 63);
      if ((lane & 3) == 0) {
        LAS float* ri = (LAS float*)(L + SM_ROW + (lane >> 3) * 16 + (lane & 4));
        ri[0] = -dl * rinv;
        ri[2] = rinv;
        const unsigned fk = key | (key > second ? 0u : (key == second ? 1u : 2u));
        *(LAS unsigned*)(L + SM_ROW + 128 + lane) = fk;
        __builtin_amdgcn_raw_buffer_store_b64(u32x2{fk, tag}, rs, WS_KF + b * 128 + lane * 2, 0, SC1);
      }
      __builtin_amdgcn_s_setprio(0);
    }
    TS(0, 9);
    TWB(7);
    __syncthreads();
    {
      const f32x4 ri = *(const LAS f32x4*)(L + SM_ROW + (tid & 7) * 16);
      const u32x2 kk = *(const LAS u32x2*)(L + SM_ROW + 128 + (tid & 7) * 8);
      const f32x2 c2 = {ri[0], ri[1]}, r2 = {ri[2], ri[3]};
      unsigned wp[6];
#pragma unroll
      for (int i = 0; i < 6; i++) {
        const f32x2 w2 = __builtin_elementwise_fma(hv[i], r2, c2);
        wp[i] = pkbf(w2[0], w2[1]);
      }
#pragma unroll
      for (int i = 0; i < 2; i++)
        if (!(kk[i] & 2)) {
          u32x3 o;
#pragma unroll
          for (int k = 0; k < 3; k++) o[k] = min(__builtin_amdgcn_perm(wp[2 * k + 1], wp[2 * k], i ? 0x07060302u : 0x05040100u), 0xfffffffeu);
          __builtin_amdgcn_raw_buffer_store_b96(o, rs, xso + (int)(kk[i] & 1) * XSS, 0, SC1);
        }
      SCHED_FENCE();
      LAS char* dst = L + SM_T2 + (tid & 7) * T2S + (tid >> 3) * 48;
#pragma unroll
      for (int i = 0; i < 6; i += 2) *(LAS u32x4*)(dst + 8 * i) = u32x4{i < 3 ? v0[i] : v1[i - 3], wp[i], i < 2 ? v0[i + 1] : v1[i - 2], wp[i + 1]};
    }
    __syncthreads();
    const int pr = tid / 72, ck = tid - pr * 72;
    u32x4 hq[2], wq[2];
    u32x2 key = {2u, 2u};
    auto put_row = [&](int i, int slot) {
      const int o = key_addr(key[i], slot) + ck * 16;
      const u32x4 cap = {0xfffffffeu, 0xfffffffeu, 0xfffffffeu, 0xfffffffeu};
      if (key[i] & 2) stg16(rs, o, __builtin_elementwise_min(wq[i], cap));
      stg16(rs, o + DM * 2, __builtin_elementwise_min(hq[i], cap));
    };
    if (wave < 9) {
      key = *(const LAS u32x2*)(L + SM_ROW + 128 + pr * 8);
#pragma unroll
      for (int k = 0; k < 4; k++) {
        const u32x4 t = *(const LAS u32x4*)(L + SM_T2 + pr * T2S + ck * 64 + k * 16);
        hq[0][k] = __builtin_amdgcn_perm(t[2], t[0], 0x05040100u);
        hq[1][k] = __builtin_amdgcn_perm(t[2], t[0], 0x07060302u);
        wq[0][k] = __builtin_amdgcn_perm(t[3], t[1], 0x05040100u);
        wq[1][k] = __builtin_amdgcn_perm(t[3], t[1], 0x07060302u);
      }
#pragma unroll
      for (int i = 0; i < 2; i++)
        if (!(key[i] & 2)) put_row(i, 0);
    } else if (wave == 9) {
      select_rows();
    }
    __syncthreads();
    TS(0, 10);
    if (wave < 9) {
#pragma unroll
      for (int i = 0; i < 2; i++) {
        int slot = 8;
#pragma unroll
        for (int r = 0; r < 8; r++) slot = selp[r] == key[i] ? r : slot;
        if (slot < 8) {
          if (key[i] & 2) put_row(i, slot);
        } else {
          u32x4 o;
#pragma unroll
          for (int k = 0; k < 4; k++) {
            const f32x2 o2 = unpk(hq[i][k]) + unpk(wq[i][k]);
            o[k] = pkbf(o2[0], o2[1]);
          }
          *(GAS u32x4*)(out + (unsigned)((16 * b + 2 * pr + i) * (DM * 2) + ck * 16)) = o;
        }
      }
    }
  } else if (wave == 9) {
    select_rows();
  }
  if (b < RED0) {
    TS(0, 12);
    TC(0, 15);
    return;
  }
  {
    const int T = (b - RED0) >> 2, tp = (b - RED0) & 3;
    unsigned hv = 0u, wv = 0u, oo = 0u;
    float sg = 0.f;
    if (wave == 9) {
      const int slot = 2 * tp + ((lane >> 3) & 1);
      const unsigned key = *(volatile LAS unsigned*)(selp + slot);
      int o = key_addr(key, slot) + (8 * T + (lane & 7)) * 4;
      _Pragma("unroll 1") for (int spin = 0; spin < SPIN_CAP; spin++) {
        asm volatile("" : "+v"(o) : : "memory");
        wv = __builtin_amdgcn_raw_buffer_load_b32(rs, o, 0, SC1);
        hv = __builtin_amdgcn_raw_buffer_load_b32(rs, o + DM * 2, 0, SC1);
        if (__all(wv != ~0u && hv != ~0u)) break;
        __builtin_amdgcn_s_sleep(4);
      }
      const unsigned rb = key ^ ((int)key < 0 ? 0x80000000u : 0xffffffffu);
      sg = rbf(__builtin_amdgcn_rcpf(1.0f + __expf(-bfhi(rb))));
      oo = (unsigned)(key_row(key) * (DM * 2) + (8 * T + (lane & 7)) * 4);
      asm volatile("" : "+v"(sg), "+v"(oo));
      while (__builtin_amdgcn_s_memrealtime() - t_key < (unsigned long long)DLY_P) __builtin_amdgcn_s_sleep(1);
    }
    LAS float* red = (LAS float*)(L + SM_RED);
    LAS char* rw = (LAS char*)red + wave * 128 + (lane & 7) * 16 + (lane >> 4) * 4;
    asm volatile("" : "+v"(rw));
    __syncthreads();
    TS(0, 11);
    if (wave < 8) {
      u32x4 v;
      _Pragma("unroll 1") for (int spin = 0; spin < SPIN_CAP; spin++) {
        v = ldg16(rs, part_off);
        if (__builtin_expect(__all(v[0] != ~0u), 1)) break;
        __builtin_amdgcn_s_sleep(4);
      }
      auto r0 = __builtin_amdgcn_permlane32_swap(v[0], v[2], false, false);
      auto r1 = __builtin_amdgcn_permlane32_swap(v[1], v[3], false, false);
      const float s0 = __uint_as_float(r0[0]) + __uint_as_float(r0[1]), s1 = __uint_as_float(r1[0]) + __uint_as_float(r1[1]);
      auto r2 = __builtin_amdgcn_permlane16_swap(__float_as_uint(s0), __float_as_uint(s1), false, false);
      float a = __uint_as_float(r2[0]) + __uint_as_float(r2[1]);
      a += dppf<0x128>(a);
      *(LAS float*)rw = a;
    }
    __syncthreads();
    if (wave == 9 && lane < 16) {
      f32x2 p[8];
#pragma unroll
      for (int w = 0; w < 8; w++) p[w] = *(const LAS f32x2*)(red + w * 32 + lane * 2);
      const f32x2 y = ((p[0] + p[1]) + (p[2] + p[3])) + ((p[4] + p[5]) + (p[6] + p[7]));
      const f32x2 y2 = rbf2(rbf2(y) * sg);
      const f32x2 o2 = unpk(hv) + rbf2(unpk(wv) + y2);
      *(GAS unsigned*)(out + oo) = pkbf(o2[0], o2[1]);
      if (b == RED0 && lane == 0) __builtin_amdgcn_raw_buffer_store_b32(tag, rs, WS_EPOCH, 0, SC1);
    }
  }
  TS(0, 12);
  TC(0, 15);
}

}

extern "C" void kernel_launch(void* const* d_in, const int* in_sizes, int n_in, void* d_out, int out_size,
                              void* d_ws, size_t ws_size, hipStream_t stream) {
  k_all<<<NCONV + NFFN, NTHR, 0, stream>>>((const u16*)d_in[0], (const u16*)d_in[1], (const u16*)d_in[2], (const u16*)d_in[3],
                                             (const u16*)d_in[4], (const u16*)d_in[5], (char*)d_ws, (const u16*)d_in[6],
                                             (const u16*)d_in[8], (const u16*)d_in[9], (const u16*)d_in[10], (const u16*)d_in[11],
                                             (u16*)d_out);
}
```

```cpp
#include <hip/hip_runtime.h>
#include <stdint.h>
#include <type_traits>

#define PRA 0
#define CPF 212
#define WPAD 0
#define PFNT 0
#define WNT 0
#define WNTS ""
#define PTH 4
#define PTL 2
#define EB 0
#define LAUX 16
#define SAUX 16
#define PFA 0
#define XFL 0
#define CMAP 0
#define PRB 0
#define WORD 1
#define DR0 6
#define SENT1 0
#define S4M 0
#define S4D 50
#define XP47 0
#define XP8 1
#define SP47 0
#define SP8 1
#define LSKIP 0
#define DNT 1
#define SCN 0
#define SCX 0
#define ED 3
#define GNT 1
#define PP 1
#define PPS 6
#define S0M 0
#define S1M 32
#define S2M 0
#define S3M 0
#define STA 16
#define XM 0
#define XF 0
#define PFD 10
namespace {

constexpr int SEQ = 2048;
constexpr int DM = 576;
constexpr int NM = 128;
constexpr int FF = 4608;

constexpr int NTHR = 768;
constexpr int NFFN = 64;
constexpr int NPART = 80;
constexpr int NCONV = 192;
constexpr int NLN = 128;
constexpr int RED0 = 32;
constexpr int LATE0 = 176;

typedef unsigned short u16;
#define LAS __attribute__((address_space(3)))
#define GAS __attribute__((address_space(1)))
#define CAS __attribute__((address_space(4)))
using half8 = __attribute__((ext_vector_type(8))) _Float16;
using half2v = __attribute__((ext_vector_type(2))) _Float16;
using bf2v = __attribute__((ext_vector_type(2))) __bf16;
using bf8v = __attribute__((ext_vector_type(8))) __bf16;
using bf16v = __attribute__((ext_vector_type(16))) __bf16;
using s16x8 = __attribute__((ext_vector_type(8))) short;
using s16x4 = __attribute__((ext_vector_type(4))) short;
using f32x2 = __attribute__((ext_vector_type(2))) float;
using f32x4 = __attribute__((ext_vector_type(4))) float;
using u32x2 = __attribute__((ext_vector_type(2))) unsigned;
using u32x3 = __attribute__((ext_vector_type(3))) unsigned;
typedef u32x3 u32x3u __attribute__((aligned(4)));
using u32x4 = __attribute__((ext_vector_type(4))) unsigned;
using u64x2 = __attribute__((ext_vector_type(2))) unsigned long long;
using u64x8 = __attribute__((ext_vector_type(8))) unsigned long long;
using u64x4 = __attribute__((ext_vector_type(4))) unsigned long long;

constexpr int WS_EPOCH = 0;
constexpr int WS_PF = 4096;
constexpr int WS_KR = WS_PF + NFFN * 128;
constexpr int WS_KF = WS_KR + 8 * 2048;
constexpr int WS_ST = WS_KF + 16384;
constexpr int WS_CK = WS_ST + 16384;
constexpr int WS_FL = WS_CK + 8 * 2048;
constexpr int WS_UP = 131072;
constexpr int WS_ZP = WS_UP + NCONV * NLN * 128;
constexpr int WS_PART = WS_ZP + NCONV * NLN * 128;
constexpr int WS_XS = WS_PART + NPART * 8 * DM * 4;
constexpr int XSS = 4 * DM;
constexpr int WS_END = WS_XS + (2 * NLN + 8) * XSS;
static_assert(WS_FL + 8 * 1024 <= WS_UP, "ws");
constexpr size_t WS_TS = 64u << 20;
constexpr int SC1 = 16, NT = 2;
constexpr int SPIN_CAP = 1 << 16;

#define TW(k)
#define TWP(k)
#define TWA(k)
#define TWB(k)
#define TWDECL
#define TSX(k, s)
#define TWFLUSH()
#define TC(k, s)
#define TSW(w, s)
#define TSL(k, s)
#define TS(k, s)

#define TWK(k)
#define TWKV(k, ...)
__device__ __forceinline__ float bflo(unsigned v) { return __uint_as_float(v << 16); }
__device__ __forceinline__ float bfhi(unsigned v) { return __uint_as_float(v & 0xffff0000u); }
__device__ __forceinline__ unsigned pkbf(float a, float b) {
  f32x2 f = {a, b};
  return __builtin_bit_cast(unsigned, __builtin_convertvector(f, bf2v));
}
__device__ __forceinline__ unsigned pkh(float a, float b) {
  f32x2 f = {a, b};
  return __builtin_bit_cast(unsigned, __builtin_convertvector(f, half2v));
}
__device__ __forceinline__ float rbf(float f) { return __uint_as_float(pkbf(0.f, f)); }
__device__ __forceinline__ f32x2 rbf2(f32x2 v) { return f32x2{rbf(v[0]), rbf(v[1])}; }
__device__ __forceinline__ f32x2 unpk(unsigned v) { return f32x2{bflo(v), bfhi(v)}; }
__device__ __forceinline__ float dot2(unsigned a, unsigned b, float c) {
  return __builtin_amdgcn_fdot2_f32_bf16(__builtin_bit_cast(bf2v, a), __builtin_bit_cast(bf2v, b), c, false);
}
__device__ __forceinline__ float hadd(f32x2 v) {
  float a = v[0], b = v[1];
  asm volatile("" : "+v"(a), "+v"(b));
  return a + b;
}

template <int CTRL>
__device__ __forceinline__ float dppf(float v) {
  return __int_as_float(__builtin_amdgcn_update_dpp(0, __float_as_int(v), CTRL, 0xf, 0xf, true));
}
template <int CTRL>
__device__ __forceinline__ unsigned dppu(unsigned v) {
  return (unsigned)__builtin_amdgcn_update_dpp(0, (int)v, CTRL, 0xf, 0xf, true);
}
__device__ __forceinline__ float swap16_sum(float v) {
  auto r = __builtin_amdgcn_permlane16_swap(__float_as_uint(v), __float_as_uint(v), false, false);
  return __uint_as_float(r[0]) + __uint_as_float(r[1]);
}
__device__ __forceinline__ float swap32_sum(float v) {
  auto r = __builtin_amdgcn_permlane32_swap(__float_as_uint(v), __float_as_uint(v), false, false);
  return __uint_as_float(r[0]) + __uint_as_float(r[1]);
}
__device__ __forceinline__ float red32_sum(float v) {
  v += dppf<0xB1>(v);
  v += dppf<0x4E>(v);
  v += dppf<0x141>(v);
  v += dppf<0x140>(v);
  return swap16_sum(v);
}
#define SCHED_FENCE() __builtin_amdgcn_sched_barrier(0)
#define DRAIN() asm volatile("s_waitcnt vmcnt(0)" ::: "memory")

__device__ __forceinline__ float red16_sum(float v) {
  v += dppf<0xB1>(v);
  v += dppf<0x4E>(v);
  v += dppf<0x141>(v);
  return v + dppf<0x140>(v);
}
__device__ __forceinline__ float ln_rinv(float q, float dm) {
  const float var = fmaxf(fmaf(q, 1.0f / DM, -dm * dm), 0.f);
  return __builtin_amdgcn_rcpf(rbf(__builtin_amdgcn_sqrtf(rbf(rbf(var) + 1e-5f))));
}

constexpr int TAB_STRIDE = 544;
constexpr int TAB_ROWS = 58;
constexpr int TR_M = 26, TR_F = 34, TR_H = 42;
constexpr int TABB = TAB_ROWS * TAB_STRIDE;
constexpr int SM_KC = 3 * TABB;
constexpr int RDS = 336 * 4;
constexpr int KC_RD = SM_KC;
constexpr int KC_R5 = KC_RD + 4 * RDS;
constexpr int KC_R3 = KC_R5 + 64 * 4;
constexpr int KC_R9 = KC_R3 + 64 * 4;
constexpr int KC_R7 = KC_R9 + 96 * 4;
constexpr int SM_US = KC_R7 + 96 * 4;
constexpr int UPAD = 256;
constexpr int CH2 = SM_US + (UPAD + SEQ) * 2 - SM_KC;
static_assert(SM_US + 2 * UPAD <= SM_KC + 8192 && 8192 <= CH2 && 2 * TABB + 256 < 65536, "zero fill");
constexpr int YT = 1280;
constexpr int SM_YD = 0, SM_Y2 = 8 * YT - 2 * YT, SM_Y3 = 14 * YT - 4 * YT;
constexpr int SM_T2 = 0, T2S = 4624;
constexpr int SM_RED = SM_KC + 3 * CH2;
constexpr int SM_ROW = SM_RED + 3200;
constexpr int SM_ISC = SM_ROW + 128 + 128 + 32;
constexpr int SM_MX = SM_ISC + 16;
constexpr int SM_WR = SM_MX + 32;
constexpr int SM_DUMMY = SM_WR + DM * 2;
constexpr int RG = 8192;
constexpr int XRS = 1216;
constexpr int FS_SEL = 19 * RG;
constexpr int FS_GU = FS_SEL + 32;
constexpr int SM_TOTAL = FS_GU + 8 * 160;
static_assert(SM_DUMMY + 256 <= SM_TOTAL && SM_ISC % 16 == 0 && CH2 % 16 == 0 && TABB % 16 == 0, "lds");

typedef f32x2 cf;
#define TM(t, a, b) "v_pk_mul_f32 v[" #t ":" #t "+1], v[" #b ":" #b "+1], v[" #a ":" #a "+1] op_sel:[1,0]\n\t"
#define TF(d, a, b, t, n) \
  "v_pk_fma_f32 v[" #d ":" #d "+1], v[" #a ":" #a "+1], v[" #b ":" #b "+1], v[" #t ":" #t "+1] op_sel:[0,0,1] op_sel_hi:[1,0,0] neg_" #n ":[0,0,1]\n\t"
#define TC16(c, v) "v_cvt_pk_f16_f32 v" #c ", v[" #v "], v[" #v "+1]\n\t"
#define TWR(c, row) "ds_write_addtid_b32 v" #c " offset:544*" #row "\n\t"
#define TAB_A \
  TC16(134, 100) TM(106, 102, 102) TM(108, 100, 104) TWR(134, 0) TF(110, 102, 102, 106, lo) TF(112, 100, 104, 108, lo) \
  TM(114, 100, 102) TM(116, 100, 110) TC16(135, 112) TF(118, 100, 102, 114, lo) TF(120, 100, 110, 116, lo) TWR(135, 8) \
  TC16(136, 118) TC16(137, 120) TM(122, 112, 110) TWR(136, 1) TM(124, 118, 104) TF(126, 112, 110, 122, lo) TWR(137, 2) \
  TF(128, 118, 104, 124, lo) TC16(134, 126) TM(130, 126, 110) TC16(135, 128) TWR(134, 10) TF(132, 126, 110, 130, lo) \
  TM(106, 118, 110) TWR(135, 9) TC16(136, 132) TF(108, 118, 110, 106, lo) TM(114, 132, 104) TWR(136, 12) TC16(137, 108) \
  TF(102, 132, 104, 114, lo) TM(116, 132, 110) TWR(137, 3) TC16(134, 102) TF(100, 132, 110, 116, lo) TM(122, 102, 104) TWR(134, 18) \
  TC16(135, 100) TF(124, 102, 104, 122, lo) TM(130, 128, 110) TWR(135, 14) TC16(136, 124) TF(106, 128, 110, 130, lo) \
  TM(118, 124, 104) TWR(136, 19) TC16(137, 106) TF(114, 124, 104, 118, lo) TM(116, 100, 104) TWR(137, 11) TC16(134, 114) \
  TF(132, 100, 104, 116, lo) TM(122, 114, 104) TWR(134, 26) TC16(135, 132) TF(102, 114, 104, 122, lo) TM(130, 120, 110) \
  TWR(135, 17) TC16(136, 102) TF(128, 120, 110, 130, lo) TM(118, 108, 110) TWR(136, 28) TC16(137, 128) TF(124, 108, 110, 118, lo) \
  TM(116, 106, 110) TWR(137, 4) TC16(134, 124) TF(100, 106, 110, 116, lo) TM(122, 112, 104) TWR(134, 5) TC16(135, 100) \
  TF(114, 112, 104, 122, lo) TM(130, 126, 104) TWR(135, 13) TC16(136, 114) TF(120, 126, 104, 130, lo) TM(118, 132, 110) \
  TWR(136, 21) TC16(137, 120) TF(108, 132, 110, 118, lo) TM(116, 102, 104) TWR(137, 20) TC16(134, 108) TF(106, 102, 104, 116, lo) \
  TM(122, 128, 110) TWR(134, 16) TC16(135, 106) TF(112, 128, 110, 122, lo) TM(130, 124, 110) TWR(135, 30) TC16(136, 112) \
  TF(126, 124, 110, 130, lo) TM(118, 100, 110) TWR(136, 6) TC16(137, 126) TF(116, 100, 110, 118, lo) TM(102, 114, 104) TWR(137, 7) \
  TC16(134, 116) TF(122, 114, 104, 102, lo) TM(128, 120, 104) TWR(134, 15) TC16(135, 122) TF(112, 120, 104, 128, lo) \
  TM(130, 132, 104) TWR(135, 23) TC16(136, 112) TF(124, 132, 104, 130, lo) TM(126, 108, 104) TWR(136, 22) TC16(137, 124) \
  TF(118, 108, 104, 126, lo) TM(110, 106, 104) TWR(137, 25) TC16(134, 118) TF(100, 106, 104, 110, lo) TWR(134, 24) TC16(135, 100) \
  TWR(135, 32)
#define TAB_B \
  "v_mov_b32 v137, 0x3c00\n\t" TWR(137, 42) TC16(134, 102) TM(106, 104, 104) TM(108, 100, 102) TWR(134, 43) \
  TF(110, 104, 104, 106, lo) TF(112, 100, 102, 108, hi) TM(114, 102, 102) TM(116, 110, 110) TF(118, 110, 110, 116, lo) \
  TF(120, 102, 102, 114, lo) TC16(135, 120) TM(122, 112, 118) TM(124, 120, 102) TWR(135, 44) TF(126, 112, 118, 122, lo) \
  TF(128, 120, 102, 124, lo) TM(130, 120, 120) TC16(136, 126) TC16(137, 128) TF(132, 120, 120, 130, lo) TWR(136, 27) \
  TM(106, 126, 104) TC16(134, 132) TWR(137, 45) TF(108, 126, 104, 106, lo) TM(100, 126, 110) TWR(134, 46) TC16(135, 108) \
  TF(116, 126, 110, 100, lo) TM(114, 108, 110) TWR(135, 29) TC16(136, 116) TF(122, 108, 110, 114, lo) TM(112, 102, 132) \
  TWR(136, 31) TC16(137, 122) TF(124, 102, 132, 112, lo) TM(130, 120, 132) TWR(137, 33) TC16(134, 124) TF(106, 120, 132, 130, lo) \
  TM(104, 128, 132) TWR(134, 47) TC16(135, 106) TF(100, 128, 132, 104, lo) TM(114, 132, 132) TWR(135, 48) TC16(136, 100) \
  TF(110, 132, 132, 114, lo) TM(112, 124, 132) TWR(136, 49) TC16(137, 110) TF(102, 124, 132, 112, lo) TM(130, 106, 132) \
  TWR(137, 50) TC16(134, 102) TF(120, 106, 132, 130, lo) TM(104, 100, 132) TWR(134, 51) TC16(135, 120) TF(128, 100, 132, 104, lo) \
  TM(114, 126, 118) TWR(135, 52) TC16(136, 128) TF(112, 126, 118, 114, lo) TM(124, 108, 118) TWR(136, 53) TC16(137, 112) \
  TF(130, 108, 118, 124, lo) TM(106, 116, 118) TWR(137, 34) TC16(134, 130) TF(104, 116, 118, 106, lo) TM(100, 122, 118) \
  TWR(134, 35) TC16(135, 104) TF(114, 122, 118, 100, lo) TM(126, 110, 132) TWR(135, 36) TC16(136, 114) TF(124, 110, 132, 126, lo) \
  TM(108, 102, 132) TWR(136, 37) TC16(137, 124) TF(106, 102, 132, 108, lo) TM(116, 120, 132) TWR(137, 54) TC16(134, 106) \
  TF(100, 120, 132, 116, lo) TM(122, 128, 132) TWR(134, 55) TC16(135, 100) TF(126, 128, 132, 122, lo) TM(110, 112, 118) \
  TWR(135, 56) TC16(136, 126) TF(124, 112, 118, 110, lo) TM(108, 130, 118) TWR(136, 57) TC16(137, 124) TF(102, 130, 118, 108, lo) \
  TM(106, 104, 118) TWR(137, 38) TC16(134, 102) TF(116, 104, 118, 106, lo) TM(120, 114, 118) TWR(134, 39) TC16(135, 116) \
  TF(100, 114, 118, 120, lo) TWR(135, 40) TC16(136, 100) TWR(136, 41)
#define TAB_CLOB "v106", "v107", "v108", "v109", "v110", "v111", "v112", "v113", "v114", "v115", "v116", "v117", "v118", "v119", "v120", "v121", "v122", "v123", "v124", "v125", "v126", "v127", "v128", "v129", "v130", "v131", "v132", "v133", "v134", "v135", "v136", "v137"
__device__ __forceinline__ unsigned pkc(cf a) { return __builtin_bit_cast(unsigned, __builtin_convertvector(a, half2v)); }
__device__ __forceinline__ cf cexp(f32x2 hi, f32x2 lo) {
  float mag = __builtin_amdgcn_exp2f(hi[0]);
  mag = fmaf(mag, lo[0], mag);
  const float fr = __builtin_amdgcn_fractf(hi[1]) + lo[1];
  return cf{__builtin_amdgcn_cosf(fr), __builtin_amdgcn_sinf(fr)} * mag;
}
__device__ __forceinline__ unsigned wave_max_u32(unsigned v) {
  v = max(v, (unsigned)__builtin_amdgcn_update_dpp(0, (int)v, 0x111, 0xf, 0xf, false));
  v = max(v, (unsigned)__builtin_amdgcn_update_dpp(0, (int)v, 0x112, 0xf, 0xf, false));
  v = max(v, (unsigned)__builtin_amdgcn_update_dpp(0, (int)v, 0x114, 0xf, 0xf, false));
  v = max(v, (unsigned)__builtin_amdgcn_update_dpp(0, (int)v, 0x118, 0xf, 0xf, false));
  v = max(v, (unsigned)__builtin_amdgcn_update_dpp(0, (int)v, 0x142, 0xa, 0xf, false));
  v = max(v, (unsigned)__builtin_amdgcn_update_dpp(0, (int)v, 0x143, 0xc, 0xf, false));
  return (unsigned)__builtin_amdgcn_readlane((int)v, 63);
}

#define MFMA16(a, b, c) __builtin_amdgcn_mfma_f32_16x16x32_f16((a), (b), (c), 0, 0, 0)
#define MFMAB(a, b, c) __builtin_amdgcn_mfma_f32_16x16x32_bf16((a), (b), (c), 0, 0, 0)

__device__ __forceinline__ half8 ld_frag(const LAS char* p) { return *(const LAS half8*)p; }
__device__ __forceinline__ const LAS unsigned* opaque_lds(const LAS char* p) {
  const LAS unsigned* q = (const LAS unsigned*)p;
  asm volatile("" : "+v"(q));
  return q;
}
template <int O2>
__device__ __forceinline__ half8 ld_toep(const LAS unsigned* q) {
  u32x4 v = {q[0], q[1], q[O2], q[O2 + 1]};
  return __builtin_bit_cast(half8, v);
}
__device__ __forceinline__ half8 band_frag(int lane, unsigned pat) {
  unsigned t = __builtin_amdgcn_ubfe(pat << (lane & 15), 8 * (lane >> 4), 8);
  t |= t << 15;
  u32x4 v;
#pragma unroll
  for (int j = 0; j < 4; j++) v[j] = __umul24(t & (0x10001u << (2 * j)), 0x3c00u >> (2 * j));
  return __builtin_bit_cast(half8, v);
}
__device__ __forceinline__ half8 pack_win(f32x4 a, f32x4 b) {
  u32x4 v = {pkh(a[0], a[1]), pkh(a[2], a[3]), pkh(b[0], b[1]), pkh(b[2], b[3])};
  return __builtin_bit_cast(half8, v);
}

template <int V>
using IC = std::integral_constant<int, V>;
template <int S, int E, class F>
__device__ __forceinline__ void static_for(F&& f) {
  if constexpr (S < E) {
    f(std::integral_constant<int, S>{});
    static_for<S + 1, E>(f);
  }
}
__device__ __forceinline__ half8 ld_td(const LAS char* tp, int i) {
  const volatile LAS u32x2* tb = (const volatile LAS u32x2*)tp;
  const u32x2 lo = tb[8 * (16 - i)], hi = tb[8 * (16 - i) + 1];
  return __builtin_bit_cast(half8, __builtin_shufflevector(lo, hi, 0, 1, 2, 3));
}
__device__ __forceinline__ void conv_dense(half8 (&td)[17], half8 tmask, const LAS char* f1024, const LAS char* ub, LAS char* yd) {
  constexpr int S0 = 9, NS = 73, PF = PFD;
  const f32x4 zero4 = {0.f, 0.f, 0.f, 0.f};
  f32x4 acc[8] = {zero4, zero4, zero4, zero4, zero4, zero4, zero4, zero4};
  half8 u[NS], te;
  unsigned fe = 0;
#pragma unroll
  for (int s = S0; s < S0 + PF; s++) {
    u[s] = ld_frag(ub + 64 * s);
    if (s == S0 + 3) fe = *(const LAS u16*)f1024;
    SCHED_FENCE();
  }
  static_for<S0, NS>([&](auto sc) {
    constexpr int s = decltype(sc)::value, t0 = s < 16 ? 0 : (s - 9) / 8, t1 = s / 8 < 7 ? s / 8 : 7;
    acc[t0] = MFMA16(td[16 + 8 * t0 - s], u[s], acc[t0]);
    SCHED_FENCE();
    if constexpr (s + PF < NS) u[s + PF] = ld_frag(ub + 64 * (s + PF));
    if constexpr (s == 67) *(LAS f32x4*)(yd + 6 * YT) = acc[6];
    SCHED_FENCE();
    if constexpr (t0 + 1 <= t1) acc[t0 + 1] = MFMA16(td[24 + 8 * t0 - s], u[s], acc[t0 + 1]);
    SCHED_FENCE();
    if constexpr (s >= 24 && (s & 7) == 0 && s < 72) *(LAS f32x4*)(yd + (s / 8 - 3) * YT) = acc[s / 8 - 3];
    if constexpr (s == 14) {
      fe |= fe << 16;
      te = tmask * __builtin_bit_cast(half8, u32x4{fe, fe, fe, fe});
    }
    SCHED_FENCE();
    if constexpr (t0 + 2 <= t1) acc[t0 + 2] = MFMA16(td[32 + 8 * t0 - s], u[s], acc[t0 + 2]);
    if constexpr (s >= 16 && s <= 40 && (s & 7) == 0) acc[s / 8 + 2] = MFMA16(te, u[s], acc[s / 8 + 2]);
    SCHED_FENCE();
  });
  *(LAS f32x4*)(yd + 7 * YT) = acc[7];
}

typedef __amdgpu_buffer_rsrc_t rsrc_t;
template <int AUX = LAUX>
__device__ __forceinline__ u32x4 ldg16(rsrc_t rs, int off, int so = 0) {
  asm volatile("" : "+s"(so) : : "memory");
  return __builtin_amdgcn_raw_buffer_load_b128(rs, off, so, AUX);
}
__device__ __forceinline__ void stg16(rsrc_t rs, int off, u32x4 v) { __builtin_amdgcn_raw_buffer_store_b128(v, rs, off, 0, SAUX); }
__device__ __forceinline__ void dma16(const char* g, unsigned lds) {
  unsigned keep;
  asm volatile("s_mov_b32 %0, m0\n\ts_mov_b32 m0, %2\n\ts_nop 0\n\tglobal_load_lds_dwordx4 %1, off" WNTS "\n\ts_mov_b32 m0, %0"
               : "=&s"(keep) : "v"(g), "s"(lds) : "memory");
}
__device__ __forceinline__ void dma16nt(const char* g, unsigned lds) {
  unsigned keep;
  asm volatile("s_mov_b32 %0, m0\n\ts_mov_b32 m0, %2\n\ts_nop 0\n\tglobal_load_lds_dwordx4 %1, off nt\n\ts_mov_b32 m0, %0"
               : "=&s"(keep) : "v"(g), "s"(lds) : "memory");
}
__device__ __forceinline__ void dma4(const char* g, unsigned lds) {
  unsigned keep;
  asm volatile("s_mov_b32 %0, m0\n\ts_mov_b32 m0, %2\n\ts_nop 0\n\tglobal_load_lds_dword %1, off\n\ts_mov_b32 m0, %0"
               : "=&s"(keep) : "v"(g), "s"(lds) : "memory");
}
__device__ __forceinline__ void dma4s(size_t base, unsigned off, unsigned lds) {
  unsigned keep;
  asm volatile("s_mov_b32 %0, m0\n\ts_mov_b32 m0, %3\n\ts_nop 0\n\tglobal_load_lds_dword %1, %2\n\ts_mov_b32 m0, %0"
               : "=&s"(keep) : "v"(off), "s"(base), "s"(lds) : "memory");
}
__device__ __forceinline__ void wait_ticks(int t) {
  const unsigned long long t0 = __builtin_amdgcn_s_memrealtime();
  while (__builtin_amdgcn_s_memrealtime() - t0 < (unsigned long long)t) __builtin_amdgcn_s_sleep(1);
}
#define UPOS 1
#define DLY_X 30
#define DLY_W 0
#define HB 1
#define DLY_H 55
#define DLY_R 0
#define DLY_L 260
#define DLY_P 120

__device__ __forceinline__ int key_row(unsigned k) { return (int)(~(k >> 2) & (SEQ - 1)); }
__device__ __forceinline__ int key_slot(int lane) { return (lane & 3) | ((lane >> 1) & 4); }
__device__ __forceinline__ int key_addr(unsigned k, int slot) {
  return WS_XS + ((k & 2) ? 2 * NLN + slot : (int)((~(k >> 5) & (2 * NLN - 2)) | (k & 1))) * XSS;
}
struct SelMasks {
  unsigned m[15];
  __device__ __forceinline__ SelMasks(int lane) {
    const unsigned b0 = -(lane & 1), b1 = -((lane >> 1) & 1), b3 = -((lane >> 3) & 1), b4 = -((lane >> 4) & 1), b5 = -(lane >> 5);
    const unsigned t[15] = {~(b0 ^ b1), ~(b1 ^ b3), ~(b0 ^ b3), ~b3, ~b1, ~b0, b3, b1, b0, ~(b3 ^ b4), ~(b1 ^ b4), ~(b0 ^ b4), ~(b3 ^ b5), ~(b1 ^ b5), ~(b0 ^ b5)};
#pragma unroll
    for (int i = 0; i < 15; i++) {
      m[i] = t[i];
      asm volatile("" : "+v"(m[i]));
    }
  }
};
template <int CTRL>
__device__ __forceinline__ unsigned cex(unsigned v, unsigned m) {
  const unsigned o = dppu<CTRL>(v);
  return max(min(v, o), min(max(v, o), m));
}
__device__ __forceinline__ unsigned clean8(unsigned v, const unsigned* m) { return cex<0xB1>(cex<0x4E>(cex<0x128>(v, m[0]), m[1]), m[2]); }
__device__ __forceinline__ unsigned select8(rsrc_t rs, unsigned tag, int lane, const SelMasks& sm, char* ws, int bid) {
  const int off = WS_KR + (bid & 7) * 2048 + lane * 16;
  u32x4 a, c;
  _Pragma("unroll 1") for (int spin = 0; spin < SPIN_CAP; spin++) {
    a = ldg16(rs, off);
    c = ldg16(rs, off, 1024);
    if (__builtin_expect(__all(a[3] == tag && c[3] == tag), 1)) break;
    __builtin_amdgcn_s_sleep(2);
  }
  TSL(1, 8);
  const unsigned t1 = min(a[0], c[0]), t2 = max(a[1], c[1]), third = max(a[2], c[2]);
  unsigned m1 = max(a[0], c[0]), m2 = max(t1, t2), m3 = min(t1, t2);
  unsigned x = clean8(cex<0xB1>(cex<0x4E>(cex<0xB1>(m1, sm.m[0]), sm.m[1]), sm.m[2]), sm.m + 3);
  const unsigned y = clean8(cex<0xB1>(cex<0x4E>(cex<0xB1>(m2, sm.m[0]), sm.m[1]), sm.m[2]), sm.m + 6);
  x = clean8(max(x, y), sm.m + 3);
  x = clean8(max(x, dppu<0x140>(x)), sm.m + 9);
  {
    auto r = __builtin_amdgcn_permlane16_swap(x, x, false, false);
    x = clean8(max(r[0], r[1]), sm.m + 12);
    auto q = __builtin_amdgcn_permlane32_swap(x, x, false, false);
    x = max(q[0], q[1]);
  }
  unsigned k = min(x, dppu<0xB1>(x));
  k = min(k, dppu<0x4E>(k));
  k = min(k, dppu<0x128>(k));
  if (__builtin_expect(__any(max(m3, third) > k), 0)) {
    const int slot = key_slot(lane);
    unsigned m4 = min(a[1], c[1]);
    _Pragma("unroll 1") for (int r = 0; r < 8; r++) {
      k = wave_max_u32(m1);
      x = slot == r ? k : x;
      const bool e = m1 == k;
      m1 = e ? m2 : m1;
      m2 = e ? m3 : m2;
      m3 = e ? m4 : m3;
      m4 = e ? 0u : m4;
    }
    if (__any(third > k)) {
      k = 0u;
      _Pragma("unroll 1") for (int r = 0; r < 8; r++) {
        unsigned v;
        _Pragma("unroll 1") for (int spin = 0; spin < SPIN_CAP; spin++) {
          bool ok = true;
          v = 0u;
          _Pragma("unroll 4") for (int i = 0; i < 16; i++) {
            const u32x4 f = ldg16(rs, WS_KF + lane * 16, i * 1024);
            ok = ok && f[1] == tag && f[3] == tag;
            v = max(v, max(f[0] <= k - 1u ? f[0] : 0u, f[2] <= k - 1u ? f[2] : 0u));
          }
          if (__all(ok)) break;
          __builtin_amdgcn_s_sleep(1);
        }
        k = wave_max_u32(v);
        x = slot == r ? k : x;
      }
    }
  }
  return x;
}

__device__ __forceinline__ int late_group(size_t wg, int bid) { return (3 - (int)(wg >> 7) - bid) & 7; }
__device__ __forceinline__ int ffn_group(size_t wg, int bid) {
  const int l = late_group(wg, bid), i = bid >> 3;
  return (bid & 7) * 9 + (bid < NFFN ? (i == l ? 8 : i) : l);
}
__device__ __forceinline__ void down_dma(size_t wd, int j, LAS char* L, int wave, int lane, int r0, int r1) {
#pragma unroll
  for (int k = 0; k < 3; k++) {
    const int slot = 3 * (wave - 9) + k, t = slot * 64 + lane, r8 = t / 72, q8 = (t - r8 * 72) ^ (r8 & 2);
    const char* p = (const char*)wd + (unsigned)((j * 64 + r8) * 1152);
    _Pragma("unroll 1") for (int i = r0; i < r1; i++)
        (DNT ? dma16nt : dma16)(p + (unsigned)(i * 9216 + (q8 ^ ((i & 1) << 2)) * 16), (unsigned)(size_t)L + slot * 1024 + i * 9216);
  }
}
__device__ __forceinline__ unsigned stats_role(size_t argp, unsigned cpf, LAS char* L, unsigned long long r_entry) {
  const int tid = threadIdx.x, lane = tid & 63, bid = blockIdx.x, fw = bid;
  size_t x, wsp, wd, wg;
  asm volatile(
      "s_waitcnt lgkmcnt(0)\n\ts_load_dwordx2 %0, %4, 0x0\n\ts_load_dwordx2 %1, %4, 0x30\n\ts_load_dwordx2 %2, %4, 0x58\n\t"
      "s_load_dwordx2 %3, %4, 0x48"
      : "=&s"(x), "=&s"(wsp), "=&s"(wd), "=&s"(wg) : "s"(argp));
  SCHED_FENCE();
  unsigned xo0 = (bid * 32 + (tid >> 4)) * (DM * 2) + (lane & 15) * 8;
  asm volatile("" : "+v"(xo0));
  asm volatile("s_waitcnt lgkmcnt(0)" : "+s"(x), "+s"(wsp), "+s"(wd), "+s"(wg));
  const int wave = __builtin_amdgcn_readfirstlane(tid >> 6);
  char* const ws = (char*)(GAS char*)wsp;
  const rsrc_t rs = __builtin_amdgcn_make_buffer_rsrc(ws, 0, WS_END, 0x00020000);
  unsigned tagv = WS_EPOCH;
  asm volatile("global_load_dword %0, %0, %1 sc1" : "+v"(tagv) : "s"(wsp) : "memory");
  SCHED_FENCE();
  unsigned tag;
  if (wave >= 8) {
    if (ED) {
      __builtin_amdgcn_s_barrier();
      if (wave > 8) down_dma(wd, ffn_group(wg, bid), L, wave, lane, 0, ED);
      else DRAIN();
    }
    asm volatile("s_waitcnt vmcnt(%2)" : "+v"(tagv) : "v"(cpf), "n"(3 * ED));
    tag = __builtin_amdgcn_readfirstlane(tagv) + 1u;
    asm volatile("" : "+s"(tag));
  } else {
    u32x2 xw[9];
#pragma unroll
    for (int k = 0; k < 9; k++) xw[k] = *(GAS const u32x2*)(x + xo0 + 128 * k);
    SCHED_FENCE();
    if (ED) __builtin_amdgcn_s_barrier();
    unsigned one2 = 0x3f803f80u;
    asm volatile("" : "+s"(one2));
    float s0 = 0.f, s1 = 0.f, q0 = 0.f, q1 = 0.f;
#pragma unroll
    for (int k = 0; k < 9; k++) {
      const unsigned a = xw[k][0], c = xw[k][1];
      s0 = dot2(a, one2, s0);
      q0 = dot2(a, a, q0);
      s1 = dot2(c, one2, s1);
      q1 = dot2(c, c, q1);
    }
    const float mean = red16_sum(s0 + s1) * (1.0f / DM), mbf = rbf(mean);
    float q = red16_sum(q0 + q1), dm = mean;
    if (__builtin_expect(__any(!(q * (1.0f / DM) >= mean * mean * 1.002f)), 0)) {
      f32x2 q2 = {0.f, 0.f};
#pragma unroll
      for (int k = 0; k < 18; k++) {
        const f32x2 dd = unpk(xw[k >> 1][k & 1]) - mbf;
        q2 = __builtin_elementwise_fma(dd, dd, q2);
      }
      q = red16_sum(hadd(q2));
      dm = mean - mbf;
    }
    float ri = ln_rinv(q, dm);
    asm volatile("" : "+v"(tagv), "+v"(ri));
    auto rr = __builtin_amdgcn_permlane16_swap(__float_as_uint(ri), __float_as_uint(ri), false, false);
    auto m2 = __builtin_amdgcn_permlane16_swap(__float_as_uint(mbf), __float_as_uint(mbf), false, false);
    const unsigned mp = __builtin_amdgcn_perm(m2[1], m2[0], 0x07060302u);
    if ((lane & 31) == 0) stg16(rs, WS_ST + (16 * fw + (tid >> 5)) * 16, u32x4{rr[0], rr[1], mp, tagv + 1u});
    tag = __builtin_amdgcn_readfirstlane(tagv) + 1u;
    asm volatile("" : : "v"(cpf), "s"(tag));
  }
  TS(1, 10);
  return tag;
}

__device__ __forceinline__ void ffn_role(size_t argp, unsigned tag, LAS char* L) {
  const int tid = threadIdx.x, lane = tid & 63, bid = blockIdx.x;
  size_t wsp, wdp;
  u64x2 gu;
  asm volatile("s_load_dwordx2 %0, %3, 0x30\n\ts_load_dwordx4 %1, %3, 0x48\n\ts_load_dwordx2 %2, %3, 0x58" : "=&s"(wsp), "=&s"(gu), "=&s"(wdp) : "s"(argp));
  SCHED_FENCE();
  const int wave = __builtin_amdgcn_readfirstlane(tid >> 6);
  const int hm = bid < NFFN ? 3 : (bid & 8 ? 2 : 1);
  const bool mine = wave < 8 && (hm >> (wave >> 2) & 1);
  const int g = lane >> 4, tq = (lane & 15) >> 2, tp = lane & 3;
  const bool full = hm == 3;
  const int r8 = tid / 72;
  const int gr = full ? tid >> 4 : tid >> 3;
  const int gc = full ? (lane & 7) ^ ((gr & 1) | ((gr >> 1) & 6)) : (hm & 2) * 2 + ((lane & 3) ^ ((gr >> 2) & 3));
  unsigned go = gr * (FF * 2) + gc * 16;
  asm volatile("" : "+v"(go));
  asm volatile("s_waitcnt lgkmcnt(0)" : "+s"(wsp), "+s"(gu), "+s"(wdp));
  char* const ws = (char*)(GAS char*)wsp;
  const rsrc_t rs = __builtin_amdgcn_make_buffer_rsrc(ws, 0, WS_END, 0x00020000);
  const int j = ffn_group(gu[0], bid);
  const char* gsrc = (const char*)((lane & (full ? 8 : 4)) ? gu[1] : gu[0]) + (go + j * 128);
  const unsigned lb = (unsigned)(size_t)L + wave * 1024 + 9 * RG;
  auto issue_gu = [&](int n, unsigned step) {
    if (wave < 8) {
      _Pragma("unroll 1") for (int i = 0; i < n; i++) {
        (GNT ? dma16nt : dma16)(gsrc, lb + i * RG);
        gsrc += step;
      }
    }
  };
  __syncthreads();
  TS(1, 11);
  if (DLY_W) wait_ticks(DLY_W);
  issue_gu(full ? 10 : 9, full ? 32 * FF * 2 : 64 * FF * 2);
  if (wave > 8) down_dma(wdp, j, L, wave, lane, full ? ED : (hm & 2) * 2, hm == 1 ? 4 : 8);
  s16x4 fa[18][2], fd[3][4];
  const int fr = 16 * (g >> 1) + 4 * (g & 1) + (tq & 1) + 8 * (tq >> 1), fx = (g & 1) | ((tq >> 1) << 1);
  const LAS char* gup = L + 9 * RG + (tp & 1) * 8 +
                        (full ? fr * 256 + ((wave ^ ((tq & 1) | (fx << 1))) + 8 * (tp >> 1)) * 16 : fr * 128 + (((wave & 3) ^ fx) + 4 * (tp >> 1)) * 16);
  const LAS char* gup2 = gup + 8 * RG;
  asm volatile("" : "+v"(gup), "+v"(gup2));
  auto read_gu = [&](auto ks0, auto n, auto reg0, auto sz) {
#pragma unroll
    for (int i = 0; i < n; i++)
#pragma unroll
      for (int h = 0; h < 2; h++) {
        const int o = reg0 * RG + i * sz + h * (sz / 16);
        fa[ks0 + i][h] = __builtin_amdgcn_ds_read_tr16_b64_v4i16((LAS s16x4*)(o < 8 * RG ? gup + o : gup2 + (o - 8 * RG)));
      }
  };
  auto read_down = [&]() {
    const int f = (tq >> 1) | ((g & 1) << 1);
#pragma unroll
    for (int t = 0; t < 3; t++) {
      const LAS char* p = L + (8 * g + tq) * 1152 + ((3 * wave + t) ^ f) * 32 + tp * 8;
#pragma unroll
      for (int h = 0; h < 4; h++)
        fd[t][h] = __builtin_amdgcn_ds_read_tr16_b64_v4i16((LAS s16x4*)(p + (32 * (h >> 1) + 4 * (h & 1)) * 1152));
    }
  };
  if (full) {
    if (wave < 8) asm volatile("s_waitcnt vmcnt(2)" ::: "memory");
    __syncthreads();
    if (mine) read_gu(IC<0>{}, IC<8>{}, IC<0>{}, IC<RG>{});
    __syncthreads();
    TS(1, 6);
    issue_gu(8, 32 * FF * 2);
    if (wave > 8) DRAIN();
    asm volatile("s_waitcnt vmcnt(4)" ::: "memory");
    __syncthreads();
    TS(1, 1);
    if (mine) {
      read_gu(IC<8>{}, IC<2>{}, IC<8>{}, IC<RG>{});
      read_gu(IC<10>{}, IC<4>{}, IC<0>{}, IC<RG>{});
    }
  } else {
    DRAIN();
    __syncthreads();
    TS(1, 1);
    if (mine) read_gu(IC<0>{}, IC<14>{}, IC<0>{}, IC<RG / 2>{});
  }
  read_down();
  const int c15 = lane & 15;
  const int xs = r8, xc = tid - xs * 72;
  const LAS char* selr = L + FS_SEL + (xs & 7) * 4;
  LAS char* xw2 = L + xs * XRS + xc * 16;
  const LAS char* xr = L + (lane & 7) * XRS + (c15 >> 3) * 16 + g * 32;
  LAS char* guw = L + FS_GU + (4 * (g & 1) + 2 * (g >> 1) + (c15 >> 3)) * 160 + (wave * 8 + (lane & 7)) * 2;
  const LAS char* gur = L + FS_GU + (lane & 7) * 160 + 16 * g;
  int xo = xc * 16, po = WS_PART + (bid < NFFN ? bid : bid - LATE0) * (8 * DM * 4) + 3 * wave * 512 + (lane & 7) * 64 + 16 * g;
  int sidx = c15 < 8 ? 0x4444 : 0xeeee;
  asm volatile("" : "+v"(selr), "+v"(guw), "+v"(gur), "+v"(xo), "+v"(po), "+v"(sidx), "+v"(xw2), "+v"(xr));
  DRAIN();
  TS(1, 2);
  if (wave == 9) {
    const SelMasks sm(lane);
    LAS char* selw = L + FS_SEL + key_slot(lane) * 4;
    asm volatile("" : "+v"(selw));
    int spin = 0;
    if (!LSKIP || bid < NFFN) _Pragma("unroll 1") for (; spin < SPIN_CAP; spin++) {
      const unsigned t = ldg16(rs, WS_PF + (bid & 63) * 128)[0];
      if (__builtin_amdgcn_readfirstlane(t) == tag) break;
      __builtin_amdgcn_s_sleep(2);
    }
    if (spin) wait_ticks(DLY_X);
    TSL(1, 3);
    *(LAS int*)selw = key_addr(select8(rs, tag, lane, sm, ws, bid), key_slot(lane));
  }
  __syncthreads();
  TS(1, 9);
  if (wave < 9) {
    xo += *(const LAS int*)selr;
    u32x4 v = ldg16(rs, xo);
    SCHED_FENCE();
    if (mine) {
      if (full) read_gu(IC<14>{}, IC<4>{}, IC<4>{}, IC<RG>{});
      else read_gu(IC<14>{}, IC<4>{}, IC<7>{}, IC<RG / 2>{});
    }
    SCHED_FENCE();
    _Pragma("unroll 1") for (int spin = 0; spin < SPIN_CAP; spin++) {
      if (__builtin_expect(__all(max(max(v[0], v[1]), max(v[2], v[3])) != ~0u), 1)) break;
      __builtin_amdgcn_s_sleep(2);
      v = ldg16(rs, xo);
    }
    *(LAS u32x4*)xw2 = v;
  }
  __syncthreads();
  TS(1, 4);
  if (mine) {
    bf8v xf[9];
#pragma unroll
    for (int i = 0; i < 9; i++) xf[i] = *(const LAS bf8v*)(xr + 128 * i);
    SCHED_FENCE();
    f32x4 acc = {0.f, 0.f, 0.f, 0.f};
#pragma unroll
    for (int i = 0; i < 9; i++) {
      const s16x8 lo = __builtin_shufflevector(fa[2 * i][0], fa[2 * i][1], 0, 1, 2, 3, 4, 5, 6, 7);
      const s16x8 hi = __builtin_shufflevector(fa[2 * i + 1][0], fa[2 * i + 1][1], 0, 1, 2, 3, 4, 5, 6, 7);
      acc = __builtin_amdgcn_smfmac_f32_16x16x64_bf16(
          xf[i], __builtin_bit_cast(bf16v, __builtin_shufflevector(lo, hi, 0, 1, 2, 3, 4, 5, 6, 7, 8, 9, 10, 11, 12, 13, 14, 15)), acc, sidx, 0, 0);
    }
    const float a0 = acc[0], a1 = acc[1], a2 = acc[2], a3 = acc[3];
    auto r0 = __builtin_amdgcn_permlane32_swap(__float_as_uint(a0), __float_as_uint(a2), false, false);
    auto r1 = __builtin_amdgcn_permlane32_swap(__float_as_uint(a1), __float_as_uint(a3), false, false);
    const float s0 = __uint_as_float(r0[0]) + __uint_as_float(r0[1]), s1 = __uint_as_float(r1[0]) + __uint_as_float(r1[1]);
    const float gs = __int_as_float(__builtin_amdgcn_update_dpp(__float_as_int(s0), __float_as_int(s1), 0x128, 0xf, 0xc, false));
    const float us = __int_as_float(__builtin_amdgcn_update_dpp(__float_as_int(s1), __float_as_int(s0), 0x128, 0xf, 0x3, false));
    const float gp = rbf(gs);
    const float gq = rbf(gp * __builtin_amdgcn_rcpf(1.0f + __expf(-gp)));
    *(LAS u16*)guw = (u16)pkbf(gq * rbf(us), 0.f);
  }
  __syncthreads();
  {
    const bf8v b0 = *(const LAS bf8v*)gur, b1 = *(const LAS bf8v*)(gur + 64);
    const f32x4 z4 = {0.f, 0.f, 0.f, 0.f};
    f32x4 acc[3] = {z4, z4, z4};
    if (hm & 1) {
#pragma unroll
      for (int t = 0; t < 3; t++)
        acc[t] = MFMAB(__builtin_bit_cast(bf8v, __builtin_shufflevector(fd[t][0], fd[t][1], 0, 1, 2, 3, 4, 5, 6, 7)), b0, z4);
    }
    if (hm & 2) {
#pragma unroll
      for (int t = 0; t < 3; t++)
        acc[t] = MFMAB(__builtin_bit_cast(bf8v, __builtin_shufflevector(fd[t][2], fd[t][3], 0, 1, 2, 3, 4, 5, 6, 7)), b1, acc[t]);
    }
    SCHED_FENCE();
    if (c15 < 8) {
#pragma unroll
      for (int t = 0; t < 3; t++) {
        u32x4 o = __builtin_bit_cast(u32x4, acc[t]);
        o[0] = min(o[0], 0xfffffffeu);
        stg16(rs, po + t * 512, o);
      }
    }
  }
  TS(1, 5);
}

__global__ __launch_bounds__(NTHR, 3) void k_all(const u16*, const u16*, const u16*, const u16*, const u16*, const u16*, char*, const u16*,
                                                 const u16*, const u16*, const u16*, const u16*, u16*) {
  __shared__ __attribute__((aligned(16))) char smem[SM_TOTAL];
  LAS char* const L = (LAS char*)smem;
  const int bid = blockIdx.x;
  const size_t argp = (size_t)__builtin_amdgcn_kernarg_segment_ptr();
  u64x8 a8;
  u64x4 a4;
  size_t a1;
  unsigned cpf;
  {
    size_t pc;
    int t;
    asm volatile("s_load_dwordx16 %0, %3, 0x0\n\ts_load_dwordx8 %1, %3, 0x40\n\ts_load_dwordx2 %2, %3, 0x60" : "=&s"(a8), "=&s"(a4), "=&s"(a1) : "s"(argp));
    asm volatile(
        "s_getpc_b64 %1\n\ts_lshr_b32 %2, %3, 3\n\tv_min_u32 %0, 6, %4\n\ts_mul_i32 %2, %2, 7\n\tv_add_lshl_u32 %0, %0, %2, 7\n\t"
        "global_load_dword %0, %0, %1"
        : "=&v"(cpf), "=&s"(pc), "=&s"(t) : "s"(bid), "v"(threadIdx.x) : "scc");
  }
  SCHED_FENCE();
  const int tid = threadIdx.x, lane = tid & 63, b = bid - NFFN;
  unsigned ftag;
  if (b < 0) {
    ftag = stats_role(argp, cpf, L, 0);
  } else {
  const int wave = __builtin_amdgcn_readfirstlane(tid >> 6);
  const bool is_ln = b < NLN;
  const int hl = lane & 31;
  const int ch = (wave & 3) < 3 ? (wave & 3) : (wave >> 2), sub = (wave & 3) < 3 ? (wave >> 2) : 3;
  const int d = 3 * b + ch;
  const int xt = 4 * b + wave, hrow = 8 * ((xt >> 1) & 127) + (lane >> (3 + XM));
  const int grp = ((xt >> 8) << 5) + ((xt & 1) << 4) + (lane & (XM ? 15 : 7));
  unsigned xo = hrow * (DM * 4) + grp * 12, po = d * NM * 2 + lane * 4, lo = 2 * d & ~3;
  int cpo = WS_UP + (grp * 2048 + hrow) * 16;
  {
    LAS char* zc = L + (wave >> 2) * CH2 + SM_KC + 16 * (tid & 255);
    const u32x4 z = {0u, 0u, 0u, 0u};
    *(LAS u32x4*)zc = z;
    *(LAS u32x4*)(zc + 4096) = z;
  }
  const int xg = b & 7, sl = b >> 3, sl12 = sl < 12 ? sl : sl - 12;
  unsigned s0o = (144 * min(lane >> 4, S0M - 1) + 12 * sl12 + wave) * (FF * 2) + xg * 1152 + min(lane & 15, 8) * 128;
  if (S0M) asm volatile("" : "+v"(s0o));
  unsigned dso = 6 * b & ~3;
  asm volatile("" : "+v"(xo), "+v"(po), "+v"(lo), "+v"(cpo), "+v"(dso));
  SCHED_FENCE();
  asm volatile("s_waitcnt lgkmcnt(0)" : "+s"(a8), "+s"(a4), "+s"(a1));
  u32x3 cp[4];
  auto load_x = [&]() {
    if (wave < 4) {
#pragma unroll
      for (int i = 0; i < 4; i++) cp[i] = *(GAS const u32x3u*)(a8[0] + xo + ((i & 1) * (DM * 2) + (i >> 1) * (XM ? DM * 16 : 96)));
    }
    SCHED_FENCE();
  };
  if (XF) load_x();
  const unsigned r_ldt = *(GAS const unsigned*)(a8[5] + lo), r_lar = *(GAS const unsigned*)(a8[1] + po), r_aim = *(GAS const unsigned*)(a8[2] + po);
  const unsigned r_cr = *(GAS const unsigned*)(a8[3] + po), r_ci = *(GAS const unsigned*)(a8[4] + po);
  typedef u32x2 u32x2u __attribute__((aligned(4)));
  u32x2 r_dsk = *(GAS const u32x2u*)(a8[7] + dso);
  char* const ws = (char*)(GAS char*)a8[6];
  GAS char* const out = (GAS char*)a1;
  const rsrc_t rs = __builtin_amdgcn_make_buffer_rsrc(ws, 0, WS_END, 0x00020000);
  const unsigned tagv = __builtin_amdgcn_raw_buffer_load_b32(rs, WS_EPOCH, 0, SC1);
  SCHED_FENCE();
  if (!XF) load_x();
  unsigned s0v = 0;
  if (S0M) {
    __builtin_amdgcn_s_barrier();
    s0v = *(GAS const unsigned*)((sl < 12 ? a4[1] : a4[2]) + s0o);
    SCHED_FENCE();
  }
  TWDECL;
  TWP(4);
  auto prefetch = [&](int i) {
    const char* base = (const char*)(sl < 12 ? a4[1] : a4[2]) + (144 * i + 48 * ch + 4 * sl12) * (FF * 2) + xg * 1152;
    dma4(base + (unsigned)((lane >> 4) * (FF * 2) + min(lane & 15, 8) * 128), (unsigned)(size_t)L + SM_DUMMY);
  };
  const int part_off = WS_PART + (tid >> 3) * (8 * DM * 4) + (b - RED0) * 128 + (tid & 7) * 16;
  LAS char *const C = L + ch * TABB, *const K = L + ch * CH2;
  const int c15 = lane & 15, g = lane >> 4;
  unsigned pfo = ((9 * xg + (sl & 7)) * 576 + DR0 * 72 + ((sl >> 3) * 4 + (wave & 3)) * ((8 - DR0) * 6) + min(lane, (8 - DR0) * 6 - 1)) * 128;
  unsigned pfq = (12 * sl12 + 3 * (wave & 3) + min(lane >> 4, 2)) * (FF * 2) + xg * 1152 + min(lane & 15, 8) * 128;
  const int lg = late_group(a4[1], bid);
  const char* pfx = (lane & 31) < 12 ? (const char*)(sl < 12 ? a4[1] : a4[2]) + (unsigned)(((sl12 * 4 + (wave & 3)) * 12 + (lane & 31)) * (FF * 2) + xg * 1152 +
                                                                                            (lg ? lg : (lane >> 5) * 8) * 128)
                                     : (const char*)a4[3] + (unsigned)(((9 * xg + lg) * 576 + (sl * 4 + (wave & 3)) * 6 + min((lane & 31) - 12, 5)) * 128);
  asm volatile("" : "+v"(pfx));
  auto pf4 = [&](int m) {
#pragma unroll
    for (int i = 0; i < 4; i++)
      if (m >> i & 1) dma4s((sl < 12 ? a4[1] : a4[2]) + 144 * i * (FF * 2), pfq, (unsigned)(size_t)L + SM_DUMMY);
    if (m & 16) dma4s(a4[3], pfo, (unsigned)(size_t)L + SM_DUMMY);
    if (m & 32) dma4(pfx, (unsigned)(size_t)L + SM_DUMMY);
  };
  unsigned hs = (sub & 1) ? 0x03020c0cu : 0x01000c0cu, hs2 = (d & 1) ? 0x03020c0cu : 0x01000c0cu;
  LAS char* yb = C + g * 320 + c15 * 16;
  const LAS char *pa, *pa2, *pb, *uf16 = K + SM_US + 2 * UPAD + 32 * c15 + 16 * g;
  LAS char *kd, *kd2;
  unsigned ks0, ks1;
  half8 bA, bB;
  if (sub == 0) {
    pa = C + (((unsigned)(0x33982210cdef4567ull >> (4 * c15)) & 15) + 2 * (c15 & 8)) * TAB_STRIDE + 16 * g;
    pa2 = C + (((unsigned)(0x7763554289ab0123ull >> (4 * c15)) & 15) + 2 * (c15 & 8)) * TAB_STRIDE + 16 * g;
    pb = C + (TR_H + c15) * TAB_STRIDE + 16 * g;
    kd = K + KC_RD + 2 * 544 - 16 * lane;
    kd2 = K + KC_RD + (~c15 & 3) * RDS + 2 * (535 - (c15 | 3) + 8 * g) - 64 * 16;
    uf16 -= 32 * 33;
    bA = band_frag(lane, 0x3u << 12);
    const int s = lane & 3;
    const u32x4 t = {s < 2 ? 0x0c0c0c0cu : (s == 2 ? 0x01000c0cu : 0x01000100u), s == 0 ? 0x01000c0cu : 0x01000100u,
                     s < 2 ? 0x01000100u : (s == 2 ? 0x03020100u : 0x05040302u), (unsigned)(size_t)(K + KC_RD + s * RDS + 64)};
    bB = __builtin_bit_cast(half8, t);
    asm volatile("" : "+v"(bA), "+v"(pa), "+v"(pa2), "+v"(pb), "+v"(kd), "+v"(kd2), "=v"(ks0), "+v"(uf16), "=v"(ks1), "+v"(bB));
  } else if (sub == 1) {
    asm volatile("" : "=v"(bA), "=v"(pa), "=v"(pa2), "=v"(pb), "=v"(kd), "=v"(kd2), "=v"(ks0), "=v"(uf16), "=v"(ks1), "=v"(bB));
  } else if (sub == 2) {
    kd = K + KC_R9 + 190 * ((c15 + 1) & 1) + 2 * (15 - c15 + 4 * g);
    bA = band_frag(lane, 0x1ffu << 4);
    bB = band_frag(lane, 0x7fu << 5);
    asm volatile("" : "+v"(bA), "=v"(pa), "=v"(pa2), "=v"(pb), "+v"(kd), "=v"(kd2), "=v"(ks0), "+v"(uf16), "=v"(ks1), "+v"(bB));
  } else {
    pa = C + (TR_H + 15 - c15) * TAB_STRIDE + 16 * g;
    pa2 = K + KC_R5 + 2 * (158 - 64 - 2 * c15 + 4 * g) - 128;
    pb = C + (TR_M + c15) * TAB_STRIDE + 16 * g;
    kd = K + (c15 < 8 ? ((c15 & 1) ? KC_R5 : KC_R3) + 2 * (80 - 8 * (c15 & 6) + 4 * g) : KC_R9 + 2 * (120 - 8 * c15 + 2 * g));
    kd2 = kd + (c15 < 8 ? 4 : KC_R7 - KC_R9);
    ks0 = c15 < 8 ? 0x03020100u : 0x05040100u;
    ks1 = c15 < 8 ? 0x07060504u : 0x07060302u;
    uf16 -= 16 * c15;
    bA = band_frag(lane, 0x1fu << 6);
    bB = band_frag(lane, 0x7u << 11);
    asm volatile("" : "+v"(bA), "+v"(pa), "+v"(pa2), "+v"(pb), "+v"(kd), "+v"(kd2), "+v"(ks0), "+v"(uf16), "+v"(ks1), "+v"(bB));
  }
  const unsigned pe = sub < 2 ? 0u : 3u << 23;
  f32x2 kh = {-1.44269502f, 0.159154936f}, kl = {-1.92596299e-8f, 7.13048051e-9f}, k1 = {128.f * 0.693147181f, 128.f}, ln2 = {0.693147181f, 128.f};
  const unsigned pi = sub < 2 ? 0x3f800000u : 0xc1000000u;
  u32x2 k0u = {0x3f317218u + pe, pi}, p0u = {0x3f800000u + pe, pi};
  const int vt = tid - 256;
  int uoff = WS_UP + b * 16384 + vt * 16, soff = WS_ST + vt * 16;
  asm volatile("" : "+v"(pfo), "+v"(pfq), "+v"(yb), "+v"(uoff), "+v"(soff));
  asm volatile("s_mov_b32 m0, %8" : "+s"(hs), "+s"(hs2), "+s"(kh), "+s"(kl), "+s"(k0u), "+s"(k1), "+s"(ln2), "+s"(p0u)
               : "s"((unsigned)(size_t)C + (sub & 1) * 256));
  SCHED_FENCE();
  TWP(5);
  SCHED_FENCE();
  const unsigned tag = __builtin_amdgcn_readfirstlane(tagv) + 1u;
  auto half = [&](unsigned v) { return __uint_as_float(__builtin_amdgcn_perm(v, v, hs)); };
  f32x2 f_l = {half(r_lar), __uint_as_float(__builtin_amdgcn_perm(r_ldt, r_ldt, hs2))}, f_c = {half(r_cr), half(r_ci)};
  float f_aim = half(r_aim);
  unsigned f_cm;
  asm volatile("v_max3_f32 %0, |%1|, |%2|, |%3|\n\tv_max_f32 %0, %0, |%4|" : "=&v"(f_cm) : "v"(r_cr), "v"(r_ci), "v"(r_cr << 16), "v"(r_ci << 16));
  asm volatile("" : "+v"(f_l), "+v"(f_aim), "+v"(f_c), "+v"(r_dsk) : "v"(cpf));
  SCHED_FENCE();
  TWP(6);
  SCHED_FENCE();
  if (wave < 4) {
#pragma unroll
    for (int i = 0; i < 4; i += 2) {
      unsigned lo[3], hi[3];
#pragma unroll
      for (int k = 0; k < 3; k++) {
        lo[k] = __builtin_amdgcn_perm(cp[i + 1][k], cp[i][k], 0x05040100u);
        hi[k] = __builtin_amdgcn_perm(cp[i + 1][k], cp[i][k], 0x07060302u);
      }
      const int o = cpo + i * (XM ? 32 : 8 * 1024 * 16);
      stg16(rs, o, u32x4{lo[0], hi[0], lo[1], tag});
      stg16(rs, o + 16384, u32x4{hi[1], lo[2], hi[2], tag});
    }
    if (is_ln && tid >= 184) dma16((const char*)a4[0] + (tid - 184) * 16, (unsigned)(size_t)L + SM_WR + (wave - 3) * 1024 + 128);
    pf4(S1M);
  }
  SCHED_FENCE();
  TWA(2);
  TWP(7);
  u32x4 v0, v1, st0, st1;
  const u32x4 rq = {(unsigned)a8[6], (unsigned)(a8[6] >> 32) & 0xffffu, (unsigned)WS_END, 0x00020000u};
  asm volatile("" : "=v"(v0), "=v"(v1), "=v"(st0), "=v"(st1));
  auto ask = [&](u32x4& p, u32x4& q, int off) {
    asm volatile("buffer_load_dwordx4 %0, %2, %3, 0 offen sc1\n\tbuffer_load_dwordx4 %1, %2, %3, %4 offen sc1"
                 : "+v"(p), "+v"(q) : "v"(off), "s"(rq), "s"(8192) : "memory");
  };
  auto polls = [&](int k) {
    if (wave >= 4 && (wave < 8 ? XP47 : XP8) == k) ask(v0, v1, uoff);
    if (wave >= 4 && (wave < 8 ? SP47 : SP8) == k) ask(st0, st1, soff);
    SCHED_FENCE();
  };
  auto get = [&](bool both) {
    _Pragma("unroll 1") for (int spin = 0; spin < SPIN_CAP; spin++) {
      const bool ok = both ? v0[3] == tag && v1[3] == tag : st0[3] == tag && st1[3] == tag;
      if (__builtin_expect(__all(ok), 1)) break;
      __builtin_amdgcn_s_sleep(4);
      if (both) {
        asm volatile("s_waitcnt vmcnt(0)" : "+v"(st0), "+v"(st1));
        if (!ok) ask(v0, v1, uoff);
      }
      if (st0[3] != tag || st1[3] != tag) ask(st0, st1, soff);
      if (both) asm volatile("s_waitcnt vmcnt(0)" : "+v"(v0), "+v"(v1), "+v"(st0), "+v"(st1));
      else asm volatile("s_waitcnt vmcnt(0)" : "+v"(st0), "+v"(st1));
    }
  };
  {
    const f32x2 l2e = -f32x2{kh[0], kh[0]}, th = f_l * l2e;
    const f32x2 tl = __builtin_elementwise_fma(f_l, -f32x2{kl[0], kl[0]}, __builtin_elementwise_fma(f_l, l2e, -th)) * ln2[0];
    const f32x2 e0 = {__builtin_amdgcn_exp2f(th[0]), __builtin_amdgcn_exp2f(th[1])}, ex = __builtin_elementwise_fma(e0, tl, e0);
    const unsigned eb = pkbf(ex[0], ex[1]);
    const float dt = bfhi(eb);
    const f32x2 av = f32x2{bflo(eb), f_aim} * dt;
    const f32x2 hi = av * kh, lo = __builtin_elementwise_fma(av, kl, __builtin_elementwise_fma(av, kh, -hi));
    int se = 385 - (int)(wave_max_u32(f_cm) >> 23) - (int)((unsigned)__builtin_amdgcn_readfirstlane(eb) >> 23);
    se = max(se, 2);
    asm("" : "+s"(se));
    se = min(se, 252);
    const float dts = dt * __uint_as_float((unsigned)(se + 1) << 23);
    cf cc = f_c * dts;
    cf ea = cexp(hi * __builtin_bit_cast(f32x2, p0u), lo * __builtin_bit_cast(f32x2, k0u)), eb2 = cexp(hi * 128.f, lo * k1);
    polls(2);
    asm volatile("s_cmp_lt_u32 %3, 2\n\ts_cbranch_scc0 1f\n\t" TAB_A "s_branch 2f\n1:\n\t" TAB_B "\n2:"
                 : "+{v[100:101]}"(cc), "+{v[102:103]}"(ea), "+{v[104:105]}"(eb2) : "s"(sub) : TAB_CLOB, "scc", "memory");
    if (sub == 2) *(volatile LAS unsigned*)(L + SM_ISC + 4 * ch) = (unsigned)(254 - se) << 23;
  }
  SCHED_FENCE();
  if (S0M) asm volatile("" : : "v"(s0v));
  polls(1);
  TWA(3);
  asm volatile("s_waitcnt lgkmcnt(0)" ::: "memory");
  __syncthreads();
  const f32x4 zero4 = {0.f, 0.f, 0.f, 0.f};
  f32x2 sb[2][3];
  f32x2 isc[3];
  const LAS char *yp, *yp2;
  int zoff, zoff2;
  unsigned t0;
  if (sub == 0) {
    half8 td[17];
    {
      half8 fa[8], fc[8], fb[8];
      TWK(3);
#pragma unroll
      for (int ks = 0; ks < 8; ks++) { fa[ks] = ld_frag(pa + 64 * ks); fb[ks] = ld_frag(pb + 64 * ks); fc[ks] = ld_frag(pa2 + 64 * ks); }
      SCHED_FENCE();
      TWKV(4, "v"(fc[7]));
      f32x4 f = zero4, e = zero4;
#pragma unroll
      for (int ks = 0; ks < 8; ks++) { f = MFMA16(fa[ks], fb[ks], f); e = MFMA16(fc[ks], fb[ks], e); }
      const unsigned d0 = pkh(f[0], f[1]);
      TWKV(5, "v"(d0));
      const unsigned d1 = pkh(f[2], f[3]), d2 = pkh(e[0], e[1]), d3 = pkh(e[2], e[3]);
      const unsigned p0 = dppu<0x138>(d0), p1 = dppu<0x138>(d1);
      *(volatile LAS u32x4*)kd = u32x4{d0, d1, d2, d3};
      const unsigned a1 = __builtin_amdgcn_alignbit(d2, d1, 16), a2 = __builtin_amdgcn_alignbit(d3, d2, 16), a3 = __builtin_amdgcn_alignbit(p0, d3, 16);
      *(volatile LAS u32x4*)(kd + RDS) = u32x4{__builtin_amdgcn_alignbit(d1, d0, 16), a1, a2, a3};
      *(volatile LAS u32x4*)(kd + 2 * RDS) = u32x4{d1, d2, d3, p0};
      *(volatile LAS u32x4*)(kd + 3 * RDS) = u32x4{a1, a2, a3, __builtin_amdgcn_alignbit(p1, p0, 16)};
      TWA(4);
#pragma unroll
      for (int i = 7; i >= 0; i--) { td[i] = ld_td(kd2, i); td[i + 8] = ld_td(kd2, i + 8); }
      const unsigned q0 = dppu<0xFF>(d0), q1 = dppu<0xFF>(d1);
      const u32x4 t = __builtin_bit_cast(u32x4, bB);
      if (lane >= 60)
        *(volatile LAS u32x4*)(size_t)t[3] =
            u32x4{0u, __builtin_amdgcn_perm(q1, q0, t[0]), __builtin_amdgcn_perm(q1, q0, t[1]), __builtin_amdgcn_perm(q1, q0, t[2])};
      td[16] = ld_td(kd2, 16);
    }
    __builtin_amdgcn_s_waitcnt(0x0F70);
    pf4(S2M);
    TWA(5);
    __syncthreads();
    t0 = (unsigned)__builtin_amdgcn_s_memrealtime();
    TSX(0, 5);
    TWA(6);
    conv_dense(td, bA, K + KC_R5 + 64, uf16, yb + SM_YD);
  } else {
  polls(0);
  half8 td[8];
  auto mid_td = [&]() {
    const LAS unsigned* t5 = (const LAS unsigned*)pa2;
#pragma unroll
    for (int e = 0; e < 3; e++) {
      td[e] = ld_toep<8>(t5 + 16 * (2 - e));
      td[3 + e] = ld_toep<8>(t5 + (KC_R3 - KC_R5) / 4 + 16 * (2 - e));
    }
  };
  if (wave == 3) {
    half8 fa[24], fb[24];
    f32x4 f[3] = {zero4, zero4, zero4};
    constexpr int PF = 10;
    TWK(3);
#pragma unroll
    for (int i = 0; i < PF; i++) { fa[i] = ld_frag(pa + (i >> 3) * TABB + 64 * (i & 7)); fb[i] = ld_frag(pb + (i >> 3) * TABB + 64 * (i & 7)); }
    SCHED_FENCE();
    auto put = [&](int c) {
      const unsigned d0 = pkh(f[c][0], f[c][1]), d1 = pkh(f[c][2], f[c][3]);
      const unsigned w0 = __builtin_amdgcn_perm(d1, d0, ks0), w1 = __builtin_amdgcn_perm(d1, d0, ks1);
      *(LAS unsigned*)(kd + c * CH2) = w0;
      *(LAS unsigned*)(kd2 + c * CH2) = w1;
      if (c15 >= 8) {
        *(LAS u16*)(kd + c * CH2 + 190) = (u16)w0;
        *(LAS u16*)(kd + c * CH2 + 192) = (u16)(w0 >> 16);
        *(LAS u16*)(kd + c * CH2 + (KC_R7 - KC_R9) + 190) = (u16)w1;
        *(LAS u16*)(kd + c * CH2 + (KC_R7 - KC_R9) + 192) = (u16)(w1 >> 16);
      }
    };
    static_for<0, 24>([&](auto ic) {
      constexpr int i = decltype(ic)::value, n = i + PF;
      f[i >> 3] = MFMA16(fa[i], fb[i], f[i >> 3]);
      SCHED_FENCE();
      if constexpr (n < 24) { fa[n] = ld_frag(pa + (n >> 3) * TABB + 64 * (n & 7)); fb[n] = ld_frag(pb + (n >> 3) * TABB + 64 * (n & 7)); }
      SCHED_FENCE();
    });
    TWK(5);
    put(0);
    put(1);
    put(2);
    asm volatile("" ::: "memory");
    mid_td();
    __builtin_amdgcn_s_waitcnt(0x0F70);
    pf4(S2M);
  }
  TWA(4);
  SCHED_FENCE();
  if (wave >= 4) {
    const u32x4 iscv = *(const LAS u32x4*)(L + SM_ISC);
    yp = L + ((wave >> 1) - 2) * YT + ((vt >> 1) & 3) * 320 + ((vt >> 3) & 15) * 16 + (vt & 1) * 8;
    yp2 = yp + 2 * TABB;
    zoff = WS_ZP + ((vt >> 3) * NCONV + b) * 128 + (vt & 7) * 16;
    zoff2 = zoff + 64 * NCONV * 128;
    asm volatile("" : "+v"(yp), "+v"(yp2), "+v"(zoff), "+v"(zoff2));
    f32x2 dsk[3], xf[2][3];
    LAS char* dst = L + SM_US + 2 * UPAD + 4 * vt;
#pragma unroll
    for (int c = 0; c < 3; c++) {
      dsk[c][0] = __uint_as_float(__builtin_amdgcn_perm(r_dsk[1], r_dsk[0], 0x01000c0cu + 0x02020000u * (c + (b & 1))));
      isc[c] = f32x2{__uint_as_float(iscv[c]), __uint_as_float(iscv[c])};
      asm volatile("" : "+v"(dsk[c]), "+v"(isc[c]), "+v"(dst));
    }
    asm volatile("s_waitcnt vmcnt(2)" : "+v"(v0), "+v"(v1));
    get(true);
#pragma unroll
    for (int c = 0; c < 3; c++) {
      xf[0][c] = unpk(v0[c]);
      xf[1][c] = unpk(v1[c]);
      asm volatile("" : "+v"(xf[0][c]), "+v"(xf[1][c]));
    }
    SCHED_FENCE();
    asm volatile("s_waitcnt vmcnt(0)" : "+v"(st0), "+v"(st1));
    get(false);
    TWA(5);
    f32x2 uf[2][3];
#pragma unroll
    for (int i = 0; i < 2; i++) {
      const u32x4& st = i ? st1 : st0;
      const f32x2 ri = {__uint_as_float(st[0]), __uint_as_float(st[1])};
      const f32x2 cc = -unpk(st[2]) * ri;
#pragma unroll
      for (int c = 0; c < 3; c++) {
        uf[i][c] = __builtin_elementwise_fma(xf[i][c], ri, cc);
        *(LAS unsigned*)(dst + i * 2048 + c * CH2) = pkc(uf[i][c]);
      }
    }
    SCHED_FENCE();
#pragma unroll
    for (int i = 0; i < 2; i++)
#pragma unroll
      for (int c = 0; c < 3; c++) {
        sb[i][c] = __builtin_elementwise_fma(uf[i][c], f32x2{dsk[c][0], dsk[c][0]}, xf[i][c]);
        asm volatile("" : "+v"(sb[i][c]));
      }
    if (is_ln && (vt >> 3) == (b & 63)) *(LAS unsigned*)(L + SM_MX + (vt & 7) * 4) = b < 64 ? st0[2] : st1[2];
  } else {
    TWA(5);
  }
  if (sub == 1) __builtin_amdgcn_s_setprio(3);
  __syncthreads();
  t0 = (unsigned)__builtin_amdgcn_s_memrealtime();
  TWA(6);
  {
    if (sub == 1) {
      if (wave == 4 && lane < 8) __builtin_amdgcn_raw_buffer_store_b64(u32x2{t0, tag}, rs, WS_CK + lane * 2048 + b * 8, 0, SC1);
      auto pf3 = [&](int m) {
        if (m & 16)
          dma4s(a4[3], ((9 * xg + (sl & 7)) * 576 + DR0 * 72 + ((sl >> 3) * 3 + ch) * ((8 - DR0) * 8) + min(lane, (8 - DR0) * 8 - 1)) * 128,
                (unsigned)(size_t)L + SM_DUMMY);
        if (m & 32) {
          const char* pg = (const char*)(sl < 12 ? a4[1] : a4[2]) + (unsigned)(((sl12 * 3 + ch) * 8 + (lane & 7)) * (FF * 2) + xg * 1152 + 1024);
          const char* pd = (const char*)a4[3] + (unsigned)(((9 * xg + 8) * 576 + (sl * 3 + ch) * 8 + (lane & 7)) * 128);
          dma4((lane & 8) ? pd : pg, (unsigned)(size_t)L + SM_DUMMY);
        }
#pragma unroll
        for (int i = 0; i < 4; i++)
          if (m >> i & 1) prefetch(i);
      };
      if (!SENT1) pf3(S3M);
      if (is_ln) {
        const u32x4 ones = {~0u, ~0u, ~0u, ~0u};
#pragma unroll
        for (int i = 0; i < 3; i++) stg16(rs, WS_PART + b * 11520 + vt * 16 + i * 3072, ones);
        if (vt < 144) stg16(rs, WS_PART + b * 11520 + vt * 16 + 9216, ones);
        stg16(rs, WS_XS + b * (2 * XSS) + vt * 16, ones);
        if (vt < 96) stg16(rs, WS_XS + b * (2 * XSS) + 3072 + vt * 16, ones);
        if (b < 6) stg16(rs, WS_XS + 2 * NLN * XSS + b * 3072 + vt * 16, ones);
      }
      if (SENT1) pf3(S3M);
      if (S4M) {
        while ((unsigned)__builtin_amdgcn_s_memrealtime() - t0 < S4D) __builtin_amdgcn_s_sleep(1);
        pf3(S4M);
      }
      __builtin_amdgcn_s_setprio(0);
    } else if (sub == 3) {
      if (wave != 3) mid_td();
      f32x4 a2[6], w[4];
#pragma unroll
      for (int i = 0; i < 6; i++) a2[i] = zero4;
      half8 uw[12];
#pragma unroll
      for (int jj = 0; jj < 6; jj++) uw[jj] = ld_frag(uf16 - 48 + 256 * jj);
      SCHED_FENCE();
      w[0] = MFMA16(uw[0], bA, zero4); w[1] = MFMA16(uw[1], bA, zero4);
      w[2] = MFMA16(uw[0], bB, zero4); w[3] = MFMA16(uw[1], bB, zero4);
      half8 X5 = pack_win(w[0], w[1]), X3 = pack_win(w[2], w[3]);
      SCHED_FENCE();
#pragma unroll
      for (int jj = 0; jj < 6; jj++) {
        if (jj < 5) {
          w[0] = MFMA16(uw[2 * jj + 2], bA, zero4);
          if (jj < 3) uw[2 * jj + 6] = ld_frag(uf16 - 48 + 256 * (2 * jj + 6));
          SCHED_FENCE();
          w[1] = MFMA16(uw[2 * jj + 3], bA, zero4);
          if (jj < 3) uw[2 * jj + 7] = ld_frag(uf16 - 48 + 256 * (2 * jj + 7));
          SCHED_FENCE();
          w[2] = MFMA16(uw[2 * jj + 2], bB, zero4);
        }
        if (jj > 0) *(LAS f32x4*)(yb + SM_Y2 + (1 + jj) * YT) = a2[jj - 1];
        SCHED_FENCE();
        if (jj < 5) w[3] = MFMA16(uw[2 * jj + 3], bB, zero4);
        SCHED_FENCE();
        u32x4 n5, n3;
#pragma unroll
        for (int q = 0; q < 6; q++) {
          const int e = q >> 1;
          if (jj + e < 6) a2[jj + e] = MFMA16((q & 1) ? X3 : X5, td[3 * (q & 1) + e], a2[jj + e]);
          if (jj < 5 && q < 4) {
            u32x4& nn = q < 2 ? n5 : n3;
            nn[2 * (q & 1)] = pkh(w[q][0], w[q][1]);
            nn[2 * (q & 1) + 1] = pkh(w[q][2], w[q][3]);
          }
          SCHED_FENCE();
        }
        if (jj < 5) {
          X5 = __builtin_bit_cast(half8, n5);
          X3 = __builtin_bit_cast(half8, n3);
        }
      }
      *(LAS f32x4*)(yb + SM_Y2 + 7 * YT) = a2[5];
    } else {
      half8 uw[8];
#pragma unroll
      for (int i = 0; i < 4; i++) {
        uw[2 * i] = ld_frag(uf16 + 512 * i - 48);
        uw[2 * i + 1] = ld_frag(uf16 + 512 * i - 32);
      }
      SCHED_FENCE();
      f32x4 w[8], a3[4];
#pragma unroll
      for (int e = 0; e < 4; e++) {
        const LAS unsigned* t9 = opaque_lds(kd);
        td[e] = ld_toep<8>(t9 + 8 * (3 - e));
        td[4 + e] = ld_toep<8>(t9 + (KC_R7 - KC_R9) / 4 + 8 * (3 - e));
      }
#pragma unroll
      for (int i = 0; i < 4; i++) {
        w[2 * i] = MFMA16(uw[2 * i], bA, zero4);
        w[2 * i + 1] = MFMA16(uw[2 * i + 1], bB, zero4);
      }
      const half8 X90 = pack_win(w[0], w[2]), X70 = pack_win(w[1], w[3]), X91 = pack_win(w[4], w[6]), X71 = pack_win(w[5], w[7]);
#pragma unroll
      for (int t = 0; t < 4; t++) {
        a3[t] = MFMA16(X90, td[t], zero4);
        a3[t] = MFMA16(X70, td[4 + t], a3[t]);
        if (t >= 2) {
          a3[t] = MFMA16(X91, td[t - 2], a3[t]);
          a3[t] = MFMA16(X71, td[4 + t - 2], a3[t]);
        }
      }
#pragma unroll
      for (int t = 0; t < 4; t++) *(LAS f32x4*)(yb + SM_Y3 + (4 + t) * YT) = a3[t];
    }
  }
  }
  TWA(7);
  __syncthreads();
  const unsigned long long t_end = __builtin_amdgcn_s_memrealtime();
  TS(0, 6);
  TWB(2);
  if (wave >= 4) {
    auto ld = [&](int c, int off) { return *(const volatile LAS f32x2*)((c < 2 ? yp + c * TABB : yp2) + off); };
    auto epi = [&](auto mid) {
      f32x2 ya[3], yc[3], yb0[3], yb1[3], yb2[3];
#pragma unroll
      for (int c = 0; c < 3; c++) {
        ya[c] = ld(c, SM_YD);
        if (mid) yc[c] = ld(c, SM_Y2);
      }
#pragma unroll
      for (int c = 0; c < 3; c++) {
        yb0[c] = ld(c, 4 * YT + SM_YD);
        yb1[c] = ld(c, 4 * YT + SM_Y2);
        yb2[c] = ld(c, 4 * YT + SM_Y3);
      }
      SCHED_FENCE();
      u32x4 o, o2;
#pragma unroll
      for (int c = 0; c < 3; c++) {
        const f32x2 z = __builtin_elementwise_fma(mid ? ya[c] + yc[c] : ya[c], isc[c], sb[0][c]);
        o[c] = pkbf(z[0], z[1]);
      }
      o[3] = tag;
      stg16(rs, zoff, o);
#pragma unroll
      for (int c = 0; c < 3; c++) {
        const f32x2 z = __builtin_elementwise_fma((yb0[c] + yb1[c]) + yb2[c], isc[c], sb[1][c]);
        o2[c] = pkbf(z[0], z[1]);
      }
      o2[3] = tag;
      stg16(rs, zoff2, o2);
    };
    if (wave >= 8) epi(std::true_type{});
    else epi(std::false_type{});
  }
  TS(0, 7);
  TWB(3);
  TWFLUSH();
  ftag = tag;
  if (b < LATE0) {
  unsigned long long t_all = t_end;
  auto get_lag = [&]() {
    u32x4 p, q;
    _Pragma("unroll 1") for (int spin = 0; spin < SPIN_CAP; spin++) {
      p = ldg16(rs, WS_CK + (b & 7) * 2048 + lane * 16);
      q = ldg16(rs, WS_CK + (b & 7) * 2048 + 1024 + hl * 16);
      if (__all(p[1] == tag && p[3] == tag && q[1] == tag && q[3] == tag)) break;
      __builtin_amdgcn_s_sleep(4);
    }
    const int lag = max(max(max((int)(p[0] - t0), (int)(p[2] - t0)), max((int)(q[0] - t0), (int)(q[2] - t0))), 0);
    t_all += min(wave_max_u32((unsigned)lag), 1000u);
  };
  auto wait_until = [&](int t) {
    while ((long long)(__builtin_amdgcn_s_memrealtime() - t_all) < t) __builtin_amdgcn_s_sleep(1);
  };
  LAS unsigned* const selp = (LAS unsigned*)(L + SM_ROW + 256);
  unsigned long long t_key = 0;
  auto select_rows = [&]() {
    const SelMasks sm(lane);
    get_lag();
    wait_until(DLY_L);
    selp[key_slot(lane)] = select8(rs, tag, lane, sm, ws, bid);
    t_key = __builtin_amdgcn_s_memrealtime();
  };
  if (is_ln) {
    f32x2 wf[6], hv[6];
    const unsigned mxp = *(const LAS unsigned*)(L + SM_MX + (tid & 7) * 4);
#pragma unroll
    for (int i = 0; i < 6; i++) {
      const float t = bflo(*(const LAS u16*)(L + SM_WR + (tid >> 3) * 12 + i * 2));
      wf[i] = f32x2{t, t};
    }
    float wsum = 0.f, mxr = bflo(*(const LAS u16*)(L + SM_MX + (lane >> 2) * 2));
    unsigned one2 = 0x3f803f80u, sel0 = 0x00003f80u, sel1 = 0x3f800000u;
    asm volatile("" : "+s"(one2), "+s"(sel0), "+s"(sel1));
    if (wave == 0) {
#pragma unroll
      for (int k = 0; k < 9; k++) wsum = dot2(*(const LAS unsigned*)(L + SM_WR + (hl + 32 * k) * 4), one2, wsum);
      wsum = red32_sum(wsum);
      __builtin_amdgcn_s_setprio(2);
    }
#pragma unroll
    for (int i = 0; i < 6; i++) {
      hv[i] = -unpk(mxp);
      asm volatile("" : "+v"(hv[i]), "+v"(wf[i]));
    }
    LAS char* redw = L + SM_RED + wave * 256 + (wave >= 6 ? 128 : 0) + (lane & 7) * 32 + (lane >> 5) * 16 + ((lane >> 4) & 1) * 8;
    const LAS char* redr = L + SM_RED + ((lane >> 1) & 1) * (6 * 256 + 128) + (lane >> 2) * 16 + (lane & 1) * 8;
    unsigned kidx = (unsigned)(SEQ - 1 - 16 * b - (lane >> 2)) << 2;
    int kro = WS_KR + (lane & 7) * 2048 + b * 16, xso = WS_XS + b * (2 * XSS) + (tid >> 3) * 12;
    asm volatile("" : "+v"(wsum), "+v"(mxr), "+v"(redw), "+v"(redr), "+v"(kidx), "+v"(kro), "+v"(xso));
    if (wave == 1) {
      get_lag();
      wait_until(DLY_H);
    }
    __syncthreads();
    TWB(4);
    {
      const int off = WS_ZP + b * (NCONV * 128) + (tid >> 3) * 256 + (tid & 7) * 16;
      v0 = ldg16(rs, off);
      v1 = ldg16(rs, off + 128);
      _Pragma("unroll 1") for (int spin = 0; spin < SPIN_CAP; spin++) {
        if (__builtin_expect(__all(v0[3] == tag && v1[3] == tag), 1)) break;
        __builtin_amdgcn_s_sleep(4);
        if (v0[3] != tag) v0 = ldg16(rs, off);
        if (v1[3] != tag) v1 = ldg16(rs, off + 128);
      }
    }
    TS(0, 8);
    TWB(5);
    {
      f32x2 ss, qq, pp;
#pragma unroll
      for (int i = 0; i < 6; i++) {
        const unsigned t = i < 3 ? v0[i] : v1[i - 3];
        hv[i] = f32x2{dot2(t, sel0, hv[i][0]), dot2(t, sel1, hv[i][1])};
        ss = i ? ss + hv[i] : hv[i];
        qq = i ? __builtin_elementwise_fma(hv[i], hv[i], qq) : hv[i] * hv[i];
        pp = i ? __builtin_elementwise_fma(hv[i], wf[i], pp) : hv[i] * wf[i];
      }
      float s0 = ss[0], s1 = ss[1], q0 = qq[0], q1 = qq[1], p0 = pp[0], p1 = pp[1];
      asm volatile(
          "s_nop 1\n\tv_permlane32_swap_b32 %0, %1\n\tv_permlane32_swap_b32 %2, %3\n\tv_permlane32_swap_b32 %4, %5\n\t"
          "v_add_f32 %0, %0, %1\n\tv_add_f32 %2, %2, %3\n\tv_add_f32 %4, %4, %5\n\ts_nop 0\n\t"
          "v_permlane16_swap_b32 %0, %2\n\tv_add_f32_dpp %1, %4, %4 row_ror:8 row_mask:0xf bank_mask:0xf\n\t"
          "v_add_f32 %2, %0, %2\n\ts_nop 1\n\tv_add_f32_dpp %0, %2, %2 row_ror:8 row_mask:0xf bank_mask:0xf"
          : "+v"(s0), "+v"(s1), "+v"(q0), "+v"(q1), "+v"(p0), "+v"(p1));
      *(LAS f32x2*)redw = f32x2{s0, s1};
    }
    TWB(6);
    if (wave == 11 && lane == 0 && !(b & 1)) __builtin_amdgcn_raw_buffer_store_b32(tag, rs, WS_PF + (b >> 1) * 128, 0, SC1);
    __syncthreads();
    if (wave == 0) {
      const volatile LAS f32x2* rp = (const volatile LAS f32x2*)redr;
      const f32x2 t0 = rp[0], t1 = rp[32], t2 = rp[64], t3 = rp[96], t4 = rp[128], t5 = rp[160];
      const f32x2 t = ((t0 + t1) + (t2 + t3)) + (t4 + t5);
      float a = t[0], p = t[1];
      asm volatile("" : "+v"(a), "+v"(p));
      a += dppf<0x4E>(a);
      p += dppf<0x4E>(p);
      p += dppf<0xB1>(p);
      const float mv = dppf<0x00>(a) * (1.0f / DM);
      const float rinv = ln_rinv(dppf<0x55>(a), mv);
      const float dl = rbf(mxr + mv) - mxr;
      const unsigned rb = pkbf(0.f, fmaf(-dl, wsum, p) * rinv);
      const unsigned key = ((rb ^ (unsigned)(((int)rb >> 31) | (int)0x80000000)) & 0xffff0000u) | kidx;
      unsigned k1 = max(key, dppu<0x141>(key)), k2 = min(key, dppu<0x141>(key)), k3;
      {
        const unsigned lo = min(k1, dppu<0x140>(k1)), hi = max(k2, dppu<0x140>(k2));
        k1 = max(k1, dppu<0x140>(k1));
        k2 = max(lo, hi);
        k3 = min(lo, hi);
      }
      {
        const unsigned lo = min(k1, dppu<0x142>(k1)), hi = max(k2, dppu<0x142>(k2)), w = max(k3, dppu<0x142>(k3));
        k1 = max(k1, dppu<0x142>(k1));
        k2 = max(lo, hi);
        k3 = max(min(lo, hi), w);
      }
      {
        const unsigned lo = min(k1, dppu<0x143>(k1)), hi = max(k2, dppu<0x143>(k2)), w = max(k3, dppu<0x143>(k3));
        k1 = max(k1, dppu<0x143>(k1));
        k2 = max(lo, hi);
        k3 = max(min(lo, hi), w);
      }
      if (lane >= 56) stg16(rs, kro, u32x4{k1, k2 | 1u, k3, tag});
      SCHED_FENCE();
      const unsigned second = (unsigned)__builtin_amdgcn_readlane((int)k2, 63);
      if ((lane & 3) == 0) {
        LAS float* ri = (LAS float*)(L + SM_ROW + (lane >> 3) * 16 + (lane & 4));
        ri[0] = -dl * rinv;
        ri[2] = rinv;
        const unsigned fk = key | (key > second ? 0u : (key == second ? 1u : 2u));
        *(LAS unsigned*)(L + SM_ROW + 128 + lane) = fk;
        __builtin_amdgcn_raw_buffer_store_b64(u32x2{fk, tag}, rs, WS_KF + b * 128 + lane * 2, 0, SC1);
      }
      __builtin_amdgcn_s_setprio(0);
    }
    TS(0, 9);
    TWB(7);
    __syncthreads();
    {
      const f32x4 ri = *(const LAS f32x4*)(L + SM_ROW + (tid & 7) * 16);
      const u32x2 kk = *(const LAS u32x2*)(L + SM_ROW + 128 + (tid & 7) * 8);
      const f32x2 c2 = {ri[0], ri[1]}, r2 = {ri[2], ri[3]};
      unsigned wp[6];
#pragma unroll
      for (int i = 0; i < 6; i++) {
        const f32x2 w2 = __builtin_elementwise_fma(hv[i], r2, c2);
        wp[i] = pkbf(w2[0], w2[1]);
      }
#pragma unroll
      for (int i = 0; i < 2; i++)
        if (!(kk[i] & 2)) {
          u32x3 o;
#pragma unroll
          for (int k = 0; k < 3; k++) o[k] = min(__builtin_amdgcn_perm(wp[2 * k + 1], wp[2 * k], i ? 0x07060302u : 0x05040100u), 0xfffffffeu);
          __builtin_amdgcn_raw_buffer_store_b96(o, rs, xso + (int)(kk[i] & 1) * XSS, 0, SC1);
        }
      SCHED_FENCE();
      LAS char* dst = L + SM_T2 + (tid & 7) * T2S + (tid >> 3) * 48;
#pragma unroll
      for (int i = 0; i < 6; i += 2) *(LAS u32x4*)(dst + 8 * i) = u32x4{i < 3 ? v0[i] : v1[i - 3], wp[i], i < 2 ? v0[i + 1] : v1[i - 2], wp[i + 1]};
    }
    __syncthreads();
    const int pr = tid / 72, ck = tid - pr * 72;
    u32x4 hq[2], wq[2];
    u32x2 key = {2u, 2u};
    auto put_row = [&](int i, int slot) {
      const int o = key_addr(key[i], slot) + ck * 16;
      const u32x4 cap = {0xfffffffeu, 0xfffffffeu, 0xfffffffeu, 0xfffffffeu};
      if (key[i] & 2) stg16(rs, o, __builtin_elementwise_min(wq[i], cap));
      stg16(rs, o + DM * 2, __builtin_elementwise_min(hq[i], cap));
    };
    if (wave < 9) {
      key = *(const LAS u32x2*)(L + SM_ROW + 128 + pr * 8);
#pragma unroll
      for (int k = 0; k < 4; k++) {
        const u32x4 t = *(const LAS u32x4*)(L + SM_T2 + pr * T2S + ck * 64 + k * 16);
        hq[0][k] = __builtin_amdgcn_perm(t[2], t[0], 0x05040100u);
        hq[1][k] = __builtin_amdgcn_perm(t[2], t[0], 0x07060302u);
        wq[0][k] = __builtin_amdgcn_perm(t[3], t[1], 0x05040100u);
        wq[1][k] = __builtin_amdgcn_perm(t[3], t[1], 0x07060302u);
      }
#pragma unroll
      for (int i = 0; i < 2; i++)
        if (!(key[i] & 2)) put_row(i, 0);
    } else if (wave == 10) {
      select_rows();
    }
    __syncthreads();
    TS(0, 10);
    if (wave < 9) {
#pragma unroll
      for (int i = 0; i < 2; i++) {
        int slot = 8;
#pragma unroll
        for (int r = 0; r < 8; r++) slot = selp[r] == key[i] ? r : slot;
        if (slot < 8) {
          if (key[i] & 2) put_row(i, slot);
        } else {
          u32x4 o;
#pragma unroll
          for (int k = 0; k < 4; k++) {
            const f32x2 o2 = unpk(hq[i][k]) + unpk(wq[i][k]);
            o[k] = pkbf(o2[0], o2[1]);
          }
          *(GAS u32x4*)(out + (unsigned)((16 * b + 2 * pr + i) * (DM * 2) + ck * 16)) = o;
        }
      }
    }
  } else if (wave == 10) {
    select_rows();
  }
  if (b < RED0) {
    TS(0, 12);
    TC(0, 15);
    __builtin_amdgcn_endpgm();
  }
  {
    const int T = (b - RED0) >> 2, tp = (b - RED0) & 3;
    unsigned hv = 0u, wv = 0u, oo = 0u;
    float sg = 0.f;
    if (wave == 10) {
      const int slot = 2 * tp + ((lane >> 3) & 1);
      const unsigned key = *(volatile LAS unsigned*)(selp + slot);
      int o = key_addr(key, slot) + (8 * T + (lane & 7)) * 4;
      _Pragma("unroll 1") for (int spin = 0; spin < SPIN_CAP; spin++) {
        asm volatile("" : "+v"(o) : : "memory");
        wv = __builtin_amdgcn_raw_buffer_load_b32(rs, o, 0, SC1);
        hv = __builtin_amdgcn_raw_buffer_load_b32(rs, o + DM * 2, 0, SC1);
        if (__all(wv != ~0u && hv != ~0u)) break;
        __builtin_amdgcn_s_sleep(4);
      }
      const unsigned rb = key ^ ((int)key < 0 ? 0x80000000u : 0xffffffffu);
      sg = rbf(__builtin_amdgcn_rcpf(1.0f + __expf(-bfhi(rb))));
      oo = (unsigned)(key_row(key) * (DM * 2) + (8 * T + (lane & 7)) * 4);
      asm volatile("" : "+v"(sg), "+v"(oo));
      while (__builtin_amdgcn_s_memrealtime() - t_key < (unsigned long long)DLY_P) __builtin_amdgcn_s_sleep(1);
    }
    LAS float* red = (LAS float*)(L + SM_RED);
    LAS char* rw = (LAS char*)red + wave * 128 + (lane & 7) * 16 + (lane >> 4) * 4;
    asm volatile("" : "+v"(rw));
    __syncthreads();
    TS(0, 11);
    if (wave < 10) {
      u32x4 v, w;
      asm volatile("buffer_load_dwordx4 %0, %2, %3, 0 offen sc1\n\ts_sleep %4\n\tbuffer_load_dwordx4 %1, %2, %3, 0 offen sc1"
                   : "=&v"(v), "=&v"(w) : "v"(part_off), "s"(rq), "n"(PPS) : "memory");
      u32x4 got = {~0u, 0u, 0u, 0u};
      _Pragma("unroll 1") for (int spin = 0; spin < SPIN_CAP; spin++) {
        asm volatile("s_waitcnt vmcnt(1)" : "+v"(v));
        if (got[0] == ~0u) got = v;
        if (__all(got[0] != ~0u)) break;
        if (got[0] == ~0u) asm volatile("buffer_load_dwordx4 %0, %1, %2, 0 offen sc1" : "+v"(v) : "v"(part_off), "s"(rq) : "memory");
        asm volatile("s_waitcnt vmcnt(1)" : "+v"(w));
        if (got[0] == ~0u) got = w;
        if (__all(got[0] != ~0u)) break;
        if (got[0] == ~0u) asm volatile("buffer_load_dwordx4 %0, %1, %2, 0 offen sc1" : "+v"(w) : "v"(part_off), "s"(rq) : "memory");
      }
#define v got
      auto r0 = __builtin_amdgcn_permlane32_swap(v[0], v[2], false, false);
      auto r1 = __builtin_amdgcn_permlane32_swap(v[1], v[3], false, false);
      const float s0 = __uint_as_float(r0[0]) + __uint_as_float(r0[1]), s1 = __uint_as_float(r1[0]) + __uint_as_float(r1[1]);
      auto r2 = __builtin_amdgcn_permlane16_swap(__float_as_uint(s0), __float_as_uint(s1), false, false);
      float a = __uint_as_float(r2[0]) + __uint_as_float(r2[1]);
      a += dppf<0x128>(a);
      *(LAS float*)rw = a;
#undef v
      asm volatile("" : : "v"(v), "v"(w));
    }
    __syncthreads();
    if (wave == 10 && lane < 16) {
      f32x2 p[10];
#pragma unroll
      for (int w = 0; w < 10; w++) p[w] = *(const LAS f32x2*)(red + w * 32 + lane * 2);
      const f32x2 y = (((p[0] + p[1]) + (p[2] + p[3])) + (p[8] + p[9])) + ((p[4] + p[5]) + (p[6] + p[7]));
      const f32x2 y2 = rbf2(rbf2(y) * sg);
      const f32x2 o2 = unpk(hv) + rbf2(unpk(wv) + y2);
      *(GAS unsigned*)(out + oo) = pkbf(o2[0], o2[1]);
      if (b == RED0 && lane == 0) __builtin_amdgcn_raw_buffer_store_b32(tag, rs, WS_EPOCH, 0, SC1);
    }
  }
  TS(0, 12);
  TC(0, 15);
  __builtin_amdgcn_endpgm();
  }
  }
  ffn_role(argp, ftag, L);
}

}

extern "C" void kernel_launch(void* const* d_in, const int* in_sizes, int n_in, void* d_out, int out_size,
                              void* d_ws, size_t ws_size, hipStream_t stream) {
  k_all<<<NCONV + NFFN, NTHR, 0, stream>>>((const u16*)d_in[0], (const u16*)d_in[1], (const u16*)d_in[2], (const u16*)d_in[3],
                                             (const u16*)d_in[4], (const u16*)d_in[5], (char*)d_ws, (const u16*)d_in[6],
                                             (const u16*)d_in[8], (const u16*)d_in[9], (const u16*)d_in[10], (const u16*)d_in[11],
                                             (u16*)d_out);
}
```

```cpp
#include <hip/hip_runtime.h>
#include <stdint.h>
#include <type_traits>

#define PRA 0
#define CPF 212
#define WPAD 0
#define PFNT 0
#define WNT 0
#define WNTS ""
#define PTH 4
#define PTL 2
#define EB 0
#define LAUX 16
#define SAUX 16
#define PFA 0
#define XFL 0
#define CMAP 0
#define PRB 0
#define WORD 1
#define DR0 6
#define SENT1 0
#define S4M 0
#define S4D 50
#define XP47 0
#define XP8 1
#define SP47 0
#define SP8 1
#define LSKIP 0
#define DNT 1
#define SCN 0
#define SCX 0
#define ED 3
#define GNT 1
#define PP 1
#define PPS 6
#define DLY_Q 430
#define DLY_K 375
#define DLY_C 232
#define KPP 4
#define TDB 4
#define TDI 1
#define PF3 9
#define T0A 1
#define S0M 0
#define S1M 32
#define S2M 0
#define S3M 0
#define STA 16
#define XM 0
#define XF 0
#define PFD 10
namespace {

constexpr int SEQ = 2048;
constexpr int DM = 576;
constexpr int NM = 128;
constexpr int FF = 4608;

constexpr int NTHR = 768;
constexpr int NFFN = 64;
constexpr int NPART = 80;
constexpr int NCONV = 192;
constexpr int NLN = 128;
constexpr int RED0 = 32;
constexpr int LATE0 = 176;

typedef unsigned short u16;
#define LAS __attribute__((address_space(3)))
#define GAS __attribute__((address_space(1)))
#define CAS __attribute__((address_space(4)))
using half8 = __attribute__((ext_vector_type(8))) _Float16;
using half2v = __attribute__((ext_vector_type(2))) _Float16;
using bf2v = __attribute__((ext_vector_type(2))) __bf16;
using bf8v = __attribute__((ext_vector_type(8))) __bf16;
using bf16v = __attribute__((ext_vector_type(16))) __bf16;
using s16x8 = __attribute__((ext_vector_type(8))) short;
using s16x4 = __attribute__((ext_vector_type(4))) short;
using f32x2 = __attribute__((ext_vector_type(2))) float;
using f32x4 = __attribute__((ext_vector_type(4))) float;
using u32x2 = __attribute__((ext_vector_type(2))) unsigned;
using u32x3 = __attribute__((ext_vector_type(3))) unsigned;
typedef u32x3 u32x3u __attribute__((aligned(4)));
using u32x4 = __attribute__((ext_vector_type(4))) unsigned;
using u64x2 = __attribute__((ext_vector_type(2))) unsigned long long;
using u64x8 = __attribute__((ext_vector_type(8))) unsigned long long;
using u64x4 = __attribute__((ext_vector_type(4))) unsigned long long;

constexpr int WS_EPOCH = 0;
constexpr int WS_PF = 4096;
constexpr int WS_KR = WS_PF + NFFN * 128;
constexpr int WS_KF = WS_KR + 8 * 2048;
constexpr int WS_ST = WS_KF + 16384;
constexpr int WS_CK = WS_ST + 16384;
constexpr int WS_FL = WS_CK + 8 * 2048;
constexpr int WS_UP = 131072;
constexpr int WS_ZP = WS_UP + NCONV * NLN * 128;
constexpr int WS_PART = WS_ZP + NCONV * NLN * 128;
constexpr int WS_XS = WS_PART + NPART * 8 * DM * 4;
constexpr int XSS = 4 * DM;
constexpr int WS_END = WS_XS + (2 * NLN + 8) * XSS;
static_assert(WS_FL + 8 * 1024 <= WS_UP, "ws");
constexpr size_t WS_TS = 64u << 20;
constexpr int SC1 = 16, NT = 2;
constexpr int SPIN_CAP = 1 << 16;

#define TW(k)
#define TWP(k)
#define TWA(k)
#define TWB(k)
#define TWDECL
#define TSX(k, s)
#define TWFLUSH()
#define TC(k, s)
#define TSW(w, s)
#define TSL(k, s)
#define TS(k, s)

#define TWK(k)
#define TWKV(k, ...)
__device__ __forceinline__ float bflo(unsigned v) { return __uint_as_float(v << 16); }
__device__ __forceinline__ float bfhi(unsigned v) { return __uint_as_float(v & 0xffff0000u); }
__device__ __forceinline__ unsigned pkbf(float a, float b) {
  f32x2 f = {a, b};
  return __builtin_bit_cast(unsigned, __builtin_convertvector(f, bf2v));
}
__device__ __forceinline__ unsigned pkh(float a, float b) {
  f32x2 f = {a, b};
  return __builtin_bit_cast(unsigned, __builtin_convertvector(f, half2v));
}
__device__ __forceinline__ float rbf(float f) { return __uint_as_float(pkbf(0.f, f)); }
__device__ __forceinline__ f32x2 rbf2(f32x2 v) { return f32x2{rbf(v[0]), rbf(v[1])}; }
__device__ __forceinline__ f32x2 unpk(unsigned v) { return f32x2{bflo(v), bfhi(v)}; }
__device__ __forceinline__ float dot2(unsigned a, unsigned b, float c) {
  return __builtin_amdgcn_fdot2_f32_bf16(__builtin_bit_cast(bf2v, a), __builtin_bit_cast(bf2v, b), c, false);
}
__device__ __forceinline__ float hadd(f32x2 v) {
  float a = v[0], b = v[1];
  asm volatile("" : "+v"(a), "+v"(b));
  return a + b;
}

template <int CTRL>
__device__ __forceinline__ float dppf(float v) {
  return __int_as_float(__builtin_amdgcn_update_dpp(0, __float_as_int(v), CTRL, 0xf, 0xf, true));
}
template <int CTRL>
__device__ __forceinline__ unsigned dppu(unsigned v) {
  return (unsigned)__builtin_amdgcn_update_dpp(0, (int)v, CTRL, 0xf, 0xf, true);
}
__device__ __forceinline__ float swap16_sum(float v) {
  auto r = __builtin_amdgcn_permlane16_swap(__float_as_uint(v), __float_as_uint(v), false, false);
  return __uint_as_float(r[0]) + __uint_as_float(r[1]);
}
__device__ __forceinline__ float swap32_sum(float v) {
  auto r = __builtin_amdgcn_permlane32_swap(__float_as_uint(v), __float_as_uint(v), false, false);
  return __uint_as_float(r[0]) + __uint_as_float(r[1]);
}
__device__ __forceinline__ float red32_sum(float v) {
  v += dppf<0xB1>(v);
  v += dppf<0x4E>(v);
  v += dppf<0x141>(v);
  v += dppf<0x140>(v);
  return swap16_sum(v);
}
#define SCHED_FENCE() __builtin_amdgcn_sched_barrier(0)
#define DRAIN() asm volatile("s_waitcnt vmcnt(0)" ::: "memory")

__device__ __forceinline__ float red16_sum(float v) {
  v += dppf<0xB1>(v);
  v += dppf<0x4E>(v);
  v += dppf<0x141>(v);
  return v + dppf<0x140>(v);
}
__device__ __forceinline__ float ln_rinv(float q, float dm) {
  const float var = fmaxf(fmaf(q, 1.0f / DM, -dm * dm), 0.f);
  return __builtin_amdgcn_rcpf(rbf(__builtin_amdgcn_sqrtf(rbf(rbf(var) + 1e-5f))));
}

constexpr int TAB_STRIDE = 544;
constexpr int TAB_ROWS = 58;
constexpr int TR_M = 26, TR_F = 34, TR_H = 42;
constexpr int TABB = TAB_ROWS * TAB_STRIDE;
constexpr int SM_KC = 3 * TABB;
constexpr int RDS = 336 * 4;
constexpr int KC_RD = SM_KC;
constexpr int KC_R5 = KC_RD + 4 * RDS;
constexpr int KC_R3 = KC_R5 + 64 * 4;
constexpr int KC_R9 = KC_R3 + 64 * 4;
constexpr int KC_R7 = KC_R9 + 96 * 4;
constexpr int SM_US = KC_R7 + 96 * 4;
constexpr int UPAD = 256;
constexpr int CH2 = SM_US + (UPAD + SEQ) * 2 - SM_KC;
static_assert(SM_US + 2 * UPAD <= SM_KC + 8192 && 8192 <= CH2 && 2 * TABB + 256 < 65536, "zero fill");
constexpr int YT = 1280;
constexpr int SM_YD = 0, SM_Y2 = 8 * YT - 2 * YT, SM_Y3 = 14 * YT - 4 * YT;
constexpr int SM_T2 = 0, T2S = 4624;
constexpr int SM_RED = SM_KC + 3 * CH2;
constexpr int SM_ROW = SM_RED + 3200;
constexpr int SM_ISC = SM_ROW + 128 + 128 + 32;
constexpr int SM_MX = SM_ISC + 16;
constexpr int SM_WR = SM_MX + 32;
constexpr int SM_DUMMY = SM_WR + DM * 2;
constexpr int RG = 8192;
constexpr int XRS = 1216;
constexpr int FS_SEL = 19 * RG;
constexpr int FS_GU = FS_SEL + 48;
constexpr int SM_TOTAL = FS_GU + 8 * 160;
static_assert(SM_DUMMY + 256 <= SM_TOTAL && SM_ISC % 16 == 0 && CH2 % 16 == 0 && TABB % 16 == 0, "lds");

typedef f32x2 cf;
#define TM(t, a, b) "v_pk_mul_f32 v[" #t ":" #t "+1], v[" #b ":" #b "+1], v[" #a ":" #a "+1] op_sel:[1,0]\n\t"
#define TF(d, a, b, t, n) \
  "v_pk_fma_f32 v[" #d ":" #d "+1], v[" #a ":" #a "+1], v[" #b ":" #b "+1], v[" #t ":" #t "+1] op_sel:[0,0,1] op_sel_hi:[1,0,0] neg_" #n ":[0,0,1]\n\t"
#define TC16(c, v) "v_cvt_pk_f16_f32 v" #c ", v[" #v "], v[" #v "+1]\n\t"
#define TWR(c, row) "ds_write_addtid_b32 v" #c " offset:544*" #row "\n\t"
#define TAB_A \
  TC16(134, 100) TM(106, 102, 102) TM(108, 100, 104) TWR(134, 0) TF(110, 102, 102, 106, lo) TF(112, 100, 104, 108, lo) \
  TM(114, 100, 102) TM(116, 100, 110) TC16(135, 112) TF(118, 100, 102, 114, lo) TF(120, 100, 110, 116, lo) TWR(135, 8) \
  TC16(136, 118) TC16(137, 120) TM(122, 112, 110) TWR(136, 1) TM(124, 118, 104) TF(126, 112, 110, 122, lo) TWR(137, 2) \
  TF(128, 118, 104, 124, lo) TC16(134, 126) TM(130, 126, 110) TC16(135, 128) TWR(134, 10) TF(132, 126, 110, 130, lo) \
  TM(106, 118, 110) TWR(135, 9) TC16(136, 132) TF(108, 118, 110, 106, lo) TM(114, 132, 104) TWR(136, 12) TC16(137, 108) \
  TF(102, 132, 104, 114, lo) TM(116, 132, 110) TWR(137, 3) TC16(134, 102) TF(100, 132, 110, 116, lo) TM(122, 102, 104) TWR(134, 18) \
  TC16(135, 100) TF(124, 102, 104, 122, lo) TM(130, 128, 110) TWR(135, 14) TC16(136, 124) TF(106, 128, 110, 130, lo) \
  TM(118, 124, 104) TWR(136, 19) TC16(137, 106) TF(114, 124, 104, 118, lo) TM(116, 100, 104) TWR(137, 11) TC16(134, 114) \
  TF(132, 100, 104, 116, lo) TM(122, 114, 104) TWR(134, 26) TC16(135, 132) TF(102, 114, 104, 122, lo) TM(130, 120, 110) \
  TWR(135, 17) TC16(136, 102) TF(128, 120, 110, 130, lo) TM(118, 108, 110) TWR(136, 28) TC16(137, 128) TF(124, 108, 110, 118, lo) \
  TM(116, 106, 110) TWR(137, 4) TC16(134, 124) TF(100, 106, 110, 116, lo) TM(122, 112, 104) TWR(134, 5) TC16(135, 100) \
  TF(114, 112, 104, 122, lo) TM(130, 126, 104) TWR(135, 13) TC16(136, 114) TF(120, 126, 104, 130, lo) TM(118, 132, 110) \
  TWR(136, 21) TC16(137, 120) TF(108, 132, 110, 118, lo) TM(116, 102, 104) TWR(137, 20) TC16(134, 108) TF(106, 102, 104, 116, lo) \
  TM(122, 128, 110) TWR(134, 16) TC16(135, 106) TF(112, 128, 110, 122, lo) TM(130, 124, 110) TWR(135, 30) TC16(136, 112) \
  TF(126, 124, 110, 130, lo) TM(118, 100, 110) TWR(136, 6) TC16(137, 126) TF(116, 100, 110, 118, lo) TM(102, 114, 104) TWR(137, 7) \
  TC16(134, 116) TF(122, 114, 104, 102, lo) TM(128, 120, 104) TWR(134, 15) TC16(135, 122) TF(112, 120, 104, 128, lo) \
  TM(130, 132, 104) TWR(135, 23) TC16(136, 112) TF(124, 132, 104, 130, lo) TM(126, 108, 104) TWR(136, 22) TC16(137, 124) \
  TF(118, 108, 104, 126, lo) TM(110, 106, 104) TWR(137, 25) TC16(134, 118) TF(100, 106, 104, 110, lo) TWR(134, 24) TC16(135, 100) \
  TWR(135, 32)
#define TAB_B \
  "v_mov_b32 v137, 0x3c00\n\t" TWR(137, 42) TC16(134, 102) TM(106, 104, 104) TM(108, 100, 102) TWR(134, 43) \
  TF(110, 104, 104, 106, lo) TF(112, 100, 102, 108, hi) TM(114, 102, 102) TM(116, 110, 110) TF(118, 110, 110, 116, lo) \
  TF(120, 102, 102, 114, lo) TC16(135, 120) TM(122, 112, 118) TM(124, 120, 102) TWR(135, 44) TF(126, 112, 118, 122, lo) \
  TF(128, 120, 102, 124, lo) TM(130, 120, 120) TC16(136, 126) TC16(137, 128) TF(132, 120, 120, 130, lo) TWR(136, 27) \
  TM(106, 126, 104) TC16(134, 132) TWR(137, 45) TF(108, 126, 104, 106, lo) TM(100, 126, 110) TWR(134, 46) TC16(135, 108) \
  TF(116, 126, 110, 100, lo) TM(114, 108, 110) TWR(135, 29) TC16(136, 116) TF(122, 108, 110, 114, lo) TM(112, 102, 132) \
  TWR(136, 31) TC16(137, 122) TF(124, 102, 132, 112, lo) TM(130, 120, 132) TWR(137, 33) TC16(134, 124) TF(106, 120, 132, 130, lo) \
  TM(104, 128, 132) TWR(134, 47) TC16(135, 106) TF(100, 128, 132, 104, lo) TM(114, 132, 132) TWR(135, 48) TC16(136, 100) \
  TF(110, 132, 132, 114, lo) TM(112, 124, 132) TWR(136, 49) TC16(137, 110) TF(102, 124, 132, 112, lo) TM(130, 106, 132) \
  TWR(137, 50) TC16(134, 102) TF(120, 106, 132, 130, lo) TM(104, 100, 132) TWR(134, 51) TC16(135, 120) TF(128, 100, 132, 104, lo) \
  TM(114, 126, 118) TWR(135, 52) TC16(136, 128) TF(112, 126, 118, 114, lo) TM(124, 108, 118) TWR(136, 53) TC16(137, 112) \
  TF(130, 108, 118, 124, lo) TM(106, 116, 118) TWR(137, 34) TC16(134, 130) TF(104, 116, 118, 106, lo) TM(100, 122, 118) \
  TWR(134, 35) TC16(135, 104) TF(114, 122, 118, 100, lo) TM(126, 110, 132) TWR(135, 36) TC16(136, 114) TF(124, 110, 132, 126, lo) \
  TM(108, 102, 132) TWR(136, 37) TC16(137, 124) TF(106, 102, 132, 108, lo) TM(116, 120, 132) TWR(137, 54) TC16(134, 106) \
  TF(100, 120, 132, 116, lo) TM(122, 128, 132) TWR(134, 55) TC16(135, 100) TF(126, 128, 132, 122, lo) TM(110, 112, 118) \
  TWR(135, 56) TC16(136, 126) TF(124, 112, 118, 110, lo) TM(108, 130, 118) TWR(136, 57) TC16(137, 124) TF(102, 130, 118, 108, lo) \
  TM(106, 104, 118) TWR(137, 38) TC16(134, 102) TF(116, 104, 118, 106, lo) TM(120, 114, 118) TWR(134, 39) TC16(135, 116) \
  TF(100, 114, 118, 120, lo) TWR(135, 40) TC16(136, 100) TWR(136, 41)
#define TAB_CLOB "v106", "v107", "v108", "v109", "v110", "v111", "v112", "v113", "v114", "v115", "v116", "v117", "v118", "v119", "v120", "v121", "v122", "v123", "v124", "v125", "v126", "v127", "v128", "v129", "v130", "v131", "v132", "v133", "v134", "v135", "v136", "v137"
__device__ __forceinline__ unsigned pkc(cf a) { return __builtin_bit_cast(unsigned, __builtin_convertvector(a, half2v)); }
__device__ __forceinline__ cf cexp(f32x2 hi, f32x2 lo) {
  float mag = __builtin_amdgcn_exp2f(hi[0]);
  mag = fmaf(mag, lo[0], mag);
  const float fr = __builtin_amdgcn_fractf(hi[1]) + lo[1];
  return cf{__builtin_amdgcn_cosf(fr), __builtin_amdgcn_sinf(fr)} * mag;
}
__device__ __forceinline__ unsigned wave_max_u32(unsigned v) {
  v = max(v, (unsigned)__builtin_amdgcn_update_dpp(0, (int)v, 0x111, 0xf, 0xf, false));
  v = max(v, (unsigned)__builtin_amdgcn_update_dpp(0, (int)v, 0x112, 0xf, 0xf, false));
  v = max(v, (unsigned)__builtin_amdgcn_update_dpp(0, (int)v, 0x114, 0xf, 0xf, false));
  v = max(v, (unsigned)__builtin_amdgcn_update_dpp(0, (int)v, 0x118, 0xf, 0xf, false));
  v = max(v, (unsigned)__builtin_amdgcn_update_dpp(0, (int)v, 0x142, 0xa, 0xf, false));
  v = max(v, (unsigned)__builtin_amdgcn_update_dpp(0, (int)v, 0x143, 0xc, 0xf, false));
  return (unsigned)__builtin_amdgcn_readlane((int)v, 63);
}

#define MFMA16(a, b, c) __builtin_amdgcn_mfma_f32_16x16x32_f16((a), (b), (c), 0, 0, 0)
#define MFMAB(a, b, c) __builtin_amdgcn_mfma_f32_16x16x32_bf16((a), (b), (c), 0, 0, 0)

__device__ __forceinline__ half8 ld_frag(const LAS char* p) { return *(const LAS half8*)p; }
__device__ __forceinline__ const LAS unsigned* opaque_lds(const LAS char* p) {
  const LAS unsigned* q = (const LAS unsigned*)p;
  asm volatile("" : "+v"(q));
  return q;
}
template <int O2>
__device__ __forceinline__ half8 ld_toep(const LAS unsigned* q) {
  u32x4 v = {q[0], q[1], q[O2], q[O2 + 1]};
  return __builtin_bit_cast(half8, v);
}
__device__ __forceinline__ half8 band_frag(int lane, unsigned pat) {
  unsigned t = __builtin_amdgcn_ubfe(pat << (lane & 15), 8 * (lane >> 4), 8);
  t |= t << 15;
  u32x4 v;
#pragma unroll
  for (int j = 0; j < 4; j++) v[j] = __umul24(t & (0x10001u << (2 * j)), 0x3c00u >> (2 * j));
  return __builtin_bit_cast(half8, v);
}
__device__ __forceinline__ half8 pack_win(f32x4 a, f32x4 b) {
  u32x4 v = {pkh(a[0], a[1]), pkh(a[2], a[3]), pkh(b[0], b[1]), pkh(b[2], b[3])};
  return __builtin_bit_cast(half8, v);
}

template <int V>
using IC = std::integral_constant<int, V>;
template <int S, int E, class F>
__device__ __forceinline__ void static_for(F&& f) {
  if constexpr (S < E) {
    f(std::integral_constant<int, S>{});
    static_for<S + 1, E>(f);
  }
}
__device__ __forceinline__ half8 ld_td(const LAS char* tp, int i) {
  const volatile LAS u32x2* tb = (const volatile LAS u32x2*)tp;
  const u32x2 lo = tb[8 * (16 - i)], hi = tb[8 * (16 - i) + 1];
  return __builtin_bit_cast(half8, __builtin_shufflevector(lo, hi, 0, 1, 2, 3));
}
__device__ __forceinline__ void conv_dense(half8 (&td)[17], const LAS char* tp, half8 tmask, const LAS char* f1024, const LAS char* ub, LAS char* yd) {
  constexpr int S0 = 9, NS = 73, PF = PFD;
  const f32x4 zero4 = {0.f, 0.f, 0.f, 0.f};
  f32x4 acc[8] = {zero4, zero4, zero4, zero4, zero4, zero4, zero4, zero4};
  half8 u[NS], te;
  unsigned fe = 0;
#pragma unroll
  for (int s = S0; s < S0 + PF; s++) {
    if (TDI && s >= S0 + TDB && s <= 16) {
      td[16 - s] = ld_td(tp, 16 - s);
      td[24 - s] = ld_td(tp, 24 - s);
    }
    if (TDI && s == 16) td[16] = ld_td(tp, 16);
    u[s] = ld_frag(ub + 64 * s);
    if (s == S0 + 3) fe = *(const LAS u16*)f1024;
    SCHED_FENCE();
  }
  static_for<S0, NS>([&](auto sc) {
    constexpr int s = decltype(sc)::value, t0 = s < 16 ? 0 : (s - 9) / 8, t1 = s / 8 < 7 ? s / 8 : 7;
    acc[t0] = MFMA16(td[16 + 8 * t0 - s], u[s], acc[t0]);
    SCHED_FENCE();
    if constexpr (s + PF < NS) u[s + PF] = ld_frag(ub + 64 * (s + PF));
    if constexpr (s == 67) *(LAS f32x4*)(yd + 6 * YT) = acc[6];
    SCHED_FENCE();
    if constexpr (t0 + 1 <= t1) acc[t0 + 1] = MFMA16(td[24 + 8 * t0 - s], u[s], acc[t0 + 1]);
    SCHED_FENCE();
    if constexpr (s >= 24 && (s & 7) == 0 && s < 72) *(LAS f32x4*)(yd + (s / 8 - 3) * YT) = acc[s / 8 - 3];
    if constexpr (s == 14) {
      fe |= fe << 16;
      te = tmask * __builtin_bit_cast(half8, u32x4{fe, fe, fe, fe});
    }
    SCHED_FENCE();
    if constexpr (t0 + 2 <= t1) acc[t0 + 2] = MFMA16(td[32 + 8 * t0 - s], u[s], acc[t0 + 2]);
    if constexpr (s >= 16 && s <= 40 && (s & 7) == 0) acc[s / 8 + 2] = MFMA16(te, u[s], acc[s / 8 + 2]);
    SCHED_FENCE();
  });
  *(LAS f32x4*)(yd + 7 * YT) = acc[7];
}

typedef __amdgpu_buffer_rsrc_t rsrc_t;
template <int AUX = LAUX>
__device__ __forceinline__ u32x4 ldg16(rsrc_t rs, int off, int so = 0) {
  asm volatile("" : "+s"(so) : : "memory");
  return __builtin_amdgcn_raw_buffer_load_b128(rs, off, so, AUX);
}
__device__ __forceinline__ void stg16(rsrc_t rs, int off, u32x4 v) { __builtin_amdgcn_raw_buffer_store_b128(v, rs, off, 0, SAUX); }
__device__ __forceinline__ void dma16(const char* g, unsigned lds) {
  unsigned keep;
  asm volatile("s_mov_b32 %0, m0\n\ts_mov_b32 m0, %2\n\ts_nop 0\n\tglobal_load_lds_dwordx4 %1, off" WNTS "\n\ts_mov_b32 m0, %0"
               : "=&s"(keep) : "v"(g), "s"(lds) : "memory");
}
__device__ __forceinline__ void dma16nt(const char* g, unsigned lds) {
  unsigned keep;
  asm volatile("s_mov_b32 %0, m0\n\ts_mov_b32 m0, %2\n\ts_nop 0\n\tglobal_load_lds_dwordx4 %1, off nt\n\ts_mov_b32 m0, %0"
               : "=&s"(keep) : "v"(g), "s"(lds) : "memory");
}
__device__ __forceinline__ void dma4(const char* g, unsigned lds) {
  unsigned keep;
  asm volatile("s_mov_b32 %0, m0\n\ts_mov_b32 m0, %2\n\ts_nop 0\n\tglobal_load_lds_dword %1, off\n\ts_mov_b32 m0, %0"
               : "=&s"(keep) : "v"(g), "s"(lds) : "memory");
}
__device__ __forceinline__ void dma4s(size_t base, unsigned off, unsigned lds) {
  unsigned keep;
  asm volatile("s_mov_b32 %0, m0\n\ts_mov_b32 m0, %3\n\ts_nop 0\n\tglobal_load_lds_dword %1, %2\n\ts_mov_b32 m0, %0"
               : "=&s"(keep) : "v"(off), "s"(base), "s"(lds) : "memory");
}
__device__ __forceinline__ void wait_ticks(int t) {
  const unsigned long long t0 = __builtin_amdgcn_s_memrealtime();
  while (__builtin_amdgcn_s_memrealtime() - t0 < (unsigned long long)t) __builtin_amdgcn_s_sleep(1);
}
#define UPOS 1
#define DLY_X 30
#define DLY_W 0
#define HB 1
#define DLY_H 55
#define DLY_R 0
#define DLY_L 260
#define DLY_P 120

__device__ __forceinline__ int key_row(unsigned k) { return (int)(~(k >> 2) & (SEQ - 1)); }
__device__ __forceinline__ int key_slot(int lane) { return (lane & 3) | ((lane >> 1) & 4); }
__device__ __forceinline__ int key_addr(unsigned k, int slot) {
  return WS_XS + ((k & 2) ? 2 * NLN + slot : (int)((~(k >> 5) & (2 * NLN - 2)) | (k & 1))) * XSS;
}
struct SelMasks {
  unsigned m[15];
  __device__ __forceinline__ SelMasks(int lane) {
    const unsigned b0 = -(lane & 1), b1 = -((lane >> 1) & 1), b3 = -((lane >> 3) & 1), b4 = -((lane >> 4) & 1), b5 = -(lane >> 5);
    const unsigned t[15] = {~(b0 ^ b1), ~(b1 ^ b3), ~(b0 ^ b3), ~b3, ~b1, ~b0, b3, b1, b0, ~(b3 ^ b4), ~(b1 ^ b4), ~(b0 ^ b4), ~(b3 ^ b5), ~(b1 ^ b5), ~(b0 ^ b5)};
#pragma unroll
    for (int i = 0; i < 15; i++) {
      m[i] = t[i];
      asm volatile("" : "+v"(m[i]));
    }
  }
};
template <int CTRL>
__device__ __forceinline__ unsigned cex(unsigned v, unsigned m) {
  const unsigned o = dppu<CTRL>(v);
  return max(min(v, o), min(max(v, o), m));
}
__device__ __forceinline__ unsigned clean8(unsigned v, const unsigned* m) { return cex<0xB1>(cex<0x4E>(cex<0x128>(v, m[0]), m[1]), m[2]); }
template <bool EAGER>
__device__ __forceinline__ unsigned select8(rsrc_t rs, unsigned tag, int lane, const SelMasks& sm, char* ws, int bid, unsigned& stamp) {
  const int off = WS_KR + (bid & 7) * 2048 + lane * 16;
  unsigned m1, t1, t2, m4, third;
  u32x4 a, c, d, e;
  if (EAGER) {
    const u32x4 rq = {(unsigned)(size_t)ws, (unsigned)((size_t)ws >> 32) & 0xffffu, (unsigned)WS_END, 0x00020000u};
    unsigned long long t;
    int n = SPIN_CAP;
#define KASK(r) "buffer_load_dwordx4 v[" #r ":" #r "+3], %11, %12, 0 offen sc1\n\tbuffer_load_dwordx4 v[" #r "+4:" #r "+7], %11, %12, 0 offen offset:1024 sc1\n\t"
#define KCHK(r, l)                                                                                                          \
  "s_waitcnt vmcnt(2)\n\tv_cmp_eq_u32 vcc, %13, v[" #r "+3]\n\tv_cmp_eq_u32 %9, %13, v[" #r "+7]\n\ts_and_b64 %9, %9, vcc\n\t" \
  "s_cmp_eq_u64 %9, -1\n\ts_cbranch_scc1 " #l "\n\t"
#define KUSE(r)                                                                                                             \
  "v_max_u32 %0, v[" #r "], v[" #r "+4]\n\tv_min_u32 %1, v[" #r "], v[" #r "+4]\n\tv_max_u32 %2, v[" #r "+1], v[" #r "+5]\n\t"     \
  "v_min_u32 %3, v[" #r "+1], v[" #r "+5]\n\tv_max_u32 %4, v[" #r "+2], v[" #r "+6]\n\t"
    asm volatile(KASK(148) "s_sleep %14\n1:\n\t" KASK(156) KCHK(148, 2f) KASK(148) KCHK(156, 3f) "s_sub_u32 %10, %10, 1\n\ts_cbranch_scc0 1b\n"
                 "3:\n\t" KUSE(156) "s_branch 4f\n2:\n\t" KUSE(148) "\n4:"
                 : "=&v"(m1), "=&v"(t1), "=&v"(t2), "=&v"(m4), "=&v"(third), "=&{v[148:151]}"(a), "=&{v[152:155]}"(c), "=&{v[156:159]}"(d),
                   "=&{v[160:163]}"(e), "=&s"(t), "+s"(n)
                 : "v"(off), "s"(rq), "s"(tag), "n"(KPP) : "vcc", "scc", "memory");
  } else {
    _Pragma("unroll 1") for (int spin = 0; spin < SPIN_CAP; spin++) {
      a = ldg16(rs, off);
      c = ldg16(rs, off, 1024);
      if (__builtin_expect(__all(a[3] == tag && c[3] == tag), 1)) break;
      __builtin_amdgcn_s_sleep(2);
    }
    const unsigned now = (unsigned)__builtin_amdgcn_s_memrealtime();
    stamp = now - 0xffffu + wave_max_u32(max((a[2] - now - 1u) & 0xffffu, (c[2] - now - 1u) & 0xffffu));
    m1 = max(a[0], c[0]), t1 = min(a[0], c[0]), t2 = max(a[1], c[1]), m4 = min(a[1], c[1]), third = max(a[2], c[2]);
  }
  TSL(1, 8);
  third |= 0xffffu;
  unsigned m2 = max(t1, t2), m3 = min(t1, t2);
  unsigned x = clean8(cex<0xB1>(cex<0x4E>(cex<0xB1>(m1, sm.m[0]), sm.m[1]), sm.m[2]), sm.m + 3);
  const unsigned y = clean8(cex<0xB1>(cex<0x4E>(cex<0xB1>(m2, sm.m[0]), sm.m[1]), sm.m[2]), sm.m + 6);
  x = clean8(max(x, y), sm.m + 3);
  x = clean8(max(x, dppu<0x140>(x)), sm.m + 9);
  {
    auto r = __builtin_amdgcn_permlane16_swap(x, x, false, false);
    x = clean8(max(r[0], r[1]), sm.m + 12);
    auto q = __builtin_amdgcn_permlane32_swap(x, x, false, false);
    x = max(q[0], q[1]);
  }
  unsigned k = min(x, dppu<0xB1>(x));
  k = min(k, dppu<0x4E>(k));
  k = min(k, dppu<0x128>(k));
  if (__builtin_expect(__any(max(m3, third) > k), 0)) {
    const int slot = key_slot(lane);
    _Pragma("unroll 1") for (int r = 0; r < 8; r++) {
      k = wave_max_u32(m1);
      x = slot == r ? k : x;
      const bool e = m1 == k;
      m1 = e ? m2 : m1;
      m2 = e ? m3 : m2;
      m3 = e ? m4 : m3;
      m4 = e ? 0u : m4;
    }
    if (__any(third > k)) {
      k = 0u;
      _Pragma("unroll 1") for (int r = 0; r < 8; r++) {
        unsigned v;
        _Pragma("unroll 1") for (int spin = 0; spin < SPIN_CAP; spin++) {
          bool ok = true;
          v = 0u;
          _Pragma("unroll 4") for (int i = 0; i < 16; i++) {
            const u32x4 f = ldg16(rs, WS_KF + lane * 16, i * 1024);
            ok = ok && f[1] == tag && f[3] == tag;
            v = max(v, max(f[0] <= k - 1u ? f[0] : 0u, f[2] <= k - 1u ? f[2] : 0u));
          }
          if (__all(ok)) break;
          __builtin_amdgcn_s_sleep(1);
        }
        k = wave_max_u32(v);
        x = slot == r ? k : x;
      }
    }
  }
  if (EAGER) asm volatile("s_waitcnt vmcnt(0)" : "+v"(x) : "{v[148:151]}"(a), "{v[152:155]}"(c), "{v[156:159]}"(d), "{v[160:163]}"(e));
  return x;
}

__device__ __forceinline__ int last_start(rsrc_t rs, unsigned tag, int lane, int bid, unsigned ref) {
  u32x4 p, q;
  _Pragma("unroll 1") for (int spin = 0; spin < SPIN_CAP; spin++) {
    p = ldg16(rs, WS_CK + (bid & 7) * 2048 + lane * 16);
    q = ldg16(rs, WS_CK + (bid & 7) * 2048 + 1024 + (lane & 31) * 16);
    if (__all(p[1] == tag && p[3] == tag && q[1] == tag && q[3] == tag)) break;
    __builtin_amdgcn_s_sleep(4);
  }
  const int lag = max(max((int)(p[0] - ref), (int)(p[2] - ref)), max((int)(q[0] - ref), (int)(q[2] - ref)));
  return min((int)wave_max_u32((unsigned)max(lag + 4096, 0)) - 4096, 1000);
}
__device__ __forceinline__ int late_group(size_t wg, int bid) { return (3 - (int)(wg >> 7) - bid) & 7; }
__device__ __forceinline__ int ffn_group(size_t wg, int bid) {
  const int l = late_group(wg, bid), i = bid >> 3;
  return (bid & 7) * 9 + (bid < NFFN ? (i == l ? 8 : i) : l);
}
__device__ __forceinline__ void down_dma(size_t wd, int j, LAS char* L, int wave, int lane, int r0, int r1) {
#pragma unroll
  for (int k = 0; k < 3; k++) {
    const int slot = 3 * (wave - 9) + k, t = slot * 64 + lane, r8 = t / 72, q8 = (t - r8 * 72) ^ (r8 & 2);
    const char* p = (const char*)wd + (unsigned)((j * 64 + r8) * 1152);
    _Pragma("unroll 1") for (int i = r0; i < r1; i++)
        (DNT ? dma16nt : dma16)(p + (unsigned)(i * 9216 + (q8 ^ ((i & 1) << 2)) * 16), (unsigned)(size_t)L + slot * 1024 + i * 9216);
  }
}
__device__ __forceinline__ unsigned stats_role(size_t argp, unsigned cpf, LAS char* L, unsigned long long r_entry) {
  const int tid = threadIdx.x, lane = tid & 63, bid = blockIdx.x, fw = bid;
  size_t x, wsp, wd, wg;
  asm volatile(
      "s_waitcnt lgkmcnt(0)\n\ts_load_dwordx2 %0, %4, 0x0\n\ts_load_dwordx2 %1, %4, 0x30\n\ts_load_dwordx2 %2, %4, 0x58\n\t"
      "s_load_dwordx2 %3, %4, 0x48"
      : "=&s"(x), "=&s"(wsp), "=&s"(wd), "=&s"(wg) : "s"(argp));
  SCHED_FENCE();
  unsigned xo0 = (bid * 32 + (tid >> 4)) * (DM * 2) + (lane & 15) * 8;
  asm volatile("" : "+v"(xo0));
  asm volatile("s_waitcnt lgkmcnt(0)" : "+s"(x), "+s"(wsp), "+s"(wd), "+s"(wg));
  const int wave = __builtin_amdgcn_readfirstlane(tid >> 6);
  char* const ws = (char*)(GAS char*)wsp;
  const rsrc_t rs = __builtin_amdgcn_make_buffer_rsrc(ws, 0, WS_END, 0x00020000);
  unsigned tagv = WS_EPOCH;
  asm volatile("global_load_dword %0, %0, %1 sc1" : "+v"(tagv) : "s"(wsp) : "memory");
  SCHED_FENCE();
  unsigned tag;
  if (wave >= 8) {
    if (ED) {
      __builtin_amdgcn_s_barrier();
      if (wave > 8) down_dma(wd, ffn_group(wg, bid), L, wave, lane, 0, ED);
      else DRAIN();
    }
    asm volatile("s_waitcnt vmcnt(%2)" : "+v"(tagv) : "v"(cpf), "n"(3 * ED));
    tag = __builtin_amdgcn_readfirstlane(tagv) + 1u;
    asm volatile("" : "+s"(tag));
  } else {
    u32x2 xw[9];
#pragma unroll
    for (int k = 0; k < 9; k++) xw[k] = *(GAS const u32x2*)(x + xo0 + 128 * k);
    SCHED_FENCE();
    if (ED) __builtin_amdgcn_s_barrier();
    unsigned one2 = 0x3f803f80u;
    asm volatile("" : "+s"(one2));
    float s0 = 0.f, s1 = 0.f, q0 = 0.f, q1 = 0.f;
#pragma unroll
    for (int k = 0; k < 9; k++) {
      const unsigned a = xw[k][0], c = xw[k][1];
      s0 = dot2(a, one2, s0);
      q0 = dot2(a, a, q0);
      s1 = dot2(c, one2, s1);
      q1 = dot2(c, c, q1);
    }
    const float mean = red16_sum(s0 + s1) * (1.0f / DM), mbf = rbf(mean);
    float q = red16_sum(q0 + q1), dm = mean;
    if (__builtin_expect(__any(!(q * (1.0f / DM) >= mean * mean * 1.002f)), 0)) {
      f32x2 q2 = {0.f, 0.f};
#pragma unroll
      for (int k = 0; k < 18; k++) {
        const f32x2 dd = unpk(xw[k >> 1][k & 1]) - mbf;
        q2 = __builtin_elementwise_fma(dd, dd, q2);
      }
      q = red16_sum(hadd(q2));
      dm = mean - mbf;
    }
    float ri = ln_rinv(q, dm);
    asm volatile("" : "+v"(tagv), "+v"(ri));
    auto rr = __builtin_amdgcn_permlane16_swap(__float_as_uint(ri), __float_as_uint(ri), false, false);
    auto m2 = __builtin_amdgcn_permlane16_swap(__float_as_uint(mbf), __float_as_uint(mbf), false, false);
    const unsigned mp = __builtin_amdgcn_perm(m2[1], m2[0], 0x07060302u);
    if ((lane & 31) == 0) stg16(rs, WS_ST + (16 * fw + (tid >> 5)) * 16, u32x4{rr[0], rr[1], mp, tagv + 1u});
    tag = __builtin_amdgcn_readfirstlane(tagv) + 1u;
    asm volatile("" : : "v"(cpf), "s"(tag));
  }
  TS(1, 10);
  return tag;
}

__device__ __forceinline__ void ffn_role(size_t argp, unsigned tag, LAS char* L) {
  const int tid = threadIdx.x, lane = tid & 63, bid = blockIdx.x;
  size_t wsp, wdp;
  u64x2 gu;
  asm volatile("s_load_dwordx2 %0, %3, 0x30\n\ts_load_dwordx4 %1, %3, 0x48\n\ts_load_dwordx2 %2, %3, 0x58" : "=&s"(wsp), "=&s"(gu), "=&s"(wdp) : "s"(argp));
  SCHED_FENCE();
  const int wave = __builtin_amdgcn_readfirstlane(tid >> 6);
  const int hm = bid < NFFN ? 3 : (bid & 8 ? 2 : 1);
  const bool mine = wave < 8 && (hm >> (wave >> 2) & 1);
  const int g = lane >> 4, tq = (lane & 15) >> 2, tp = lane & 3;
  const bool full = hm == 3;
  const int r8 = tid / 72;
  const int gr = full ? tid >> 4 : tid >> 3;
  const int gc = full ? (lane & 7) ^ ((gr & 1) | ((gr >> 1) & 6)) : (hm & 2) * 2 + ((lane & 3) ^ ((gr >> 2) & 3));
  unsigned go = gr * (FF * 2) + gc * 16;
  asm volatile("" : "+v"(go));
  asm volatile("s_waitcnt lgkmcnt(0)" : "+s"(wsp), "+s"(gu), "+s"(wdp));
  char* const ws = (char*)(GAS char*)wsp;
  const rsrc_t rs = __builtin_amdgcn_make_buffer_rsrc(ws, 0, WS_END, 0x00020000);
  const int j = ffn_group(gu[0], bid);
  const char* gsrc = (const char*)((lane & (full ? 8 : 4)) ? gu[1] : gu[0]) + (go + j * 128);
  const unsigned lb = (unsigned)(size_t)L + wave * 1024 + 9 * RG;
  auto issue_gu = [&](int n, unsigned step) {
    if (wave < 8) {
      _Pragma("unroll 1") for (int i = 0; i < n; i++) {
        (GNT ? dma16nt : dma16)(gsrc, lb + i * RG);
        gsrc += step;
      }
    }
  };
  __syncthreads();
  TS(1, 11);
  if (DLY_W) wait_ticks(DLY_W);
  issue_gu(full ? 10 : 9, full ? 32 * FF * 2 : 64 * FF * 2);
  if (wave > 8) down_dma(wdp, j, L, wave, lane, full ? ED : (hm & 2) * 2, hm == 1 ? 4 : 8);
  s16x4 fa[18][2], fd[3][4];
  const int fr = 16 * (g >> 1) + 4 * (g & 1) + (tq & 1) + 8 * (tq >> 1), fx = (g & 1) | ((tq >> 1) << 1);
  const LAS char* gup = L + 9 * RG + (tp & 1) * 8 +
                        (full ? fr * 256 + ((wave ^ ((tq & 1) | (fx << 1))) + 8 * (tp >> 1)) * 16 : fr * 128 + (((wave & 3) ^ fx) + 4 * (tp >> 1)) * 16);
  const LAS char* gup2 = gup + 8 * RG;
  asm volatile("" : "+v"(gup), "+v"(gup2));
  auto read_gu = [&](auto ks0, auto n, auto reg0, auto sz) {
#pragma unroll
    for (int i = 0; i < n; i++)
#pragma unroll
      for (int h = 0; h < 2; h++) {
        const int o = reg0 * RG + i * sz + h * (sz / 16);
        fa[ks0 + i][h] = __builtin_amdgcn_ds_read_tr16_b64_v4i16((LAS s16x4*)(o < 8 * RG ? gup + o : gup2 + (o - 8 * RG)));
      }
  };
  auto read_down = [&]() {
    const int f = (tq >> 1) | ((g & 1) << 1);
#pragma unroll
    for (int t = 0; t < 3; t++) {
      const LAS char* p = L + (8 * g + tq) * 1152 + ((3 * wave + t) ^ f) * 32 + tp * 8;
#pragma unroll
      for (int h = 0; h < 4; h++)
        fd[t][h] = __builtin_amdgcn_ds_read_tr16_b64_v4i16((LAS s16x4*)(p + (32 * (h >> 1) + 4 * (h & 1)) * 1152));
    }
  };
  if (full) {
    if (wave < 8) asm volatile("s_waitcnt vmcnt(2)" ::: "memory");
    __syncthreads();
    if (mine) read_gu(IC<0>{}, IC<8>{}, IC<0>{}, IC<RG>{});
    __syncthreads();
    TS(1, 6);
    issue_gu(8, 32 * FF * 2);
    if (wave > 8) DRAIN();
    asm volatile("s_waitcnt vmcnt(4)" ::: "memory");
    __syncthreads();
    TS(1, 1);
    if (mine) {
      read_gu(IC<8>{}, IC<2>{}, IC<8>{}, IC<RG>{});
      read_gu(IC<10>{}, IC<4>{}, IC<0>{}, IC<RG>{});
    }
  } else {
    DRAIN();
    __syncthreads();
    TS(1, 1);
    if (mine) read_gu(IC<0>{}, IC<14>{}, IC<0>{}, IC<RG / 2>{});
  }
  read_down();
  const int c15 = lane & 15;
  const int xs = r8, xc = tid - xs * 72;
  const LAS char* selr = L + FS_SEL + (xs & 7) * 4;
  LAS char* xw2 = L + xs * XRS + xc * 16;
  const LAS char* xr = L + (lane & 7) * XRS + (c15 >> 3) * 16 + g * 32;
  LAS char* guw = L + FS_GU + (4 * (g & 1) + 2 * (g >> 1) + (c15 >> 3)) * 160 + (wave * 8 + (lane & 7)) * 2;
  const LAS char* gur = L + FS_GU + (lane & 7) * 160 + 16 * g;
  int xo = xc * 16, po = WS_PART + (bid < NFFN ? bid : bid - LATE0) * (8 * DM * 4) + 3 * wave * 512 + (lane & 7) * 64 + 16 * g;
  int sidx = c15 < 8 ? 0x4444 : 0xeeee;
  asm volatile("" : "+v"(selr), "+v"(guw), "+v"(gur), "+v"(xo), "+v"(po), "+v"(sidx), "+v"(xw2), "+v"(xr));
  DRAIN();
  TS(1, 2);
  if (wave == 9) {
    const SelMasks sm(lane);
    LAS char* selw = L + FS_SEL + key_slot(lane) * 4;
    asm volatile("" : "+v"(selw));
    const unsigned now = (unsigned)__builtin_amdgcn_s_memrealtime();
    const int due = (full ? last_start(rs, tag, lane, bid, now) : __builtin_amdgcn_readfirstlane(*(const LAS int*)(L + FS_SEL + 32)) - (int)now) + DLY_K;
    while ((int)((unsigned)__builtin_amdgcn_s_memrealtime() - now) < due) __builtin_amdgcn_s_sleep(1);
    TSL(1, 3);
    unsigned unused;
    *(LAS int*)selw = key_addr(select8<KPP != 0>(rs, tag, lane, sm, ws, bid, unused), key_slot(lane));
  }
  __syncthreads();
  TS(1, 9);
  if (wave < 9) {
    xo += *(const LAS int*)selr;
    u32x4 v = ldg16(rs, xo);
    SCHED_FENCE();
    if (mine) {
      if (full) read_gu(IC<14>{}, IC<4>{}, IC<4>{}, IC<RG>{});
      else read_gu(IC<14>{}, IC<4>{}, IC<7>{}, IC<RG / 2>{});
    }
    SCHED_FENCE();
    _Pragma("unroll 1") for (int spin = 0; spin < SPIN_CAP; spin++) {
      if (__builtin_expect(__all(max(max(v[0], v[1]), max(v[2], v[3])) != ~0u), 1)) break;
      __builtin_amdgcn_s_sleep(2);
      v = ldg16(rs, xo);
    }
    *(LAS u32x4*)xw2 = v;
  }
  __syncthreads();
  TS(1, 4);
  if (mine) {
    bf8v xf[9];
#pragma unroll
    for (int i = 0; i < 9; i++) xf[i] = *(const LAS bf8v*)(xr + 128 * i);
    SCHED_FENCE();
    f32x4 acc = {0.f, 0.f, 0.f, 0.f};
#pragma unroll
    for (int i = 0; i < 9; i++) {
      const s16x8 lo = __builtin_shufflevector(fa[2 * i][0], fa[2 * i][1], 0, 1, 2, 3, 4, 5, 6, 7);
      const s16x8 hi = __builtin_shufflevector(fa[2 * i + 1][0], fa[2 * i + 1][1], 0, 1, 2, 3, 4, 5, 6, 7);
      acc = __builtin_amdgcn_smfmac_f32_16x16x64_bf16(
          xf[i], __builtin_bit_cast(bf16v, __builtin_shufflevector(lo, hi, 0, 1, 2, 3, 4, 5, 6, 7, 8, 9, 10, 11, 12, 13, 14, 15)), acc, sidx, 0, 0);
    }
    const float a0 = acc[0], a1 = acc[1], a2 = acc[2], a3 = acc[3];
    auto r0 = __builtin_amdgcn_permlane32_swap(__float_as_uint(a0), __float_as_uint(a2), false, false);
    auto r1 = __builtin_amdgcn_permlane32_swap(__float_as_uint(a1), __float_as_uint(a3), false, false);
    const float s0 = __uint_as_float(r0[0]) + __uint_as_float(r0[1]), s1 = __uint_as_float(r1[0]) + __uint_as_float(r1[1]);
    const float gs = __int_as_float(__builtin_amdgcn_update_dpp(__float_as_int(s0), __float_as_int(s1), 0x128, 0xf, 0xc, false));
    const float us = __int_as_float(__builtin_amdgcn_update_dpp(__float_as_int(s1), __float_as_int(s0), 0x128, 0xf, 0x3, false));
    const float gp = rbf(gs);
    const float gq = rbf(gp * __builtin_amdgcn_rcpf(1.0f + __expf(-gp)));
    *(LAS u16*)guw = (u16)pkbf(gq * rbf(us), 0.f);
  }
  __syncthreads();
  {
    const bf8v b0 = *(const LAS bf8v*)gur, b1 = *(const LAS bf8v*)(gur + 64);
    const f32x4 z4 = {0.f, 0.f, 0.f, 0.f};
    f32x4 acc[3] = {z4, z4, z4};
    if (hm & 1) {
#pragma unroll
      for (int t = 0; t < 3; t++)
        acc[t] = MFMAB(__builtin_bit_cast(bf8v, __builtin_shufflevector(fd[t][0], fd[t][1], 0, 1, 2, 3, 4, 5, 6, 7)), b0, z4);
    }
    if (hm & 2) {
#pragma unroll
      for (int t = 0; t < 3; t++)
        acc[t] = MFMAB(__builtin_bit_cast(bf8v, __builtin_shufflevector(fd[t][2], fd[t][3], 0, 1, 2, 3, 4, 5, 6, 7)), b1, acc[t]);
    }
    SCHED_FENCE();
    if (c15 < 8) {
#pragma unroll
      for (int t = 0; t < 3; t++) {
        u32x4 o = __builtin_bit_cast(u32x4, acc[t]);
        o[0] = min(o[0], 0xfffffffeu);
        stg16(rs, po + t * 512, o);
      }
    }
  }
  TS(1, 5);
}

__global__ __launch_bounds__(NTHR, 3) void k_all(const u16*, const u16*, const u16*, const u16*, const u16*, const u16*, char*, const u16*,
                                                 const u16*, const u16*, const u16*, const u16*, u16*) {
  __shared__ __attribute__((aligned(16))) char smem[SM_TOTAL];
  LAS char* const L = (LAS char*)smem;
  const int bid = blockIdx.x;
  const size_t argp = (size_t)__builtin_amdgcn_kernarg_segment_ptr();
  u64x8 a8;
  u64x4 a4;
  size_t a1;
  unsigned cpf;
  {
    size_t pc;
    int t;
    asm volatile("s_load_dwordx16 %0, %3, 0x0\n\ts_load_dwordx8 %1, %3, 0x40\n\ts_load_dwordx2 %2, %3, 0x60" : "=&s"(a8), "=&s"(a4), "=&s"(a1) : "s"(argp));
    asm volatile(
        "s_getpc_b64 %1\n\ts_lshr_b32 %2, %3, 3\n\tv_min_u32 %0, 6, %4\n\ts_mul_i32 %2, %2, 7\n\tv_add_lshl_u32 %0, %0, %2, 7\n\t"
        "global_load_dword %0, %0, %1"
        : "=&v"(cpf), "=&s"(pc), "=&s"(t) : "s"(bid), "v"(threadIdx.x) : "scc");
  }
  SCHED_FENCE();
  const int tid = threadIdx.x, lane = tid & 63, b = bid - NFFN;
  unsigned ftag;
  if (b < 0) {
    ftag = stats_role(argp, cpf, L, 0);
  } else {
  const int wave = __builtin_amdgcn_readfirstlane(tid >> 6);
  const bool is_ln = b < NLN;
  const int hl = lane & 31;
  const int ch = (wave & 3) < 3 ? (wave & 3) : (wave >> 2), sub = (wave & 3) < 3 ? (wave >> 2) : 3;
  const int d = 3 * b + ch;
  const int xt = 4 * b + wave, hrow = 8 * ((xt >> 1) & 127) + (lane >> (3 + XM));
  const int grp = ((xt >> 8) << 5) + ((xt & 1) << 4) + (lane & (XM ? 15 : 7));
  unsigned xo = hrow * (DM * 4) + grp * 12, po = d * NM * 2 + lane * 4, lo = 2 * d & ~3;
  int cpo = WS_UP + (grp * 2048 + hrow) * 16;
  {
    LAS char* zc = L + (wave >> 2) * CH2 + SM_KC + 16 * (tid & 255);
    const u32x4 z = {0u, 0u, 0u, 0u};
    *(LAS u32x4*)zc = z;
    *(LAS u32x4*)(zc + 4096) = z;
  }
  const int xg = b & 7, sl = b >> 3, sl12 = sl < 12 ? sl : sl - 12;
  unsigned s0o = (144 * min(lane >> 4, S0M - 1) + 12 * sl12 + wave) * (FF * 2) + xg * 1152 + min(lane & 15, 8) * 128;
  if (S0M) asm volatile("" : "+v"(s0o));
  unsigned dso = 6 * b & ~3;
  asm volatile("" : "+v"(xo), "+v"(po), "+v"(lo), "+v"(cpo), "+v"(dso));
  SCHED_FENCE();
  asm volatile("s_waitcnt lgkmcnt(0)" : "+s"(a8), "+s"(a4), "+s"(a1));
  u32x3 cp[4];
  auto load_x = [&]() {
    if (wave < 4) {
#pragma unroll
      for (int i = 0; i < 4; i++) cp[i] = *(GAS const u32x3u*)(a8[0] + xo + ((i & 1) * (DM * 2) + (i >> 1) * (XM ? DM * 16 : 96)));
    }
    SCHED_FENCE();
  };
  if (XF) load_x();
  const unsigned r_ldt = *(GAS const unsigned*)(a8[5] + lo), r_lar = *(GAS const unsigned*)(a8[1] + po), r_aim = *(GAS const unsigned*)(a8[2] + po);
  const unsigned r_cr = *(GAS const unsigned*)(a8[3] + po), r_ci = *(GAS const unsigned*)(a8[4] + po);
  typedef u32x2 u32x2u __attribute__((aligned(4)));
  u32x2 r_dsk = *(GAS const u32x2u*)(a8[7] + dso);
  char* const ws = (char*)(GAS char*)a8[6];
  GAS char* const out = (GAS char*)a1;
  const rsrc_t rs = __builtin_amdgcn_make_buffer_rsrc(ws, 0, WS_END, 0x00020000);
  const unsigned tagv = __builtin_amdgcn_raw_buffer_load_b32(rs, WS_EPOCH, 0, SC1);
  SCHED_FENCE();
  if (!XF) load_x();
  unsigned s0v = 0;
  if (S0M) {
    __builtin_amdgcn_s_barrier();
    s0v = *(GAS const unsigned*)((sl < 12 ? a4[1] : a4[2]) + s0o);
    SCHED_FENCE();
  }
  TWDECL;
  TWP(4);
  auto prefetch = [&](int i) {
    const char* base = (const char*)(sl < 12 ? a4[1] : a4[2]) + (144 * i + 48 * ch + 4 * sl12) * (FF * 2) + xg * 1152;
    dma4(base + (unsigned)((lane >> 4) * (FF * 2) + min(lane & 15, 8) * 128), (unsigned)(size_t)L + SM_DUMMY);
  };
  const int part_off = WS_PART + (tid >> 3) * (8 * DM * 4) + (b - RED0) * 128 + (tid & 7) * 16;
  LAS char *const C = L + ch * TABB, *const K = L + ch * CH2;
  const int c15 = lane & 15, g = lane >> 4;
  unsigned pfo = ((9 * xg + (sl & 7)) * 576 + DR0 * 72 + ((sl >> 3) * 4 + (wave & 3)) * ((8 - DR0) * 6) + min(lane, (8 - DR0) * 6 - 1)) * 128;
  unsigned pfq = (12 * sl12 + 3 * (wave & 3) + min(lane >> 4, 2)) * (FF * 2) + xg * 1152 + min(lane & 15, 8) * 128;
  const int lg = late_group(a4[1], bid);
  const char* pfx = (lane & 31) < 12 ? (const char*)(sl < 12 ? a4[1] : a4[2]) + (unsigned)(((sl12 * 4 + (wave & 3)) * 12 + (lane & 31)) * (FF * 2) + xg * 1152 +
                                                                                            (lg ? lg : (lane >> 5) * 8) * 128)
                                     : (const char*)a4[3] + (unsigned)(((9 * xg + lg) * 576 + (sl * 4 + (wave & 3)) * 6 + min((lane & 31) - 12, 5)) * 128);
  asm volatile("" : "+v"(pfx));
  auto pf4 = [&](int m) {
#pragma unroll
    for (int i = 0; i < 4; i++)
      if (m >> i & 1) dma4s((sl < 12 ? a4[1] : a4[2]) + 144 * i * (FF * 2), pfq, (unsigned)(size_t)L + SM_DUMMY);
    if (m & 16) dma4s(a4[3], pfo, (unsigned)(size_t)L + SM_DUMMY);
    if (m & 32) dma4(pfx, (unsigned)(size_t)L + SM_DUMMY);
  };
  unsigned hs = (sub & 1) ? 0x03020c0cu : 0x01000c0cu, hs2 = (d & 1) ? 0x03020c0cu : 0x01000c0cu;
  LAS char* yb = C + g * 320 + c15 * 16;
  const LAS char *pa, *pa2, *pb, *uf16 = K + SM_US + 2 * UPAD + 32 * c15 + 16 * g;
  LAS char *kd, *kd2;
  unsigned ks0, ks1;
  half8 bA, bB;
  if (sub == 0) {
    pa = C + (((unsigned)(0x33982210cdef4567ull >> (4 * c15)) & 15) + 2 * (c15 & 8)) * TAB_STRIDE + 16 * g;
    pa2 = C + (((unsigned)(0x7763554289ab0123ull >> (4 * c15)) & 15) + 2 * (c15 & 8)) * TAB_STRIDE + 16 * g;
    pb = C + (TR_H + c15) * TAB_STRIDE + 16 * g;
    kd = K + KC_RD + 2 * 544 - 16 * lane;
    kd2 = K + KC_RD + (~c15 & 3) * RDS + 2 * (535 - (c15 | 3) + 8 * g) - 64 * 16;
    uf16 -= 32 * 33;
    bA = band_frag(lane, 0x3u << 12);
    const int s = lane & 3;
    const u32x4 t = {s < 2 ? 0x0c0c0c0cu : (s == 2 ? 0x01000c0cu : 0x01000100u), s == 0 ? 0x01000c0cu : 0x01000100u,
                     s < 2 ? 0x01000100u : (s == 2 ? 0x03020100u : 0x05040302u), (unsigned)(size_t)(K + KC_RD + s * RDS + 64)};
    bB = __builtin_bit_cast(half8, t);
    asm volatile("" : "+v"(bA), "+v"(pa), "+v"(pa2), "+v"(pb), "+v"(kd), "+v"(kd2), "=v"(ks0), "+v"(uf16), "=v"(ks1), "+v"(bB));
  } else if (sub == 1) {
    asm volatile("" : "=v"(bA), "=v"(pa), "=v"(pa2), "=v"(pb), "=v"(kd), "=v"(kd2), "=v"(ks0), "=v"(uf16), "=v"(ks1), "=v"(bB));
  } else if (sub == 2) {
    kd = K + KC_R9 + 190 * ((c15 + 1) & 1) + 2 * (15 - c15 + 4 * g);
    bA = band_frag(lane, 0x1ffu << 4);
    bB = band_frag(lane, 0x7fu << 5);
    asm volatile("" : "+v"(bA), "=v"(pa), "=v"(pa2), "=v"(pb), "+v"(kd), "=v"(kd2), "=v"(ks0), "+v"(uf16), "=v"(ks1), "+v"(bB));
  } else {
    pa = C + (TR_H + 15 - c15) * TAB_STRIDE + 16 * g;
    pa2 = K + KC_R5 + 2 * (158 - 64 - 2 * c15 + 4 * g) - 128;
    pb = C + (TR_M + c15) * TAB_STRIDE + 16 * g;
    kd = K + (c15 < 8 ? ((c15 & 1) ? KC_R5 : KC_R3) + 2 * (80 - 8 * (c15 & 6) + 4 * g) : KC_R9 + 2 * (120 - 8 * c15 + 2 * g));
    kd2 = kd + (c15 < 8 ? 4 : KC_R7 - KC_R9);
    ks0 = c15 < 8 ? 0x03020100u : 0x05040100u;
    ks1 = c15 < 8 ? 0x07060504u : 0x07060302u;
    uf16 -= 16 * c15;
    bA = band_frag(lane, 0x1fu << 6);
    bB = band_frag(lane, 0x7u << 11);
    asm volatile("" : "+v"(bA), "+v"(pa), "+v"(pa2), "+v"(pb), "+v"(kd), "+v"(kd2), "+v"(ks0), "+v"(uf16), "+v"(ks1), "+v"(bB));
  }
  const unsigned pe = sub < 2 ? 0u : 3u << 23;
  f32x2 kh = {-1.44269502f, 0.159154936f}, kl = {-1.92596299e-8f, 7.13048051e-9f}, k1 = {128.f * 0.693147181f, 128.f}, ln2 = {0.693147181f, 128.f};
  const unsigned pi = sub < 2 ? 0x3f800000u : 0xc1000000u;
  u32x2 k0u = {0x3f317218u + pe, pi}, p0u = {0x3f800000u + pe, pi};
  const int vt = tid - 256;
  int uoff = WS_UP + b * 16384 + vt * 16, soff = WS_ST + vt * 16;
  asm volatile("" : "+v"(pfo), "+v"(pfq), "+v"(yb), "+v"(uoff), "+v"(soff));
  asm volatile("s_mov_b32 m0, %8" : "+s"(hs), "+s"(hs2), "+s"(kh), "+s"(kl), "+s"(k0u), "+s"(k1), "+s"(ln2), "+s"(p0u)
               : "s"((unsigned)(size_t)C + (sub & 1) * 256));
  SCHED_FENCE();
  TWP(5);
  SCHED_FENCE();
  const unsigned tag = __builtin_amdgcn_readfirstlane(tagv) + 1u;
  auto half = [&](unsigned v) { return __uint_as_float(__builtin_amdgcn_perm(v, v, hs)); };
  f32x2 f_l = {half(r_lar), __uint_as_float(__builtin_amdgcn_perm(r_ldt, r_ldt, hs2))}, f_c = {half(r_cr), half(r_ci)};
  float f_aim = half(r_aim);
  unsigned f_cm;
  asm volatile("v_max3_f32 %0, |%1|, |%2|, |%3|\n\tv_max_f32 %0, %0, |%4|" : "=&v"(f_cm) : "v"(r_cr), "v"(r_ci), "v"(r_cr << 16), "v"(r_ci << 16));
  asm volatile("" : "+v"(f_l), "+v"(f_aim), "+v"(f_c), "+v"(r_dsk) : "v"(cpf));
  SCHED_FENCE();
  TWP(6);
  SCHED_FENCE();
  if (wave < 4) {
#pragma unroll
    for (int i = 0; i < 4; i += 2) {
      unsigned lo[3], hi[3];
#pragma unroll
      for (int k = 0; k < 3; k++) {
        lo[k] = __builtin_amdgcn_perm(cp[i + 1][k], cp[i][k], 0x05040100u);
        hi[k] = __builtin_amdgcn_perm(cp[i + 1][k], cp[i][k], 0x07060302u);
      }
      const int o = cpo + i * (XM ? 32 : 8 * 1024 * 16);
      stg16(rs, o, u32x4{lo[0], hi[0], lo[1], tag});
      stg16(rs, o + 16384, u32x4{hi[1], lo[2], hi[2], tag});
    }
    if (is_ln && tid >= 184) dma16((const char*)a4[0] + (tid - 184) * 16, (unsigned)(size_t)L + SM_WR + (wave - 3) * 1024 + 128);
    pf4(S1M);
  }
  SCHED_FENCE();
  TWA(2);
  TWP(7);
  u32x4 v0, v1, st0, st1;
  const u32x4 rq = {(unsigned)a8[6], (unsigned)(a8[6] >> 32) & 0xffffu, (unsigned)WS_END, 0x00020000u};
  asm volatile("" : "=v"(v0), "=v"(v1), "=v"(st0), "=v"(st1));
  auto ask = [&](u32x4& p, u32x4& q, int off) {
    asm volatile("buffer_load_dwordx4 %0, %2, %3, 0 offen sc1\n\tbuffer_load_dwordx4 %1, %2, %3, %4 offen sc1"
                 : "+v"(p), "+v"(q) : "v"(off), "s"(rq), "s"(8192) : "memory");
  };
  auto polls = [&](int k) {
    if (wave >= 4 && (wave < 8 ? XP47 : XP8) == k) ask(v0, v1, uoff);
    if (wave >= 4 && (wave < 8 ? SP47 : SP8) == k) ask(st0, st1, soff);
    SCHED_FENCE();
  };
  auto get = [&](bool both) {
    _Pragma("unroll 1") for (int spin = 0; spin < SPIN_CAP; spin++) {
      const bool ok = both ? v0[3] == tag && v1[3] == tag : st0[3] == tag && st1[3] == tag;
      if (__builtin_expect(__all(ok), 1)) break;
      __builtin_amdgcn_s_sleep(4);
      if (both) {
        asm volatile("s_waitcnt vmcnt(0)" : "+v"(st0), "+v"(st1));
        if (!ok) ask(v0, v1, uoff);
      }
      if (st0[3] != tag || st1[3] != tag) ask(st0, st1, soff);
      if (both) asm volatile("s_waitcnt vmcnt(0)" : "+v"(v0), "+v"(v1), "+v"(st0), "+v"(st1));
      else asm volatile("s_waitcnt vmcnt(0)" : "+v"(st0), "+v"(st1));
    }
  };
  {
    const f32x2 l2e = -f32x2{kh[0], kh[0]}, th = f_l * l2e;
    const f32x2 tl = __builtin_elementwise_fma(f_l, -f32x2{kl[0], kl[0]}, __builtin_elementwise_fma(f_l, l2e, -th)) * ln2[0];
    const f32x2 e0 = {__builtin_amdgcn_exp2f(th[0]), __builtin_amdgcn_exp2f(th[1])}, ex = __builtin_elementwise_fma(e0, tl, e0);
    const unsigned eb = pkbf(ex[0], ex[1]);
    const float dt = bfhi(eb);
    const f32x2 av = f32x2{bflo(eb), f_aim} * dt;
    const f32x2 hi = av * kh, lo = __builtin_elementwise_fma(av, kl, __builtin_elementwise_fma(av, kh, -hi));
    int se = 385 - (int)(wave_max_u32(f_cm) >> 23) - (int)((unsigned)__builtin_amdgcn_readfirstlane(eb) >> 23);
    se = max(se, 2);
    asm("" : "+s"(se));
    se = min(se, 252);
    const float dts = dt * __uint_as_float((unsigned)(se + 1) << 23);
    cf cc = f_c * dts;
    cf ea = cexp(hi * __builtin_bit_cast(f32x2, p0u), lo * __builtin_bit_cast(f32x2, k0u)), eb2 = cexp(hi * 128.f, lo * k1);
    polls(2);
    asm volatile("s_cmp_lt_u32 %3, 2\n\ts_cbranch_scc0 1f\n\t" TAB_A "s_branch 2f\n1:\n\t" TAB_B "\n2:"
                 : "+{v[100:101]}"(cc), "+{v[102:103]}"(ea), "+{v[104:105]}"(eb2) : "s"(sub) : TAB_CLOB, "scc", "memory");
    if (sub == 2) *(volatile LAS unsigned*)(L + SM_ISC + 4 * ch) = (unsigned)(254 - se) << 23;
  }
  SCHED_FENCE();
  if (S0M) asm volatile("" : : "v"(s0v));
  polls(1);
  TWA(3);
  const f32x4 zero4 = {0.f, 0.f, 0.f, 0.f};
  f32x2 sb[2][3];
  f32x2 isc[3];
  const LAS char *yp, *yp2;
  int zoff, zoff2;
  unsigned t0;
  if (sub == 0) {
    half8 td[17];
    {
      half8 fa[8], fc[8], fb[8];
#pragma unroll
      for (int ks = 0; ks < 4; ks++) { fa[ks] = ld_frag(pa + 64 * ks); fc[ks] = ld_frag(pa2 + 64 * ks); }
      asm volatile("s_waitcnt lgkmcnt(0)" ::: "memory");
      __syncthreads();
      TWK(3);
#pragma unroll
      for (int ks = 0; ks < 4; ks++) fb[ks] = ld_frag(pb + 64 * ks);
      SCHED_FENCE();
#pragma unroll
      for (int ks = 4; ks < 8; ks++) { fa[ks] = ld_frag(pa + 64 * ks); fb[ks] = ld_frag(pb + 64 * ks); fc[ks] = ld_frag(pa2 + 64 * ks); }
      SCHED_FENCE();
      TWKV(4, "v"(fc[7]));
      f32x4 f = zero4, e = zero4;
#pragma unroll
      for (int ks = 0; ks < 8; ks++) { f = MFMA16(fa[ks], fb[ks], f); e = MFMA16(fc[ks], fb[ks], e); }
      const unsigned d0 = pkh(f[0], f[1]);
      TWKV(5, "v"(d0));
      const unsigned d1 = pkh(f[2], f[3]), d2 = pkh(e[0], e[1]), d3 = pkh(e[2], e[3]);
      const unsigned p0 = dppu<0x138>(d0), p1 = dppu<0x138>(d1);
      *(volatile LAS u32x4*)kd = u32x4{d0, d1, d2, d3};
      const unsigned a1 = __builtin_amdgcn_alignbit(d2, d1, 16), a2 = __builtin_amdgcn_alignbit(d3, d2, 16), a3 = __builtin_amdgcn_alignbit(p0, d3, 16);
      *(volatile LAS u32x4*)(kd + RDS) = u32x4{__builtin_amdgcn_alignbit(d1, d0, 16), a1, a2, a3};
      *(volatile LAS u32x4*)(kd + 2 * RDS) = u32x4{d1, d2, d3, p0};
      *(volatile LAS u32x4*)(kd + 3 * RDS) = u32x4{a1, a2, a3, __builtin_amdgcn_alignbit(p1, p0, 16)};
      TWA(4);
      const unsigned q0 = dppu<0xFF>(d0), q1 = dppu<0xFF>(d1);
      const u32x4 t = __builtin_bit_cast(u32x4, bB);
      if (lane >= 60)
        *(volatile LAS u32x4*)(size_t)t[3] =
            u32x4{0u, __builtin_amdgcn_perm(q1, q0, t[0]), __builtin_amdgcn_perm(q1, q0, t[1]), __builtin_amdgcn_perm(q1, q0, t[2])};
#pragma unroll
      for (int i = 7; i >= 0; i--) {
        if (i == 7 - TDB) {
          TWA(5);
          asm volatile("s_waitcnt vmcnt(0)\n\ts_barrier" ::: "memory");
        }
        if (TDI && i <= 7 - TDB) continue;
        td[i] = ld_td(kd2, i);
        td[i + 8] = ld_td(kd2, i + 8);
      }
      if (!TDI) td[16] = ld_td(kd2, 16);
      if (TDB >= 8) {
        TWA(5);
        asm volatile("s_waitcnt vmcnt(0)\n\ts_barrier" ::: "memory");
      }
    }
    unsigned long long t0a;
    asm volatile("s_memrealtime %0" : "=s"(t0a));
    TSX(0, 5);
    TWA(6);
    conv_dense(td, kd2, bA, K + KC_R5 + 64, uf16, yb + SM_YD);
    asm volatile("s_waitcnt lgkmcnt(0)" : "+s"(t0a));
    t0 = (unsigned)t0a;
  } else {
  asm volatile("s_waitcnt lgkmcnt(0)" ::: "memory");
  __syncthreads();
  polls(0);
  half8 td[8];
  auto mid_td = [&]() {
    const LAS unsigned* t5 = (const LAS unsigned*)pa2;
#pragma unroll
    for (int e = 0; e < 3; e++) {
      td[e] = ld_toep<8>(t5 + 16 * (2 - e));
      td[3 + e] = ld_toep<8>(t5 + (KC_R3 - KC_R5) / 4 + 16 * (2 - e));
    }
  };
  if (wave == 3) {
    half8 fa[24], fb[24];
    f32x4 f[3] = {zero4, zero4, zero4};
    constexpr int PF = PF3;
    TWK(3);
#pragma unroll
    for (int i = 0; i < PF; i++) { fa[i] = ld_frag(pa + (i >> 3) * TABB + 64 * (i & 7)); fb[i] = ld_frag(pb + (i >> 3) * TABB + 64 * (i & 7)); }
    SCHED_FENCE();
    auto put = [&](int c) {
      const unsigned d0 = pkh(f[c][0], f[c][1]), d1 = pkh(f[c][2], f[c][3]);
      const unsigned w0 = __builtin_amdgcn_perm(d1, d0, ks0), w1 = __builtin_amdgcn_perm(d1, d0, ks1);
      *(LAS unsigned*)(kd + c * CH2) = w0;
      *(LAS unsigned*)(kd2 + c * CH2) = w1;
      if (c15 >= 8) {
        *(LAS u16*)(kd + c * CH2 + 190) = (u16)w0;
        *(LAS u16*)(kd + c * CH2 + 192) = (u16)(w0 >> 16);
        *(LAS u16*)(kd + c * CH2 + (KC_R7 - KC_R9) + 190) = (u16)w1;
        *(LAS u16*)(kd + c * CH2 + (KC_R7 - KC_R9) + 192) = (u16)(w1 >> 16);
      }
    };
    static_for<0, 24>([&](auto ic) {
      constexpr int i = decltype(ic)::value, n = i + PF;
      f[i >> 3] = MFMA16(fa[i], fb[i], f[i >> 3]);
      SCHED_FENCE();
      if constexpr (n < 24) { fa[n] = ld_frag(pa + (n >> 3) * TABB + 64 * (n & 7)); fb[n] = ld_frag(pb + (n >> 3) * TABB + 64 * (n & 7)); }
      if constexpr (i == 14) {
        put(0);
        asm volatile("" ::: "memory");
        mid_td();
      }
      if constexpr (i == 18) put(1);
      SCHED_FENCE();
    });
    TWK(5);
    put(2);
    __builtin_amdgcn_s_waitcnt(0x0F70);
  }
  TWA(4);
  SCHED_FENCE();
  if (wave >= 4) {
    const u32x4 iscv = *(const LAS u32x4*)(L + SM_ISC);
    yp = L + ((wave >> 1) - 2) * YT + ((vt >> 1) & 3) * 320 + ((vt >> 3) & 15) * 16 + (vt & 1) * 8;
    yp2 = yp + 2 * TABB;
    zoff = WS_ZP + ((vt >> 3) * NCONV + b) * 128 + (vt & 7) * 16;
    zoff2 = zoff + 64 * NCONV * 128;
    asm volatile("" : "+v"(yp), "+v"(yp2), "+v"(zoff), "+v"(zoff2));
    f32x2 dsk[3], xf[2][3];
    LAS char* dst = L + SM_US + 2 * UPAD + 4 * vt;
#pragma unroll
    for (int c = 0; c < 3; c++) {
      dsk[c][0] = __uint_as_float(__builtin_amdgcn_perm(r_dsk[1], r_dsk[0], 0x01000c0cu + 0x02020000u * (c + (b & 1))));
      isc[c] = f32x2{__uint_as_float(iscv[c]), __uint_as_float(iscv[c])};
      asm volatile("" : "+v"(dsk[c]), "+v"(isc[c]), "+v"(dst));
    }
    asm volatile("s_waitcnt vmcnt(2)" : "+v"(v0), "+v"(v1));
    get(true);
#pragma unroll
    for (int c = 0; c < 3; c++) {
      xf[0][c] = unpk(v0[c]);
      xf[1][c] = unpk(v1[c]);
      asm volatile("" : "+v"(xf[0][c]), "+v"(xf[1][c]));
    }
    SCHED_FENCE();
    asm volatile("s_waitcnt vmcnt(0)" : "+v"(st0), "+v"(st1));
    get(false);
    TWA(5);
    f32x2 uf[2][3];
#pragma unroll
    for (int i = 0; i < 2; i++) {
      const u32x4& st = i ? st1 : st0;
      const f32x2 ri = {__uint_as_float(st[0]), __uint_as_float(st[1])};
      const f32x2 cc = -unpk(st[2]) * ri;
#pragma unroll
      for (int c = 0; c < 3; c++) {
        uf[i][c] = __builtin_elementwise_fma(xf[i][c], ri, cc);
        *(LAS unsigned*)(dst + i * 2048 + c * CH2) = pkc(uf[i][c]);
      }
    }
    SCHED_FENCE();
#pragma unroll
    for (int i = 0; i < 2; i++)
#pragma unroll
      for (int c = 0; c < 3; c++) {
        sb[i][c] = __builtin_elementwise_fma(uf[i][c], f32x2{dsk[c][0], dsk[c][0]}, xf[i][c]);
        asm volatile("" : "+v"(sb[i][c]));
      }
    if (is_ln && (vt >> 3) == (b & 63)) *(LAS unsigned*)(L + SM_MX + (vt & 7) * 4) = b < 64 ? st0[2] : st1[2];
  } else {
    TWA(5);
  }
  if (sub == 1) __builtin_amdgcn_s_setprio(3);
  __syncthreads();
  t0 = (unsigned)__builtin_amdgcn_s_memrealtime();
  TWA(6);
  {
    if (sub == 1) {
      if (wave == 4 && lane < 8) __builtin_amdgcn_raw_buffer_store_b64(u32x2{t0, tag}, rs, WS_CK + lane * 2048 + b * 8, 0, SC1);
      auto pf3 = [&](int m) {
        if (m & 16)
          dma4s(a4[3], ((9 * xg + (sl & 7)) * 576 + DR0 * 72 + ((sl >> 3) * 3 + ch) * ((8 - DR0) * 8) + min(lane, (8 - DR0) * 8 - 1)) * 128,
                (unsigned)(size_t)L + SM_DUMMY);
        if (m & 32) {
          const char* pg = (const char*)(sl < 12 ? a4[1] : a4[2]) + (unsigned)(((sl12 * 3 + ch) * 8 + (lane & 7)) * (FF * 2) + xg * 1152 + 1024);
          const char* pd = (const char*)a4[3] + (unsigned)(((9 * xg + 8) * 576 + (sl * 3 + ch) * 8 + (lane & 7)) * 128);
          dma4((lane & 8) ? pd : pg, (unsigned)(size_t)L + SM_DUMMY);
        }
#pragma unroll
        for (int i = 0; i < 4; i++)
          if (m >> i & 1) prefetch(i);
      };
      if (!SENT1) pf3(S3M);
      if (is_ln) {
        const u32x4 ones = {~0u, ~0u, ~0u, ~0u};
#pragma unroll
        for (int i = 0; i < 3; i++) stg16(rs, WS_PART + b * 11520 + vt * 16 + i * 3072, ones);
        if (vt < 144) stg16(rs, WS_PART + b * 11520 + vt * 16 + 9216, ones);
        stg16(rs, WS_XS + b * (2 * XSS) + vt * 16, ones);
        if (vt < 96) stg16(rs, WS_XS + b * (2 * XSS) + 3072 + vt * 16, ones);
        if (b < 6) stg16(rs, WS_XS + 2 * NLN * XSS + b * 3072 + vt * 16, ones);
      }
      if (SENT1) pf3(S3M);
      if (S4M) {
        while ((unsigned)__builtin_amdgcn_s_memrealtime() - t0 < S4D) __builtin_amdgcn_s_sleep(1);
        pf3(S4M);
      }
      __builtin_amdgcn_s_setprio(0);
    } else if (sub == 3) {
      if (wave != 3) mid_td();
      f32x4 a2[6], w[4];
#pragma unroll
      for (int i = 0; i < 6; i++) a2[i] = zero4;
      half8 uw[12];
#pragma unroll
      for (int jj = 0; jj < 6; jj++) uw[jj] = ld_frag(uf16 - 48 + 256 * jj);
      SCHED_FENCE();
      w[0] = MFMA16(uw[0], bA, zero4); w[1] = MFMA16(uw[1], bA, zero4);
      w[2] = MFMA16(uw[0], bB, zero4); w[3] = MFMA16(uw[1], bB, zero4);
      half8 X5 = pack_win(w[0], w[1]), X3 = pack_win(w[2], w[3]);
      SCHED_FENCE();
#pragma unroll
      for (int jj = 0; jj < 6; jj++) {
        if (jj < 5) {
          w[0] = MFMA16(uw[2 * jj + 2], bA, zero4);
          if (jj < 3) uw[2 * jj + 6] = ld_frag(uf16 - 48 + 256 * (2 * jj + 6));
          SCHED_FENCE();
          w[1] = MFMA16(uw[2 * jj + 3], bA, zero4);
          if (jj < 3) uw[2 * jj + 7] = ld_frag(uf16 - 48 + 256 * (2 * jj + 7));
          SCHED_FENCE();
          w[2] = MFMA16(uw[2 * jj + 2], bB, zero4);
        }
        if (jj > 0) *(LAS f32x4*)(yb + SM_Y2 + (1 + jj) * YT) = a2[jj - 1];
        SCHED_FENCE();
        if (jj < 5) w[3] = MFMA16(uw[2 * jj + 3], bB, zero4);
        SCHED_FENCE();
        u32x4 n5, n3;
#pragma unroll
        for (int q = 0; q < 6; q++) {
          const int e = q >> 1;
          if (jj + e < 6) a2[jj + e] = MFMA16((q & 1) ? X3 : X5, td[3 * (q & 1) + e], a2[jj + e]);
          if (jj < 5 && q < 4) {
            u32x4& nn = q < 2 ? n5 : n3;
            nn[2 * (q & 1)] = pkh(w[q][0], w[q][1]);
            nn[2 * (q & 1) + 1] = pkh(w[q][2], w[q][3]);
          }
          SCHED_FENCE();
        }
        if (jj < 5) {
          X5 = __builtin_bit_cast(half8, n5);
          X3 = __builtin_bit_cast(half8, n3);
        }
      }
      *(LAS f32x4*)(yb + SM_Y2 + 7 * YT) = a2[5];
    } else {
      half8 uw[8];
#pragma unroll
      for (int i = 0; i < 4; i++) {
        uw[2 * i] = ld_frag(uf16 + 512 * i - 48);
        uw[2 * i + 1] = ld_frag(uf16 + 512 * i - 32);
      }
      SCHED_FENCE();
      f32x4 w[8], a3[4];
#pragma unroll
      for (int e = 0; e < 4; e++) {
        const LAS unsigned* t9 = opaque_lds(kd);
        td[e] = ld_toep<8>(t9 + 8 * (3 - e));
        td[4 + e] = ld_toep<8>(t9 + (KC_R7 - KC_R9) / 4 + 8 * (3 - e));
      }
#pragma unroll
      for (int i = 0; i < 4; i++) {
        w[2 * i] = MFMA16(uw[2 * i], bA, zero4);
        w[2 * i + 1] = MFMA16(uw[2 * i + 1], bB, zero4);
      }
      const half8 X90 = pack_win(w[0], w[2]), X70 = pack_win(w[1], w[3]), X91 = pack_win(w[4], w[6]), X71 = pack_win(w[5], w[7]);
#pragma unroll
      for (int t = 0; t < 4; t++) {
        a3[t] = MFMA16(X90, td[t], zero4);
        a3[t] = MFMA16(X70, td[4 + t], a3[t]);
        if (t >= 2) {
          a3[t] = MFMA16(X91, td[t - 2], a3[t]);
          a3[t] = MFMA16(X71, td[4 + t - 2], a3[t]);
        }
      }
#pragma unroll
      for (int t = 0; t < 4; t++) *(LAS f32x4*)(yb + SM_Y3 + (4 + t) * YT) = a3[t];
    }
  }
  }
  TWA(7);
  __syncthreads();
  const unsigned long long t_end = __builtin_amdgcn_s_memrealtime();
  TS(0, 6);
  TWB(2);
  if (wave >= 4) {
    auto ld = [&](int c, int off) { return *(const volatile LAS f32x2*)((c < 2 ? yp + c * TABB : yp2) + off); };
    auto epi = [&](auto mid) {
      f32x2 ya[3], yc[3], yb0[3], yb1[3], yb2[3];
#pragma unroll
      for (int c = 0; c < 3; c++) {
        ya[c] = ld(c, SM_YD);
        if (mid) yc[c] = ld(c, SM_Y2);
      }
#pragma unroll
      for (int c = 0; c < 3; c++) {
        yb0[c] = ld(c, 4 * YT + SM_YD);
        yb1[c] = ld(c, 4 * YT + SM_Y2);
        yb2[c] = ld(c, 4 * YT + SM_Y3);
      }
      SCHED_FENCE();
      u32x4 o, o2;
#pragma unroll
      for (int c = 0; c < 3; c++) {
        const f32x2 z = __builtin_elementwise_fma(mid ? ya[c] + yc[c] : ya[c], isc[c], sb[0][c]);
        o[c] = pkbf(z[0], z[1]);
      }
      o[3] = tag;
      stg16(rs, zoff, o);
#pragma unroll
      for (int c = 0; c < 3; c++) {
        const f32x2 z = __builtin_elementwise_fma((yb0[c] + yb1[c]) + yb2[c], isc[c], sb[1][c]);
        o2[c] = pkbf(z[0], z[1]);
      }
      o2[3] = tag;
      stg16(rs, zoff2, o2);
    };
    if (wave >= 8) epi(std::true_type{});
    else epi(std::false_type{});
  }
  TS(0, 7);
  TWB(3);
  TWFLUSH();
  ftag = tag;
  if (b >= LATE0 && wave == 1) *(LAS int*)(L + FS_SEL + 32) = (int)t0 + last_start(rs, tag, lane, bid, t0);
  if (b < LATE0) {
  unsigned long long t_all = t_end;
  auto get_lag = [&]() { t_all += last_start(rs, tag, lane, bid, t0); };
  auto wait_until = [&](int t) {
    while ((long long)(__builtin_amdgcn_s_memrealtime() - t_all) < t) __builtin_amdgcn_s_sleep(1);
  };
  LAS unsigned* const selp = (LAS unsigned*)(L + SM_ROW + 256);
  unsigned t_key = 0;
  auto select_rows = [&]() {
    const SelMasks sm(lane);
    get_lag();
    wait_until(DLY_L);
    selp[key_slot(lane)] = select8<false>(rs, tag, lane, sm, ws, bid, t_key);
  };
  if (is_ln) {
    f32x2 wf[6], hv[6];
    const unsigned mxp = *(const LAS unsigned*)(L + SM_MX + (tid & 7) * 4);
#pragma unroll
    for (int i = 0; i < 6; i++) {
      const float t = bflo(*(const LAS u16*)(L + SM_WR + (tid >> 3) * 12 + i * 2));
      wf[i] = f32x2{t, t};
    }
    float wsum = 0.f, mxr = bflo(*(const LAS u16*)(L + SM_MX + (lane >> 2) * 2));
    unsigned one2 = 0x3f803f80u, sel0 = 0x00003f80u, sel1 = 0x3f800000u;
    asm volatile("" : "+s"(one2), "+s"(sel0), "+s"(sel1));
    if (wave == 0) {
#pragma unroll
      for (int k = 0; k < 9; k++) wsum = dot2(*(const LAS unsigned*)(L + SM_WR + (hl + 32 * k) * 4), one2, wsum);
      wsum = red32_sum(wsum);
      __builtin_amdgcn_s_setprio(2);
    }
#pragma unroll
    for (int i = 0; i < 6; i++) {
      hv[i] = -unpk(mxp);
      asm volatile("" : "+v"(hv[i]), "+v"(wf[i]));
    }
    LAS char* redw = L + SM_RED + wave * 256 + (wave >= 6 ? 128 : 0) + (lane & 7) * 32 + (lane >> 5) * 16 + ((lane >> 4) & 1) * 8;
    const LAS char* redr = L + SM_RED + ((lane >> 1) & 1) * (6 * 256 + 128) + (lane >> 2) * 16 + (lane & 1) * 8;
    unsigned kidx = (unsigned)(SEQ - 1 - 16 * b - (lane >> 2)) << 2;
    int kro = WS_KR + (lane & 7) * 2048 + b * 16, xso = WS_XS + b * (2 * XSS) + (tid >> 3) * 12;
    asm volatile("" : "+v"(wsum), "+v"(mxr), "+v"(redw), "+v"(redr), "+v"(kidx), "+v"(kro), "+v"(xso));
    if (wave == 1) {
      get_lag();
      wait_until(DLY_H);
    }
    __syncthreads();
    TWB(4);
    {
      const int off = WS_ZP + b * (NCONV * 128) + (tid >> 3) * 256 + (tid & 7) * 16;
      v0 = ldg16(rs, off);
      v1 = ldg16(rs, off + 128);
      _Pragma("unroll 1") for (int spin = 0; spin < SPIN_CAP; spin++) {
        if (__builtin_expect(__all(v0[3] == tag && v1[3] == tag), 1)) break;
        __builtin_amdgcn_s_sleep(4);
        if (v0[3] != tag) v0 = ldg16(rs, off);
        if (v1[3] != tag) v1 = ldg16(rs, off + 128);
      }
    }
    TS(0, 8);
    TWB(5);
    {
      f32x2 ss, qq, pp;
#pragma unroll
      for (int i = 0; i < 6; i++) {
        const unsigned t = i < 3 ? v0[i] : v1[i - 3];
        hv[i] = f32x2{dot2(t, sel0, hv[i][0]), dot2(t, sel1, hv[i][1])};
        ss = i ? ss + hv[i] : hv[i];
        qq = i ? __builtin_elementwise_fma(hv[i], hv[i], qq) : hv[i] * hv[i];
        pp = i ? __builtin_elementwise_fma(hv[i], wf[i], pp) : hv[i] * wf[i];
      }
      float s0 = ss[0], s1 = ss[1], q0 = qq[0], q1 = qq[1], p0 = pp[0], p1 = pp[1];
      asm volatile(
          "s_nop 1\n\tv_permlane32_swap_b32 %0, %1\n\tv_permlane32_swap_b32 %2, %3\n\tv_permlane32_swap_b32 %4, %5\n\t"
          "v_add_f32 %0, %0, %1\n\tv_add_f32 %2, %2, %3\n\tv_add_f32 %4, %4, %5\n\ts_nop 0\n\t"
          "v_permlane16_swap_b32 %0, %2\n\tv_add_f32_dpp %1, %4, %4 row_ror:8 row_mask:0xf bank_mask:0xf\n\t"
          "v_add_f32 %2, %0, %2\n\ts_nop 1\n\tv_add_f32_dpp %0, %2, %2 row_ror:8 row_mask:0xf bank_mask:0xf"
          : "+v"(s0), "+v"(s1), "+v"(q0), "+v"(q1), "+v"(p0), "+v"(p1));
      *(LAS f32x2*)redw = f32x2{s0, s1};
    }
    TWB(6);
    __syncthreads();
    if (wave == 0) {
      const unsigned now = (unsigned)__builtin_amdgcn_s_memrealtime();
      const volatile LAS f32x2* rp = (const volatile LAS f32x2*)redr;
      const f32x2 t0 = rp[0], t1 = rp[32], t2 = rp[64], t3 = rp[96], t4 = rp[128], t5 = rp[160];
      const f32x2 t = ((t0 + t1) + (t2 + t3)) + (t4 + t5);
      float a = t[0], p = t[1];
      asm volatile("" : "+v"(a), "+v"(p));
      a += dppf<0x4E>(a);
      p += dppf<0x4E>(p);
      p += dppf<0xB1>(p);
      const float mv = dppf<0x00>(a) * (1.0f / DM);
      const float rinv = ln_rinv(dppf<0x55>(a), mv);
      const float dl = rbf(mxr + mv) - mxr;
      const unsigned rb = pkbf(0.f, fmaf(-dl, wsum, p) * rinv);
      const unsigned key = ((rb ^ (unsigned)(((int)rb >> 31) | (int)0x80000000)) & 0xffff0000u) | kidx;
      unsigned k1 = max(key, dppu<0x141>(key)), k2 = min(key, dppu<0x141>(key)), k3;
      {
        const unsigned lo = min(k1, dppu<0x140>(k1)), hi = max(k2, dppu<0x140>(k2));
        k1 = max(k1, dppu<0x140>(k1));
        k2 = max(lo, hi);
        k3 = min(lo, hi);
      }
      {
        const unsigned lo = min(k1, dppu<0x142>(k1)), hi = max(k2, dppu<0x142>(k2)), w = max(k3, dppu<0x142>(k3));
        k1 = max(k1, dppu<0x142>(k1));
        k2 = max(lo, hi);
        k3 = max(min(lo, hi), w);
      }
      {
        const unsigned lo = min(k1, dppu<0x143>(k1)), hi = max(k2, dppu<0x143>(k2)), w = max(k3, dppu<0x143>(k3));
        k1 = max(k1, dppu<0x143>(k1));
        k2 = max(lo, hi);
        k3 = max(min(lo, hi), w);
      }
      if (lane >= 56) stg16(rs, kro, u32x4{k1, k2 | 1u, __builtin_amdgcn_perm(k3, now, 0x07060100u), tag});
      SCHED_FENCE();
      const unsigned second = (unsigned)__builtin_amdgcn_readlane((int)k2, 63);
      if ((lane & 3) == 0) {
        LAS float* ri = (LAS float*)(L + SM_ROW + (lane >> 3) * 16 + (lane & 4));
        ri[0] = -dl * rinv;
        ri[2] = rinv;
        const unsigned fk = key | (key > second ? 0u : (key == second ? 1u : 2u));
        *(LAS unsigned*)(L + SM_ROW + 128 + lane) = fk;
        __builtin_amdgcn_raw_buffer_store_b64(u32x2{fk, tag}, rs, WS_KF + b * 128 + lane * 2, 0, SC1);
      }
      __builtin_amdgcn_s_setprio(0);
    }
    TS(0, 9);
    TWB(7);
    __syncthreads();
    {
      const f32x4 ri = *(const LAS f32x4*)(L + SM_ROW + (tid & 7) * 16);
      const u32x2 kk = *(const LAS u32x2*)(L + SM_ROW + 128 + (tid & 7) * 8);
      const f32x2 c2 = {ri[0], ri[1]}, r2 = {ri[2], ri[3]};
      unsigned wp[6];
#pragma unroll
      for (int i = 0; i < 6; i++) {
        const f32x2 w2 = __builtin_elementwise_fma(hv[i], r2, c2);
        wp[i] = pkbf(w2[0], w2[1]);
      }
#pragma unroll
      for (int i = 0; i < 2; i++)
        if (!(kk[i] & 2)) {
          u32x3 o;
#pragma unroll
          for (int k = 0; k < 3; k++) o[k] = min(__builtin_amdgcn_perm(wp[2 * k + 1], wp[2 * k], i ? 0x07060302u : 0x05040100u), 0xfffffffeu);
          __builtin_amdgcn_raw_buffer_store_b96(o, rs, xso + (int)(kk[i] & 1) * XSS, 0, SC1);
        }
      SCHED_FENCE();
      LAS char* dst = L + SM_T2 + (tid & 7) * T2S + (tid >> 3) * 48;
#pragma unroll
      for (int i = 0; i < 6; i += 2) *(LAS u32x4*)(dst + 8 * i) = u32x4{i < 3 ? v0[i] : v1[i - 3], wp[i], i < 2 ? v0[i + 1] : v1[i - 2], wp[i + 1]};
    }
    __syncthreads();
    const int pr = tid / 72, ck = tid - pr * 72;
    u32x4 hq[2], wq[2];
    u32x2 key = {2u, 2u};
    auto put_row = [&](int i, int slot) {
      const int o = key_addr(key[i], slot) + ck * 16;
      const u32x4 cap = {0xfffffffeu, 0xfffffffeu, 0xfffffffeu, 0xfffffffeu};
      if (key[i] & 2) stg16(rs, o, __builtin_elementwise_min(wq[i], cap));
      stg16(rs, o + DM * 2, __builtin_elementwise_min(hq[i], cap));
    };
    if (wave < 9) {
      key = *(const LAS u32x2*)(L + SM_ROW + 128 + pr * 8);
#pragma unroll
      for (int k = 0; k < 4; k++) {
        const u32x4 t = *(const LAS u32x4*)(L + SM_T2 + pr * T2S + ck * 64 + k * 16);
        hq[0][k] = __builtin_amdgcn_perm(t[2], t[0], 0x05040100u);
        hq[1][k] = __builtin_amdgcn_perm(t[2], t[0], 0x07060302u);
        wq[0][k] = __builtin_amdgcn_perm(t[3], t[1], 0x05040100u);
        wq[1][k] = __builtin_amdgcn_perm(t[3], t[1], 0x07060302u);
      }
#pragma unroll
      for (int i = 0; i < 2; i++)
        if (!(key[i] & 2)) put_row(i, 0);
    } else if (wave == 10) {
      select_rows();
    }
    __syncthreads();
    TS(0, 10);
    if (wave < 9) {
#pragma unroll
      for (int i = 0; i < 2; i++) {
        int slot = 8;
#pragma unroll
        for (int r = 0; r < 8; r++) slot = selp[r] == key[i] ? r : slot;
        if (slot < 8) {
          if (key[i] & 2) put_row(i, slot);
        } else {
          u32x4 o;
#pragma unroll
          for (int k = 0; k < 4; k++) {
            const f32x2 o2 = unpk(hq[i][k]) + unpk(wq[i][k]);
            o[k] = pkbf(o2[0], o2[1]);
          }
          *(GAS u32x4*)(out + (unsigned)((16 * b + 2 * pr + i) * (DM * 2) + ck * 16)) = o;
        }
      }
    }
  } else if (wave == 10) {
    select_rows();
  }
  if (b < RED0) {
    TS(0, 12);
    TC(0, 15);
    __builtin_amdgcn_endpgm();
  }
  {
    const int T = (b - RED0) >> 2, tp = (b - RED0) & 3;
    unsigned hv = 0u, wv = 0u, oo = 0u;
    float sg = 0.f;
    if (wave == 10) {
      const int slot = 2 * tp + ((lane >> 3) & 1);
      const unsigned key = *(volatile LAS unsigned*)(selp + slot);
      int o = key_addr(key, slot) + (8 * T + (lane & 7)) * 4;
      _Pragma("unroll 1") for (int spin = 0; spin < SPIN_CAP; spin++) {
        asm volatile("" : "+v"(o) : : "memory");
        wv = __builtin_amdgcn_raw_buffer_load_b32(rs, o, 0, SC1);
        hv = __builtin_amdgcn_raw_buffer_load_b32(rs, o + DM * 2, 0, SC1);
        if (__all(wv != ~0u && hv != ~0u)) break;
        __builtin_amdgcn_s_sleep(4);
      }
      const unsigned rb = key ^ ((int)key < 0 ? 0x80000000u : 0xffffffffu);
      sg = rbf(__builtin_amdgcn_rcpf(1.0f + __expf(-bfhi(rb))));
      oo = (unsigned)(key_row(key) * (DM * 2) + (8 * T + (lane & 7)) * 4);
      asm volatile("" : "+v"(sg), "+v"(oo));
      if (DLY_C) {
        while ((int)((unsigned)__builtin_amdgcn_s_memrealtime() - t_key) < DLY_C) __builtin_amdgcn_s_sleep(1);
      } else {
        wait_until(DLY_Q);
      }
    }
    LAS float* red = (LAS float*)(L + SM_RED);
    LAS char* rw = (LAS char*)red + wave * 128 + (lane & 7) * 16 + (lane >> 4) * 4;
    asm volatile("" : "+v"(rw));
    __syncthreads();
    TS(0, 11);
    if (wave < 10) {
      u32x4 v, w;
      asm volatile("buffer_load_dwordx4 %0, %2, %3, 0 offen sc1\n\ts_sleep %4\n\tbuffer_load_dwordx4 %1, %2, %3, 0 offen sc1"
                   : "=&v"(v), "=&v"(w) : "v"(part_off), "s"(rq), "n"(PPS) : "memory");
      u32x4 got = {~0u, 0u, 0u, 0u};
      _Pragma("unroll 1") for (int spin = 0; spin < SPIN_CAP; spin++) {
        asm volatile("s_waitcnt vmcnt(1)" : "+v"(v));
        if (got[0] == ~0u) got = v;
        if (__all(got[0] != ~0u)) break;
        if (got[0] == ~0u) asm volatile("buffer_load_dwordx4 %0, %1, %2, 0 offen sc1" : "+v"(v) : "v"(part_off), "s"(rq) : "memory");
        asm volatile("s_waitcnt vmcnt(1)" : "+v"(w));
        if (got[0] == ~0u) got = w;
        if (__all(got[0] != ~0u)) break;
        if (got[0] == ~0u) asm volatile("buffer_load_dwordx4 %0, %1, %2, 0 offen sc1" : "+v"(w) : "v"(part_off), "s"(rq) : "memory");
      }
#define v got
      auto r0 = __builtin_amdgcn_permlane32_swap(v[0], v[2], false, false);
      auto r1 = __builtin_amdgcn_permlane32_swap(v[1], v[3], false, false);
      const float s0 = __uint_as_float(r0[0]) + __uint_as_float(r0[1]), s1 = __uint_as_float(r1[0]) + __uint_as_float(r1[1]);
      auto r2 = __builtin_amdgcn_permlane16_swap(__float_as_uint(s0), __float_as_uint(s1), false, false);
      float a = __uint_as_float(r2[0]) + __uint_as_float(r2[1]);
      a += dppf<0x128>(a);
      *(LAS float*)rw = a;
#undef v
      asm volatile("" : : "v"(v), "v"(w));
    }
    __syncthreads();
    if (wave == 10 && lane < 16) {
      f32x2 p[10];
#pragma unroll
      for (int w = 0; w < 10; w++) p[w] = *(const LAS f32x2*)(red + w * 32 + lane * 2);
      const f32x2 y = (((p[0] + p[1]) + (p[2] + p[3])) + (p[8] + p[9])) + ((p[4] + p[5]) + (p[6] + p[7]));
      const f32x2 y2 = rbf2(rbf2(y) * sg);
      const f32x2 o2 = unpk(hv) + rbf2(unpk(wv) + y2);
      *(GAS unsigned*)(out + oo) = pkbf(o2[0], o2[1]);
      if (b == RED0 && lane == 0) __builtin_amdgcn_raw_buffer_store_b32(tag, rs, WS_EPOCH, 0, SC1);
    }
  }
  TS(0, 12);
  TC(0, 15);
  __builtin_amdgcn_endpgm();
  }
  }
  ffn_role(argp, ftag, L);
}

}

extern "C" void kernel_launch(void* const* d_in, const int* in_sizes, int n_in, void* d_out, int out_size,
                              void* d_ws, size_t ws_size, hipStream_t stream) {
  k_all<<<NCONV + NFFN, NTHR, 0, stream>>>((const u16*)d_in[0], (const u16*)d_in[1], (const u16*)d_in[2], (const u16*)d_in[3],
                                             (const u16*)d_in[4], (const u16*)d_in[5], (char*)d_ws, (const u16*)d_in[6],
                                             (const u16*)d_in[8], (const u16*)d_in[9], (const u16*)d_in[10], (const u16*)d_in[11],
                                             (u16*)d_out);
}
```

```cpp
#include <hip/hip_runtime.h>
#include <stdint.h>
#include <type_traits>

#define PRA 0
#define CPF 212
#define WPAD 0
#define PFNT 0
#define WNT 0
#define WNTS ""
#define PTH 4
#define PTL 2
#define EB 0
#define LAUX 16
#define SAUX 16
#define PFA 0
#define XFL 0
#define CMAP 0
#define PRB 0
#define WORD 1
#define DR0 6
#define SENT1 0
#define S4M 0
#define S4D 50
#define XP47 0
#define XP8 1
#define SP47 0
#define SP8 1
#define LSKIP 0
#define DNT 1
#define SCN 0
#define SCX 0
#define ED 3
#define GNT 1
#define PP 1
#define PPS 6
#define DLY_Q 430
#define DLY_K 340
#define DLY_C 200
#define KPP 4
#define TDB 4
#define PM 1
#define XL 3
#define NC 1
#define XB 0
#define S2W 1
#define PSL 2
#define HE5 10
#define HE4 25
#define HE3 40
#define FPR 0
#define MPR 0x1233
#define TWO 3
#define WPR 0
#define PPR 3
#define SPR 3
#define PSE 1
#define FP2 3
#define SELW 8
#define EO 1
#define OAUX 16
#define HEA 1
#define DLG 0
#define MJP 4
#define FB 1
#define FP3 0
#define FSL 0
#define TDI 1
#define PF3 8
#define T0A 1
#define S0M 0
#define S1M 32
#define S2M 0
#define S3M 0
#define STA 16
#define XM 0
#define XF 0
#define PFD 10
namespace {

constexpr int SEQ = 2048;
constexpr int DM = 576;
constexpr int NM = 128;
constexpr int FF = 4608;

constexpr int NTHR = 768;
constexpr int NFFN = 64;
constexpr int NPART = 80;
constexpr int NCONV = 192;
constexpr int NLN = 128;
constexpr int RED1 = 96;
constexpr int LATE0 = 176;

typedef unsigned short u16;
#define LAS __attribute__((address_space(3)))
#define GAS __attribute__((address_space(1)))
#define CAS __attribute__((address_space(4)))
using half8 = __attribute__((ext_vector_type(8))) _Float16;
using half2v = __attribute__((ext_vector_type(2))) _Float16;
using bf2v = __attribute__((ext_vector_type(2))) __bf16;
using bf8v = __attribute__((ext_vector_type(8))) __bf16;
using bf16v = __attribute__((ext_vector_type(16))) __bf16;
using s16x8 = __attribute__((ext_vector_type(8))) short;
using s16x4 = __attribute__((ext_vector_type(4))) short;
using f32x2 = __attribute__((ext_vector_type(2))) float;
using f32x4 = __attribute__((ext_vector_type(4))) float;
using u32x2 = __attribute__((ext_vector_type(2))) unsigned;
using u32x3 = __attribute__((ext_vector_type(3))) unsigned;
typedef u32x3 u32x3u __attribute__((aligned(4)));
using u32x4 = __attribute__((ext_vector_type(4))) unsigned;
using u64x2 = __attribute__((ext_vector_type(2))) unsigned long long;
using u64x8 = __attribute__((ext_vector_type(8))) unsigned long long;
using u64x4 = __attribute__((ext_vector_type(4))) unsigned long long;

constexpr int WS_EPOCH = 0;
constexpr int WS_PF = 4096;
constexpr int WS_KR = WS_PF + NFFN * 128;
constexpr int WS_KF = WS_KR + 8 * 2048;
constexpr int WS_ST = WS_KF + 16384;
constexpr int WS_CK = WS_ST + 16384;
constexpr int WS_FL = WS_CK + 8 * 2048;
constexpr int WS_UP = 131072;
constexpr int WS_ZP = WS_UP + NCONV * NLN * 128;
constexpr int WS_PART = WS_ZP + NCONV * NLN * 128;
constexpr int WS_XS = WS_PART + NPART * 8 * DM * 4;
constexpr int XSS = (2 + 2 * NC) * DM;
constexpr int WS_END = WS_XS + (2 * NLN + 8) * XSS;
static_assert(WS_FL + 8 * 1024 <= WS_UP, "ws");
constexpr size_t WS_TS = 64u << 20;
constexpr int SC1 = 16, NT = 2;
constexpr int SPIN_CAP = 1 << 16;

#define TW(k)
#define TWP(k)
#define TWA(k)
#define TWB(k)
#define TWDECL
#define TSX(k, s)
#define TWFLUSH()
#define TC(k, s)
#define TSW(w, s)
#define TSL(k, s)
#define TS(k, s)

#define TWF(k)
#define TWK(k)
#define TWKV(k, ...)
__device__ __forceinline__ float bflo(unsigned v) { return __uint_as_float(v << 16); }
__device__ __forceinline__ float bfhi(unsigned v) { return __uint_as_float(v & 0xffff0000u); }
__device__ __forceinline__ unsigned pkbf(float a, float b) {
  f32x2 f = {a, b};
  return __builtin_bit_cast(unsigned, __builtin_convertvector(f, bf2v));
}
__device__ __forceinline__ unsigned pkh(float a, float b) {
  f32x2 f = {a, b};
  return __builtin_bit_cast(unsigned, __builtin_convertvector(f, half2v));
}
__device__ __forceinline__ float rbf(float f) { return __uint_as_float(pkbf(0.f, f)); }
__device__ __forceinline__ f32x2 rbf2(f32x2 v) { return f32x2{rbf(v[0]), rbf(v[1])}; }
__device__ __forceinline__ f32x2 unpk(unsigned v) { return f32x2{bflo(v), bfhi(v)}; }
__device__ __forceinline__ float dot2(unsigned a, unsigned b, float c) {
  return __builtin_amdgcn_fdot2_f32_bf16(__builtin_bit_cast(bf2v, a), __builtin_bit_cast(bf2v, b), c, false);
}
__device__ __forceinline__ float hadd(f32x2 v) {
  float a = v[0], b = v[1];
  asm volatile("" : "+v"(a), "+v"(b));
  return a + b;
}

template <int CTRL>
__device__ __forceinline__ float dppf(float v) {
  return __int_as_float(__builtin_amdgcn_update_dpp(0, __float_as_int(v), CTRL, 0xf, 0xf, true));
}
template <int CTRL>
__device__ __forceinline__ unsigned dppu(unsigned v) {
  return (unsigned)__builtin_amdgcn_update_dpp(0, (int)v, CTRL, 0xf, 0xf, true);
}
__device__ __forceinline__ float swap16_sum(float v) {
  auto r = __builtin_amdgcn_permlane16_swap(__float_as_uint(v), __float_as_uint(v), false, false);
  return __uint_as_float(r[0]) + __uint_as_float(r[1]);
}
__device__ __forceinline__ float swap32_sum(float v) {
  auto r = __builtin_amdgcn_permlane32_swap(__float_as_uint(v), __float_as_uint(v), false, false);
  return __uint_as_float(r[0]) + __uint_as_float(r[1]);
}
__device__ __forceinline__ float red32_sum(float v) {
  v += dppf<0xB1>(v);
  v += dppf<0x4E>(v);
  v += dppf<0x141>(v);
  v += dppf<0x140>(v);
  return swap16_sum(v);
}
#define SCHED_FENCE() __builtin_amdgcn_sched_barrier(0)
#define DRAIN() asm volatile("s_waitcnt vmcnt(0)" ::: "memory")

__device__ __forceinline__ float red16_sum(float v) {
  v += dppf<0xB1>(v);
  v += dppf<0x4E>(v);
  v += dppf<0x141>(v);
  return v + dppf<0x140>(v);
}
__device__ __forceinline__ float ln_rinv(float q, float dm) {
  const float var = fmaxf(fmaf(q, 1.0f / DM, -dm * dm), 0.f);
  return __builtin_amdgcn_rcpf(rbf(__builtin_amdgcn_sqrtf(rbf(rbf(var) + 1e-5f))));
}

constexpr int TAB_STRIDE = 544;
constexpr int TAB_ROWS = 58;
constexpr int TR_M = 26, TR_F = 34, TR_H = 42;
constexpr int TABB = TAB_ROWS * TAB_STRIDE;
constexpr int SM_KC = 3 * TABB;
constexpr int RDS = 336 * 4;
constexpr int KC_RD = SM_KC;
constexpr int KC_R5 = KC_RD + 4 * RDS;
constexpr int KC_R3 = KC_R5 + 64 * 4;
constexpr int KC_R9 = KC_R3 + 64 * 4;
constexpr int KC_R7 = KC_R9 + 96 * 4;
constexpr int SM_US = KC_R7 + 96 * 4;
constexpr int UPAD = 256;
constexpr int CH2 = SM_US + (UPAD + SEQ) * 2 - SM_KC;
static_assert(SM_US + 2 * UPAD <= SM_KC + 8192 && 8192 <= CH2 && 2 * TABB + 256 < 65536, "zero fill");
constexpr int YT = 1280;
constexpr int SM_YD = 0, SM_Y2 = 8 * YT - 2 * YT, SM_Y3 = 14 * YT - 4 * YT;
constexpr int SM_T2 = 0, T2S = 4624;
constexpr int SM_RED = SM_KC + 3 * CH2;
constexpr int SM_ROW = SM_RED + 3200;
constexpr int SM_ISC = SM_ROW + 128 + 128 + 32;
constexpr int SM_MX = SM_ISC + 16;
constexpr int SM_WR = SM_MX + 32;
constexpr int SM_DUMMY = SM_WR + DM * 2;
constexpr int SM_PRG = SM_DUMMY + 256;
constexpr int SM_SB = SM_PRG + 64;
constexpr int RG = 8192;
constexpr int FS_X = 18 * RG;
constexpr int FS_SEL = FS_X + 9216;
constexpr int FS_GU = FS_SEL + 16;
constexpr int SM_TOTAL = FS_GU + 8 * 160;
static_assert(SM_SB + 3072 <= SM_TOTAL && SM_ISC % 16 == 0 && CH2 % 16 == 0 && TABB % 16 == 0, "lds");

typedef f32x2 cf;
#define TM(t, a, b) "v_pk_mul_f32 v[" #t ":" #t "+1], v[" #b ":" #b "+1], v[" #a ":" #a "+1] op_sel:[1,0]\n\t"
#define TF(d, a, b, t, n) \
  "v_pk_fma_f32 v[" #d ":" #d "+1], v[" #a ":" #a "+1], v[" #b ":" #b "+1], v[" #t ":" #t "+1] op_sel:[0,0,1] op_sel_hi:[1,0,0] neg_" #n ":[0,0,1]\n\t"
#define TC16(c, v) "v_cvt_pk_f16_f32 v" #c ", v[" #v "], v[" #v "+1]\n\t"
#define TWR(c, row) "ds_write_addtid_b32 v" #c " offset:544*" #row "\n\t"
#define TAB_A \
  TC16(134, 100) TM(106, 102, 102) TM(108, 100, 104) TWR(134, 0) TF(110, 102, 102, 106, lo) TF(112, 100, 104, 108, lo) \
  TM(114, 100, 102) TM(116, 100, 110) TC16(135, 112) TF(118, 100, 102, 114, lo) TF(120, 100, 110, 116, lo) TWR(135, 8) \
  TC16(136, 118) TC16(137, 120) TM(122, 112, 110) TWR(136, 1) TM(124, 118, 104) TF(126, 112, 110, 122, lo) TWR(137, 2) \
  TF(128, 118, 104, 124, lo) TC16(134, 126) TM(130, 126, 110) TC16(135, 128) TWR(134, 10) TF(132, 126, 110, 130, lo) \
  TM(106, 118, 110) TWR(135, 9) TC16(136, 132) TF(108, 118, 110, 106, lo) TM(114, 132, 104) TWR(136, 12) TC16(137, 108) \
  TF(102, 132, 104, 114, lo) TM(116, 132, 110) TWR(137, 3) TC16(134, 102) TF(100, 132, 110, 116, lo) TM(122, 102, 104) TWR(134, 18) \
  TC16(135, 100) TF(124, 102, 104, 122, lo) TM(130, 128, 110) TWR(135, 14) TC16(136, 124) TF(106, 128, 110, 130, lo) \
  TM(118, 124, 104) TWR(136, 19) TC16(137, 106) TF(114, 124, 104, 118, lo) TM(116, 100, 104) TWR(137, 11) TC16(134, 114) \
  TF(132, 100, 104, 116, lo) TM(122, 114, 104) TWR(134, 26) TC16(135, 132) TF(102, 114, 104, 122, lo) TM(130, 120, 110) \
  TWR(135, 17) TC16(136, 102) TF(128, 120, 110, 130, lo) TM(118, 108, 110) TWR(136, 28) TC16(137, 128) TF(124, 108, 110, 118, lo) \
  TM(116, 106, 110) TWR(137, 4) TC16(134, 124) TF(100, 106, 110, 116, lo) TM(122, 112, 104) TWR(134, 5) TC16(135, 100) \
  TF(114, 112, 104, 122, lo) TM(130, 126, 104) TWR(135, 13) TC16(136, 114) TF(120, 126, 104, 130, lo) TM(118, 132, 110) \
  TWR(136, 21) TC16(137, 120) TF(108, 132, 110, 118, lo) TM(116, 102, 104) TWR(137, 20) TC16(134, 108) TF(106, 102, 104, 116, lo) \
  TM(122, 128, 110) TWR(134, 16) TC16(135, 106) TF(112, 128, 110, 122, lo) TM(130, 124, 110) TWR(135, 30) TC16(136, 112) \
  TF(126, 124, 110, 130, lo) TM(118, 100, 110) TWR(136, 6) TC16(137, 126) TF(116, 100, 110, 118, lo) TM(102, 114, 104) TWR(137, 7) \
  TC16(134, 116) TF(122, 114, 104, 102, lo) TM(128, 120, 104) TWR(134, 15) TC16(135, 122) TF(112, 120, 104, 128, lo) \
  TM(130, 132, 104) TWR(135, 23) TC16(136, 112) TF(124, 132, 104, 130, lo) TM(126, 108, 104) TWR(136, 22) TC16(137, 124) \
  TF(118, 108, 104, 126, lo) TM(110, 106, 104) TWR(137, 25) TC16(134, 118) TF(100, 106, 104, 110, lo) TWR(134, 24) TC16(135, 100) \
  TWR(135, 32)
#define TAB_B \
  "v_mov_b32 v137, 0x3c00\n\t" TWR(137, 42) TC16(134, 102) TM(106, 104, 104) TM(108, 100, 102) TWR(134, 43) \
  TF(110, 104, 104, 106, lo) TF(112, 100, 102, 108, hi) TM(114, 102, 102) TM(116, 110, 110) TF(118, 110, 110, 116, lo) \
  TF(120, 102, 102, 114, lo) TC16(135, 120) TM(122, 112, 118) TM(124, 120, 102) TWR(135, 44) TF(126, 112, 118, 122, lo) \
  TF(128, 120, 102, 124, lo) TM(130, 120, 120) TC16(136, 126) TC16(137, 128) TF(132, 120, 120, 130, lo) TWR(136, 27) \
  TM(106, 126, 104) TC16(134, 132) TWR(137, 45) TF(108, 126, 104, 106, lo) TM(100, 126, 110) TWR(134, 46) TC16(135, 108) \
  TF(116, 126, 110, 100, lo) TM(114, 108, 110) TWR(135, 29) TC16(136, 116) TF(122, 108, 110, 114, lo) TM(112, 102, 132) \
  TWR(136, 31) TC16(137, 122) TF(124, 102, 132, 112, lo) TM(130, 120, 132) TWR(137, 33) TC16(134, 124) TF(106, 120, 132, 130, lo) \
  TM(104, 128, 132) TWR(134, 47) TC16(135, 106) TF(100, 128, 132, 104, lo) TM(114, 132, 132) TWR(135, 48) TC16(136, 100) \
  TF(110, 132, 132, 114, lo) TM(112, 124, 132) TWR(136, 49) TC16(137, 110) TF(102, 124, 132, 112, lo) TM(130, 106, 132) \
  TWR(137, 50) TC16(134, 102) TF(120, 106, 132, 130, lo) TM(104, 100, 132) TWR(134, 51) TC16(135, 120) TF(128, 100, 132, 104, lo) \
  TM(114, 126, 118) TWR(135, 52) TC16(136, 128) TF(112, 126, 118, 114, lo) TM(124, 108, 118) TWR(136, 53) TC16(137, 112) \
  TF(130, 108, 118, 124, lo) TM(106, 116, 118) TWR(137, 34) TC16(134, 130) TF(104, 116, 118, 106, lo) TM(100, 122, 118) \
  TWR(134, 35) TC16(135, 104) TF(114, 122, 118, 100, lo) TM(126, 110, 132) TWR(135, 36) TC16(136, 114) TF(124, 110, 132, 126, lo) \
  TM(108, 102, 132) TWR(136, 37) TC16(137, 124) TF(106, 102, 132, 108, lo) TM(116, 120, 132) TWR(137, 54) TC16(134, 106) \
  TF(100, 120, 132, 116, lo) TM(122, 128, 132) TWR(134, 55) TC16(135, 100) TF(126, 128, 132, 122, lo) TM(110, 112, 118) \
  TWR(135, 56) TC16(136, 126) TF(124, 112, 118, 110, lo) TM(108, 130, 118) TWR(136, 57) TC16(137, 124) TF(102, 130, 118, 108, lo) \
  TM(106, 104, 118) TWR(137, 38) TC16(134, 102) TF(116, 104, 118, 106, lo) TM(120, 114, 118) TWR(134, 39) TC16(135, 116) \
  TF(100, 114, 118, 120, lo) TWR(135, 40) TC16(136, 100) TWR(136, 41)
#define TAB_CLOB "v106", "v107", "v108", "v109", "v110", "v111", "v112", "v113", "v114", "v115", "v116", "v117", "v118", "v119", "v120", "v121", "v122", "v123", "v124", "v125", "v126", "v127", "v128", "v129", "v130", "v131", "v132", "v133", "v134", "v135", "v136", "v137"
__device__ __forceinline__ unsigned pkc(cf a) { return __builtin_bit_cast(unsigned, __builtin_convertvector(a, half2v)); }
__device__ __forceinline__ cf cexp(f32x2 hi, f32x2 lo) {
  float mag = __builtin_amdgcn_exp2f(hi[0]);
  mag = fmaf(mag, lo[0], mag);
  const float fr = __builtin_amdgcn_fractf(hi[1]) + lo[1];
  return cf{__builtin_amdgcn_cosf(fr), __builtin_amdgcn_sinf(fr)} * mag;
}
__device__ __forceinline__ unsigned wave_max_u32(unsigned v) {
  v = max(v, (unsigned)__builtin_amdgcn_update_dpp(0, (int)v, 0x111, 0xf, 0xf, false));
  v = max(v, (unsigned)__builtin_amdgcn_update_dpp(0, (int)v, 0x112, 0xf, 0xf, false));
  v = max(v, (unsigned)__builtin_amdgcn_update_dpp(0, (int)v, 0x114, 0xf, 0xf, false));
  v = max(v, (unsigned)__builtin_amdgcn_update_dpp(0, (int)v, 0x118, 0xf, 0xf, false));
  v = max(v, (unsigned)__builtin_amdgcn_update_dpp(0, (int)v, 0x142, 0xa, 0xf, false));
  v = max(v, (unsigned)__builtin_amdgcn_update_dpp(0, (int)v, 0x143, 0xc, 0xf, false));
  return (unsigned)__builtin_amdgcn_readlane((int)v, 63);
}

#define MFMA16(a, b, c) __builtin_amdgcn_mfma_f32_16x16x32_f16((a), (b), (c), 0, 0, 0)
#define MFMAB(a, b, c) __builtin_amdgcn_mfma_f32_16x16x32_bf16((a), (b), (c), 0, 0, 0)

__device__ __forceinline__ half8 ld_frag(const LAS char* p) { return *(const LAS half8*)p; }
__device__ __forceinline__ const LAS unsigned* opaque_lds(const LAS char* p) {
  const LAS unsigned* q = (const LAS unsigned*)p;
  asm volatile("" : "+v"(q));
  return q;
}
template <int O2>
__device__ __forceinline__ half8 ld_toep(const LAS unsigned* q) {
  u32x4 v = {q[0], q[1], q[O2], q[O2 + 1]};
  return __builtin_bit_cast(half8, v);
}
__device__ __forceinline__ half8 band_frag(int lane, unsigned pat) {
  unsigned t = __builtin_amdgcn_ubfe(pat << (lane & 15), 8 * (lane >> 4), 8);
  t |= t << 15;
  u32x4 v;
#pragma unroll
  for (int j = 0; j < 4; j++) v[j] = __umul24(t & (0x10001u << (2 * j)), 0x3c00u >> (2 * j));
  return __builtin_bit_cast(half8, v);
}
__device__ __forceinline__ half8 pack_win(f32x4 a, f32x4 b) {
  u32x4 v = {pkh(a[0], a[1]), pkh(a[2], a[3]), pkh(b[0], b[1]), pkh(b[2], b[3])};
  return __builtin_bit_cast(half8, v);
}

__device__ __forceinline__ void set_prio(int p) {
  if (p == 0) __builtin_amdgcn_s_setprio(0);
  if (p == 1) __builtin_amdgcn_s_setprio(1);
  if (p == 2) __builtin_amdgcn_s_setprio(2);
  if (p == 3) __builtin_amdgcn_s_setprio(3);
}
template <int V>
using IC = std::integral_constant<int, V>;
template <int S, int E, class F>
__device__ __forceinline__ void static_for(F&& f) {
  if constexpr (S < E) {
    f(std::integral_constant<int, S>{});
    static_for<S + 1, E>(f);
  }
}
__device__ __forceinline__ half8 ld_td(const LAS char* tp, int i) {
  const volatile LAS u32x2* tb = (const volatile LAS u32x2*)tp;
  const u32x2 lo = tb[8 * (16 - i)], hi = tb[8 * (16 - i) + 1];
  return __builtin_bit_cast(half8, __builtin_shufflevector(lo, hi, 0, 1, 2, 3));
}
__device__ __forceinline__ void conv_dense(half8 (&td)[17], const LAS char* tp, half8 tmask, const LAS char* f1024, const LAS char* ub, LAS char* yd, LAS char* prg) {
  constexpr int S0 = 9, NS = 73, PF = PFD;
  const f32x4 zero4 = {0.f, 0.f, 0.f, 0.f};
  f32x4 acc[8] = {zero4, zero4, zero4, zero4, zero4, zero4, zero4, zero4};
  half8 u[NS], te;
  unsigned fe = 0;
#pragma unroll
  for (int s = S0; s < S0 + PF; s++) {
    if (TDI && s >= S0 + TDB && s <= 16) {
      td[16 - s] = ld_td(tp, 16 - s);
      td[24 - s] = ld_td(tp, 24 - s);
    }
    if (TDI && s == 16) td[16] = ld_td(tp, 16);
    u[s] = ld_frag(ub + 64 * s);
    if (s == S0 + 3) fe = *(const LAS u16*)f1024;
    SCHED_FENCE();
  }
  static_for<S0, NS>([&](auto sc) {
    constexpr int s = decltype(sc)::value, t0 = s < 16 ? 0 : (s - 9) / 8, t1 = s / 8 < 7 ? s / 8 : 7;
    acc[t0] = MFMA16(td[16 + 8 * t0 - s], u[s], acc[t0]);
    SCHED_FENCE();
    if constexpr (s + PF < NS) u[s + PF] = ld_frag(ub + 64 * (s + PF));
    SCHED_FENCE();
    if constexpr (t0 + 1 <= t1) acc[t0 + 1] = MFMA16(td[24 + 8 * t0 - s], u[s], acc[t0 + 1]);
    SCHED_FENCE();
    if constexpr (s >= 16 && (s & 7) == TWO) *(volatile LAS f32x4*)(yd + (s / 8 - 2) * YT) = acc[s / 8 - 2];
    if constexpr (s >= 16 && (s & 7) == TWO + 1) *(volatile LAS int*)prg = s / 8 - 1;
    if constexpr (s == 14) {
      fe |= fe << 16;
      te = tmask * __builtin_bit_cast(half8, u32x4{fe, fe, fe, fe});
    }
    SCHED_FENCE();
    if constexpr (t0 + 2 <= t1) acc[t0 + 2] = MFMA16(td[32 + 8 * t0 - s], u[s], acc[t0 + 2]);
    if constexpr (s >= 16 && s <= 40 && (s & 7) == 0) acc[s / 8 + 2] = MFMA16(te, u[s], acc[s / 8 + 2]);
    SCHED_FENCE();
  });
  *(volatile LAS f32x4*)(yd + 7 * YT) = acc[7];
  *(volatile LAS int*)prg = 8;
}

typedef __amdgpu_buffer_rsrc_t rsrc_t;
template <int AUX = LAUX>
__device__ __forceinline__ u32x4 ldg16(rsrc_t rs, int off, int so = 0) {
  asm volatile("" : "+s"(so) : : "memory");
  return __builtin_amdgcn_raw_buffer_load_b128(rs, off, so, AUX);
}
__device__ __forceinline__ void stg16(rsrc_t rs, int off, u32x4 v) { __builtin_amdgcn_raw_buffer_store_b128(v, rs, off, 0, SAUX); }
__device__ __forceinline__ void dma16(const char* g, unsigned lds) {
  unsigned keep;
  asm volatile("s_mov_b32 %0, m0\n\ts_mov_b32 m0, %2\n\ts_nop 0\n\tglobal_load_lds_dwordx4 %1, off" WNTS "\n\ts_mov_b32 m0, %0"
               : "=&s"(keep) : "v"(g), "s"(lds) : "memory");
}
__device__ __forceinline__ void dma16nt(const char* g, unsigned lds) {
  unsigned keep;
  asm volatile("s_mov_b32 %0, m0\n\ts_mov_b32 m0, %2\n\ts_nop 0\n\tglobal_load_lds_dwordx4 %1, off nt\n\ts_mov_b32 m0, %0"
               : "=&s"(keep) : "v"(g), "s"(lds) : "memory");
}
__device__ __forceinline__ void dma4(const char* g, unsigned lds) {
  unsigned keep;
  asm volatile("s_mov_b32 %0, m0\n\ts_mov_b32 m0, %2\n\ts_nop 0\n\tglobal_load_lds_dword %1, off\n\ts_mov_b32 m0, %0"
               : "=&s"(keep) : "v"(g), "s"(lds) : "memory");
}
__device__ __forceinline__ void dma4s(size_t base, unsigned off, unsigned lds) {
  unsigned keep;
  asm volatile("s_mov_b32 %0, m0\n\ts_mov_b32 m0, %3\n\ts_nop 0\n\tglobal_load_lds_dword %1, %2\n\ts_mov_b32 m0, %0"
               : "=&s"(keep) : "v"(off), "s"(base), "s"(lds) : "memory");
}
__device__ __forceinline__ void wait_ticks(int t) {
  const unsigned long long t0 = __builtin_amdgcn_s_memrealtime();
  while (__builtin_amdgcn_s_memrealtime() - t0 < (unsigned long long)t) __builtin_amdgcn_s_sleep(1);
}
#define UPOS 1
#define DLY_X 30
#define DLY_W 0
#define HB 1
#define DLY_H 220
#define DLY_R 0
#define DLY_L (DLY_K + 30)
#define DLY_P 120

__device__ __forceinline__ int key_row(unsigned k) { return (int)(~(k >> 2) & (SEQ - 1)); }
__device__ __forceinline__ int key_slot(int lane) { return (lane & 3) | ((lane >> 1) & 4); }
__device__ __forceinline__ int key_addr(unsigned k, int slot) {
  return WS_XS + ((k & 2) ? 2 * NLN + slot : (int)((~(k >> 5) & (2 * NLN - 2)) | (k & 1))) * XSS;
}
struct SelMasks {
  unsigned m[15];
  __device__ __forceinline__ SelMasks(int lane) {
    const unsigned b0 = -(lane & 1), b1 = -((lane >> 1) & 1), b3 = -((lane >> 3) & 1), b4 = -((lane >> 4) & 1), b5 = -(lane >> 5);
    const unsigned t[15] = {~(b0 ^ b1), ~(b1 ^ b3), ~(b0 ^ b3), ~b3, ~b1, ~b0, b3, b1, b0, ~(b3 ^ b4), ~(b1 ^ b4), ~(b0 ^ b4), ~(b3 ^ b5), ~(b1 ^ b5), ~(b0 ^ b5)};
#pragma unroll
    for (int i = 0; i < 15; i++) {
      m[i] = t[i];
      asm volatile("" : "+v"(m[i]));
    }
  }
};
template <int CTRL>
__device__ __forceinline__ unsigned cex(unsigned v, unsigned m) {
  const unsigned o = dppu<CTRL>(v);
  return max(min(v, o), min(max(v, o), m));
}
__device__ __forceinline__ unsigned clean8(unsigned v, const unsigned* m) { return cex<0xB1>(cex<0x4E>(cex<0x128>(v, m[0]), m[1]), m[2]); }
template <bool EAGER, class F>
__device__ __forceinline__ void select8(rsrc_t rs, unsigned tag, int lane, const SelMasks& sm, char* ws, int bid, unsigned& stamp, F&& use) {
  const int off = WS_KR + (bid & 7) * 2048 + lane * 16;
  unsigned m1, t1, t2, m4, third;
  u32x4 a, c, d, e;
  if (EAGER) {
    const u32x4 rq = {(unsigned)(size_t)ws, (unsigned)((size_t)ws >> 32) & 0xffffu, (unsigned)WS_END, 0x00020000u};
    unsigned long long t;
    int n = SPIN_CAP;
#define KASK(r) "buffer_load_dwordx4 v[" #r ":" #r "+3], %11, %12, 0 offen sc1\n\tbuffer_load_dwordx4 v[" #r "+4:" #r "+7], %11, %12, 0 offen offset:1024 sc1\n\t"
#define KCHK(r, l)                                                                                                          \
  "s_waitcnt vmcnt(2)\n\tv_cmp_eq_u32 vcc, %13, v[" #r "+3]\n\tv_cmp_eq_u32 %9, %13, v[" #r "+7]\n\ts_and_b64 %9, %9, vcc\n\t" \
  "s_cmp_eq_u64 %9, -1\n\ts_cbranch_scc1 " #l "\n\t"
#define KUSE(r)                                                                                                             \
  "v_max_u32 %0, v[" #r "], v[" #r "+4]\n\tv_min_u32 %1, v[" #r "], v[" #r "+4]\n\tv_max_u32 %2, v[" #r "+1], v[" #r "+5]\n\t"     \
  "v_min_u32 %3, v[" #r "+1], v[" #r "+5]\n\tv_max_u32 %4, v[" #r "+2], v[" #r "+6]\n\t"
    asm volatile(KASK(148) "s_sleep %14\n1:\n\t" KASK(156) KCHK(148, 2f) KASK(148) KCHK(156, 3f) "s_sub_u32 %10, %10, 1\n\ts_cbranch_scc0 1b\n"
                 "3:\n\t" KUSE(156) "s_branch 4f\n2:\n\t" KUSE(148) "\n4:"
                 : "=&v"(m1), "=&v"(t1), "=&v"(t2), "=&v"(m4), "=&v"(third), "=&{v[148:151]}"(a), "=&{v[152:155]}"(c), "=&{v[156:159]}"(d),
                   "=&{v[160:163]}"(e), "=&s"(t), "+s"(n)
                 : "v"(off), "s"(rq), "s"(tag), "n"(KPP) : "vcc", "scc", "memory");
  } else {
    _Pragma("unroll 1") for (int spin = 0; spin < SPIN_CAP; spin++) {
      a = ldg16(rs, off);
      c = ldg16(rs, off, 1024);
      if (__builtin_expect(__all(a[3] == tag && c[3] == tag), 1)) break;
      __builtin_amdgcn_s_sleep(2);
    }
    TSW(10, 2);
    const unsigned now = (unsigned)__builtin_amdgcn_s_memrealtime();
    stamp = now - 0xffffu + wave_max_u32(max((a[2] - now - 1u) & 0xffffu, (c[2] - now - 1u) & 0xffffu));
    m1 = max(a[0], c[0]), t1 = min(a[0], c[0]), t2 = max(a[1], c[1]), m4 = min(a[1], c[1]), third = max(a[2], c[2]);
  }
  TSL(1, 8);
  third |= 0xffffu;
  unsigned m2 = max(t1, t2), m3 = min(t1, t2);
  unsigned x = clean8(cex<0xB1>(cex<0x4E>(cex<0xB1>(m1, sm.m[0]), sm.m[1]), sm.m[2]), sm.m + 3);
  const unsigned y = clean8(cex<0xB1>(cex<0x4E>(cex<0xB1>(m2, sm.m[0]), sm.m[1]), sm.m[2]), sm.m + 6);
  x = clean8(max(x, y), sm.m + 3);
  x = clean8(max(x, dppu<0x140>(x)), sm.m + 9);
  {
    auto r = __builtin_amdgcn_permlane16_swap(x, x, false, false);
    x = clean8(max(r[0], r[1]), sm.m + 12);
    auto q = __builtin_amdgcn_permlane32_swap(x, x, false, false);
    x = max(q[0], q[1]);
  }
  use(x, IC<0>{});
  unsigned k = min(x, dppu<0xB1>(x));
  k = min(k, dppu<0x4E>(k));
  k = min(k, dppu<0x128>(k));
  if (__builtin_expect(__any(max(m3, third) > k), 0)) {
    const int slot = key_slot(lane);
    _Pragma("unroll 1") for (int r = 0; r < 8; r++) {
      k = wave_max_u32(m1);
      x = slot == r ? k : x;
      const bool e = m1 == k;
      m1 = e ? m2 : m1;
      m2 = e ? m3 : m2;
      m3 = e ? m4 : m3;
      m4 = e ? 0u : m4;
    }
    if (__any(third > k)) {
      k = 0u;
      _Pragma("unroll 1") for (int r = 0; r < 8; r++) {
        unsigned v;
        _Pragma("unroll 1") for (int spin = 0; spin < SPIN_CAP; spin++) {
          bool ok = true;
          v = 0u;
          _Pragma("unroll 4") for (int i = 0; i < 16; i++) {
            const u32x4 f = ldg16(rs, WS_KF + lane * 16, i * 1024);
            ok = ok && f[1] == tag && f[3] == tag;
            v = max(v, max(f[0] <= k - 1u ? f[0] : 0u, f[2] <= k - 1u ? f[2] : 0u));
          }
          if (__all(ok)) break;
          __builtin_amdgcn_s_sleep(1);
        }
        k = wave_max_u32(v);
        x = slot == r ? k : x;
      }
    }
    use(x, IC<1>{});
  }
  use(x, IC<2>{});
  if (EAGER) asm volatile("s_waitcnt vmcnt(9)" : : "{v[148:151]}"(a), "{v[152:155]}"(c), "{v[156:159]}"(d), "{v[160:163]}"(e) : "memory");
}

__device__ __forceinline__ int last_start(rsrc_t rs, unsigned tag, int lane, int bid, unsigned ref) {
  u32x4 p, q;
  _Pragma("unroll 1") for (int spin = 0; spin < SPIN_CAP; spin++) {
    p = ldg16(rs, WS_CK + (bid & 7) * 2048 + lane * 16);
    q = ldg16(rs, WS_CK + (bid & 7) * 2048 + 1024 + (lane & 31) * 16);
    if (__all(p[1] == tag && p[3] == tag && q[1] == tag && q[3] == tag)) break;
    __builtin_amdgcn_s_sleep(4);
  }
  const int lag = max(max((int)(p[0] - ref), (int)(p[2] - ref)), max((int)(q[0] - ref), (int)(q[2] - ref)));
  return min((int)wave_max_u32((unsigned)max(lag + 4096, 0)) - 4096, 1000);
}
__device__ __forceinline__ int late_group(size_t wg, int bid) { return (3 - (int)(wg >> 7) - bid) & 7; }
__device__ __forceinline__ int ffn_group(size_t wg, int bid) {
  const int l = late_group(wg, bid), i = bid >> 3;
  return (bid & 7) * 9 + (bid < NFFN ? (i == l ? 8 : i) : l);
}
__device__ __forceinline__ void down_dma(size_t wd, int j, LAS char* L, int wave, int lane, int r0, int r1) {
#pragma unroll
  for (int k = 0; k < 3; k++) {
    const int slot = 3 * (wave - 9) + k, t = slot * 64 + lane, r8 = t / 72, q8 = (t - r8 * 72) ^ (r8 & 2);
    const char* p = (const char*)wd + (unsigned)((j * 64 + r8) * 1152);
    _Pragma("unroll 1") for (int i = r0; i < r1; i++)
        (DNT ? dma16nt : dma16)(p + (unsigned)(i * 9216 + (q8 ^ ((i & 1) << 2)) * 16), (unsigned)(size_t)L + slot * 1024 + i * 9216);
  }
}
__device__ __forceinline__ unsigned stats_role(size_t argp, unsigned cpf, LAS char* L, unsigned long long r_entry) {
  const int tid = threadIdx.x, lane = tid & 63, bid = blockIdx.x, fw = bid;
  size_t x, wsp, wd, wg;
  asm volatile(
      "s_waitcnt lgkmcnt(0)\n\ts_load_dwordx2 %0, %4, 0x0\n\ts_load_dwordx2 %1, %4, 0x30\n\ts_load_dwordx2 %2, %4, 0x58\n\t"
      "s_load_dwordx2 %3, %4, 0x48"
      : "=&s"(x), "=&s"(wsp), "=&s"(wd), "=&s"(wg) : "s"(argp));
  SCHED_FENCE();
  unsigned xo0 = (bid * 32 + (tid >> 4)) * (DM * 2) + (lane & 15) * 8;
  asm volatile("" : "+v"(xo0));
  asm volatile("s_waitcnt lgkmcnt(0)" : "+s"(x), "+s"(wsp), "+s"(wd), "+s"(wg));
  const int wave = __builtin_amdgcn_readfirstlane(tid >> 6);
  char* const ws = (char*)(GAS char*)wsp;
  const rsrc_t rs = __builtin_amdgcn_make_buffer_rsrc(ws, 0, WS_END, 0x00020000);
  unsigned tagv = WS_EPOCH;
  asm volatile("global_load_dword %0, %0, %1 sc1" : "+v"(tagv) : "s"(wsp) : "memory");
  SCHED_FENCE();
  unsigned tag;
  if (wave >= 8) {
    if (ED) {
      __builtin_amdgcn_s_barrier();
      if (wave > 8) down_dma(wd, ffn_group(wg, bid), L, wave, lane, 0, ED);
      else DRAIN();
    }
    asm volatile("s_waitcnt vmcnt(%2)" : "+v"(tagv) : "v"(cpf), "n"(3 * ED));
    tag = __builtin_amdgcn_readfirstlane(tagv) + 1u;
    asm volatile("" : "+s"(tag));
  } else {
    u32x2 xw[9];
#pragma unroll
    for (int k = 0; k < 9; k++) xw[k] = *(GAS const u32x2*)(x + xo0 + 128 * k);
    SCHED_FENCE();
    if (ED) __builtin_amdgcn_s_barrier();
    unsigned one2 = 0x3f803f80u;
    asm volatile("" : "+s"(one2));
    float s0 = 0.f, s1 = 0.f, q0 = 0.f, q1 = 0.f;
#pragma unroll
    for (int k = 0; k < 9; k++) {
      const unsigned a = xw[k][0], c = xw[k][1];
      s0 = dot2(a, one2, s0);
      q0 = dot2(a, a, q0);
      s1 = dot2(c, one2, s1);
      q1 = dot2(c, c, q1);
    }
    const float mean = red16_sum(s0 + s1) * (1.0f / DM), mbf = rbf(mean);
    float q = red16_sum(q0 + q1), dm = mean;
    if (__builtin_expect(__any(!(q * (1.0f / DM) >= mean * mean * 1.002f)), 0)) {
      f32x2 q2 = {0.f, 0.f};
#pragma unroll
      for (int k = 0; k < 18; k++) {
        const f32x2 dd = unpk(xw[k >> 1][k & 1]) - mbf;
        q2 = __builtin_elementwise_fma(dd, dd, q2);
      }
      q = red16_sum(hadd(q2));
      dm = mean - mbf;
    }
    float ri = ln_rinv(q, dm);
    asm volatile("" : "+v"(tagv), "+v"(ri));
    auto rr = __builtin_amdgcn_permlane16_swap(__float_as_uint(ri), __float_as_uint(ri), false, false);
    auto m2 = __builtin_amdgcn_permlane16_swap(__float_as_uint(mbf), __float_as_uint(mbf), false, false);
    const unsigned mp = __builtin_amdgcn_perm(m2[1], m2[0], 0x07060302u);
    if ((lane & 31) == 0) stg16(rs, WS_ST + (16 * fw + (tid >> 5)) * 16, u32x4{rr[0], rr[1], mp, tagv + 1u});
    tag = __builtin_amdgcn_readfirstlane(tagv) + 1u;
    asm volatile("" : : "v"(cpf), "s"(tag));
  }
  TS(1, 10);
  return tag;
}

__device__ __forceinline__ void ffn_role(size_t argp, unsigned tag, LAS char* L) {
  const int tid = threadIdx.x, lane = tid & 63, bid = blockIdx.x;
  size_t wsp, wdp;
  u64x2 gu;
  asm volatile("s_load_dwordx2 %0, %3, 0x30\n\ts_load_dwordx4 %1, %3, 0x48\n\ts_load_dwordx2 %2, %3, 0x58" : "=&s"(wsp), "=&s"(gu), "=&s"(wdp) : "s"(argp));
  SCHED_FENCE();
  const int wave = __builtin_amdgcn_readfirstlane(tid >> 6);
  const int hm = bid < NFFN ? 3 : (bid & 8 ? 2 : 1);
  const bool mine = wave < 8 && (hm >> (wave >> 2) & 1);
  const int g = lane >> 4, tq = (lane & 15) >> 2, tp = lane & 3;
  const bool full = hm == 3;
  const int gr = full ? tid >> 4 : tid >> 3;
  const int gc = full ? (lane & 7) ^ ((gr & 1) | ((gr >> 1) & 6)) : (hm & 2) * 2 + ((lane & 3) ^ ((gr >> 2) & 3));
  unsigned go = gr * (FF * 2) + gc * 16;
  asm volatile("" : "+v"(go));
  asm volatile("s_waitcnt lgkmcnt(0)" : "+s"(wsp), "+s"(gu), "+s"(wdp));
  char* const ws = (char*)(GAS char*)wsp;
  const rsrc_t rs = __builtin_amdgcn_make_buffer_rsrc(ws, 0, WS_END, 0x00020000);
  const int j = ffn_group(gu[0], bid);
  const char* gsrc = (const char*)((lane & (full ? 8 : 4)) ? gu[1] : gu[0]) + (go + j * 128);
  const unsigned lb = (unsigned)(size_t)L + wave * 1024 + 9 * RG;
  auto issue_gu = [&](int n, unsigned step) {
    if (wave < 8) {
      _Pragma("unroll 1") for (int i = 0; i < n; i++) {
        (GNT ? dma16nt : dma16)(gsrc, lb + i * RG);
        gsrc += step;
      }
    }
  };
  __syncthreads();
  TS(1, 11);
  if (DLY_W) wait_ticks(DLY_W);
  issue_gu(full ? 10 : 9, full ? 32 * FF * 2 : 64 * FF * 2);
  if (wave > 8) down_dma(wdp, j, L, wave, lane, full ? ED : (hm & 2) * 2, hm == 1 ? 4 : 8);
  s16x4 fa[18][2], fd[3][4];
  const int fr = 16 * (g >> 1) + 4 * (g & 1) + (tq & 1) + 8 * (tq >> 1), fx = (g & 1) | ((tq >> 1) << 1);
  const LAS char* gup = L + 9 * RG + (tp & 1) * 8 +
                        (full ? fr * 256 + ((wave ^ ((tq & 1) | (fx << 1))) + 8 * (tp >> 1)) * 16 : fr * 128 + (((wave & 3) ^ fx) + 4 * (tp >> 1)) * 16);
  const LAS char* gup2 = gup + 8 * RG;
  asm volatile("" : "+v"(gup), "+v"(gup2));
  auto read_gu = [&](auto ks0, auto n, auto reg0, auto sz) {
#pragma unroll
    for (int i = 0; i < n; i++)
#pragma unroll
      for (int h = 0; h < 2; h++) {
        const int o = reg0 * RG + i * sz + h * (sz / 16);
        fa[ks0 + i][h] = __builtin_amdgcn_ds_read_tr16_b64_v4i16((LAS s16x4*)(o < 8 * RG ? gup + o : gup2 + (o - 8 * RG)));
      }
  };
  auto read_down = [&]() {
    const int f = (tq >> 1) | ((g & 1) << 1);
#pragma unroll
    for (int t = 0; t < 3; t++) {
      const LAS char* p = L + (8 * g + tq + (hm == 2 ? 32 : 0)) * 1152 + ((3 * wave + t) ^ f) * 32 + tp * 8;
#pragma unroll
      for (int h = 0; h < 4; h++) {
        if (h >= 2 && !full) fd[t][h] = s16x4{0, 0, 0, 0};
        else fd[t][h] = __builtin_amdgcn_ds_read_tr16_b64_v4i16((LAS s16x4*)(p + (32 * (h >> 1) + 4 * (h & 1)) * 1152));
      }
    }
  };
  const int c15 = lane & 15;
  const LAS char* xr = L + FS_X + ((((c15 >> 3) + 2 * g) ^ (2 * tp)) + 2 * (lane & 4) + 16 * tp) * 16 + (wave < 8 ? 0 : (wave - 8) * 2048);
  LAS char* guw = L + FS_GU + (4 * (g & 1) + 2 * (g >> 1) + (c15 >> 3)) * 160 + ((wave ^ (hm == 2 ? 4 : 0)) * 8 + (lane & 7)) * 2;
  const LAS char* gur = L + FS_GU + (lane & 7) * 160 + 16 * g;
  int xo = ((lane & 7) ^ (2 * g)) * 16 + (1 + (bid & (NC - 1))) * (DM * 2);
  int po = WS_PART + (bid < NFFN ? bid : bid - LATE0) * (8 * DM * 4) + (3 * wave + PM * (c15 >> 3)) * 512 + g * 128 + (lane & 7) * 16;
  int sidx = c15 < 8 ? 0x4444 : 0xeeee;
  asm volatile("" : "+v"(guw), "+v"(gur), "+v"(xo), "+v"(po), "+v"(sidx), "+v"(xr));
  if (wave < 8) *(LAS u16*)guw = 0;
  else *(LAS int*)(L + FS_SEL + 4) = 0;
  const SelMasks sm(lane);
  unsigned due = 0;
  if (full) {
    if (wave < 8) asm volatile("s_waitcnt vmcnt(2)" ::: "memory");
    __syncthreads();
    if (mine) read_gu(IC<0>{}, IC<8>{}, IC<0>{}, IC<RG>{});
    __syncthreads();
    TS(1, 6);
    issue_gu(8, 32 * FF * 2);
    if (wave < 8) asm volatile("s_waitcnt vmcnt(8)" ::: "memory");
    else DRAIN();
    __syncthreads();
    if (mine) read_gu(IC<8>{}, IC<2>{}, IC<8>{}, IC<RG>{});
    read_down();
    __syncthreads();
    TS(1, 1);
    if (wave == SELW) {
      due = (unsigned)__builtin_amdgcn_s_memrealtime();
      due += last_start(rs, tag, lane, bid, due);
    }
  } else {
    due = __builtin_amdgcn_readfirstlane(*(const LAS int*)(L + FS_SEL));
  }
  TWDECL;
  auto fetch = [&](int xa) {
    asm volatile(
        "s_mov_b32 m0, %2\n\t.set i_, 0\n\t.rept 9\n\ts_nop 0\n\tglobal_load_lds_dwordx4 %0, %1 offset:128*i_ sc1\n\t"
        "s_add_u32 m0, m0, 896\n\t.set i_, i_+1\n\t.endr"
        : : "v"(xa), "s"(wsp + WS_XS), "s"((unsigned)(size_t)L + FS_X) : "scc", "memory");
  };
  if (wave == SELW) {
    while ((int)((unsigned)__builtin_amdgcn_s_memrealtime() - due) < DLY_K) __builtin_amdgcn_s_sleep(1);
    TSL(1, 3);
    unsigned unused;
    int xa;
    select8<KPP != 0>(rs, tag, lane, sm, ws, bid, unused, [&](unsigned k, auto step) {
      if (step < 2) {
        asm("s_nop 1\n\tv_add_u32_dpp %0, %1, %2 quad_perm:[0,0,0,0] row_mask:0x1 bank_mask:0xf\n\t"
            "v_add_u32_dpp %0, %1, %2 quad_perm:[1,1,1,1] row_mask:0x2 bank_mask:0xf\n\t"
            "v_add_u32_dpp %0, %1, %2 quad_perm:[2,2,2,2] row_mask:0x4 bank_mask:0xf\n\t"
            "v_add_u32_dpp %0, %1, %2 quad_perm:[3,3,3,3] row_mask:0x8 bank_mask:0xf"
            : "=&v"(xa) : "v"(key_addr(step ? k : k & ~2u, key_slot(lane)) - WS_XS), "v"(xo));
        if (step) DRAIN();
        fetch(xa);
        TWF(6);
      } else if (step == 3) {
        TWF(3);
      } else {
        __builtin_amdgcn_s_barrier();
        TSL(1, 9);
      }
    });
    xo = xa;
  } else {
    DRAIN();
    TS(1, 2);
    __syncthreads();
  }
  if (full) {
    if (mine) read_gu(IC<10>{}, IC<8>{}, IC<0>{}, IC<RG>{});
  } else {
    if (mine) read_gu(IC<0>{}, IC<18>{}, IC<0>{}, IC<RG / 2>{});
    read_down();
  }
  f32x4 acc[3];
  _Pragma("unroll 1") for (int spin = 1; spin < SPIN_CAP; spin++) {
    DRAIN();
    __syncthreads();
    TWF(2);
    if (mine) {
      bf8v xf[9];
#pragma unroll
      for (int i = 0; i < 9; i++) xf[i] = *(const LAS bf8v*)(xr + 1024 * i);
      SCHED_FENCE();
      f32x4 a = {0.f, 0.f, 0.f, 0.f};
#pragma unroll
      for (int i = 0; i < 9; i++) {
        const s16x8 lo = __builtin_shufflevector(fa[2 * i][0], fa[2 * i][1], 0, 1, 2, 3, 4, 5, 6, 7);
        const s16x8 hi = __builtin_shufflevector(fa[2 * i + 1][0], fa[2 * i + 1][1], 0, 1, 2, 3, 4, 5, 6, 7);
        a = __builtin_amdgcn_smfmac_f32_16x16x64_bf16(
            xf[i], __builtin_bit_cast(bf16v, __builtin_shufflevector(lo, hi, 0, 1, 2, 3, 4, 5, 6, 7, 8, 9, 10, 11, 12, 13, 14, 15)), a, sidx, 0, 0);
      }
      const float a0 = a[0], a1 = a[1], a2 = a[2], a3 = a[3];
      auto r0 = __builtin_amdgcn_permlane32_swap(__float_as_uint(a0), __float_as_uint(a2), false, false);
      auto r1 = __builtin_amdgcn_permlane32_swap(__float_as_uint(a1), __float_as_uint(a3), false, false);
      const float s0 = __uint_as_float(r0[0]) + __uint_as_float(r0[1]), s1 = __uint_as_float(r1[0]) + __uint_as_float(r1[1]);
      const float gs = __int_as_float(__builtin_amdgcn_update_dpp(__float_as_int(s0), __float_as_int(s1), 0x128, 0xf, 0xc, false));
      const float us = __int_as_float(__builtin_amdgcn_update_dpp(__float_as_int(s1), __float_as_int(s0), 0x128, 0xf, 0x3, false));
      const float gp = rbf(gs);
      const float gq = rbf(gp * __builtin_amdgcn_rcpf(1.0f + __expf(-gp)));
      *(LAS u16*)guw = (u16)pkbf(gq * rbf(us), 0.f);
    } else if (wave >= 8) {
      const u32x4 t = __builtin_elementwise_max(__builtin_elementwise_max(*(const LAS u32x4*)xr, *(const LAS u32x4*)(xr + 1024)), *(const LAS u32x4*)(xr + 2048));
      if (max(max(t[0], t[1]), max(t[2], t[3])) == ~0u) *(LAS int*)(L + FS_SEL + 4) = spin;
    }
    TWF(4);
    __syncthreads();
    TWF(5);
    const bf8v b0 = *(const LAS bf8v*)gur, b1 = *(const LAS bf8v*)(gur + 64);
    const int redo = *(const volatile LAS int*)(L + FS_SEL + 4);
    SCHED_FENCE();
    const f32x4 z4 = {0.f, 0.f, 0.f, 0.f};
#pragma unroll
    for (int t = 0; t < 3; t++) acc[t] = MFMAB(__builtin_bit_cast(bf8v, __builtin_shufflevector(fd[t][0], fd[t][1], 0, 1, 2, 3, 4, 5, 6, 7)), b0, z4);
#pragma unroll
    for (int t = 0; t < 3; t++) acc[t] = MFMAB(__builtin_bit_cast(bf8v, __builtin_shufflevector(fd[t][2], fd[t][3], 0, 1, 2, 3, 4, 5, 6, 7)), b1, acc[t]);
    asm volatile("" : "+v"(acc[0]), "+v"(acc[1]), "+v"(acc[2]));
    SCHED_FENCE();
    if (__builtin_expect(__builtin_amdgcn_readfirstlane(redo) != spin, 1)) break;
    TS(1, 7);
    if (wave == SELW) fetch(xo);
  }
  {
    SCHED_FENCE();
    auto put = [&](f32x4 a, int off) {
      u32x4 o = __builtin_bit_cast(u32x4, a);
      o[0] = min(o[0], 0xfffffffeu);
      stg16(rs, po + off, o);
    };
    if (PM) put(c15 < 8 ? acc[0] : acc[1], 0);
    if (c15 < 8) {
#pragma unroll
      for (int t = 2 * PM; t < 3; t++) put(acc[t], t * 512);
    }
  }
  TWF(7);
  TS(1, 5);
}

__global__ __launch_bounds__(NTHR, 3) void k_all(const u16*, const u16*, const u16*, const u16*, const u16*, const u16*, char*, const u16*,
                                                 const u16*, const u16*, const u16*, const u16*, u16*) {
  __shared__ __attribute__((aligned(16))) char smem[SM_TOTAL];
  LAS char* const L = (LAS char*)smem;
  const int bid = blockIdx.x;
  const size_t argp = (size_t)__builtin_amdgcn_kernarg_segment_ptr();
  u64x8 a8;
  u64x4 a4;
  size_t a1;
  unsigned cpf;
  {
    size_t pc;
    int t;
    asm volatile("s_load_dwordx16 %0, %3, 0x0\n\ts_load_dwordx8 %1, %3, 0x40\n\ts_load_dwordx2 %2, %3, 0x60" : "=&s"(a8), "=&s"(a4), "=&s"(a1) : "s"(argp));
    asm volatile(
        "s_getpc_b64 %1\n\ts_lshr_b32 %2, %3, 3\n\tv_min_u32 %0, 7, %4\n\ts_mul_i32 %2, %2, 8\n\tv_add_lshl_u32 %0, %0, %2, 7\n\t"
        "global_load_dword %0, %0, %1"
        : "=&v"(cpf), "=&s"(pc), "=&s"(t) : "s"(bid), "v"(threadIdx.x) : "scc");
  }
  SCHED_FENCE();
  const int tid = threadIdx.x, lane = tid & 63, b = bid - NFFN;
  unsigned ftag;
  if (b < 0) {
    ftag = stats_role(argp, cpf, L, 0);
  } else {
  const int wave = __builtin_amdgcn_readfirstlane(tid >> 6);
  const bool is_ln = b < NLN;
  const int hl = lane & 31;
  const int ch = (wave & 3) < 3 ? (wave & 3) : (wave >> 2), sub = (wave & 3) < 3 ? (wave >> 2) : 3;
  const int d = 3 * b + ch;
  const int xt = 4 * b + wave, hrow = 8 * ((xt >> 1) & 127) + (XL == 0 ? lane >> (3 + XM) : (XL == 1 ? lane & 7 : (XL == 2 ? (lane & 3) + 4 * (lane >> 5) : (lane & 1) + 2 * (lane >> 4))));
  const int grp = ((xt >> 8) << 5) + ((xt & 1) << 4) + (XL == 0 ? lane & (XM ? 15 : 7) : (XL == 1 ? lane >> 3 : (lane >> (4 - XL)) & 7));
  unsigned xo = hrow * (DM * 4) + grp * 12, po = d * NM * 2 + lane * 4, lo = 2 * d & ~3;
  int cpo = WS_UP + (XB ? ((grp & ~7) + (lane >> 3)) * 2048 + (hrow & ~7) + (lane & 7) : grp * 2048 + hrow) * 16;
  int xbp = (lane & 7) * 32 + (lane >> 3) * 4;
  if (XB) asm volatile("" : "+v"(xbp));
  {
    LAS char* zc = L + (wave >> 2) * CH2 + SM_KC + 16 * (tid & 255);
    const u32x4 z = {0u, 0u, 0u, 0u};
    *(LAS u32x4*)zc = z;
    *(LAS u32x4*)(zc + 4096) = z;
  }
  LAS char* prg = L + SM_PRG + 4 * wave;
  *(LAS int*)prg = sub == 1 ? 8 : (sub == 2 ? 4 : (sub == 3 && !DLG ? 2 : 0));
  *(LAS int*)(L + SM_PRG + 52) = -1;
  asm volatile("" : "+v"(prg));
  const int xg = b & 7, sl = b >> 3, sl12 = sl < 12 ? sl : sl - 12;
  unsigned s0o = (144 * min(lane >> 4, S0M - 1) + 12 * sl12 + wave) * (FF * 2) + xg * 1152 + min(lane & 15, 8) * 128;
  if (S0M) asm volatile("" : "+v"(s0o));
  unsigned dso = 6 * b & ~3;
  asm volatile("" : "+v"(xo), "+v"(po), "+v"(lo), "+v"(cpo), "+v"(dso));
  SCHED_FENCE();
  asm volatile("s_waitcnt lgkmcnt(0)" : "+s"(a8), "+s"(a4), "+s"(a1));
  u32x3 cp[4];
  auto load_x = [&]() {
    if (wave < 4) {
#pragma unroll
      for (int i = 0; i < 4; i++) cp[i] = *(GAS const u32x3u*)(a8[0] + xo + ((i & 1) * (DM * 2) + (i >> 1) * (XM ? DM * 16 : 96)));
    }
    SCHED_FENCE();
  };
  if (XF) load_x();
  const unsigned r_ldt = *(GAS const unsigned*)(a8[5] + lo), r_lar = *(GAS const unsigned*)(a8[1] + po), r_aim = *(GAS const unsigned*)(a8[2] + po);
  const unsigned r_cr = *(GAS const unsigned*)(a8[3] + po), r_ci = *(GAS const unsigned*)(a8[4] + po);
  typedef u32x2 u32x2u __attribute__((aligned(4)));
  u32x2 r_dsk = *(GAS const u32x2u*)(a8[7] + dso);
  char* const ws = (char*)(GAS char*)a8[6];
  const rsrc_t ro = __builtin_amdgcn_make_buffer_rsrc((char*)(GAS char*)a1, 0, SEQ * DM * 2, 0x00020000);
  const rsrc_t rs = __builtin_amdgcn_make_buffer_rsrc(ws, 0, WS_END, 0x00020000);
  const unsigned tagv = __builtin_amdgcn_raw_buffer_load_b32(rs, WS_EPOCH, 0, SC1);
  SCHED_FENCE();
  if (!XF) load_x();
  unsigned s0v = 0;
  if (S0M) {
    __builtin_amdgcn_s_barrier();
    s0v = *(GAS const unsigned*)((sl < 12 ? a4[1] : a4[2]) + s0o);
    SCHED_FENCE();
  }
  TWDECL;
  TWP(4);
  auto prefetch = [&](int i) {
    const char* base = (const char*)(sl < 12 ? a4[1] : a4[2]) + (144 * i + 48 * ch + 4 * sl12) * (FF * 2) + xg * 1152;
    dma4(base + (unsigned)((lane >> 4) * (FF * 2) + min(lane & 15, 8) * 128), (unsigned)(size_t)L + SM_DUMMY);
  };
  const int rb = is_ln ? b : b - (NLN - RED1);
  LAS char *const C = L + ch * TABB, *const K = L + ch * CH2;
  const int c15 = lane & 15, g = lane >> 4;
  unsigned pfo = ((9 * xg + (sl & 7)) * 576 + DR0 * 72 + ((sl >> 3) * 4 + (wave & 3)) * ((8 - DR0) * 6) + min(lane, (8 - DR0) * 6 - 1)) * 128;
  unsigned pfq = (12 * sl12 + 3 * (wave & 3) + min(lane >> 4, 2)) * (FF * 2) + xg * 1152 + min(lane & 15, 8) * 128;
  const int lg = late_group(a4[1], bid);
  const char* pfx = (lane & 31) < 12 ? (const char*)(sl < 12 ? a4[1] : a4[2]) + (unsigned)(((sl12 * 4 + (wave & 3)) * 12 + (lane & 31)) * (FF * 2) + xg * 1152 +
                                                                                            (lg ? lg : (lane >> 5) * 8) * 128)
                                     : (const char*)a4[3] + (unsigned)(((9 * xg + lg) * 576 + (sl * 4 + (wave & 3)) * 6 + min((lane & 31) - 12, 5)) * 128);
  asm volatile("" : "+v"(pfx));
  auto pf4 = [&](int m) {
#pragma unroll
    for (int i = 0; i < 4; i++)
      if (m >> i & 1) dma4s((sl < 12 ? a4[1] : a4[2]) + 144 * i * (FF * 2), pfq, (unsigned)(size_t)L + SM_DUMMY);
    if (m & 16) dma4s(a4[3], pfo, (unsigned)(size_t)L + SM_DUMMY);
    if (m & 32) dma4(pfx, (unsigned)(size_t)L + SM_DUMMY);
  };
  unsigned hs = (sub & 1) ? 0x03020c0cu : 0x01000c0cu, hs2 = (d & 1) ? 0x03020c0cu : 0x01000c0cu;
  LAS char* yb = C + g * 320 + c15 * 16;
  const LAS char *pa, *pa2, *pb, *uf16 = K + SM_US + 2 * UPAD + 32 * c15 + 16 * g;
  LAS char *kd, *kd2;
  unsigned ks0, ks1;
  half8 bA, bB;
  if (sub == 0) {
    pa = C + (((unsigned)(0x33982210cdef4567ull >> (4 * c15)) & 15) + 2 * (c15 & 8)) * TAB_STRIDE + 16 * g;
    pa2 = C + (((unsigned)(0x7763554289ab0123ull >> (4 * c15)) & 15) + 2 * (c15 & 8)) * TAB_STRIDE + 16 * g;
    pb = C + (TR_H + c15) * TAB_STRIDE + 16 * g;
    kd = K + KC_RD + 2 * 544 - 16 * lane;
    kd2 = K + KC_RD + (~c15 & 3) * RDS + 2 * (535 - (c15 | 3) + 8 * g) - 64 * 16;
    uf16 -= 32 * 33;
    bA = band_frag(lane, 0x3u << 12);
    const int s = lane & 3;
    const u32x4 t = {s < 2 ? 0x0c0c0c0cu : (s == 2 ? 0x01000c0cu : 0x01000100u), s == 0 ? 0x01000c0cu : 0x01000100u,
                     s < 2 ? 0x01000100u : (s == 2 ? 0x03020100u : 0x05040302u), (unsigned)(size_t)(K + KC_RD + s * RDS + 64)};
    bB = __builtin_bit_cast(half8, t);
    asm volatile("" : "+v"(bA), "+v"(pa), "+v"(pa2), "+v"(pb), "+v"(kd), "+v"(kd2), "=v"(ks0), "+v"(uf16), "=v"(ks1), "+v"(bB));
  } else if (sub == 1) {
    asm volatile("" : "=v"(bA), "=v"(pa), "=v"(pa2), "=v"(pb), "=v"(kd), "=v"(kd2), "=v"(ks0), "=v"(uf16), "=v"(ks1), "=v"(bB));
  } else if (sub == 2) {
    kd = K + KC_R9 + 190 * ((c15 + 1) & 1) + 2 * (15 - c15 + 4 * g);
    bA = band_frag(lane, 0x1ffu << 4);
    bB = band_frag(lane, 0x7fu << 5);
    asm volatile("" : "+v"(bA), "=v"(pa), "=v"(pa2), "=v"(pb), "+v"(kd), "=v"(kd2), "=v"(ks0), "+v"(uf16), "=v"(ks1), "+v"(bB));
  } else {
    pa = C + (TR_H + 15 - c15) * TAB_STRIDE + 16 * g;
    pa2 = K + KC_R5 + 2 * (158 - 64 - 2 * c15 + 4 * g) - 128;
    pb = C + (TR_M + c15) * TAB_STRIDE + 16 * g;
    kd = K + (c15 < 8 ? ((c15 & 1) ? KC_R5 : KC_R3) + 2 * (80 - 8 * (c15 & 6) + 4 * g) : KC_R9 + 2 * (120 - 8 * c15 + 2 * g));
    kd2 = kd + (c15 < 8 ? 4 : KC_R7 - KC_R9);
    ks0 = c15 < 8 ? 0x03020100u : 0x05040100u;
    ks1 = c15 < 8 ? 0x07060504u : 0x07060302u;
    uf16 -= 16 * c15;
    bA = band_frag(lane, 0x1fu << 6);
    bB = band_frag(lane, 0x7u << 11);
    asm volatile("" : "+v"(bA), "+v"(pa), "+v"(pa2), "+v"(pb), "+v"(kd), "+v"(kd2), "+v"(ks0), "+v"(uf16), "+v"(ks1), "+v"(bB));
  }
  const unsigned pe = sub < 2 ? 0u : 3u << 23;
  f32x2 kh = {-1.44269502f, 0.159154936f}, kl = {-1.92596299e-8f, 7.13048051e-9f}, k1 = {128.f * 0.693147181f, 128.f}, ln2 = {0.693147181f, 128.f};
  const unsigned pi = sub < 2 ? 0x3f800000u : 0xc1000000u;
  u32x2 k0u = {0x3f317218u + pe, pi}, p0u = {0x3f800000u + pe, pi};
  const int vt = tid - 256;
  int uoff = WS_UP + b * 16384 + vt * 16, soff = WS_ST + vt * 16;
  asm volatile("" : "+v"(pfo), "+v"(pfq), "+v"(yb), "+v"(uoff), "+v"(soff));
  asm volatile("s_mov_b32 m0, %8" : "+s"(hs), "+s"(hs2), "+s"(kh), "+s"(kl), "+s"(k0u), "+s"(k1), "+s"(ln2), "+s"(p0u)
               : "s"((unsigned)(size_t)C + (sub & 1) * 256));
  SCHED_FENCE();
  TWP(5);
  SCHED_FENCE();
  const unsigned tag = __builtin_amdgcn_readfirstlane(tagv) + 1u;
  auto half = [&](unsigned v) { return __uint_as_float(__builtin_amdgcn_perm(v, v, hs)); };
  f32x2 f_l = {half(r_lar), __uint_as_float(__builtin_amdgcn_perm(r_ldt, r_ldt, hs2))}, f_c = {half(r_cr), half(r_ci)};
  float f_aim = half(r_aim);
  unsigned f_cm;
  asm volatile("v_max3_f32 %0, |%1|, |%2|, |%3|\n\tv_max_f32 %0, %0, |%4|" : "=&v"(f_cm) : "v"(r_cr), "v"(r_ci), "v"(r_cr << 16), "v"(r_ci << 16));
  asm volatile("" : "+v"(f_l), "+v"(f_aim), "+v"(f_c), "+v"(r_dsk) : "v"(cpf));
  SCHED_FENCE();
  TWP(6);
  SCHED_FENCE();
  if (wave < 4) {
    u32x4 pc[4];
#pragma unroll
    for (int i = 0; i < 12; i++) {
      const int r = i / 6 * 2, k = i % 6 / 2;
      const unsigned t = __builtin_amdgcn_perm(cp[r + 1][k], cp[r][k], (i & 1) ? 0x07060302u : 0x05040100u);
      pc[i / 3][i % 3] = XB ? __builtin_amdgcn_ds_bpermute(xbp, t) : t;
      pc[i / 3][3] = tag;
    }
    if (XB) SCHED_FENCE();
#pragma unroll
    for (int i = 0; i < 4; i++) stg16(rs, cpo + (i >> 1) * (XM ? 64 : 16 * 1024 * 16) + (i & 1) * 16384, pc[i]);
    if (is_ln && tid >= 184) dma16((const char*)a4[0] + (tid - 184) * 16, (unsigned)(size_t)L + SM_WR + (wave - 3) * 1024 + 128);
    pf4(S1M);
  }
  SCHED_FENCE();
  TWA(2);
  TWP(7);
  u32x4 v0, v1, st0, st1;
  const u32x4 rq = {(unsigned)a8[6], (unsigned)(a8[6] >> 32) & 0xffffu, (unsigned)WS_END, 0x00020000u};
  asm volatile("" : "=v"(v0), "=v"(v1), "=v"(st0), "=v"(st1));
  auto ask = [&](u32x4& p, u32x4& q, int off) {
    asm volatile("buffer_load_dwordx4 %0, %2, %3, 0 offen sc1\n\tbuffer_load_dwordx4 %1, %2, %3, %4 offen sc1"
                 : "+v"(p), "+v"(q) : "v"(off), "s"(rq), "s"(8192) : "memory");
  };
  auto polls = [&](int k) {
    if (wave >= 4 && (wave < 8 ? XP47 : XP8) == k) ask(v0, v1, uoff);
    if (wave >= 4 && (wave < 8 ? SP47 : SP8) == k) ask(st0, st1, soff);
    SCHED_FENCE();
  };
  auto get = [&](bool both) {
    _Pragma("unroll 1") for (int spin = 0; spin < SPIN_CAP; spin++) {
      const bool ok = both ? v0[3] == tag && v1[3] == tag : st0[3] == tag && st1[3] == tag;
      if (__builtin_expect(__all(ok), 1)) break;
      __builtin_amdgcn_s_sleep(4);
      if (both) {
        asm volatile("s_waitcnt vmcnt(0)" : "+v"(st0), "+v"(st1));
        if (!ok) ask(v0, v1, uoff);
      }
      if (st0[3] != tag || st1[3] != tag) ask(st0, st1, soff);
      if (both) asm volatile("s_waitcnt vmcnt(0)" : "+v"(v0), "+v"(v1), "+v"(st0), "+v"(st1));
      else asm volatile("s_waitcnt vmcnt(0)" : "+v"(st0), "+v"(st1));
    }
  };
  {
    const f32x2 l2e = -f32x2{kh[0], kh[0]}, th = f_l * l2e;
    const f32x2 tl = __builtin_elementwise_fma(f_l, -f32x2{kl[0], kl[0]}, __builtin_elementwise_fma(f_l, l2e, -th)) * ln2[0];
    const f32x2 e0 = {__builtin_amdgcn_exp2f(th[0]), __builtin_amdgcn_exp2f(th[1])}, ex = __builtin_elementwise_fma(e0, tl, e0);
    const unsigned eb = pkbf(ex[0], ex[1]);
    const float dt = bfhi(eb);
    const f32x2 av = f32x2{bflo(eb), f_aim} * dt;
    const f32x2 hi = av * kh, lo = __builtin_elementwise_fma(av, kl, __builtin_elementwise_fma(av, kh, -hi));
    int se = 385 - (int)(wave_max_u32(f_cm) >> 23) - (int)((unsigned)__builtin_amdgcn_readfirstlane(eb) >> 23);
    se = max(se, 2);
    asm("" : "+s"(se));
    se = min(se, 252);
    const float dts = dt * __uint_as_float((unsigned)(se + 1) << 23);
    cf cc = f_c * dts;
    cf ea = cexp(hi * __builtin_bit_cast(f32x2, p0u), lo * __builtin_bit_cast(f32x2, k0u)), eb2 = cexp(hi * 128.f, lo * k1);
    polls(2);
    asm volatile("s_cmp_lt_u32 %3, 2\n\ts_cbranch_scc0 1f\n\t" TAB_A "s_branch 2f\n1:\n\t" TAB_B "\n2:"
                 : "+{v[100:101]}"(cc), "+{v[102:103]}"(ea), "+{v[104:105]}"(eb2) : "s"(sub) : TAB_CLOB, "scc", "memory");
    if (sub == 2) *(volatile LAS unsigned*)(L + SM_ISC + 4 * ch) = (unsigned)(254 - se) << 23;
  }
  SCHED_FENCE();
  if (S0M) asm volatile("" : : "v"(s0v));
  polls(1);
  TWA(3);
  const f32x4 zero4 = {0.f, 0.f, 0.f, 0.f};
  f32x2 sb[2][3];
  f32x2 isc[3];
  const LAS char *yp, *yp2;
  int zoff, zoff2;
  auto publish = [&](auto parts, int t, int yo, int zo, const f32x2 (&base)[3], auto nap) {
    const volatile LAS int* pp = (const volatile LAS int*)(L + SM_PRG) + min(lane, 11);
    set_prio(WPR);
    _Pragma("unroll 1") for (int spin = 0; spin < SPIN_CAP; spin++) {
      if (__all(*pp > t)) break;
      if (nap) __builtin_amdgcn_s_sleep(PSL);
      else if (PSE) __builtin_amdgcn_s_sleep(PSE);
    }
    set_prio(PPR);
    auto ld = [&](int c, int off) { return *(const volatile LAS f32x2*)((c < 2 ? yp + c * TABB : yp2) + yo + off); };
    f32x2 y[3][3];
#pragma unroll
    for (int c = 0; c < 3; c++) {
      y[c][0] = ld(c, SM_YD);
      if (parts > 1) y[c][1] = ld(c, SM_Y2);
      if (parts > 2) y[c][2] = ld(c, SM_Y3);
    }
    SCHED_FENCE();
    u32x4 o;
#pragma unroll
    for (int c = 0; c < 3; c++) {
      const f32x2 z = __builtin_elementwise_fma(parts > 2 ? (y[c][0] + y[c][1]) + y[c][2] : (parts > 1 ? y[c][0] + y[c][1] : y[c][0]), isc[c], base[c]);
      o[c] = pkbf(z[0], z[1]);
    }
    o[3] = tag;
    stg16(rs, zo, o);
  };
  if (sub == 0) {
    half8 td[17];
    {
      half8 fa[8], fc[8], fb[8];
#pragma unroll
      for (int ks = 0; ks < 4; ks++) { fa[ks] = ld_frag(pa + 64 * ks); fc[ks] = ld_frag(pa2 + 64 * ks); }
      asm volatile("s_waitcnt lgkmcnt(0)" ::: "memory");
      __syncthreads();
      TWK(3);
#pragma unroll
      for (int ks = 0; ks < 4; ks++) fb[ks] = ld_frag(pb + 64 * ks);
      SCHED_FENCE();
#pragma unroll
      for (int ks = 4; ks < 8; ks++) { fa[ks] = ld_frag(pa + 64 * ks); fb[ks] = ld_frag(pb + 64 * ks); fc[ks] = ld_frag(pa2 + 64 * ks); }
      SCHED_FENCE();
      if (S2W == 1) pf4(S2M);
      SCHED_FENCE();
      TWKV(4, "v"(fc[7]));
      f32x4 f = zero4, e = zero4;
#pragma unroll
      for (int ks = 0; ks < 8; ks++) { f = MFMA16(fa[ks], fb[ks], f); e = MFMA16(fc[ks], fb[ks], e); }
      const unsigned d0 = pkh(f[0], f[1]);
      TWKV(5, "v"(d0));
      const unsigned d1 = pkh(f[2], f[3]), d2 = pkh(e[0], e[1]), d3 = pkh(e[2], e[3]);
      const unsigned p0 = dppu<0x138>(d0), p1 = dppu<0x138>(d1);
      *(volatile LAS u32x4*)kd = u32x4{d0, d1, d2, d3};
      const unsigned a1 = __builtin_amdgcn_alignbit(d2, d1, 16), a2 = __builtin_amdgcn_alignbit(d3, d2, 16), a3 = __builtin_amdgcn_alignbit(p0, d3, 16);
      *(volatile LAS u32x4*)(kd + RDS) = u32x4{__builtin_amdgcn_alignbit(d1, d0, 16), a1, a2, a3};
      *(volatile LAS u32x4*)(kd + 2 * RDS) = u32x4{d1, d2, d3, p0};
      *(volatile LAS u32x4*)(kd + 3 * RDS) = u32x4{a1, a2, a3, __builtin_amdgcn_alignbit(p1, p0, 16)};
      if (S2W == 2) pf4(S2M);
      TWA(4);
      const unsigned q0 = dppu<0xFF>(d0), q1 = dppu<0xFF>(d1);
      const u32x4 t = __builtin_bit_cast(u32x4, bB);
      if (lane >= 60)
        *(volatile LAS u32x4*)(size_t)t[3] =
            u32x4{0u, __builtin_amdgcn_perm(q1, q0, t[0]), __builtin_amdgcn_perm(q1, q0, t[1]), __builtin_amdgcn_perm(q1, q0, t[2])};
#pragma unroll
      for (int i = 7; i >= 0; i--) {
        if (i == 7 - TDB) {
          TWA(5);
          asm volatile("s_waitcnt vmcnt(%0)\n\ts_barrier" : : "n"(S2M ? 1 : 0) : "memory");
        }
        if (TDI && i <= 7 - TDB) continue;
        td[i] = ld_td(kd2, i);
        td[i + 8] = ld_td(kd2, i + 8);
      }
      if (!TDI) td[16] = ld_td(kd2, 16);
      if (TDB >= 8) {
        TWA(5);
        asm volatile("s_waitcnt vmcnt(0)\n\ts_barrier" ::: "memory");
      }
    }
    TSX(0, 5);
    TWA(6);
    conv_dense(td, kd2, bA, K + KC_R5 + 64, uf16, yb + SM_YD, prg);
    TWA(7);
  } else {
  asm volatile("s_waitcnt lgkmcnt(0)" ::: "memory");
  __syncthreads();
  polls(0);
  half8 td[8];
  auto mid_td = [&]() {
    const LAS unsigned* t5 = (const LAS unsigned*)pa2;
#pragma unroll
    for (int e = 0; e < 3; e++) {
      td[e] = ld_toep<8>(t5 + 16 * (2 - e));
      td[3 + e] = ld_toep<8>(t5 + (KC_R3 - KC_R5) / 4 + 16 * (2 - e));
    }
  };
  if (wave == 3) {
    half8 fa[24], fb[24];
    f32x4 f[3] = {zero4, zero4, zero4};
    constexpr int PF = PF3;
    TWK(3);
#pragma unroll
    for (int i = 0; i < PF; i++) { fa[i] = ld_frag(pa + (i >> 3) * TABB + 64 * (i & 7)); fb[i] = ld_frag(pb + (i >> 3) * TABB + 64 * (i & 7)); }
    SCHED_FENCE();
    auto put = [&](int c) {
      const unsigned d0 = pkh(f[c][0], f[c][1]), d1 = pkh(f[c][2], f[c][3]);
      const unsigned w0 = __builtin_amdgcn_perm(d1, d0, ks0), w1 = __builtin_amdgcn_perm(d1, d0, ks1);
      *(LAS unsigned*)(kd + c * CH2) = w0;
      *(LAS unsigned*)(kd2 + c * CH2) = w1;
      if (c15 >= 8) {
        *(LAS u16*)(kd + c * CH2 + 190) = (u16)w0;
        *(LAS u16*)(kd + c * CH2 + 192) = (u16)(w0 >> 16);
        *(LAS u16*)(kd + c * CH2 + (KC_R7 - KC_R9) + 190) = (u16)w1;
        *(LAS u16*)(kd + c * CH2 + (KC_R7 - KC_R9) + 192) = (u16)(w1 >> 16);
      }
    };
    static_for<0, 24>([&](auto ic) {
      constexpr int i = decltype(ic)::value, n = i + PF;
      f[i >> 3] = MFMA16(fa[i], fb[i], f[i >> 3]);
      SCHED_FENCE();
      if constexpr (n < 24) { fa[n] = ld_frag(pa + (n >> 3) * TABB + 64 * (n & 7)); fb[n] = ld_frag(pb + (n >> 3) * TABB + 64 * (n & 7)); }
      if constexpr (i == 14) {
        put(0);
        asm volatile("" ::: "memory");
        mid_td();
      }
      if constexpr (i == 18) put(1);
      if constexpr (i == (S2W == 1 ? 0 : 10)) pf4(S2M);
      SCHED_FENCE();
    });
    TWK(5);
    put(2);
    __builtin_amdgcn_s_waitcnt(S2M ? 0x0F71 : 0x0F70);
  }
  TWA(4);
  SCHED_FENCE();
  if (wave >= 4) {
    const u32x4 iscv = *(const LAS u32x4*)(L + SM_ISC);
    yp = L + ((wave >> 1) - 2) * YT + ((vt >> 1) & 3) * 320 + ((vt >> 3) & 15) * 16 + (vt & 1) * 8;
    yp2 = yp + 2 * TABB;
    zoff = WS_ZP + ((vt >> 3) * NCONV + b) * 128 + (vt & 7) * 16;
    zoff2 = zoff + 64 * NCONV * 128;
    asm volatile("" : "+v"(yp), "+v"(yp2), "+v"(zoff), "+v"(zoff2));
    f32x2 dsk[3], xf[2][3];
    LAS char* dst = L + SM_US + 2 * UPAD + 4 * vt;
#pragma unroll
    for (int c = 0; c < 3; c++) {
      dsk[c][0] = __uint_as_float(__builtin_amdgcn_perm(r_dsk[1], r_dsk[0], 0x01000c0cu + 0x02020000u * (c + (b & 1))));
      isc[c] = f32x2{__uint_as_float(iscv[c]), __uint_as_float(iscv[c])};
      asm volatile("" : "+v"(dsk[c]), "+v"(isc[c]), "+v"(dst));
    }
    asm volatile("s_waitcnt vmcnt(2)" : "+v"(v0), "+v"(v1));
    get(true);
#pragma unroll
    for (int c = 0; c < 3; c++) {
      xf[0][c] = unpk(v0[c]);
      xf[1][c] = unpk(v1[c]);
      asm volatile("" : "+v"(xf[0][c]), "+v"(xf[1][c]));
    }
    SCHED_FENCE();
    asm volatile("s_waitcnt vmcnt(0)" : "+v"(st0), "+v"(st1));
    get(false);
    TWA(5);
    f32x2 uf[2][3];
#pragma unroll
    for (int i = 0; i < 2; i++) {
      const u32x4& st = i ? st1 : st0;
      const f32x2 ri = {__uint_as_float(st[0]), __uint_as_float(st[1])};
      const f32x2 cc = -unpk(st[2]) * ri;
#pragma unroll
      for (int c = 0; c < 3; c++) {
        uf[i][c] = __builtin_elementwise_fma(xf[i][c], ri, cc);
        *(LAS unsigned*)(dst + i * 2048 + c * CH2) = pkc(uf[i][c]);
      }
    }
    SCHED_FENCE();
#pragma unroll
    for (int i = 0; i < 2; i++)
#pragma unroll
      for (int c = 0; c < 3; c++) {
        sb[i][c] = __builtin_elementwise_fma(uf[i][c], f32x2{dsk[c][0], dsk[c][0]}, xf[i][c]);
        asm volatile("" : "+v"(sb[i][c]));
      }
    if (is_ln && (vt >> 3) == (b & 63)) *(LAS unsigned*)(L + SM_MX + (vt & 7) * 4) = b < 64 ? st0[2] : st1[2];
  } else {
    TWA(5);
  }
  LAS char* const sbp = L + SM_SB + (wave >> 3) * 1536 + lane * 8;
  auto publish_low = [&](auto parts) {
    const int t = (wave >> 1) - 2;
    publish(parts, t, 0, zoff, sb[0], std::true_type{});
    if (DLG && (wave & 3) == 1) {
      const f32x2 other[3] = {*(const volatile LAS f32x2*)sbp, *(const volatile LAS f32x2*)(sbp + 512), *(const volatile LAS f32x2*)(sbp + 1024)};
      publish(parts, t + 1, YT, zoff + 16 * NCONV * 128, other, std::true_type{});
    }
  };
  if (sub == 1) __builtin_amdgcn_s_setprio(SPR);
  else if (sub == 2) __builtin_amdgcn_s_setprio(FPR);
  else __builtin_amdgcn_s_setprio(MPR & 3);
  __syncthreads();
  TWA(6);
  {
    if (sub == 1) {
      unsigned long long t0 = __builtin_amdgcn_s_memrealtime();
      if (wave == 4 && lane < 8) __builtin_amdgcn_raw_buffer_store_b64(u32x2{(unsigned)t0, tag}, rs, WS_CK + lane * 2048 + b * 8, 0, SC1);
      auto pf3 = [&](int m) {
        if (m & 16)
          dma4s(a4[3], ((9 * xg + (sl & 7)) * 576 + DR0 * 72 + ((sl >> 3) * 3 + ch) * ((8 - DR0) * 8) + min(lane, (8 - DR0) * 8 - 1)) * 128,
                (unsigned)(size_t)L + SM_DUMMY);
        if (m & 32) {
          const char* pg = (const char*)(sl < 12 ? a4[1] : a4[2]) + (unsigned)(((sl12 * 3 + ch) * 8 + (lane & 7)) * (FF * 2) + xg * 1152 + 1024);
          const char* pd = (const char*)a4[3] + (unsigned)(((9 * xg + 8) * 576 + (sl * 3 + ch) * 8 + (lane & 7)) * 128);
          dma4((lane & 8) ? pd : pg, (unsigned)(size_t)L + SM_DUMMY);
        }
#pragma unroll
        for (int i = 0; i < 4; i++)
          if (m >> i & 1) prefetch(i);
      };
      if (!SENT1) pf3(S3M);
      if (is_ln) {
        const u32x4 ones = {~0u, ~0u, ~0u, ~0u};
#pragma unroll
        for (int i = 0; i < 3; i++) stg16(rs, WS_PART + b * 11520 + vt * 16 + i * 3072, ones);
        if (vt < 144) stg16(rs, WS_PART + b * 11520 + vt * 16 + 9216, ones);
#pragma unroll
        for (int i = 0; i < 2 * XSS; i += 3072)
          if (i + 3072 <= 2 * XSS || vt * 16 < 2 * XSS - i) stg16(rs, WS_XS + b * (2 * XSS) + vt * 16 + i, ones);
        if (b < 8 * XSS / 3072) stg16(rs, WS_XS + 2 * NLN * XSS + b * 3072 + vt * 16, ones);
      }
      if (SENT1) pf3(S3M);
      if (S4M) {
        while ((unsigned)__builtin_amdgcn_s_memrealtime() - t0 < S4D) __builtin_amdgcn_s_sleep(1);
        pf3(S4M);
      }
      publish_low(IC<1>{});
      if (wave == 4) {
        t0 += last_start(rs, tag, lane, bid, (unsigned)t0);
        *(volatile LAS unsigned long long*)(L + SM_PRG + 48) = t0 + (DLY_H - (b >= 96 ? 0 : (b >= 80 || (!DLG && (b & 24) == 24) ? HE5 : (b >= 64 ? HE4 : HE3))));
        *(LAS int*)(L + FS_SEL) = (int)t0;
      }
    } else if (sub == 3) {
      if (wave != 3) {
#pragma unroll
        for (int c = 0; c < 3; c++)
          if (DLG) *(volatile LAS f32x2*)(sbp + 512 * c) = sb[0][c];
        mid_td();
      }
      if (DLG) *(volatile LAS int*)prg = 2;
      auto own_low = [&]() {
        if (wave == 7) publish_low(IC<1>{});
        if (wave == 11) publish_low(IC<2>{});
      };
      f32x4 a2[6], w[4];
#pragma unroll
      for (int i = 0; i < 6; i++) a2[i] = zero4;
      half8 uw[12];
#pragma unroll
      for (int jj = 0; jj < 6; jj++) uw[jj] = ld_frag(uf16 - 48 + 256 * jj);
      SCHED_FENCE();
      w[0] = MFMA16(uw[0], bA, zero4); w[1] = MFMA16(uw[1], bA, zero4);
      w[2] = MFMA16(uw[0], bB, zero4); w[3] = MFMA16(uw[1], bB, zero4);
      half8 X5 = pack_win(w[0], w[1]), X3 = pack_win(w[2], w[3]);
      SCHED_FENCE();
#pragma unroll
      for (int jj = 0; jj < 6; jj++) {
        if (jj < 5) {
          w[0] = MFMA16(uw[2 * jj + 2], bA, zero4);
          if (jj < 3) uw[2 * jj + 6] = ld_frag(uf16 - 48 + 256 * (2 * jj + 6));
          SCHED_FENCE();
          w[1] = MFMA16(uw[2 * jj + 3], bA, zero4);
          if (jj < 3) uw[2 * jj + 7] = ld_frag(uf16 - 48 + 256 * (2 * jj + 7));
          SCHED_FENCE();
          w[2] = MFMA16(uw[2 * jj + 2], bB, zero4);
        }
        if (jj > 0) *(volatile LAS f32x4*)(yb + SM_Y2 + (1 + jj) * YT) = a2[jj - 1];
        SCHED_FENCE();
        if (jj < 5) w[3] = MFMA16(uw[2 * jj + 3], bB, zero4);
        if (jj > 0) *(volatile LAS int*)prg = 2 + jj;
        if (jj > 0 && jj < 5 && (MPR >> 4 * jj & 3) != (MPR >> 4 * (jj - 1) & 3)) set_prio(MPR >> 4 * jj & 3);
        SCHED_FENCE();
        u32x4 n5, n3;
#pragma unroll
        for (int q = 0; q < 6; q++) {
          const int e = q >> 1;
          if (jj + e < 6) a2[jj + e] = MFMA16((q & 1) ? X3 : X5, td[3 * (q & 1) + e], a2[jj + e]);
          if (jj < 5 && q < 4) {
            u32x4& nn = q < 2 ? n5 : n3;
            nn[2 * (q & 1)] = pkh(w[q][0], w[q][1]);
            nn[2 * (q & 1) + 1] = pkh(w[q][2], w[q][3]);
          }
          SCHED_FENCE();
        }
        if (jj < 5) {
          X5 = __builtin_bit_cast(half8, n5);
          X3 = __builtin_bit_cast(half8, n3);
        }
        if (!DLG && MJP == jj) {
          own_low();
          set_prio(MPR >> 4 * jj & 3);
        }
      }
      *(volatile LAS f32x4*)(yb + SM_Y2 + 7 * YT) = a2[5];
      *(volatile LAS int*)prg = 8;
      if (!DLG && MJP == 6) own_low();
    } else {
      if (FSL) __builtin_amdgcn_s_sleep(FSL);
      half8 uw[8];
#pragma unroll
      for (int i = 0; i < 4; i++) {
        uw[2 * i] = ld_frag(uf16 + 512 * i - 48);
        uw[2 * i + 1] = ld_frag(uf16 + 512 * i - 32);
      }
      SCHED_FENCE();
      f32x4 w[8], a3[4];
#pragma unroll
      for (int e = 0; e < 4; e++) {
        const LAS unsigned* t9 = opaque_lds(kd);
        td[e] = ld_toep<8>(t9 + 8 * (3 - e));
        td[4 + e] = ld_toep<8>(t9 + (KC_R7 - KC_R9) / 4 + 8 * (3 - e));
      }
      if (FP2 != FPR) {
        asm volatile("s_waitcnt lgkmcnt(0)" ::: "memory");
        SCHED_FENCE();
        set_prio(FP2);
      }
#pragma unroll
      for (int i = 0; i < 4; i++) {
        w[2 * i] = MFMA16(uw[2 * i], bA, zero4);
        w[2 * i + 1] = MFMA16(uw[2 * i + 1], bB, zero4);
      }
      if (FB) {
        SCHED_FENCE();
        set_prio(FP3);
        SCHED_FENCE();
      }
      half8 X90 = pack_win(w[0], w[2]), X70 = pack_win(w[1], w[3]), X91 = pack_win(w[4], w[6]), X71 = pack_win(w[5], w[7]);
      if (FB) {
        asm volatile("" : "+v"(X90), "+v"(X70), "+v"(X91), "+v"(X71));
        SCHED_FENCE();
        set_prio(FP2);
        SCHED_FENCE();
      }
#pragma unroll
      for (int t = 3; t >= 0; t--) a3[t] = MFMA16(X90, td[t], zero4);
#pragma unroll
      for (int t = 3; t >= 2; t--) a3[t] = MFMA16(X91, td[t - 2], a3[t]);
#pragma unroll
      for (int t = 0; t < 4; t++) a3[t] = MFMA16(X70, td[4 + t], a3[t]);
#pragma unroll
      for (int t = 2; t < 4; t++) a3[t] = MFMA16(X71, td[4 + t - 2], a3[t]);
      if (FB) {
        SCHED_FENCE();
        set_prio(FP3);
        SCHED_FENCE();
      }
#pragma unroll
      for (int t = 0; t < 4; t++) *(volatile LAS f32x4*)(yb + SM_Y3 + (4 + t) * YT) = a3[t];
      *(volatile LAS int*)prg = 8;
      publish_low(IC<2>{});
    }
  }
  TWA(7);
  }
  u32x4 h0, h1;
  int h_asked = b / NLN, hoff = WS_ZP + b * (NCONV * 128) + (tid >> 3) * 256 + (tid & 7) * 16;
  asm volatile("" : "={v[6:9]}"(h0), "={v[2:5]}"(h1), "+v"(hoff));
  auto ask_h = [&](int tries, int mark) {
    unsigned long long due, now;
    asm volatile(
        "s_cmp_lg_u32 %2, 0\n\ts_cbranch_scc1 3f\n1:\n\tds_read_b64 %4, %6\n\ts_memrealtime %5\n\ts_waitcnt lgkmcnt(0)\n\t"
        "v_cmp_ge_u64 vcc, %5, %4\n\ts_cbranch_vccnz 2f\n\t"
        "s_sub_u32 %3, %3, 1\n\ts_cbranch_scc1 3f\n\ts_sleep 1\n\ts_cmp_lg_u32 %3, 0\n\ts_cbranch_scc1 1b\n2:\n\t"
        "buffer_load_dwordx4 %0, %7, %8, 0 offen sc1\n\tbuffer_load_dwordx4 %1, %7, %8, 0 offen offset:128 sc1\n\ts_mov_b32 %2, %9\n3:"
        : "+{v[6:9]}"(h0), "+{v[2:5]}"(h1), "+s"(h_asked), "+s"(tries), "=&v"(due), "=&s"(now)
        : "v"((unsigned)(size_t)L + SM_PRG + 48), "v"(hoff), "s"(rq), "s"(mark) : "vcc", "scc", "memory");
  };
  if (HEA) ask_h(0, wave | 1);
  if (wave >= 4) publish(IC<3>{}, (wave >> 1) + 2, 4 * YT, zoff2, sb[1], wave < 8);
  __builtin_amdgcn_s_setprio(0);
  ask_h(0, 1);
  TS(0, 6);
  TWB(2);
  TS(0, 7);
  TWB(3);
  TWFLUSH();
  ftag = tag;
  if (b < LATE0) {
  LAS unsigned* const selp = (LAS unsigned*)(L + SM_ROW + 256);
  unsigned t_key = 0;
  auto select_rows = [&]() {
    const SelMasks sm(lane);
    const unsigned due = __builtin_amdgcn_readfirstlane(*(const LAS int*)(L + FS_SEL));
    while ((int)((unsigned)__builtin_amdgcn_s_memrealtime() - due) < DLY_L) __builtin_amdgcn_s_sleep(1);
    unsigned long long t_ask = 0;
    select8<false>(rs, tag, lane, sm, ws, bid, t_key, [&](unsigned k, auto step) {
      if (step == 2) selp[key_slot(lane)] = k;
    });
    TSW(10, 3);
  };
  if (is_ln) {
    f32x2 wf[6], hv[6];
    const unsigned mxp = *(const LAS unsigned*)(L + SM_MX + (tid & 7) * 4);
#pragma unroll
    for (int i = 0; i < 6; i++) {
      const float t = bflo(*(const LAS u16*)(L + SM_WR + (tid >> 3) * 12 + i * 2));
      wf[i] = f32x2{t, t};
    }
    float wsum = 0.f, mxr = bflo(*(const LAS u16*)(L + SM_MX + (lane >> 2) * 2));
    unsigned one2 = 0x3f803f80u, sel0 = 0x00003f80u, sel1 = 0x3f800000u;
    asm volatile("" : "+s"(one2), "+s"(sel0), "+s"(sel1));
    if (wave == 0) {
#pragma unroll
      for (int k = 0; k < 9; k++) wsum = dot2(*(const LAS unsigned*)(L + SM_WR + (hl + 32 * k) * 4), one2, wsum);
      wsum = red32_sum(wsum);
      __builtin_amdgcn_s_setprio(2);
    }
#pragma unroll
    for (int i = 0; i < 6; i++) {
      hv[i] = -unpk(mxp);
      asm volatile("" : "+v"(hv[i]), "+v"(wf[i]));
    }
    LAS char* redw = L + SM_RED + wave * 256 + (wave >= 6 ? 128 : 0) + (lane & 7) * 32 + (lane >> 5) * 16 + ((lane >> 4) & 1) * 8;
    const LAS char* redr = L + SM_RED + ((lane >> 1) & 1) * (6 * 256 + 128) + (lane >> 2) * 16 + (lane & 1) * 8;
    unsigned kidx = (unsigned)(SEQ - 1 - 16 * b - (lane >> 2)) << 2;
    int kro = WS_KR + (lane & 7) * 2048 + b * 16, xso = WS_XS + b * (2 * XSS) + (tid >> 3) * 12;
    asm volatile("" : "+v"(wsum), "+v"(mxr), "+v"(redw), "+v"(redr), "+v"(kidx), "+v"(kro), "+v"(xso));
    ask_h(SPIN_CAP, 1);
    TWB(4);
    {
      int n = SPIN_CAP;
      unsigned long long m;
      asm volatile(
          "s_cmp_gt_u32 %4, 3\n\ts_waitcnt vmcnt(1)\n\ts_cbranch_scc1 2f\n1:\n\ts_waitcnt vmcnt(0)\n2:\n\t"
          "v_cmp_ne_u32 vcc, %5, v9\n\tv_cmp_ne_u32 %3, %5, v5\n\ts_or_b64 vcc, vcc, %3\n\ts_cbranch_scc0 3f\n\t"
          "s_sleep 4\n\ts_mov_b64 exec, vcc\n\tbuffer_load_dwordx4 %0, %6, %7, 0 offen sc1\n\t"
          "buffer_load_dwordx4 %1, %6, %7, 0 offen offset:128 sc1\n\ts_mov_b64 exec, -1\n\ts_sub_u32 %2, %2, 1\n\ts_cbranch_scc0 1b\n\t"
          "s_waitcnt vmcnt(0)\n3:"
          : "+{v[6:9]}"(h0), "+{v[2:5]}"(h1), "+s"(n), "=&s"(m) : "s"(h_asked), "s"(tag), "v"(hoff), "s"(rq) : "vcc", "scc", "memory");
      v0 = h0;
      v1 = h1;
    }
    TS(0, 8);
    TWB(5);
    {
      f32x2 ss, qq, pp;
#pragma unroll
      for (int i = 0; i < 6; i++) {
        const unsigned t = i < 3 ? v0[i] : v1[i - 3];
        hv[i] = f32x2{dot2(t, sel0, hv[i][0]), dot2(t, sel1, hv[i][1])};
        ss = i ? ss + hv[i] : hv[i];
        qq = i ? __builtin_elementwise_fma(hv[i], hv[i], qq) : hv[i] * hv[i];
        pp = i ? __builtin_elementwise_fma(hv[i], wf[i], pp) : hv[i] * wf[i];
      }
      float s0 = ss[0], s1 = ss[1], q0 = qq[0], q1 = qq[1], p0 = pp[0], p1 = pp[1];
      asm volatile(
          "s_nop 1\n\tv_permlane32_swap_b32 %0, %1\n\tv_permlane32_swap_b32 %2, %3\n\tv_permlane32_swap_b32 %4, %5\n\t"
          "v_add_f32 %0, %0, %1\n\tv_add_f32 %2, %2, %3\n\tv_add_f32 %4, %4, %5\n\ts_nop 0\n\t"
          "v_permlane16_swap_b32 %0, %2\n\tv_add_f32_dpp %1, %4, %4 row_ror:8 row_mask:0xf bank_mask:0xf\n\t"
          "v_add_f32 %2, %0, %2\n\ts_nop 1\n\tv_add_f32_dpp %0, %2, %2 row_ror:8 row_mask:0xf bank_mask:0xf"
          : "+v"(s0), "+v"(s1), "+v"(q0), "+v"(q1), "+v"(p0), "+v"(p1));
      *(LAS f32x2*)redw = f32x2{s0, s1};
    }
    TWB(6);
    __syncthreads();
    if (wave == 0) {
      const unsigned now = (unsigned)__builtin_amdgcn_s_memrealtime();
      const volatile LAS f32x2* rp = (const volatile LAS f32x2*)redr;
      const f32x2 t0 = rp[0], t1 = rp[32], t2 = rp[64], t3 = rp[96], t4 = rp[128], t5 = rp[160];
      const f32x2 t = ((t0 + t1) + (t2 + t3)) + (t4 + t5);
      float a = t[0], p = t[1];
      asm volatile("" : "+v"(a), "+v"(p));
      a += dppf<0x4E>(a);
      p += dppf<0x4E>(p);
      p += dppf<0xB1>(p);
      const float mv = dppf<0x00>(a) * (1.0f / DM);
      const float rinv = ln_rinv(dppf<0x55>(a), mv);
      const float dl = rbf(mxr + mv) - mxr;
      const unsigned rb = pkbf(0.f, fmaf(-dl, wsum, p) * rinv);
      const unsigned key = ((rb ^ (unsigned)(((int)rb >> 31) | (int)0x80000000)) & 0xffff0000u) | kidx;
      unsigned k1 = max(key, dppu<0x141>(key)), k2 = min(key, dppu<0x141>(key)), k3;
      {
        const unsigned lo = min(k1, dppu<0x140>(k1)), hi = max(k2, dppu<0x140>(k2));
        k1 = max(k1, dppu<0x140>(k1));
        k2 = max(lo, hi);
        k3 = min(lo, hi);
      }
      {
        const unsigned lo = min(k1, dppu<0x142>(k1)), hi = max(k2, dppu<0x142>(k2)), w = max(k3, dppu<0x142>(k3));
        k1 = max(k1, dppu<0x142>(k1));
        k2 = max(lo, hi);
        k3 = max(min(lo, hi), w);
      }
      {
        const unsigned lo = min(k1, dppu<0x143>(k1)), hi = max(k2, dppu<0x143>(k2)), w = max(k3, dppu<0x143>(k3));
        k1 = max(k1, dppu<0x143>(k1));
        k2 = max(lo, hi);
        k3 = max(min(lo, hi), w);
      }
      if (lane >= 56) stg16(rs, kro, u32x4{k1, k2 | 1u, __builtin_amdgcn_perm(k3, now, 0x07060100u), tag});
      SCHED_FENCE();
      const unsigned second = (unsigned)__builtin_amdgcn_readlane((int)k2, 63);
      if ((lane & 3) == 0) {
        LAS float* ri = (LAS float*)(L + SM_ROW + (lane >> 3) * 16 + (lane & 4));
        ri[0] = -dl * rinv;
        ri[2] = rinv;
        const unsigned fk = key | (key > second ? 0u : (key == second ? 1u : 2u));
        *(LAS unsigned*)(L + SM_ROW + 128 + lane) = fk;
        __builtin_amdgcn_raw_buffer_store_b64(u32x2{fk, tag}, rs, WS_KF + b * 128 + lane * 2, 0, SC1);
      }
      __builtin_amdgcn_s_setprio(0);
    }
    TS(0, 9);
    TWB(7);
    __syncthreads();
    {
      const f32x4 ri = *(const LAS f32x4*)(L + SM_ROW + (tid & 7) * 16);
      const u32x2 kk = *(const LAS u32x2*)(L + SM_ROW + 128 + (tid & 7) * 8);
      const f32x2 c2 = {ri[0], ri[1]}, r2 = {ri[2], ri[3]};
      unsigned wp[6];
#pragma unroll
      for (int i = 0; i < 6; i++) {
        const f32x2 w2 = __builtin_elementwise_fma(hv[i], r2, c2);
        wp[i] = pkbf(w2[0], w2[1]);
      }
#pragma unroll
      for (int i = 0; i < 2; i++)
        if (!(kk[i] & 2)) {
          u32x3 o;
#pragma unroll
          for (int k = 0; k < 3; k++) o[k] = min(__builtin_amdgcn_perm(wp[2 * k + 1], wp[2 * k], i ? 0x07060302u : 0x05040100u), 0xfffffffeu);
#pragma unroll
          for (int c = 1; c <= NC; c++) __builtin_amdgcn_raw_buffer_store_b96(o, rs, xso + (int)(kk[i] & 1) * XSS + c * (DM * 2), 0, SC1);
        }
      SCHED_FENCE();
      LAS char* dst = L + SM_T2 + (tid & 7) * T2S + (tid >> 3) * 48;
#pragma unroll
      for (int i = 0; i < 6; i += 2) *(LAS u32x4*)(dst + 8 * i) = u32x4{i < 3 ? v0[i] : v1[i - 3], wp[i], i < 2 ? v0[i + 1] : v1[i - 2], wp[i + 1]};
    }
    __syncthreads();
    const int pr = tid / 72, ck = tid - pr * 72;
    u32x4 hq[2], wq[2];
    u32x2 key = {2u, 2u};
    auto put_row = [&](int i, int slot) {
      const int o = key_addr(key[i], slot) + ck * 16;
      const u32x4 cap = {0xfffffffeu, 0xfffffffeu, 0xfffffffeu, 0xfffffffeu};
      if (key[i] & 2) {
#pragma unroll
        for (int c = 1; c <= NC; c++) stg16(rs, o + c * (DM * 2), __builtin_elementwise_min(wq[i], cap));
      }
      stg16(rs, o, __builtin_elementwise_min(hq[i], cap));
    };
    auto put_out = [&](int i) {
      u32x4 o;
#pragma unroll
      for (int k = 0; k < 4; k++) {
        const f32x2 o2 = unpk(hq[i][k]) + unpk(wq[i][k]);
        o[k] = pkbf(o2[0], o2[1]);
      }
      __builtin_amdgcn_raw_buffer_store_b128(o, ro, (16 * b + 2 * pr + i) * (DM * 2) + ck * 16, 0, OAUX);
    };
    if (wave < 9) {
      key = *(const LAS u32x2*)(L + SM_ROW + 128 + pr * 8);
#pragma unroll
      for (int k = 0; k < 4; k++) {
        const u32x4 t = *(const LAS u32x4*)(L + SM_T2 + pr * T2S + ck * 64 + k * 16);
        hq[0][k] = __builtin_amdgcn_perm(t[2], t[0], 0x05040100u);
        hq[1][k] = __builtin_amdgcn_perm(t[2], t[0], 0x07060302u);
        wq[0][k] = __builtin_amdgcn_perm(t[3], t[1], 0x05040100u);
        wq[1][k] = __builtin_amdgcn_perm(t[3], t[1], 0x07060302u);
      }
#pragma unroll
      for (int i = 0; i < 2; i++) {
        if (!(key[i] & 2)) put_row(i, 0);
        else if (EO) put_out(i);
      }
    } else if (wave == 10) {
      select_rows();
    }
    __syncthreads();
    TS(0, 10);
    if (wave < 9) {
#pragma unroll
      for (int i = 0; i < 2; i++) {
        int slot = 8;
#pragma unroll
        for (int r = 0; r < 8; r++) slot = selp[r] == key[i] ? r : slot;
        if (slot < 8) {
          if (key[i] & 2) {
            DRAIN();
            put_row(i, slot);
          }
        } else if (!EO || !(key[i] & 2)) {
          put_out(i);
        }
      }
    }
  } else {
    __syncthreads();
    if (wave == 10) select_rows();
  }
  if (is_ln && b >= RED1) {
    TS(0, 12);
    TC(0, 15);
    __builtin_amdgcn_endpgm();
  }
  {
    const int co = 8 * rb + 4 * (lane & 1);
    unsigned hv = 0u, wv = 0u, oo = 0u;
    float sg = 0.f;
    LAS float* red = (LAS float*)(L + SM_RED);
    LAS char* rw = (LAS char*)red + (wave < 10 ? wave * 128 + (lane & 7) * 16 + (lane >> 4) * 4 : lane * 8);
    int part_off = WS_PART + (tid >> 3) * (8 * DM * 4) + rb * 128 + (tid & 7) * 16;
    asm volatile("" : "+v"(rw), "+v"(part_off));
    f32x2 hf = {0.f, 0.f}, wf = {0.f, 0.f};
    int o = 0;
    auto get_rows = [&]() {
      asm volatile("buffer_load_dword %0, %2, %3, 0 offen offset:1152 sc1\n\tbuffer_load_dword %1, %2, %3, 0 offen sc1"
                   : "+v"(wv), "+v"(hv) : "v"(o), "s"(rq) : "memory");
    };
    if (wave == 10) {
      const int slot = (lane >> 1) & 7;
      const unsigned key = *(volatile LAS unsigned*)(selp + slot);
      o = key_addr(key, slot) + co;
      get_rows();
      const unsigned sb = key ^ ((int)key < 0 ? 0x80000000u : 0xffffffffu);
      sg = rbf(__builtin_amdgcn_rcpf(1.0f + __expf(-bfhi(sb))));
      oo = (unsigned)(key_row(key) * (DM * 2) + co);
      asm volatile("" : "+v"(sg), "+v"(oo));
      while ((int)((unsigned)__builtin_amdgcn_s_memrealtime() - t_key) < DLY_C) __builtin_amdgcn_s_sleep(1);
    }
    __syncthreads();
    TS(0, 11);
    if (wave < 10) {
      u32x4 v, w;
      asm volatile("buffer_load_dwordx4 %0, %2, %3, 0 offen sc1\n\ts_sleep %4\n\tbuffer_load_dwordx4 %1, %2, %3, 0 offen sc1"
                   : "=&v"(v), "=&v"(w) : "v"(part_off), "s"(rq), "n"(PPS) : "memory");
      u32x4 got = {~0u, 0u, 0u, 0u};
      _Pragma("unroll 1") for (int spin = 0; spin < SPIN_CAP; spin++) {
        asm volatile("s_waitcnt vmcnt(1)" : "+v"(v));
        if (got[0] == ~0u) got = v;
        if (__all(got[0] != ~0u)) break;
        if (got[0] == ~0u) asm volatile("buffer_load_dwordx4 %0, %1, %2, 0 offen sc1" : "+v"(v) : "v"(part_off), "s"(rq) : "memory");
        asm volatile("s_waitcnt vmcnt(1)" : "+v"(w));
        if (got[0] == ~0u) got = w;
        if (__all(got[0] != ~0u)) break;
        if (got[0] == ~0u) asm volatile("buffer_load_dwordx4 %0, %1, %2, 0 offen sc1" : "+v"(w) : "v"(part_off), "s"(rq) : "memory");
      }
#define v got
      auto r0 = __builtin_amdgcn_permlane32_swap(v[0], v[2], false, false);
      auto r1 = __builtin_amdgcn_permlane32_swap(v[1], v[3], false, false);
      const float s0 = __uint_as_float(r0[0]) + __uint_as_float(r0[1]), s1 = __uint_as_float(r1[0]) + __uint_as_float(r1[1]);
      auto r2 = __builtin_amdgcn_permlane16_swap(__float_as_uint(s0), __float_as_uint(s1), false, false);
      float a = __uint_as_float(r2[0]) + __uint_as_float(r2[1]);
      a += dppf<0x128>(a);
      *(LAS float*)rw = a;
#undef v
      asm volatile("" : : "v"(v), "v"(w));
    } else if (wave == 10) {
      _Pragma("unroll 1") for (int spin = 0; spin < SPIN_CAP; spin++) {
        asm volatile("s_waitcnt vmcnt(0)" : "+v"(wv), "+v"(hv));
        if (__all(wv != ~0u && hv != ~0u)) break;
        __builtin_amdgcn_s_sleep(4);
        get_rows();
      }
      asm volatile("s_waitcnt vmcnt(0)" : "+v"(wv), "+v"(hv));
      hf = unpk(hv);
      wf = unpk(wv);
      asm volatile("" : "+v"(hf), "+v"(wf));
    }
    __syncthreads();
    if (wave == 10 && lane < 16) {
      f32x2 p[10];
#pragma unroll
      for (int w = 0; w < 10; w++) p[w] = *(const volatile LAS f32x2*)(rw + w * 128);
      const f32x2 y = (((p[0] + p[1]) + (p[2] + p[3])) + (p[8] + p[9])) + ((p[4] + p[5]) + (p[6] + p[7]));
      const f32x2 y2 = rbf2(rbf2(y) * sg);
      const f32x2 o2 = hf + rbf2(wf + y2);
      __builtin_amdgcn_raw_buffer_store_b32(pkbf(o2[0], o2[1]), ro, oo, 0, OAUX);
      if (b == 0 && lane == 0) __builtin_amdgcn_raw_buffer_store_b32(tag, rs, WS_EPOCH, 0, SC1);
    }
  }
  TS(0, 12);
  TC(0, 15);
  asm volatile("s_endpgm" ::: "memory");
  __builtin_amdgcn_s_waitcnt(0);
  }
  }
  ffn_role(argp, ftag, L);
}

}

extern "C" void kernel_launch(void* const* d_in, const int* in_sizes, int n_in, void* d_out, int out_size,
                              void* d_ws, size_t ws_size, hipStream_t stream) {
  k_all<<<NCONV + NFFN, NTHR, 0, stream>>>((const u16*)d_in[0], (const u16*)d_in[1], (const u16*)d_in[2], (const u16*)d_in[3],
                                             (const u16*)d_in[4], (const u16*)d_in[5], (char*)d_ws, (const u16*)d_in[6],
                                             (const u16*)d_in[8], (const u16*)d_in[9], (const u16*)d_in[10], (const u16*)d_in[11],
                                             (u16*)d_out);
}
```

```cpp
#include <hip/hip_runtime.h>
#include <stdint.h>
#include <type_traits>

#define PRA 0
#define CPF 212
#define WPAD 0
#define PFNT 0
#define WNT 0
#define WNTS ""
#define PTH 4
#define PTL 2
#define EB 0
#define LAUX 16
#define SAUX 16
#define PFA 0
#define XFL 0
#define CMAP 0
#define PRB 0
#define WORD 1
#define DR0 6
#define SENT1 0
#define S4M 0
#define S4D 50
#define XP47 0
#define XP8 1
#define SP47 0
#define SP8 1
#define LSKIP 0
#define DNT 1
#define SCN 0
#define SCX 0
#define ED 3
#define GNT 1
#define PP 1
#define PPS 6
#define DLY_Q 430
#define DLY_K 352
#define DLY_C 205
#define KPP 4
#define TDB 4
#define PM 1
#define XL 3
#define NC 1
#define XB 0
#define S2W 1
#define PSL 1
#define HE5 10
#define HE4 25
#define HE3 40
#define FPR 0
#define MPR 0x1233
#define TWO 3
#define WPR 0
#define PPR 3
#define SPR 3
#define PSE 1
#define FP2 3
#define SELW 8
#define EO 1
#define OAUX 16
#define HEA 1
#define DLG 0
#define MJP 4
#define FB 1
#define FP3 0
#define FSL 0
#define LS1 0
#define LSW 8
#define LSD 6
#define SNS 2
#define PKP 0
#define NOSENT 0
#define HB5 72
#define HB4 48
#define NKS 2
#define HCY 0
#define ECK 1
#define TDI 1
#define PFM 8
#define T0A 1
#define S0M 0
#define S1M 32
#define S2M 0
#define S3M 0
#define STA 16
#define XM 0
#define XF 0
#define PFD 10
#define J4(a, b, c, d) (unsigned long long)((a) + 1 + (((b) + 1) << 5) + (((c) + 1) << 10) + (((d) + 1) << 15))
#define JV 1
#define JOBS0 (J4(14, -1, -1, -1) | J4(15, -1, -1, -1) << 40)
#define JOBS1 (J4(0, 4, 8, 13) | J4(1, 5, 9, -1) << 20 | J4(2, 6, 10, -1) << 40)
#define JOBS2 (J4(3, 11, -1, -1) << 20 | J4(7, 12, -1, -1) << 40)
namespace {

constexpr int SEQ = 2048;
constexpr int DM = 576;
constexpr int NM = 128;
constexpr int FF = 4608;

constexpr int NTHR = 768;
constexpr int NFFN = 64;
constexpr int NPART = 80;
constexpr int NCONV = 192;
constexpr int NLN = 128;
constexpr int RED1 = 96;
constexpr int LATE0 = 176;

typedef unsigned short u16;
#define LAS __attribute__((address_space(3)))
#define GAS __attribute__((address_space(1)))
#define CAS __attribute__((address_space(4)))
using half8 = __attribute__((ext_vector_type(8))) _Float16;
using half2v = __attribute__((ext_vector_type(2))) _Float16;
using bf2v = __attribute__((ext_vector_type(2))) __bf16;
using bf8v = __attribute__((ext_vector_type(8))) __bf16;
using bf16v = __attribute__((ext_vector_type(16))) __bf16;
using s16x8 = __attribute__((ext_vector_type(8))) short;
using s16x4 = __attribute__((ext_vector_type(4))) short;
using f32x2 = __attribute__((ext_vector_type(2))) float;
using f32x4 = __attribute__((ext_vector_type(4))) float;
using u32x2 = __attribute__((ext_vector_type(2))) unsigned;
using u32x3 = __attribute__((ext_vector_type(3))) unsigned;
typedef u32x3 u32x3u __attribute__((aligned(4)));
using u32x4 = __attribute__((ext_vector_type(4))) unsigned;
using u64x2 = __attribute__((ext_vector_type(2))) unsigned long long;
using u64x8 = __attribute__((ext_vector_type(8))) unsigned long long;
using u64x4 = __attribute__((ext_vector_type(4))) unsigned long long;

constexpr int WS_EPOCH = 0;
constexpr int WS_PF = 4096;
constexpr int WS_KR = 131072;
constexpr int WS_KF = WS_PF;
constexpr int WS_ST = WS_KF + 16384;
constexpr int WS_CK = WS_ST + 16384;
constexpr int WS_UP = 262144;
constexpr int WS_ZP = WS_UP + NCONV * NLN * 128;
constexpr int WS_PART = WS_ZP + NCONV * NLN * 128;
constexpr int WS_XS = WS_PART + NPART * 8 * DM * 4;
constexpr int XSS = (2 + 2 * NC) * DM;
constexpr int WS_END = WS_XS + (2 * NLN + 8) * XSS;
static_assert(WS_CK + 16 * 2048 <= WS_KR, "ws");
constexpr size_t WS_TS = 64u << 20;
constexpr int SC1 = 16, NT = 2;
constexpr int SPIN_CAP = 1 << 16;

#define TW(k)
#define TWP(k)
#define TWA(k)
#define TWB(k)
#define TWDECL
#define TSX(k, s)
#define TWFLUSH()
#define TC(k, s)
#define TSW(w, s)
#define TSL(k, s)
#define TS(k, s)

#define TWF(k)
#define TWK(k)
#define TWKV(k, ...)
__device__ __forceinline__ float bflo(unsigned v) { return __uint_as_float(v << 16); }
__device__ __forceinline__ float bfhi(unsigned v) { return __uint_as_float(v & 0xffff0000u); }
__device__ __forceinline__ unsigned pkbf(float a, float b) {
  f32x2 f = {a, b};
  return __builtin_bit_cast(unsigned, __builtin_convertvector(f, bf2v));
}
__device__ __forceinline__ unsigned pkh(float a, float b) {
  f32x2 f = {a, b};
  return __builtin_bit_cast(unsigned, __builtin_convertvector(f, half2v));
}
__device__ __forceinline__ float rbf(float f) { return __uint_as_float(pkbf(0.f, f)); }
__device__ __forceinline__ f32x2 rbf2(f32x2 v) { return f32x2{rbf(v[0]), rbf(v[1])}; }
__device__ __forceinline__ f32x2 unpk(unsigned v) { return f32x2{bflo(v), bfhi(v)}; }
__device__ __forceinline__ float dot2(unsigned a, unsigned b, float c) {
  return __builtin_amdgcn_fdot2_f32_bf16(__builtin_bit_cast(bf2v, a), __builtin_bit_cast(bf2v, b), c, false);
}
__device__ __forceinline__ float hadd(f32x2 v) {
  float a = v[0], b = v[1];
  asm volatile("" : "+v"(a), "+v"(b));
  return a + b;
}

template <int CTRL>
__device__ __forceinline__ float dppf(float v) {
  return __int_as_float(__builtin_amdgcn_update_dpp(0, __float_as_int(v), CTRL, 0xf, 0xf, true));
}
template <int CTRL>
__device__ __forceinline__ unsigned dppu(unsigned v) {
  return (unsigned)__builtin_amdgcn_update_dpp(0, (int)v, CTRL, 0xf, 0xf, true);
}
__device__ __forceinline__ float swap16_sum(float v) {
  auto r = __builtin_amdgcn_permlane16_swap(__float_as_uint(v), __float_as_uint(v), false, false);
  return __uint_as_float(r[0]) + __uint_as_float(r[1]);
}
__device__ __forceinline__ float swap32_sum(float v) {
  auto r = __builtin_amdgcn_permlane32_swap(__float_as_uint(v), __float_as_uint(v), false, false);
  return __uint_as_float(r[0]) + __uint_as_float(r[1]);
}
__device__ __forceinline__ float red32_sum(float v) {
  v += dppf<0xB1>(v);
  v += dppf<0x4E>(v);
  v += dppf<0x141>(v);
  v += dppf<0x140>(v);
  return swap16_sum(v);
}
#define SCHED_FENCE() __builtin_amdgcn_sched_barrier(0)
#define DRAIN() asm volatile("s_waitcnt vmcnt(0)" ::: "memory")

__device__ __forceinline__ float red16_sum(float v) {
  v += dppf<0xB1>(v);
  v += dppf<0x4E>(v);
  v += dppf<0x141>(v);
  return v + dppf<0x140>(v);
}
__device__ __forceinline__ float ln_rinv(float q, float dm) {
  const float var = fmaxf(fmaf(q, 1.0f / DM, -dm * dm), 0.f);
  return __builtin_amdgcn_rcpf(rbf(__builtin_amdgcn_sqrtf(rbf(rbf(var) + 1e-5f))));
}

constexpr int TAB_STRIDE = 544;
constexpr int TAB_ROWS = 58;
constexpr int TR_M = 26, TR_F = 34, TR_H = 42;
constexpr int TABB = TAB_ROWS * TAB_STRIDE;
constexpr int SM_KC = 3 * TABB;
constexpr int RDS = 336 * 4;
constexpr int KC_RD = SM_KC;
constexpr int KC_R5 = KC_RD + 4 * RDS;
constexpr int KC_R3 = KC_R5 + 64 * 4;
constexpr int KC_R9 = KC_R3 + 64 * 4;
constexpr int KC_R7 = KC_R9 + 96 * 4;
constexpr int SM_US = KC_R7 + 96 * 4;
constexpr int UPAD = 256;
constexpr int CH2 = SM_US + (UPAD + SEQ) * 2 - SM_KC;
static_assert(SM_US + 2 * UPAD <= SM_KC + 8192 && 8192 <= CH2 && 2 * TABB + 256 < 65536, "zero fill");
constexpr int YT = 1280, YC = 3 * YT, YS = 3 * YC;
constexpr int SM_T2 = 0, T2S = 4624;
constexpr int SM_RED = SM_KC + 3 * CH2;
constexpr int SM_ROW = SM_RED + 3200;
constexpr int SM_ISC = SM_ROW + 128 + 128 + 32;
constexpr int SM_MX = SM_ISC + 16;
constexpr int SM_WR = SM_MX + 32;
constexpr int SM_DUMMY = SM_WR + DM * 2;
constexpr int SM_PRG = SM_DUMMY + 256;
constexpr int RG = 8192;
constexpr int FS_X = 18 * RG;
constexpr int FS_SEL = FS_X + 9216;
constexpr int FS_GU = FS_SEL + 16;
constexpr int SM_TOTAL = FS_GU + 8 * 160;
static_assert(SM_PRG + 64 <= SM_TOTAL && 8 * YS <= 3 * TABB && SM_ISC % 16 == 0 && CH2 % 16 == 0 && TABB % 16 == 0, "lds");

typedef f32x2 cf;
#define TM(t, a, b) "v_pk_mul_f32 v[" #t ":" #t "+1], v[" #b ":" #b "+1], v[" #a ":" #a "+1] op_sel:[1,0]\n\t"
#define TF(d, a, b, t, n) \
  "v_pk_fma_f32 v[" #d ":" #d "+1], v[" #a ":" #a "+1], v[" #b ":" #b "+1], v[" #t ":" #t "+1] op_sel:[0,0,1] op_sel_hi:[1,0,0] neg_" #n ":[0,0,1]\n\t"
#define TC16(c, v) "v_cvt_pk_f16_f32 v" #c ", v[" #v "], v[" #v "+1]\n\t"
#define TWR(c, row) "ds_write_addtid_b32 v" #c " offset:544*" #row "\n\t"
#define TAB_A \
  TC16(134, 100) TM(106, 102, 102) TM(108, 100, 104) TWR(134, 0) TF(110, 102, 102, 106, lo) TF(112, 100, 104, 108, lo) \
  TM(114, 100, 102) TM(116, 100, 110) TC16(135, 112) TF(118, 100, 102, 114, lo) TF(120, 100, 110, 116, lo) TWR(135, 8) \
  TC16(136, 118) TC16(137, 120) TM(122, 112, 110) TWR(136, 1) TM(124, 118, 104) TF(126, 112, 110, 122, lo) TWR(137, 2) \
  TF(128, 118, 104, 124, lo) TC16(134, 126) TM(130, 126, 110) TC16(135, 128) TWR(134, 10) TF(132, 126, 110, 130, lo) \
  TM(106, 118, 110) TWR(135, 9) TC16(136, 132) TF(108, 118, 110, 106, lo) TM(114, 132, 104) TWR(136, 12) TC16(137, 108) \
  TF(102, 132, 104, 114, lo) TM(116, 132, 110) TWR(137, 3) TC16(134, 102) TF(100, 132, 110, 116, lo) TM(122, 102, 104) TWR(134, 18) \
  TC16(135, 100) TF(124, 102, 104, 122, lo) TM(130, 128, 110) TWR(135, 14) TC16(136, 124) TF(106, 128, 110, 130, lo) \
  TM(118, 124, 104) TWR(136, 19) TC16(137, 106) TF(114, 124, 104, 118, lo) TM(116, 100, 104) TWR(137, 11) TC16(134, 114) \
  TF(132, 100, 104, 116, lo) TM(122, 114, 104) TWR(134, 26) TC16(135, 132) TF(102, 114, 104, 122, lo) TM(130, 120, 110) \
  TWR(135, 17) TC16(136, 102) TF(128, 120, 110, 130, lo) TM(118, 108, 110) TWR(136, 28) TC16(137, 128) TF(124, 108, 110, 118, lo) \
  TM(116, 106, 110) TWR(137, 4) TC16(134, 124) TF(100, 106, 110, 116, lo) TM(122, 112, 104) TWR(134, 5) TC16(135, 100) \
  TF(114, 112, 104, 122, lo) TM(130, 126, 104) TWR(135, 13) TC16(136, 114) TF(120, 126, 104, 130, lo) TM(118, 132, 110) \
  TWR(136, 21) TC16(137, 120) TF(108, 132, 110, 118, lo) TM(116, 102, 104) TWR(137, 20) TC16(134, 108) TF(106, 102, 104, 116, lo) \
  TM(122, 128, 110) TWR(134, 16) TC16(135, 106) TF(112, 128, 110, 122, lo) TM(130, 124, 110) TWR(135, 30) TC16(136, 112) \
  TF(126, 124, 110, 130, lo) TM(118, 100, 110) TWR(136, 6) TC16(137, 126) TF(116, 100, 110, 118, lo) TM(102, 114, 104) TWR(137, 7) \
  TC16(134, 116) TF(122, 114, 104, 102, lo) TM(128, 120, 104) TWR(134, 15) TC16(135, 122) TF(112, 120, 104, 128, lo) \
  TM(130, 132, 104) TWR(135, 23) TC16(136, 112) TF(124, 132, 104, 130, lo) TM(126, 108, 104) TWR(136, 22) TC16(137, 124) \
  TF(118, 108, 104, 126, lo) TM(110, 106, 104) TWR(137, 25) TC16(134, 118) TF(100, 106, 104, 110, lo) TWR(134, 24) TC16(135, 100) \
  TWR(135, 32)
#define TAB_B \
  "v_mov_b32 v137, 0x3c00\n\t" TWR(137, 42) TC16(134, 102) TM(106, 104, 104) TM(108, 100, 102) TWR(134, 43) \
  TF(110, 104, 104, 106, lo) TF(112, 100, 102, 108, hi) TM(114, 102, 102) TM(116, 110, 110) TF(118, 110, 110, 116, lo) \
  TF(120, 102, 102, 114, lo) TC16(135, 120) TM(122, 112, 118) TM(124, 120, 102) TWR(135, 44) TF(126, 112, 118, 122, lo) \
  TF(128, 120, 102, 124, lo) TM(130, 120, 120) TC16(136, 126) TC16(137, 128) TF(132, 120, 120, 130, lo) TWR(136, 27) \
  TM(106, 126, 104) TC16(134, 132) TWR(137, 45) TF(108, 126, 104, 106, lo) TM(100, 126, 110) TWR(134, 46) TC16(135, 108) \
  TF(116, 126, 110, 100, lo) TM(114, 108, 110) TWR(135, 29) TC16(136, 116) TF(122, 108, 110, 114, lo) TM(112, 102, 132) \
  TWR(136, 31) TC16(137, 122) TF(124, 102, 132, 112, lo) TM(130, 120, 132) TWR(137, 33) TC16(134, 124) TF(106, 120, 132, 130, lo) \
  TM(104, 128, 132) TWR(134, 47) TC16(135, 106) TF(100, 128, 132, 104, lo) TM(114, 132, 132) TWR(135, 48) TC16(136, 100) \
  TF(110, 132, 132, 114, lo) TM(112, 124, 132) TWR(136, 49) TC16(137, 110) TF(102, 124, 132, 112, lo) TM(130, 106, 132) \
  TWR(137, 50) TC16(134, 102) TF(120, 106, 132, 130, lo) TM(104, 100, 132) TWR(134, 51) TC16(135, 120) TF(128, 100, 132, 104, lo) \
  TM(114, 126, 118) TWR(135, 52) TC16(136, 128) TF(112, 126, 118, 114, lo) TM(124, 108, 118) TWR(136, 53) TC16(137, 112) \
  TF(130, 108, 118, 124, lo) TM(106, 116, 118) TWR(137, 34) TC16(134, 130) TF(104, 116, 118, 106, lo) TM(100, 122, 118) \
  TWR(134, 35) TC16(135, 104) TF(114, 122, 118, 100, lo) TM(126, 110, 132) TWR(135, 36) TC16(136, 114) TF(124, 110, 132, 126, lo) \
  TM(108, 102, 132) TWR(136, 37) TC16(137, 124) TF(106, 102, 132, 108, lo) TM(116, 120, 132) TWR(137, 54) TC16(134, 106) \
  TF(100, 120, 132, 116, lo) TM(122, 128, 132) TWR(134, 55) TC16(135, 100) TF(126, 128, 132, 122, lo) TM(110, 112, 118) \
  TWR(135, 56) TC16(136, 126) TF(124, 112, 118, 110, lo) TM(108, 130, 118) TWR(136, 57) TC16(137, 124) TF(102, 130, 118, 108, lo) \
  TM(106, 104, 118) TWR(137, 38) TC16(134, 102) TF(116, 104, 118, 106, lo) TM(120, 114, 118) TWR(134, 39) TC16(135, 116) \
  TF(100, 114, 118, 120, lo) TWR(135, 40) TC16(136, 100) TWR(136, 41)
#define TAB_CLOB "v106", "v107", "v108", "v109", "v110", "v111", "v112", "v113", "v114", "v115", "v116", "v117", "v118", "v119", "v120", "v121", "v122", "v123", "v124", "v125", "v126", "v127", "v128", "v129", "v130", "v131", "v132", "v133", "v134", "v135", "v136", "v137"
__device__ __forceinline__ unsigned pkc(cf a) { return __builtin_bit_cast(unsigned, __builtin_convertvector(a, half2v)); }
__device__ __forceinline__ cf cexp(f32x2 hi, f32x2 lo) {
  float mag = __builtin_amdgcn_exp2f(hi[0]);
  mag = fmaf(mag, lo[0], mag);
  const float fr = __builtin_amdgcn_fractf(hi[1]) + lo[1];
  return cf{__builtin_amdgcn_cosf(fr), __builtin_amdgcn_sinf(fr)} * mag;
}
__device__ __forceinline__ unsigned wave_max_u32(unsigned v) {
  v = max(v, (unsigned)__builtin_amdgcn_update_dpp(0, (int)v, 0x111, 0xf, 0xf, false));
  v = max(v, (unsigned)__builtin_amdgcn_update_dpp(0, (int)v, 0x112, 0xf, 0xf, false));
  v = max(v, (unsigned)__builtin_amdgcn_update_dpp(0, (int)v, 0x114, 0xf, 0xf, false));
  v = max(v, (unsigned)__builtin_amdgcn_update_dpp(0, (int)v, 0x118, 0xf, 0xf, false));
  v = max(v, (unsigned)__builtin_amdgcn_update_dpp(0, (int)v, 0x142, 0xa, 0xf, false));
  v = max(v, (unsigned)__builtin_amdgcn_update_dpp(0, (int)v, 0x143, 0xc, 0xf, false));
  return (unsigned)__builtin_amdgcn_readlane((int)v, 63);
}

#define MFMA16(a, b, c) __builtin_amdgcn_mfma_f32_16x16x32_f16((a), (b), (c), 0, 0, 0)
#define MFMAB(a, b, c) __builtin_amdgcn_mfma_f32_16x16x32_bf16((a), (b), (c), 0, 0, 0)

__device__ __forceinline__ half8 ld_frag(const LAS char* p) { return *(const LAS half8*)p; }
__device__ __forceinline__ const LAS unsigned* opaque_lds(const LAS char* p) {
  const LAS unsigned* q = (const LAS unsigned*)p;
  asm volatile("" : "+v"(q));
  return q;
}
template <int O2>
__device__ __forceinline__ half8 ld_toep(const LAS unsigned* q) {
  u32x4 v = {q[0], q[1], q[O2], q[O2 + 1]};
  return __builtin_bit_cast(half8, v);
}
__device__ __forceinline__ half8 band_frag(int lane, unsigned pat) {
  unsigned t = __builtin_amdgcn_ubfe(pat << (lane & 15), 8 * (lane >> 4), 8);
  t |= t << 15;
  u32x4 v;
#pragma unroll
  for (int j = 0; j < 4; j++) v[j] = __umul24(t & (0x10001u << (2 * j)), 0x3c00u >> (2 * j));
  return __builtin_bit_cast(half8, v);
}
__device__ __forceinline__ half8 pack_win(f32x4 a, f32x4 b) {
  u32x4 v = {pkh(a[0], a[1]), pkh(a[2], a[3]), pkh(b[0], b[1]), pkh(b[2], b[3])};
  return __builtin_bit_cast(half8, v);
}

__device__ __forceinline__ void set_prio(int p) {
  if (p == 0) __builtin_amdgcn_s_setprio(0);
  if (p == 1) __builtin_amdgcn_s_setprio(1);
  if (p == 2) __builtin_amdgcn_s_setprio(2);
  if (p == 3) __builtin_amdgcn_s_setprio(3);
}
template <int V>
using IC = std::integral_constant<int, V>;
template <int S, int E, class F>
__device__ __forceinline__ void static_for(F&& f) {
  if constexpr (S < E) {
    f(std::integral_constant<int, S>{});
    static_for<S + 1, E>(f);
  }
}
#define VR(r) "v[" #r ":" #r "+3]"
#define DT(r, o) "ds_read_b64 v[" #r ":" #r "+1], v23 offset:" #o "\n\tds_read_b64 v[" #r "+2:" #r "+3], v23 offset:" #o "+8\n\t"
#define DP(r, o) "ds_read2_b32 v[" #r ":" #r "+1], v24 offset0:" #o " offset1:" #o "+1\n\t"
#define DU(r, o) "ds_read_b128 " VR(r) ", v25 offset:" #o "\n\t"
#define DW(n) "s_waitcnt lgkmcnt(" #n ")\n\t"
#define DM(c, a, b) "v_mfma_f32_16x16x32_f16 " VR(c) ", " VR(a) ", " VR(b) ", " VR(c) "\n\t"
#define DZ(c, a, b) "v_mfma_f32_16x16x32_f16 " VR(c) ", " VR(a) ", " VR(b) ", 0\n\t"
#define DC(d, w) "v_cvt_pk_f16_f32 v" #d ", v" #w ", v[" #w "+1]\n\t"
#define DY(c, t) "ds_write_b128 v26, " VR(c) " offset:11520*" #t "\n\t"
#define DA "v_add_u32 v26, 4*11520, v26\n\t"
#define DV(n) "v_mov_b32 v22, " #n "\n\t"
#define DF "ds_write_b32 v27, v22\n\t"
#define DFE "ds_read_u16 v22, v22\n\t"
#define DFP "v_lshl_or_b32 v22, v22, 16, v22\n\t"
#define DTE(p) "v_pk_mul_f16 v" #p ", v" #p ", v22\n\t"
#define DBAR "s_waitcnt vmcnt(0)\n\ts_barrier\n\t"
#define DENSE \
  DT(56, 576) DT(88, 64) DT(52, 640) DT(84, 128) DT(48, 704) DT(80, 192) DT(44, 768) DT(76, 256) DT(40, 832) DT(72, 320) DT(36, 896) \
  DT(68, 384) DT(32, 960) DT(64, 448) DT(28, 1024) DT(60, 512) DT(92, 0) DBAR DU(104, 576) DU(108, 640) DU(112, 704) DU(116, 768) DFE DW(4) \
  DZ(124, 56, 104) DU(120, 832) DU(96, 896) DZ(128, 88, 104) DW(5) DM(124, 52, 108) DU(100, 960) DU(104, 1024) DM(128, 84, 108) DU(14, 1008) \
  DW(7) DM(124, 48, 112) DU(108, 1088) DM(128, 80, 112) DW(6) DM(124, 44, 116) DU(112, 1152) DM(128, 76, 116) DFP DW(2) DM(124, 40, 120) \
  DU(116, 1216) DM(128, 72, 120) DTE(10) DZ(14, 14, 2) DTE(11) DM(124, 36, 96) DU(120, 1280) DM(128, 68, 96) DTE(12) DM(124, 32, 100) \
  DU(96, 1344) DM(128, 64, 100) DTE(13) DM(124, 28, 104) DU(100, 1408) DM(128, 60, 104) DC(144, 14) DZ(132, 92, 104) DC(145, 16) \
  DZ(136, 10, 104) DZ(18, 104, 6) DW(5) DM(128, 56, 108) DU(104, 1472) DM(132, 88, 108) DU(14, 1520) DW(6) DM(128, 52, 112) DU(108, 1536) \
  DM(132, 84, 112) DV(1) DW(6) DM(128, 48, 116) DU(112, 1600) DM(132, 80, 116) DY(124, 0) DW(7) DM(128, 44, 120) DU(116, 1664) \
  DM(132, 76, 120) DF DW(5) DM(128, 40, 96) DU(120, 1728) DM(132, 72, 96) DZ(14, 14, 2) DM(128, 36, 100) DU(96, 1792) DM(132, 68, 100) \
  DC(148, 18) DM(128, 32, 104) DU(100, 1856) DM(132, 64, 104) DC(149, 20) DW(7) DM(128, 28, 108) DU(104, 1920) DM(132, 60, 108) DC(146, 14) \
  DZ(124, 92, 108) DC(147, 16) DZ(140, 10, 108) DZ(18, 108, 6) DW(7) DM(132, 56, 112) DU(108, 1984) DM(124, 88, 112) DU(14, 2032) DW(7) \
  DM(132, 52, 116) DU(112, 2048) DM(124, 84, 116) DV(2) DW(6) DM(132, 48, 120) DU(116, 2112) DM(124, 80, 120) DY(128, 1) DW(7) \
  DM(132, 44, 96) DU(120, 2176) DM(124, 76, 96) DF DW(5) DM(132, 40, 100) DU(96, 2240) DM(124, 72, 100) DZ(14, 14, 2) DM(132, 36, 104) \
  DU(100, 2304) DM(124, 68, 104) DC(150, 18) DM(132, 32, 108) DU(104, 2368) DM(124, 64, 108) DC(151, 20) DW(7) DM(132, 28, 112) \
  DU(108, 2432) DM(124, 60, 112) DC(152, 14) DM(136, 92, 112) DC(153, 16) DZ(128, 10, 112) DZ(18, 112, 6) DW(7) DM(124, 56, 116) \
  DU(112, 2496) DM(136, 88, 116) DU(14, 2544) DW(7) DM(124, 52, 120) DU(116, 2560) DM(136, 84, 120) DV(3) DW(6) DM(124, 48, 96) \
  DU(120, 2624) DM(136, 80, 96) DY(132, 2) DW(7) DM(124, 44, 100) DU(96, 2688) DM(136, 76, 100) DF DW(5) DM(124, 40, 104) DU(100, 2752) \
  DM(136, 72, 104) DZ(14, 14, 2) DM(124, 36, 108) DU(104, 2816) DM(136, 68, 108) DC(156, 18) DM(124, 32, 112) DU(108, 2880) DM(136, 64, 112) \
  DC(157, 20) DW(7) DM(124, 28, 116) DU(112, 2944) DM(136, 60, 116) DC(154, 14) DM(140, 92, 116) DC(155, 16) DZ(132, 10, 116) DZ(18, 116, 6) \
  DW(7) DM(136, 56, 120) DU(116, 3008) DP(10, 32) DM(140, 88, 120) DP(8, 24) DW(8) DM(136, 52, 96) DU(120, 3072) DV(4) DM(140, 84, 96) \
  DC(158, 18) DP(6, 16) DW(8) DM(136, 48, 100) DU(96, 3136) DC(159, 20) DM(140, 80, 100) DY(124, 3) DW(9) DM(136, 44, 104) DU(100, 3200) DF \
  DM(140, 76, 104) DP(20, 128) DW(11) DM(136, 40, 108) DU(104, 3264) DP(18, 120) DM(140, 72, 108) DP(16, 112) DW(10) DM(136, 36, 112) \
  DU(108, 3328) DM(140, 68, 112) DP(4, 8) DM(136, 144, 8) DP(2, 0) DW(11) DM(136, 32, 116) DU(112, 3392) DM(140, 64, 116) DP(14, 104) \
  DM(140, 144, 6) DP(12, 96) DM(136, 28, 120) DU(116, 3456) DM(140, 60, 120) DA DM(128, 92, 120) DW(8) DM(140, 56, 96) DU(120, 3520) \
  DM(128, 88, 96) DM(136, 148, 18) DW(8) DM(140, 52, 100) DU(96, 3584) DM(128, 84, 100) DM(140, 148, 16) DM(140, 48, 104) DU(100, 3648) \
  DM(128, 80, 104) DV(5) DW(9) DM(140, 44, 108) DU(104, 3712) DM(128, 76, 108) DY(136, 0) DW(8) DM(140, 40, 112) DU(108, 3776) \
  DM(128, 72, 112) DF DM(128, 144, 4) DW(7) DM(140, 36, 116) DU(112, 3840) DM(128, 68, 116) DM(128, 152, 8) DW(7) DM(140, 32, 120) \
  DU(116, 3904) DM(128, 64, 120) DM(128, 148, 14) DW(7) DM(140, 28, 96) DU(120, 3968) DM(128, 60, 96) DM(132, 92, 96) DW(7) DM(128, 56, 100) \
  DU(96, 4032) DM(132, 88, 100) DM(128, 156, 18) DW(7) DM(128, 52, 104) DU(100, 4096) DM(132, 84, 104) DV(6) DM(132, 144, 2) DW(6) \
  DM(128, 48, 108) DU(104, 4160) DM(132, 80, 108) DY(140, 1) DW(6) DM(128, 44, 112) DU(108, 4224) DM(132, 76, 112) DF DW(7) DM(128, 40, 116) \
  DU(112, 4288) DM(132, 72, 116) DM(132, 152, 6) DW(7) DM(128, 36, 120) DU(116, 4352) DM(132, 68, 120) DM(132, 148, 12) DW(7) \
  DM(128, 32, 96) DU(120, 4416) DM(132, 64, 96) DM(132, 156, 16) DW(7) DM(128, 28, 100) DU(96, 4480) DM(132, 60, 100) DW(7) DM(132, 56, 104) \
  DU(100, 4544) DW(6) DM(132, 52, 108) DU(104, 4608) DV(7) DW(5) DM(132, 48, 112) DY(128, 2) DW(5) DM(132, 44, 116) DF DW(5) \
  DM(132, 40, 120) DW(4) DM(132, 36, 96) DW(3) DM(132, 32, 100) DW(2) DM(132, 28, 104) DV(8) "s_nop 6\n\t" DY(132, 3) DF
#define DENSE_CLOB "v14", "v15", "v16", "v17", "v18", "v19", "v20", "v21", "v28", "v29", "v30", "v31", "v32", "v33", "v34", "v35", "v36", "v37", "v38", "v39", "v40", "v41", "v42", "v43", "v44", "v45", "v46", "v47", "v48", "v49", "v50", "v51", "v52", "v53", "v54", "v55", "v56", "v57", "v58", "v59", "v60", "v61", "v62", "v63", "v64", "v65", "v66", "v67", "v68", "v69", "v70", "v71", "v72", "v73", "v74", "v75", "v76", "v77", "v78", "v79", "v80", "v81", "v82", "v83", "v84", "v85", "v86", "v87", "v88", "v89", "v90", "v91", "v92", "v93", "v94", "v95", "v96", "v97", "v98", "v99", "v100", "v101", "v102", "v103", "v104", "v105", "v106", "v107", "v108", "v109", "v110", "v111", "v112", "v113", "v114", "v115", "v116", "v117", "v118", "v119", "v120", "v121", "v122", "v123", "v124", "v125", "v126", "v127", "v128", "v129", "v130", "v131", "v132", "v133", "v134", "v135", "v136", "v137", "v138", "v139", "v140", "v141", "v142", "v143", "v144", "v145", "v146", "v147", "v148", "v149", "v150", "v151", "v152", "v153", "v154", "v155", "v156", "v157", "v158", "v159"

typedef __amdgpu_buffer_rsrc_t rsrc_t;
template <int AUX = LAUX>
__device__ __forceinline__ u32x4 ldg16(rsrc_t rs, int off, int so = 0) {
  asm volatile("" : "+s"(so) : : "memory");
  return __builtin_amdgcn_raw_buffer_load_b128(rs, off, so, AUX);
}
__device__ __forceinline__ void stg16(rsrc_t rs, int off, u32x4 v) { __builtin_amdgcn_raw_buffer_store_b128(v, rs, off, 0, SAUX); }
__device__ __forceinline__ void dma16(const char* g, unsigned lds) {
  unsigned keep;
  asm volatile("s_mov_b32 %0, m0\n\ts_mov_b32 m0, %2\n\ts_nop 0\n\tglobal_load_lds_dwordx4 %1, off" WNTS "\n\ts_mov_b32 m0, %0"
               : "=&s"(keep) : "v"(g), "s"(lds) : "memory");
}
__device__ __forceinline__ void dma16nt(const char* g, unsigned lds) {
  unsigned keep;
  asm volatile("s_mov_b32 %0, m0\n\ts_mov_b32 m0, %2\n\ts_nop 0\n\tglobal_load_lds_dwordx4 %1, off nt\n\ts_mov_b32 m0, %0"
               : "=&s"(keep) : "v"(g), "s"(lds) : "memory");
}
__device__ __forceinline__ void dma4(const char* g, unsigned lds) {
  unsigned keep;
  asm volatile("s_mov_b32 %0, m0\n\ts_mov_b32 m0, %2\n\ts_nop 0\n\tglobal_load_lds_dword %1, off\n\ts_mov_b32 m0, %0"
               : "=&s"(keep) : "v"(g), "s"(lds) : "memory");
}
__device__ __forceinline__ void dma4s(size_t base, unsigned off, unsigned lds) {
  unsigned keep;
  asm volatile("s_mov_b32 %0, m0\n\ts_mov_b32 m0, %3\n\ts_nop 0\n\tglobal_load_lds_dword %1, %2\n\ts_mov_b32 m0, %0"
               : "=&s"(keep) : "v"(off), "s"(base), "s"(lds) : "memory");
}
__device__ __forceinline__ void wait_ticks(int t) {
  const unsigned long long t0 = __builtin_amdgcn_s_memrealtime();
  while (__builtin_amdgcn_s_memrealtime() - t0 < (unsigned long long)t) __builtin_amdgcn_s_sleep(1);
}
#define UPOS 1
#define DLY_X 30
#define DLY_W 0
#define HB 1
#define DLY_H 225
#define DLY_R 0
#define DLY_L (DLY_K + 30)
#define DLY_P 120

__device__ __forceinline__ int key_row(unsigned k) { return (int)(~(k >> 2) & (SEQ - 1)); }
__device__ __forceinline__ int key_slot(int lane) { return (lane & 3) | ((lane >> 1) & 4); }
__device__ __forceinline__ int key_addr(unsigned k, int slot) {
  return WS_XS + ((k & 2) ? 2 * NLN + slot : (int)((~(k >> 5) & (2 * NLN - 2)) | (k & 1))) * XSS;
}
struct SelMasks {
  unsigned m[15];
  __device__ __forceinline__ SelMasks(int lane) {
    const unsigned b0 = -(lane & 1), b1 = -((lane >> 1) & 1), b3 = -((lane >> 3) & 1), b4 = -((lane >> 4) & 1), b5 = -(lane >> 5);
    const unsigned t[15] = {~(b0 ^ b1), ~(b1 ^ b3), ~(b0 ^ b3), ~b3, ~b1, ~b0, b3, b1, b0, ~(b3 ^ b4), ~(b1 ^ b4), ~(b0 ^ b4), ~(b3 ^ b5), ~(b1 ^ b5), ~(b0 ^ b5)};
#pragma unroll
    for (int i = 0; i < 15; i++) {
      m[i] = t[i];
      asm volatile("" : "+v"(m[i]));
    }
  }
};
template <int CTRL>
__device__ __forceinline__ unsigned cex(unsigned v, unsigned m) {
  const unsigned o = dppu<CTRL>(v);
  return max(min(v, o), min(max(v, o), m));
}
__device__ __forceinline__ unsigned clean8(unsigned v, const unsigned* m) { return cex<0xB1>(cex<0x4E>(cex<0x128>(v, m[0]), m[1]), m[2]); }
template <bool EAGER, class F>
__device__ __forceinline__ void select8(rsrc_t rs, unsigned tag, int lane, const SelMasks& sm, char* ws, int bid, unsigned& stamp, F&& use) {
  int off = WS_KR + (bid & 7) * 2048 + lane * 16, so = EAGER ? 0 : (NKS - 1) * 16384;
  unsigned m1, t1, t2, m4, third;
  u32x4 a, c, d, e;
  if (EAGER) {
    const u32x4 rq = {(unsigned)(size_t)ws, (unsigned)((size_t)ws >> 32) & 0xffffu, (unsigned)WS_END, 0x00020000u};
    unsigned long long t;
    int n = SPIN_CAP;
#define KASK(r)                                                                                                                        \
  "buffer_load_dwordx4 v[" #r ":" #r "+3], %12, %13, %11 offen sc1\n\tbuffer_load_dwordx4 v[" #r "+4:" #r "+7], %12, %13, %11 offen offset:1024 sc1\n\t" \
  "s_add_u32 %11, %11, 16384\n\ts_min_u32 %11, %11, %16\n\t"
#define KCHK(r, l)                                                                                                          \
  "s_waitcnt vmcnt(2)\n\tv_cmp_eq_u32 vcc, %14, v[" #r "+3]\n\tv_cmp_eq_u32 %9, %14, v[" #r "+7]\n\ts_and_b64 %9, %9, vcc\n\t" \
  "s_cmp_eq_u64 %9, -1\n\ts_cbranch_scc1 " #l "\n\t"
#define KUSE(r)                                                                                                             \
  "v_max_u32 %0, v[" #r "], v[" #r "+4]\n\tv_min_u32 %1, v[" #r "], v[" #r "+4]\n\tv_max_u32 %2, v[" #r "+1], v[" #r "+5]\n\t"     \
  "v_min_u32 %3, v[" #r "+1], v[" #r "+5]\n\tv_max_u32 %4, v[" #r "+2], v[" #r "+6]\n\t"
    asm volatile(KASK(148) "s_sleep %15\n1:\n\t" KASK(156) KCHK(148, 2f) KASK(148) KCHK(156, 3f) "s_sub_u32 %10, %10, 1\n\ts_cbranch_scc0 1b\n"
                 "3:\n\t" KUSE(156) "s_branch 4f\n2:\n\t" KUSE(148) "\n4:"
                 : "=&v"(m1), "=&v"(t1), "=&v"(t2), "=&v"(m4), "=&v"(third), "=&{v[148:151]}"(a), "=&{v[152:155]}"(c), "=&{v[156:159]}"(d),
                   "=&{v[160:163]}"(e), "=&s"(t), "+s"(n), "+s"(so)
                 : "v"(off), "s"(rq), "s"(tag), "n"(KPP), "n"((NKS - 1) * 16384) : "vcc", "scc", "memory");
  } else {
    _Pragma("unroll 1") for (int spin = 0; spin < SPIN_CAP; spin++) {
      a = ldg16(rs, off, so);
      c = ldg16(rs, off, so + 1024);
      if (__builtin_expect(__all(a[3] == tag && c[3] == tag), 1)) break;
      __builtin_amdgcn_s_sleep(4);
    }
    TSW(10, 2);
    const unsigned now = (unsigned)__builtin_amdgcn_s_memrealtime();
    stamp = now - 0xffffu + wave_max_u32(max((a[2] - now - 1u) & 0xffffu, (c[2] - now - 1u) & 0xffffu));
    m1 = max(a[0], c[0]), t1 = min(a[0], c[0]), t2 = max(a[1], c[1]), m4 = min(a[1], c[1]), third = max(a[2], c[2]);
  }
  TSL(1, 8);
  third |= 0xffffu;
  unsigned m2 = max(t1, t2), m3 = min(t1, t2);
  unsigned x = clean8(cex<0xB1>(cex<0x4E>(cex<0xB1>(m1, sm.m[0]), sm.m[1]), sm.m[2]), sm.m + 3);
  const unsigned y = clean8(cex<0xB1>(cex<0x4E>(cex<0xB1>(m2, sm.m[0]), sm.m[1]), sm.m[2]), sm.m + 6);
  x = clean8(max(x, y), sm.m + 3);
  x = clean8(max(x, dppu<0x140>(x)), sm.m + 9);
  {
    auto r = __builtin_amdgcn_permlane16_swap(x, x, false, false);
    x = clean8(max(r[0], r[1]), sm.m + 12);
    auto q = __builtin_amdgcn_permlane32_swap(x, x, false, false);
    x = max(q[0], q[1]);
  }
  use(x, IC<0>{});
  unsigned k = min(x, dppu<0xB1>(x));
  k = min(k, dppu<0x4E>(k));
  k = min(k, dppu<0x128>(k));
  if (__builtin_expect(__any(max(m3, third) > k), 0)) {
    const int slot = key_slot(lane);
    _Pragma("unroll 1") for (int r = 0; r < 8; r++) {
      k = wave_max_u32(m1);
      x = slot == r ? k : x;
      const bool e = m1 == k;
      m1 = e ? m2 : m1;
      m2 = e ? m3 : m2;
      m3 = e ? m4 : m3;
      m4 = e ? 0u : m4;
    }
    if (__any(third > k)) {
      k = 0u;
      _Pragma("unroll 1") for (int r = 0; r < 8; r++) {
        unsigned v;
        _Pragma("unroll 1") for (int spin = 0; spin < SPIN_CAP; spin++) {
          bool ok = true;
          v = 0u;
          _Pragma("unroll 4") for (int i = 0; i < 16; i++) {
            const u32x4 f = ldg16(rs, WS_KF + lane * 16, i * 1024);
            ok = ok && f[1] == tag && f[3] == tag;
            v = max(v, max(f[0] <= k - 1u ? f[0] : 0u, f[2] <= k - 1u ? f[2] : 0u));
          }
          if (__all(ok)) break;
          __builtin_amdgcn_s_sleep(1);
        }
        k = wave_max_u32(v);
        x = slot == r ? k : x;
      }
    }
    use(x, IC<1>{});
  }
  use(x, IC<2>{});
  if (EAGER) asm volatile("s_waitcnt vmcnt(9)" : : "{v[148:151]}"(a), "{v[152:155]}"(c), "{v[156:159]}"(d), "{v[160:163]}"(e) : "memory");
}

__device__ __forceinline__ int last_start(rsrc_t rs, unsigned tag, int lane, int bid, unsigned ref, u32x4 p, u32x4 q) {
  _Pragma("unroll 1") for (int spin = 0; spin < SPIN_CAP; spin++) {
    if (__all(p[1] == tag && p[3] == tag && q[1] == tag && q[3] == tag)) break;
    __builtin_amdgcn_s_sleep(2);
    p = ldg16(rs, WS_CK + (bid & 7) * 2048 + lane * 16);
    q = ldg16(rs, WS_CK + (bid & 7) * 2048 + 512 + lane * 16);
  }
  const int lag = max(max((int)(p[0] - ref), (int)(p[2] - ref)), max((int)(q[0] - ref), (int)(q[2] - ref)));
  return min((int)wave_max_u32((unsigned)max(lag + 4096, 0)) - 4096, 1000);
}
__device__ __forceinline__ int late_group(size_t wg, int bid) { return (3 - (int)(wg >> 7) - bid) & 7; }
__device__ __forceinline__ int ffn_group(size_t wg, int bid) {
  const int l = late_group(wg, bid), i = bid >> 3;
  return (bid & 7) * 9 + (bid < NFFN ? (i == l ? 8 : i) : l);
}
__device__ __forceinline__ void down_dma(size_t wd, int j, LAS char* L, int wave, int lane, int r0, int r1) {
#pragma unroll
  for (int k = 0; k < 3; k++) {
    const int slot = 3 * (wave - 9) + k, t = slot * 64 + lane, r8 = t / 72, q8 = (t - r8 * 72) ^ (r8 & 2);
    const char* p = (const char*)wd + (unsigned)((j * 64 + r8) * 1152);
    _Pragma("unroll 1") for (int i = r0; i < r1; i++)
        (DNT ? dma16nt : dma16)(p + (unsigned)(i * 9216 + (q8 ^ ((i & 1) << 2)) * 16), (unsigned)(size_t)L + slot * 1024 + i * 9216);
  }
}
__device__ __forceinline__ unsigned stats_role(size_t argp, unsigned cpf, LAS char* L, unsigned long long r_entry) {
  const int tid = threadIdx.x, lane = tid & 63, bid = blockIdx.x, fw = bid;
  size_t x, wsp, wd, wg;
  asm volatile(
      "s_waitcnt lgkmcnt(0)\n\ts_load_dwordx2 %0, %4, 0x0\n\ts_load_dwordx2 %1, %4, 0x30\n\ts_load_dwordx2 %2, %4, 0x58\n\t"
      "s_load_dwordx2 %3, %4, 0x48"
      : "=&s"(x), "=&s"(wsp), "=&s"(wd), "=&s"(wg) : "s"(argp));
  SCHED_FENCE();
  unsigned xo0 = (bid * 32 + (tid >> 4)) * (DM * 2) + (lane & 15) * 8;
  asm volatile("" : "+v"(xo0));
  asm volatile("s_waitcnt lgkmcnt(0)" : "+s"(x), "+s"(wsp), "+s"(wd), "+s"(wg));
  const int wave = __builtin_amdgcn_readfirstlane(tid >> 6);
  char* const ws = (char*)(GAS char*)wsp;
  const rsrc_t rs = __builtin_amdgcn_make_buffer_rsrc(ws, 0, WS_END, 0x00020000);
  unsigned tagv = WS_EPOCH;
  asm volatile("global_load_dword %0, %0, %1 sc1" : "+v"(tagv) : "s"(wsp) : "memory");
  SCHED_FENCE();
  unsigned tag;
  if (wave >= 8) {
    if (ED) {
      __builtin_amdgcn_s_barrier();
      if (wave > 8) down_dma(wd, ffn_group(wg, bid), L, wave, lane, 0, ED);
      else DRAIN();
    }
    asm volatile("s_waitcnt vmcnt(%2)" : "+v"(tagv) : "v"(cpf), "n"(3 * ED));
    tag = __builtin_amdgcn_readfirstlane(tagv) + 1u;
    asm volatile("" : "+s"(tag));
  } else {
    u32x2 xw[9];
#pragma unroll
    for (int k = 0; k < 9; k++) xw[k] = *(GAS const u32x2*)(x + xo0 + 128 * k);
    SCHED_FENCE();
    if (ED) __builtin_amdgcn_s_barrier();
    unsigned one2 = 0x3f803f80u;
    asm volatile("" : "+s"(one2));
    float s0 = 0.f, s1 = 0.f, q0 = 0.f, q1 = 0.f;
#pragma unroll
    for (int k = 0; k < 9; k++) {
      const unsigned a = xw[k][0], c = xw[k][1];
      s0 = dot2(a, one2, s0);
      q0 = dot2(a, a, q0);
      s1 = dot2(c, one2, s1);
      q1 = dot2(c, c, q1);
    }
    const float mean = red16_sum(s0 + s1) * (1.0f / DM), mbf = rbf(mean);
    float q = red16_sum(q0 + q1), dm = mean;
    if (__builtin_expect(__any(!(q * (1.0f / DM) >= mean * mean * 1.002f)), 0)) {
      f32x2 q2 = {0.f, 0.f};
#pragma unroll
      for (int k = 0; k < 18; k++) {
        const f32x2 dd = unpk(xw[k >> 1][k & 1]) - mbf;
        q2 = __builtin_elementwise_fma(dd, dd, q2);
      }
      q = red16_sum(hadd(q2));
      dm = mean - mbf;
    }
    float ri = ln_rinv(q, dm);
    asm volatile("" : "+v"(tagv), "+v"(ri));
    auto rr = __builtin_amdgcn_permlane16_swap(__float_as_uint(ri), __float_as_uint(ri), false, false);
    auto m2 = __builtin_amdgcn_permlane16_swap(__float_as_uint(mbf), __float_as_uint(mbf), false, false);
    const unsigned mp = __builtin_amdgcn_perm(m2[1], m2[0], 0x07060302u);
    if ((lane & 31) == 0) stg16(rs, WS_ST + (16 * fw + (tid >> 5)) * 16, u32x4{rr[0], rr[1], mp, tagv + 1u});
    tag = __builtin_amdgcn_readfirstlane(tagv) + 1u;
    asm volatile("" : : "v"(cpf), "s"(tag));
  }
  TS(1, 10);
  return tag;
}

__device__ __forceinline__ void ffn_role(size_t argp, unsigned tag, LAS char* L) {
  const int tid = threadIdx.x, lane = tid & 63, bid = blockIdx.x;
  size_t wsp, wdp;
  u64x2 gu;
  asm volatile("s_load_dwordx2 %0, %3, 0x30\n\ts_load_dwordx4 %1, %3, 0x48\n\ts_load_dwordx2 %2, %3, 0x58" : "=&s"(wsp), "=&s"(gu), "=&s"(wdp) : "s"(argp));
  SCHED_FENCE();
  const int wave = __builtin_amdgcn_readfirstlane(tid >> 6);
  const int hm = bid < NFFN ? 3 : (bid & 8 ? 2 : 1);
  const bool mine = wave < 8 && (hm >> (wave >> 2) & 1);
  const int g = lane >> 4, tq = (lane & 15) >> 2, tp = lane & 3;
  const bool full = hm == 3;
  const int gr = full ? tid >> 4 : tid >> 3;
  const int gc = full ? (lane & 7) ^ ((gr & 1) | ((gr >> 1) & 6)) : (hm & 2) * 2 + ((lane & 3) ^ ((gr >> 2) & 3));
  unsigned go = gr * (FF * 2) + gc * 16;
  asm volatile("" : "+v"(go));
  asm volatile("s_waitcnt lgkmcnt(0)" : "+s"(wsp), "+s"(gu), "+s"(wdp));
  char* const ws = (char*)(GAS char*)wsp;
  const rsrc_t rs = __builtin_amdgcn_make_buffer_rsrc(ws, 0, WS_END, 0x00020000);
  const int j = ffn_group(gu[0], bid);
  const char* gsrc = (const char*)((lane & (full ? 8 : 4)) ? gu[1] : gu[0]) + (go + j * 128);
  const unsigned lb = (unsigned)(size_t)L + wave * 1024 + 9 * RG;
  auto issue_gu = [&](int n, unsigned step) {
    if (wave < 8) {
      _Pragma("unroll 1") for (int i = 0; i < n; i++) {
        (GNT ? dma16nt : dma16)(gsrc, lb + i * RG);
        gsrc += step;
      }
    }
  };
  __syncthreads();
  TS(1, 11);
  if (DLY_W) wait_ticks(DLY_W);
  issue_gu(full ? 10 : 9, full ? 32 * FF * 2 : 64 * FF * 2);
  if (wave > 8) down_dma(wdp, j, L, wave, lane, full ? ED : (hm & 2) * 2, hm == 1 ? 4 : 8);
  s16x4 fa[18][2], fd[3][4];
  const int fr = 16 * (g >> 1) + 4 * (g & 1) + (tq & 1) + 8 * (tq >> 1), fx = (g & 1) | ((tq >> 1) << 1);
  const LAS char* gup = L + 9 * RG + (tp & 1) * 8 +
                        (full ? fr * 256 + ((wave ^ ((tq & 1) | (fx << 1))) + 8 * (tp >> 1)) * 16 : fr * 128 + (((wave & 3) ^ fx) + 4 * (tp >> 1)) * 16);
  const LAS char* gup2 = gup + 8 * RG;
  asm volatile("" : "+v"(gup), "+v"(gup2));
  auto read_gu = [&](auto ks0, auto n, auto reg0, auto sz) {
#pragma unroll
    for (int i = 0; i < n; i++)
#pragma unroll
      for (int h = 0; h < 2; h++) {
        const int o = reg0 * RG + i * sz + h * (sz / 16);
        fa[ks0 + i][h] = __builtin_amdgcn_ds_read_tr16_b64_v4i16((LAS s16x4*)(o < 8 * RG ? gup + o : gup2 + (o - 8 * RG)));
      }
  };
  auto read_down = [&]() {
    const int f = (tq >> 1) | ((g & 1) << 1);
#pragma unroll
    for (int t = 0; t < 3; t++) {
      const LAS char* p = L + (8 * g + tq + (hm == 2 ? 32 : 0)) * 1152 + ((3 * wave + t) ^ f) * 32 + tp * 8;
#pragma unroll
      for (int h = 0; h < 4; h++) {
        if (h >= 2 && !full) fd[t][h] = s16x4{0, 0, 0, 0};
        else fd[t][h] = __builtin_amdgcn_ds_read_tr16_b64_v4i16((LAS s16x4*)(p + (32 * (h >> 1) + 4 * (h & 1)) * 1152));
      }
    }
  };
  const int c15 = lane & 15;
  const LAS char* xr = L + FS_X + ((((c15 >> 3) + 2 * g) ^ (2 * tp)) + 2 * (lane & 4) + 16 * tp) * 16 + (wave < 8 ? 0 : (wave - 8) * 2048);
  LAS char* guw = L + FS_GU + (4 * (g & 1) + 2 * (g >> 1) + (c15 >> 3)) * 160 + ((wave ^ (hm == 2 ? 4 : 0)) * 8 + (lane & 7)) * 2;
  const LAS char* gur = L + FS_GU + (lane & 7) * 160 + 16 * g;
  int xo = ((lane & 7) ^ (2 * g)) * 16 + (1 + (bid & (NC - 1))) * (DM * 2);
  int po = WS_PART + (bid < NFFN ? bid : bid - LATE0) * (8 * DM * 4) + (3 * wave + PM * (c15 >> 3)) * 512 + g * 128 + (lane & 7) * 16;
  int sidx = c15 < 8 ? 0x4444 : 0xeeee;
  asm volatile("" : "+v"(guw), "+v"(gur), "+v"(xo), "+v"(po), "+v"(sidx), "+v"(xr));
  if (wave < 8) *(LAS u16*)guw = 0;
  else *(LAS int*)(L + FS_SEL + 4) = 0;
  const SelMasks sm(lane);
  unsigned due = 0;
  if (full) {
    if (wave < 8) asm volatile("s_waitcnt vmcnt(2)" ::: "memory");
    __syncthreads();
    u32x4 cp, cq;
    asm volatile("s_cmp_lg_u32 %4, %5\n\ts_cbranch_scc1 1f\n\tglobal_load_dwordx4 %0, %2, %3 sc1\n\tglobal_load_dwordx4 %1, %2, %3 offset:512 sc1\n1:"
                 : "=&{v[148:151]}"(cp), "=&{v[152:155]}"(cq) : "v"(WS_CK + (8 + (bid & 7)) * 2048 + lane * 16), "s"(wsp), "s"(wave), "n"(ECK ? SELW : 99) : "scc", "memory");
    if (mine) read_gu(IC<0>{}, IC<8>{}, IC<0>{}, IC<RG>{});
    __syncthreads();
    TS(1, 6);
    issue_gu(8, 32 * FF * 2);
    if (wave < 8) asm volatile("s_waitcnt vmcnt(8)" ::: "memory");
    else if (wave != SELW) DRAIN();
    __syncthreads();
    if (mine) read_gu(IC<8>{}, IC<2>{}, IC<8>{}, IC<RG>{});
    read_down();
    __syncthreads();
    TS(1, 1);
    if (wave == SELW) {
      due = (unsigned)__builtin_amdgcn_s_memrealtime();
      asm volatile("s_waitcnt vmcnt(0)" : "+{v[148:151]}"(cp), "+{v[152:155]}"(cq));
      due += last_start(rs, tag, lane, bid, due, cp, cq);
    }
  } else {
    due = __builtin_amdgcn_readfirstlane(*(const LAS int*)(L + FS_SEL));
  }
  TWDECL;
  auto fetch = [&](int xa) {
    asm volatile(
        "s_mov_b32 m0, %2\n\t.set i_, 0\n\t.rept 9\n\ts_nop 0\n\tglobal_load_lds_dwordx4 %0, %1 offset:128*i_ sc1\n\t"
        "s_add_u32 m0, m0, 896\n\t.set i_, i_+1\n\t.endr"
        : : "v"(xa), "s"(wsp + WS_XS), "s"((unsigned)(size_t)L + FS_X) : "scc", "memory");
  };
  if (wave == SELW) {
    while ((int)((unsigned)__builtin_amdgcn_s_memrealtime() - due) < DLY_K) __builtin_amdgcn_s_sleep(1);
    TSL(1, 3);
    unsigned unused;
    int xa;
    select8<KPP != 0>(rs, tag, lane, sm, ws, bid, unused, [&](unsigned k, auto step) {
      if (step < 2) {
        asm("s_nop 1\n\tv_add_u32_dpp %0, %1, %2 quad_perm:[0,0,0,0] row_mask:0x1 bank_mask:0xf\n\t"
            "v_add_u32_dpp %0, %1, %2 quad_perm:[1,1,1,1] row_mask:0x2 bank_mask:0xf\n\t"
            "v_add_u32_dpp %0, %1, %2 quad_perm:[2,2,2,2] row_mask:0x4 bank_mask:0xf\n\t"
            "v_add_u32_dpp %0, %1, %2 quad_perm:[3,3,3,3] row_mask:0x8 bank_mask:0xf"
            : "=&v"(xa) : "v"(key_addr(step ? k : k & ~2u, key_slot(lane)) - WS_XS), "v"(xo));
        if (step) DRAIN();
        fetch(xa);
        TWF(6);
      } else if (step == 3) {
        TWF(3);
      } else {
        __builtin_amdgcn_s_barrier();
        TSL(1, 9);
      }
    });
    xo = xa;
  } else {
    DRAIN();
    TS(1, 2);
    __syncthreads();
  }
  if (full) {
    if (mine) read_gu(IC<10>{}, IC<8>{}, IC<0>{}, IC<RG>{});
  } else {
    if (mine) read_gu(IC<0>{}, IC<18>{}, IC<0>{}, IC<RG / 2>{});
    read_down();
  }
  f32x4 acc[3];
  _Pragma("unroll 1") for (int spin = 1; spin < SPIN_CAP; spin++) {
    DRAIN();
    __syncthreads();
    TWF(2);
    if (mine) {
      bf8v xf[9];
#pragma unroll
      for (int i = 0; i < 9; i++) xf[i] = *(const LAS bf8v*)(xr + 1024 * i);
      SCHED_FENCE();
      f32x4 a = {0.f, 0.f, 0.f, 0.f};
#pragma unroll
      for (int i = 0; i < 9; i++) {
        const s16x8 lo = __builtin_shufflevector(fa[2 * i][0], fa[2 * i][1], 0, 1, 2, 3, 4, 5, 6, 7);
        const s16x8 hi = __builtin_shufflevector(fa[2 * i + 1][0], fa[2 * i + 1][1], 0, 1, 2, 3, 4, 5, 6, 7);
        a = __builtin_amdgcn_smfmac_f32_16x16x64_bf16(
            xf[i], __builtin_bit_cast(bf16v, __builtin_shufflevector(lo, hi, 0, 1, 2, 3, 4, 5, 6, 7, 8, 9, 10, 11, 12, 13, 14, 15)), a, sidx, 0, 0);
      }
      const float a0 = a[0], a1 = a[1], a2 = a[2], a3 = a[3];
      auto r0 = __builtin_amdgcn_permlane32_swap(__float_as_uint(a0), __float_as_uint(a2), false, false);
      auto r1 = __builtin_amdgcn_permlane32_swap(__float_as_uint(a1), __float_as_uint(a3), false, false);
      const float s0 = __uint_as_float(r0[0]) + __uint_as_float(r0[1]), s1 = __uint_as_float(r1[0]) + __uint_as_float(r1[1]);
      const float gs = __int_as_float(__builtin_amdgcn_update_dpp(__float_as_int(s0), __float_as_int(s1), 0x128, 0xf, 0xc, false));
      const float us = __int_as_float(__builtin_amdgcn_update_dpp(__float_as_int(s1), __float_as_int(s0), 0x128, 0xf, 0x3, false));
      const float gp = rbf(gs);
      const float gq = rbf(gp * __builtin_amdgcn_rcpf(1.0f + __expf(-gp)));
      *(LAS u16*)guw = (u16)pkbf(gq * rbf(us), 0.f);
    } else if (wave >= 8) {
      const u32x4 t = __builtin_elementwise_max(__builtin_elementwise_max(*(const LAS u32x4*)xr, *(const LAS u32x4*)(xr + 1024)), *(const LAS u32x4*)(xr + 2048));
      if (max(max(t[0], t[1]), max(t[2], t[3])) == ~0u) *(LAS int*)(L + FS_SEL + 4) = spin;
    }
    TWF(4);
    __syncthreads();
    TWF(5);
    const bf8v b0 = *(const LAS bf8v*)gur, b1 = *(const LAS bf8v*)(gur + 64);
    const int redo = *(const volatile LAS int*)(L + FS_SEL + 4);
    SCHED_FENCE();
    const f32x4 z4 = {0.f, 0.f, 0.f, 0.f};
#pragma unroll
    for (int t = 0; t < 3; t++) acc[t] = MFMAB(__builtin_bit_cast(bf8v, __builtin_shufflevector(fd[t][0], fd[t][1], 0, 1, 2, 3, 4, 5, 6, 7)), b0, z4);
#pragma unroll
    for (int t = 0; t < 3; t++) acc[t] = MFMAB(__builtin_bit_cast(bf8v, __builtin_shufflevector(fd[t][2], fd[t][3], 0, 1, 2, 3, 4, 5, 6, 7)), b1, acc[t]);
    asm volatile("" : "+v"(acc[0]), "+v"(acc[1]), "+v"(acc[2]));
    SCHED_FENCE();
    if (__builtin_expect(__builtin_amdgcn_readfirstlane(redo) != spin, 1)) break;
    TS(1, 7);
    if (wave == SELW) fetch(xo);
  }
  {
    SCHED_FENCE();
    auto put = [&](f32x4 a, int off) {
      u32x4 o = __builtin_bit_cast(u32x4, a);
      o[0] = min(o[0], 0xfffffffeu);
      stg16(rs, po + off, o);
    };
    if (PM) put(c15 < 8 ? acc[0] : acc[1], 0);
    if (c15 < 8) {
#pragma unroll
      for (int t = 2 * PM; t < 3; t++) put(acc[t], t * 512);
    }
  }
  TWF(7);
  TS(1, 5);
}

__global__ __launch_bounds__(NTHR, 3) void k_all(const u16*, const u16*, const u16*, const u16*, const u16*, const u16*, char*, const u16*,
                                                 const u16*, const u16*, const u16*, const u16*, u16*) {
  __shared__ __attribute__((aligned(16))) char smem[SM_TOTAL];
  LAS char* const L = (LAS char*)smem;
  const int bid = blockIdx.x;
  const size_t argp = (size_t)__builtin_amdgcn_kernarg_segment_ptr();
  u64x8 a8;
  u64x4 a4;
  size_t a1;
  unsigned cpf;
  {
    size_t pc;
    int t;
    asm volatile("s_load_dwordx16 %0, %3, 0x0\n\ts_load_dwordx8 %1, %3, 0x40\n\ts_load_dwordx2 %2, %3, 0x60" : "=&s"(a8), "=&s"(a4), "=&s"(a1) : "s"(argp));
    asm volatile(
        "s_getpc_b64 %1\n\ts_lshr_b32 %2, %3, 3\n\tv_min_u32 %0, 7, %4\n\ts_mul_i32 %2, %2, 8\n\tv_add_lshl_u32 %0, %0, %2, 7\n\t"
        "global_load_dword %0, %0, %1"
        : "=&v"(cpf), "=&s"(pc), "=&s"(t) : "s"(bid), "v"(threadIdx.x) : "scc");
  }
  SCHED_FENCE();
  const int tid = threadIdx.x, lane = tid & 63, b = bid - NFFN;
  unsigned ftag;
  if (b < 0) {
    ftag = stats_role(argp, cpf, L, 0);
  } else {
  const int wave = __builtin_amdgcn_readfirstlane(tid >> 6);
  const bool is_ln = b < NLN;
  const int hl = lane & 31;
  const int ch = (wave & 3) < 3 ? (wave & 3) : (wave >> 2), sub = (wave & 3) < 3 ? (wave >> 2) : 3;
  const int d = 3 * b + ch;
  const int xt = 4 * b + wave, hrow = 8 * ((xt >> 1) & 127) + (XL == 0 ? lane >> (3 + XM) : (XL == 1 ? lane & 7 : (XL == 2 ? (lane & 3) + 4 * (lane >> 5) : (lane & 1) + 2 * (lane >> 4))));
  const int grp = ((xt >> 8) << 5) + ((xt & 1) << 4) + (XL == 0 ? lane & (XM ? 15 : 7) : (XL == 1 ? lane >> 3 : (lane >> (4 - XL)) & 7));
  unsigned xo = hrow * (DM * 4) + grp * 12, po = d * NM * 2 + lane * 4, lo = 2 * d & ~3;
  int cpo = WS_UP + (XB ? ((grp & ~7) + (lane >> 3)) * 2048 + (hrow & ~7) + (lane & 7) : grp * 2048 + hrow) * 16;
  int xbp = (lane & 7) * 32 + (lane >> 3) * 4;
  if (XB) asm volatile("" : "+v"(xbp));
  {
    LAS char* zc = L + (wave >> 2) * CH2 + SM_KC + 16 * (tid & 255);
    const u32x4 z = {0u, 0u, 0u, 0u};
    *(LAS u32x4*)zc = z;
    *(LAS u32x4*)(zc + 4096) = z;
  }
  LAS char* prg = L + SM_PRG + 4 * wave;
  *(LAS int*)prg = wave == 3 ? 2 : 0;
  *(LAS int*)(L + SM_PRG + 52) = -1;
  asm volatile("" : "+v"(prg));
  const int xg = b & 7, sl = b >> 3, sl12 = sl < 12 ? sl : sl - 12;
  unsigned s0o = (144 * min(lane >> 4, S0M - 1) + 12 * sl12 + wave) * (FF * 2) + xg * 1152 + min(lane & 15, 8) * 128;
  if (S0M) asm volatile("" : "+v"(s0o));
  unsigned dso = 6 * b & ~3;
  asm volatile("" : "+v"(xo), "+v"(po), "+v"(lo), "+v"(cpo), "+v"(dso));
  SCHED_FENCE();
  asm volatile("s_waitcnt lgkmcnt(0)" : "+s"(a8), "+s"(a4), "+s"(a1));
  u32x3 cp[4];
  auto load_x = [&]() {
    if (wave < 4) {
#pragma unroll
      for (int i = 0; i < 4; i++) cp[i] = *(GAS const u32x3u*)(a8[0] + xo + ((i & 1) * (DM * 2) + (i >> 1) * (XM ? DM * 16 : 96)));
    }
    SCHED_FENCE();
  };
  if (XF) load_x();
  const unsigned r_ldt = *(GAS const unsigned*)(a8[5] + lo), r_lar = *(GAS const unsigned*)(a8[1] + po), r_aim = *(GAS const unsigned*)(a8[2] + po);
  const unsigned r_cr = *(GAS const unsigned*)(a8[3] + po), r_ci = *(GAS const unsigned*)(a8[4] + po);
  typedef u32x2 u32x2u __attribute__((aligned(4)));
  u32x2 r_dsk = *(GAS const u32x2u*)(a8[7] + dso);
  char* const ws = (char*)(GAS char*)a8[6];
  const rsrc_t ro = __builtin_amdgcn_make_buffer_rsrc((char*)(GAS char*)a1, 0, SEQ * DM * 2, 0x00020000);
  const rsrc_t rs = __builtin_amdgcn_make_buffer_rsrc(ws, 0, WS_END, 0x00020000);
  const unsigned tagv = __builtin_amdgcn_raw_buffer_load_b32(rs, WS_EPOCH, 0, SC1);
  SCHED_FENCE();
  if (!XF) load_x();
  unsigned s0v = 0;
  if (S0M) {
    __builtin_amdgcn_s_barrier();
    s0v = *(GAS const unsigned*)((sl < 12 ? a4[1] : a4[2]) + s0o);
    SCHED_FENCE();
  }
  TWDECL;
  TWP(4);
  auto prefetch = [&](int i) {
    const char* base = (const char*)(sl < 12 ? a4[1] : a4[2]) + (144 * i + 48 * ch + 4 * sl12) * (FF * 2) + xg * 1152;
    dma4(base + (unsigned)((lane >> 4) * (FF * 2) + min(lane & 15, 8) * 128), (unsigned)(size_t)L + SM_DUMMY);
  };
  const int rb = is_ln ? b : b - (NLN - RED1);
  LAS char *const C = L + ch * TABB, *const K = L + ch * CH2;
  const int c15 = lane & 15, g = lane >> 4;
  unsigned pfo = ((9 * xg + (sl & 7)) * 576 + DR0 * 72 + ((sl >> 3) * 4 + (wave & 3)) * ((8 - DR0) * 6) + min(lane, (8 - DR0) * 6 - 1)) * 128;
  unsigned pfq = (12 * sl12 + 3 * (wave & 3) + min(lane >> 4, 2)) * (FF * 2) + xg * 1152 + min(lane & 15, 8) * 128;
  const int lg = late_group(a4[1], bid);
  const char* pfx = (lane & 31) < 12 ? (const char*)(sl < 12 ? a4[1] : a4[2]) + (unsigned)(((sl12 * 4 + (wave & 3)) * 12 + (lane & 31)) * (FF * 2) + xg * 1152 +
                                                                                            (lg ? lg : (lane >> 5) * 8) * 128)
                                     : (const char*)a4[3] + (unsigned)(((9 * xg + lg) * 576 + (sl * 4 + (wave & 3)) * 6 + min((lane & 31) - 12, 5)) * 128);
  asm volatile("" : "+v"(pfx));
  auto pf4 = [&](int m) {
#pragma unroll
    for (int i = 0; i < 4; i++)
      if (m >> i & 1) dma4s((sl < 12 ? a4[1] : a4[2]) + 144 * i * (FF * 2), pfq, (unsigned)(size_t)L + SM_DUMMY);
    if (m & 16) dma4s(a4[3], pfo, (unsigned)(size_t)L + SM_DUMMY);
    if (m & 32) dma4(pfx, (unsigned)(size_t)L + SM_DUMMY);
  };
  unsigned hs = (sub & 1) ? 0x03020c0cu : 0x01000c0cu, hs2 = (d & 1) ? 0x03020c0cu : 0x01000c0cu;
  LAS char* yb = L + ch * YC + g * 320 + c15 * 16 + (sub ? 2 * YS + YT : 0);
  const LAS char *pa, *pa2, *pb, *uf16 = K + SM_US + 2 * UPAD + 32 * c15 + 16 * g;
  LAS char *kd, *kd2;
  unsigned ks0, ks1;
  half8 bA, bB, b9, b7;
  if (sub == 0) {
    pa = C + (((unsigned)(0x33982210cdef4567ull >> (4 * c15)) & 15) + 2 * (c15 & 8)) * TAB_STRIDE + 16 * g;
    pa2 = C + (((unsigned)(0x7763554289ab0123ull >> (4 * c15)) & 15) + 2 * (c15 & 8)) * TAB_STRIDE + 16 * g;
    pb = C + (TR_H + c15) * TAB_STRIDE + 16 * g;
    kd = K + KC_RD + 2 * 544 - 16 * lane;
    kd2 = K + KC_RD + (~c15 & 3) * RDS + 2 * (535 - (c15 | 3) + 8 * g) - 64 * 16;
    uf16 -= 32 * 33;
    bA = band_frag(lane, 0x3u << 12);
    const int s = lane & 3;
    const u32x4 t = {s < 2 ? 0x0c0c0c0cu : (s == 2 ? 0x01000c0cu : 0x01000100u), s == 0 ? 0x01000c0cu : 0x01000100u,
                     s < 2 ? 0x01000100u : (s == 2 ? 0x03020100u : 0x05040302u), (unsigned)(size_t)(K + KC_RD + s * RDS + 64)};
    bB = __builtin_bit_cast(half8, t);
    ks0 = (unsigned)(size_t)(K + KC_R9 + 190 * ((c15 + 1) & 1) + 2 * (15 - c15 + 4 * g));
    b9 = band_frag(lane, 0x1ffu << 4);
    b7 = band_frag(lane, 0x7fu << 5);
    asm volatile("" : "+v"(bA), "+v"(pa), "+v"(pa2), "+v"(pb), "+v"(kd), "+v"(kd2), "+v"(ks0), "+v"(uf16), "=v"(ks1), "+v"(bB), "+v"(b9), "+v"(b7));
  } else if (sub < 3) {
    asm volatile("" : "=v"(bA), "=v"(pa), "=v"(pa2), "=v"(pb), "=v"(kd), "=v"(kd2), "=v"(ks0), "=v"(uf16), "=v"(ks1), "=v"(bB));
  } else {
    pa = C + (TR_H + 15 - c15) * TAB_STRIDE + 16 * g;
    pa2 = K + KC_R5 + 2 * (158 - 64 - 2 * c15 + 4 * g) - 128;
    pb = C + (TR_M + c15) * TAB_STRIDE + 16 * g;
    kd = K + (c15 < 8 ? ((c15 & 1) ? KC_R5 : KC_R3) + 2 * (80 - 8 * (c15 & 6) + 4 * g) : KC_R9 + 2 * (120 - 8 * c15 + 2 * g));
    kd2 = kd + (c15 < 8 ? 4 : KC_R7 - KC_R9);
    ks0 = c15 < 8 ? 0x03020100u : 0x05040100u;
    ks1 = c15 < 8 ? 0x07060504u : 0x07060302u;
    uf16 -= 16 * c15;
    bA = band_frag(lane, 0x1fu << 6);
    bB = band_frag(lane, 0x7u << 11);
    asm volatile("" : "+v"(bA), "+v"(pa), "+v"(pa2), "+v"(pb), "+v"(kd), "+v"(kd2), "+v"(ks0), "+v"(uf16), "+v"(ks1), "+v"(bB));
  }
  const unsigned pe = sub < 2 ? 0u : 3u << 23;
  f32x2 kh = {-1.44269502f, 0.159154936f}, kl = {-1.92596299e-8f, 7.13048051e-9f}, k1 = {128.f * 0.693147181f, 128.f}, ln2 = {0.693147181f, 128.f};
  const unsigned pi = sub < 2 ? 0x3f800000u : 0xc1000000u;
  u32x2 k0u = {0x3f317218u + pe, pi}, p0u = {0x3f800000u + pe, pi};
  const int vt = tid - 256;
  int uoff = WS_UP + b * 16384 + vt * 16, soff = WS_ST + vt * 16;
  asm volatile("" : "+v"(pfo), "+v"(pfq), "+v"(yb), "+v"(uoff), "+v"(soff));
  asm volatile("s_mov_b32 m0, %8" : "+s"(hs), "+s"(hs2), "+s"(kh), "+s"(kl), "+s"(k0u), "+s"(k1), "+s"(ln2), "+s"(p0u)
               : "s"((unsigned)(size_t)C + (sub & 1) * 256));
  SCHED_FENCE();
  TWP(5);
  SCHED_FENCE();
  const unsigned tag = __builtin_amdgcn_readfirstlane(tagv) + 1u;
  auto half = [&](unsigned v) { return __uint_as_float(__builtin_amdgcn_perm(v, v, hs)); };
  f32x2 f_l = {half(r_lar), __uint_as_float(__builtin_amdgcn_perm(r_ldt, r_ldt, hs2))}, f_c = {half(r_cr), half(r_ci)};
  float f_aim = half(r_aim);
  unsigned f_cm;
  asm volatile("v_max3_f32 %0, |%1|, |%2|, |%3|\n\tv_max_f32 %0, %0, |%4|" : "=&v"(f_cm) : "v"(r_cr), "v"(r_ci), "v"(r_cr << 16), "v"(r_ci << 16));
  asm volatile("" : "+v"(f_l), "+v"(f_aim), "+v"(f_c), "+v"(r_dsk) : "v"(cpf));
  SCHED_FENCE();
  TWP(6);
  SCHED_FENCE();
  if (wave < 4) {
    u32x4 pc[4];
#pragma unroll
    for (int i = 0; i < 12; i++) {
      const int r = i / 6 * 2, k = i % 6 / 2;
      const unsigned t = __builtin_amdgcn_perm(cp[r + 1][k], cp[r][k], (i & 1) ? 0x07060302u : 0x05040100u);
      pc[i / 3][i % 3] = XB ? __builtin_amdgcn_ds_bpermute(xbp, t) : t;
      pc[i / 3][3] = tag;
    }
    if (XB) SCHED_FENCE();
#pragma unroll
    for (int i = 0; i < 4; i++) stg16(rs, cpo + (i >> 1) * (XM ? 64 : 16 * 1024 * 16) + (i & 1) * 16384, pc[i]);
    if (is_ln && tid >= 184) dma16((const char*)a4[0] + (tid - 184) * 16, (unsigned)(size_t)L + SM_WR + (wave - 3) * 1024 + 128);
    pf4(S1M);
  }
  SCHED_FENCE();
  TWA(2);
  TWP(7);
  u32x4 v0, v1, st0, st1;
  const u32x4 rq = {(unsigned)a8[6], (unsigned)(a8[6] >> 32) & 0xffffu, (unsigned)WS_END, 0x00020000u};
  asm volatile("" : "=v"(v0), "=v"(v1), "=v"(st0), "=v"(st1));
  auto ask = [&](u32x4& p, u32x4& q, int off) {
    asm volatile("buffer_load_dwordx4 %0, %2, %3, 0 offen sc1\n\tbuffer_load_dwordx4 %1, %2, %3, %4 offen sc1"
                 : "+v"(p), "+v"(q) : "v"(off), "s"(rq), "s"(8192) : "memory");
  };
  auto polls = [&](int k) {
    if (wave >= 4 && (wave < 8 ? XP47 : XP8) == k) ask(v0, v1, uoff);
    if (wave >= 4 && (wave < 8 ? SP47 : SP8) == k) ask(st0, st1, soff);
    SCHED_FENCE();
  };
  auto get = [&](bool both) {
    _Pragma("unroll 1") for (int spin = 0; spin < SPIN_CAP; spin++) {
      const bool ok = both ? v0[3] == tag && v1[3] == tag : st0[3] == tag && st1[3] == tag;
      if (__builtin_expect(__all(ok), 1)) break;
      __builtin_amdgcn_s_sleep(4);
      if (both) {
        asm volatile("s_waitcnt vmcnt(0)" : "+v"(st0), "+v"(st1));
        if (!ok) ask(v0, v1, uoff);
      }
      if (st0[3] != tag || st1[3] != tag) ask(st0, st1, soff);
      if (both) asm volatile("s_waitcnt vmcnt(0)" : "+v"(v0), "+v"(v1), "+v"(st0), "+v"(st1));
      else asm volatile("s_waitcnt vmcnt(0)" : "+v"(st0), "+v"(st1));
    }
  };
  {
    const f32x2 l2e = -f32x2{kh[0], kh[0]}, th = f_l * l2e;
    const f32x2 tl = __builtin_elementwise_fma(f_l, -f32x2{kl[0], kl[0]}, __builtin_elementwise_fma(f_l, l2e, -th)) * ln2[0];
    const f32x2 e0 = {__builtin_amdgcn_exp2f(th[0]), __builtin_amdgcn_exp2f(th[1])}, ex = __builtin_elementwise_fma(e0, tl, e0);
    const unsigned eb = pkbf(ex[0], ex[1]);
    const float dt = bfhi(eb);
    const f32x2 av = f32x2{bflo(eb), f_aim} * dt;
    const f32x2 hi = av * kh, lo = __builtin_elementwise_fma(av, kl, __builtin_elementwise_fma(av, kh, -hi));
    int se = 385 - (int)(wave_max_u32(f_cm) >> 23) - (int)((unsigned)__builtin_amdgcn_readfirstlane(eb) >> 23);
    se = max(se, 2);
    asm("" : "+s"(se));
    se = min(se, 252);
    const float dts = dt * __uint_as_float((unsigned)(se + 1) << 23);
    cf cc = f_c * dts;
    cf ea = cexp(hi * __builtin_bit_cast(f32x2, p0u), lo * __builtin_bit_cast(f32x2, k0u)), eb2 = cexp(hi * 128.f, lo * k1);
    polls(2);
    asm volatile("s_cmp_lt_u32 %3, 2\n\ts_cbranch_scc0 1f\n\t" TAB_A "s_branch 2f\n1:\n\t" TAB_B "\n2:"
                 : "+{v[100:101]}"(cc), "+{v[102:103]}"(ea), "+{v[104:105]}"(eb2) : "s"(sub) : TAB_CLOB, "scc", "memory");
    if (sub == 2) *(volatile LAS unsigned*)(L + SM_ISC + 4 * ch) = (unsigned)(254 - se) << 23;
  }
  SCHED_FENCE();
  if (S0M) asm volatile("" : : "v"(s0v));
  polls(1);
  TWA(3);
  const f32x4 zero4 = {0.f, 0.f, 0.f, 0.f};
  f32x2 sb[2][3];
  if (sub == 0) {
    {
      half8 fa[8], fc[8], fb[8];
#pragma unroll
      for (int ks = 0; ks < 4; ks++) { fa[ks] = ld_frag(pa + 64 * ks); fc[ks] = ld_frag(pa2 + 64 * ks); }
      asm volatile("s_waitcnt lgkmcnt(0)" ::: "memory");
      __syncthreads();
      TWK(3);
#pragma unroll
      for (int ks = 0; ks < 4; ks++) fb[ks] = ld_frag(pb + 64 * ks);
      SCHED_FENCE();
#pragma unroll
      for (int ks = 4; ks < 8; ks++) { fa[ks] = ld_frag(pa + 64 * ks); fb[ks] = ld_frag(pb + 64 * ks); fc[ks] = ld_frag(pa2 + 64 * ks); }
      SCHED_FENCE();
      if (S2W == 1) pf4(S2M);
      SCHED_FENCE();
      TWKV(4, "v"(fc[7]));
      f32x4 f = zero4, e = zero4;
#pragma unroll
      for (int ks = 0; ks < 8; ks++) { f = MFMA16(fa[ks], fb[ks], f); e = MFMA16(fc[ks], fb[ks], e); }
      const unsigned d0 = pkh(f[0], f[1]);
      TWKV(5, "v"(d0));
      const unsigned d1 = pkh(f[2], f[3]), d2 = pkh(e[0], e[1]), d3 = pkh(e[2], e[3]);
      const unsigned p0 = dppu<0x138>(d0), p1 = dppu<0x138>(d1);
      *(volatile LAS u32x4*)kd = u32x4{d0, d1, d2, d3};
      const unsigned a1 = __builtin_amdgcn_alignbit(d2, d1, 16), a2 = __builtin_amdgcn_alignbit(d3, d2, 16), a3 = __builtin_amdgcn_alignbit(p0, d3, 16);
      *(volatile LAS u32x4*)(kd + RDS) = u32x4{__builtin_amdgcn_alignbit(d1, d0, 16), a1, a2, a3};
      *(volatile LAS u32x4*)(kd + 2 * RDS) = u32x4{d1, d2, d3, p0};
      *(volatile LAS u32x4*)(kd + 3 * RDS) = u32x4{a1, a2, a3, __builtin_amdgcn_alignbit(p1, p0, 16)};
      if (S2W == 2) pf4(S2M);
      TWA(4);
      const unsigned q0 = dppu<0xFF>(d0), q1 = dppu<0xFF>(d1);
      const u32x4 t = __builtin_bit_cast(u32x4, bB);
      if (lane >= 60)
        *(volatile LAS u32x4*)(size_t)t[3] =
            u32x4{0u, __builtin_amdgcn_perm(q1, q0, t[0]), __builtin_amdgcn_perm(q1, q0, t[1]), __builtin_amdgcn_perm(q1, q0, t[2])};
    }
    TWA(5);
    {
      const LAS char* fe = K + KC_R5 + 64;
      asm volatile(DENSE : "+{v[2:5]}"(b9), "+{v[6:9]}"(b7), "+{v[10:13]}"(bA), "+{v22}"(fe), "+{v26}"(yb)
                   : "{v23}"(kd2), "{v24}"(ks0), "{v25}"(uf16), "{v27}"(prg) : DENSE_CLOB, "memory");
    }
    TWA(7);
  } else {
  asm volatile("s_waitcnt lgkmcnt(0)" ::: "memory");
  __syncthreads();
  polls(0);
  half8 td[6];
  auto mid_td = [&]() {
    const LAS unsigned* t5 = (const LAS unsigned*)pa2;
#pragma unroll
    for (int e = 0; e < 3; e++) {
      td[e] = ld_toep<8>(t5 + 16 * (2 - e));
      td[3 + e] = ld_toep<8>(t5 + (KC_R3 - KC_R5) / 4 + 16 * (2 - e));
    }
  };
  if (wave == 3) {
    half8 fa[24], fb[24];
    f32x4 f[3] = {zero4, zero4, zero4};
    constexpr int PF = PFM;
    TWK(3);
#pragma unroll
    for (int i = 0; i < PF; i++) { fa[i] = ld_frag(pa + (i >> 3) * TABB + 64 * (i & 7)); fb[i] = ld_frag(pb + (i >> 3) * TABB + 64 * (i & 7)); }
    SCHED_FENCE();
    auto put = [&](int c) {
      const unsigned d0 = pkh(f[c][0], f[c][1]), d1 = pkh(f[c][2], f[c][3]);
      const unsigned w0 = __builtin_amdgcn_perm(d1, d0, ks0), w1 = __builtin_amdgcn_perm(d1, d0, ks1);
      *(LAS unsigned*)(kd + c * CH2) = w0;
      *(LAS unsigned*)(kd2 + c * CH2) = w1;
      if (c15 >= 8) {
        *(LAS u16*)(kd + c * CH2 + 190) = (u16)w0;
        *(LAS u16*)(kd + c * CH2 + 192) = (u16)(w0 >> 16);
        *(LAS u16*)(kd + c * CH2 + (KC_R7 - KC_R9) + 190) = (u16)w1;
        *(LAS u16*)(kd + c * CH2 + (KC_R7 - KC_R9) + 192) = (u16)(w1 >> 16);
      }
    };
    static_for<0, 24>([&](auto ic) {
      constexpr int i = decltype(ic)::value, n = i + PF;
      f[i >> 3] = MFMA16(fa[i], fb[i], f[i >> 3]);
      SCHED_FENCE();
      if constexpr (n < 24) { fa[n] = ld_frag(pa + (n >> 3) * TABB + 64 * (n & 7)); fb[n] = ld_frag(pb + (n >> 3) * TABB + 64 * (n & 7)); }
      if constexpr (i == 14) {
        put(0);
        asm volatile("" ::: "memory");
        mid_td();
      }
      if constexpr (i == 18) put(1);
      if constexpr (i == (S2W == 1 ? 0 : 10)) pf4(S2M);
      SCHED_FENCE();
    });
    TWK(5);
    put(2);
    __builtin_amdgcn_s_waitcnt(S2M ? 0x0F71 : 0x0F70);
  }
  TWA(4);
  SCHED_FENCE();
  if (wave >= 4) {
    f32x2 dsk[3], xf[2][3];
    LAS char* dst = L + SM_US + 2 * UPAD + 4 * vt;
#pragma unroll
    for (int c = 0; c < 3; c++) {
      dsk[c][0] = __uint_as_float(__builtin_amdgcn_perm(r_dsk[1], r_dsk[0], 0x01000c0cu + 0x02020000u * (c + (b & 1))));
      asm volatile("" : "+v"(dsk[c]), "+v"(dst));
    }
    asm volatile("s_waitcnt vmcnt(2)" : "+v"(v0), "+v"(v1));
    get(true);
#pragma unroll
    for (int c = 0; c < 3; c++) {
      xf[0][c] = unpk(v0[c]);
      xf[1][c] = unpk(v1[c]);
      asm volatile("" : "+v"(xf[0][c]), "+v"(xf[1][c]));
    }
    SCHED_FENCE();
    asm volatile("s_waitcnt vmcnt(0)" : "+v"(st0), "+v"(st1));
    get(false);
    TWA(5);
    f32x2 uf[2][3];
#pragma unroll
    for (int i = 0; i < 2; i++) {
      const u32x4& st = i ? st1 : st0;
      const f32x2 ri = {__uint_as_float(st[0]), __uint_as_float(st[1])};
      const f32x2 cc = -unpk(st[2]) * ri;
#pragma unroll
      for (int c = 0; c < 3; c++) {
        uf[i][c] = __builtin_elementwise_fma(xf[i][c], ri, cc);
        *(LAS unsigned*)(dst + i * 2048 + c * CH2) = pkc(uf[i][c]);
      }
    }
    SCHED_FENCE();
#pragma unroll
    for (int i = 0; i < 2; i++)
#pragma unroll
      for (int c = 0; c < 3; c++) {
        sb[i][c] = __builtin_elementwise_fma(uf[i][c], f32x2{dsk[c][0], dsk[c][0]}, xf[i][c]);
        asm volatile("" : "+v"(sb[i][c]));
      }
    if (is_ln && (vt >> 3) == (b & 63)) *(LAS unsigned*)(L + SM_MX + (vt & 7) * 4) = b < 64 ? st0[2] : st1[2];
  } else {
    TWA(5);
  }
  LAS char* sbw = L + ((wave >> 1) - 2) * YS + ((vt >> 1) & 3) * 320 + ((vt >> 3) & 15) * 16 + (vt & 1) * 8;
  int two = sub == 3 ? 2 : 8;
  asm volatile("" : "+v"(sbw), "+v"(two));
  auto put_sb = [&]() {
#pragma unroll
    for (int c = 0; c < 6; c++) *(volatile LAS f32x2*)(sbw + c % 3 * YC + 2 * YT + c / 3 * 4 * YS) = sb[c / 3][c % 3];
    *(volatile LAS int*)prg = two;
  };
  if (sub < 3) __builtin_amdgcn_s_setprio(SPR);
  else __builtin_amdgcn_s_setprio(MPR & 3);
  __syncthreads();
  TWA(6);
  {
    if (sub < 3) {
      unsigned long long t0 = __builtin_amdgcn_s_memrealtime();
      const unsigned c0 = (unsigned)__builtin_amdgcn_s_memtime();
      if (wave == LSW && lane < 16) __builtin_amdgcn_raw_buffer_store_b64(u32x2{(unsigned)t0, tag}, rs, WS_CK + lane * 2048 + b * 8, 0, SC1);
      if (is_ln && sub == SNS && !NOSENT) {
        const u32x4 ones = {~0u, ~0u, ~0u, ~0u};
        const int o = (vt & 255) * 16;
#pragma unroll
        for (int i = 0; i < 3; i++) stg16(rs, WS_PART + b * 11520 + o + i * 3072, ones);
        if (o < 2304) stg16(rs, WS_PART + b * 11520 + o + 9216, ones);
#pragma unroll
        for (int i = 0; i < 2 * XSS; i += 3072)
          if (i + 3072 <= 2 * XSS || o < 2 * XSS - i) stg16(rs, WS_XS + b * (2 * XSS) + o + i, ones);
        if (b < 8 * XSS / 3072) stg16(rs, WS_XS + 2 * NLN * XSS + b * 3072 + o, ones);
      }
      SCHED_FENCE();
      __builtin_amdgcn_s_setprio(0);
      put_sb();
      if (wave == LSW) {
        __builtin_amdgcn_s_sleep(LSD);
        const int lag = last_start(rs, tag, lane, bid, (unsigned)t0, u32x4{0u, ~tag, 0u, 0u}, u32x4{0u, 0u, 0u, 0u});
        const float dt = (float)((unsigned)__builtin_amdgcn_s_memrealtime() - (unsigned)t0), dc = (float)((unsigned)__builtin_amdgcn_s_memtime() - c0);
        t0 += lag;
        *(volatile LAS unsigned long long*)(L + SM_PRG + 48) =
            t0 + (unsigned)(HCY * dt * __builtin_amdgcn_rcpf(dc)) + (DLY_H - (b >= 96 ? 0 : (b >= HB5 ? HE5 : (b >= HB4 ? HE4 : HE3))));
        *(LAS int*)(L + FS_SEL) = (int)t0;
      }
    } else {
      if (wave != 3) {
        put_sb();
        mid_td();
      }
      f32x4 a2[6], w[4];
#pragma unroll
      for (int i = 0; i < 6; i++) a2[i] = zero4;
      half8 uw[12];
#pragma unroll
      for (int jj = 0; jj < 6; jj++) uw[jj] = ld_frag(uf16 - 48 + 256 * jj);
      SCHED_FENCE();
      w[0] = MFMA16(uw[0], bA, zero4); w[1] = MFMA16(uw[1], bA, zero4);
      w[2] = MFMA16(uw[0], bB, zero4); w[3] = MFMA16(uw[1], bB, zero4);
      half8 X5 = pack_win(w[0], w[1]), X3 = pack_win(w[2], w[3]);
      SCHED_FENCE();
#pragma unroll
      for (int jj = 0; jj < 6; jj++) {
        if (jj < 5) {
          w[0] = MFMA16(uw[2 * jj + 2], bA, zero4);
          if (jj < 3) uw[2 * jj + 6] = ld_frag(uf16 - 48 + 256 * (2 * jj + 6));
          SCHED_FENCE();
          w[1] = MFMA16(uw[2 * jj + 3], bA, zero4);
          if (jj < 3) uw[2 * jj + 7] = ld_frag(uf16 - 48 + 256 * (2 * jj + 7));
          SCHED_FENCE();
          w[2] = MFMA16(uw[2 * jj + 2], bB, zero4);
        }
        if (jj > 0) *(volatile LAS f32x4*)(yb + (jj - 1) * YS) = a2[jj - 1];
        SCHED_FENCE();
        if (jj < 5) w[3] = MFMA16(uw[2 * jj + 3], bB, zero4);
        if (jj > 0) *(volatile LAS int*)prg = 2 + jj;
        if (jj > 0 && jj < 5 && (MPR >> 4 * jj & 3) != (MPR >> 4 * (jj - 1) & 3)) set_prio(MPR >> 4 * jj & 3);
        SCHED_FENCE();
        u32x4 n5, n3;
#pragma unroll
        for (int q = 0; q < 6; q++) {
          const int e = q >> 1;
          if (jj + e < 6) a2[jj + e] = MFMA16((q & 1) ? X3 : X5, td[3 * (q & 1) + e], a2[jj + e]);
          if (jj < 5 && q < 4) {
            u32x4& nn = q < 2 ? n5 : n3;
            nn[2 * (q & 1)] = pkh(w[q][0], w[q][1]);
            nn[2 * (q & 1) + 1] = pkh(w[q][2], w[q][3]);
          }
          SCHED_FENCE();
        }
        if (jj < 5) {
          X5 = __builtin_bit_cast(half8, n5);
          X3 = __builtin_bit_cast(half8, n3);
        }
      }
      *(volatile LAS f32x4*)(yb + 5 * YS) = a2[5];
      *(volatile LAS int*)prg = 8;
    }
  }
  TWA(7);
  }
  u32x4 h0, h1;
  int h_asked = b / NLN, hoff = WS_ZP + b * (NCONV * 128) + (tid >> 3) * 256 + (tid & 7) * 16;
  asm volatile("" : "={v[6:9]}"(h0), "={v[2:5]}"(h1), "+v"(hoff));
  unsigned jobs = (unsigned)((wave < 4 ? JOBS0 : (wave < 8 ? JOBS1 : JOBS2)) >> 20 * (wave & 3)) & 0xfffffu;
  if (jobs) {
    const u32x4 isc = *(const LAS u32x4*)(L + SM_ISC);
    unsigned p, t, n = SPIN_CAP;
    const LAS char* yp = L + ((lane >> 1) & 3) * 320 + (lane >> 3) * 16 + (lane & 1) * 8;
    const int zo = WS_ZP + ((lane >> 3) * NCONV + b) * 128 + (lane & 7) * 16;
    set_prio(WPR);
#define PR3(r, o)                                                                                       \
  "ds_read_b64 v[" #r ":" #r "+1], v10 offset:" #o "\n\tds_read_b64 v[" #r "+2:" #r "+3], v10 offset:" #o "+3840\n\t" \
  "ds_read_b64 v[" #r "+4:" #r "+5], v10 offset:" #o "+7680\n\t"
#define PF3(r)                                                                                                 \
  "v_fmac_f32 v16, v12, v[" #r "]\n\tv_fmac_f32 v17, v12, v[" #r "+1]\n\tv_fmac_f32 v18, v13, v[" #r "+2]\n\t" \
  "v_fmac_f32 v19, v13, v[" #r "+3]\n\tv_fmac_f32 v20, v14, v[" #r "+4]\n\tv_fmac_f32 v21, v14, v[" #r "+5]\n\t"
    unsigned long long now;
    asm volatile(
        "v_mov_b32 v11, -1\n\tv_mov_b32 v37, %13\n"
        "1:\n\ts_and_b32 %1, %0, 31\n\ts_sub_u32 %1, %1, 1\n\ts_lshr_b32 %2, %1, 1\n\ts_lshl_b32 vcc_lo, %1, 7\n\ts_mul_i32 vcc_hi, %2, 11520-256\n\t"
        "s_add_u32 vcc_lo, vcc_lo, vcc_hi\n\tv_add_u32 v10, vcc_lo, %8\n\ts_mul_i32 %1, %1, 196608\n"
        "2:\n\tv_cmp_gt_i32 vcc, %2, v11\n\ts_cbranch_vccz 3f\n\ts_sleep %14\n\tds_read_b32 v11, %10\n\ts_sub_u32 %3, %3, 1\n\t"
        "s_cmp_gt_i32 %3, 0\n\ts_waitcnt lgkmcnt(0)\n\ts_cbranch_scc1 2b\n"
        "3:\n\tds_read_b32 v11, %10\n\t" PR3(16, 2560) PR3(22, 0) "s_cmp_lt_u32 %2, 2\n\ts_cbranch_scc1 4f\n\t" PR3(28, 1280) "\n"
        "4:\n\tds_read_b64 v[34:35], %19\n\ts_memrealtime %7\n\ts_waitcnt lgkmcnt(0)\n\tv_cmp_ge_i32 vcc, %2, v11\n\ts_cbranch_vccz 5f\n\t"
        "s_sleep %15\n\ts_sub_u32 %3, %3, 1\n\ts_cmp_gt_i32 %3, 0\n\ts_cbranch_scc1 3b\n"
        "5:\n\ts_setprio %16\n\tv_cmp_ge_u64 vcc, %7, v[34:35]\n\ts_cmp_lg_u32 %6, 0\n\ts_cbranch_scc1 7f\n\ts_cbranch_vccz 7f\n\t"
        "buffer_load_dwordx4 %4, %18, %12, 0 offen sc1\n\tbuffer_load_dwordx4 %5, %18, %12, 0 offen offset:128 sc1\n\ts_mov_b32 %6, 4\n"
        "7:\n\t" PF3(22) "s_cmp_lt_u32 %2, 2\n\ts_cbranch_scc1 6f\n\t" PF3(28) "\n"
        "6:\n\tv_cvt_pk_bf16_f32 v34, v16, v17\n\tv_cvt_pk_bf16_f32 v35, v18, v19\n\tv_cvt_pk_bf16_f32 v36, v20, v21\n\t"
        "s_lshr_b32 %0, %0, 5\n\tbuffer_store_dwordx4 v[34:37], %9, %12, %1 offen sc1\n\ts_setprio %17\n\ts_cbranch_scc1 1b\n\ts_nop 1"
        : "+s"(jobs), "=&s"(p), "=&s"(t), "+s"(n), "+{v[6:9]}"(h0), "+{v[2:5]}"(h1), "+s"(h_asked), "=&s"(now)
        : "v"(yp), "v"(zo), "v"(L + SM_PRG + 4 * min(lane, 11)), "{v[12:15]}"(isc), "s"(rq), "s"(tag), "n"(PSL), "n"(PSE), "n"(PPR), "n"(WPR),
          "v"(hoff), "v"(L + SM_PRG + 48)
        : "v10", "v11", "v16", "v17", "v18", "v19", "v20", "v21", "v22", "v23", "v24", "v25", "v26", "v27", "v28", "v29", "v30", "v31", "v32",
          "v33", "v34", "v35", "v36", "v37", "vcc", "scc", "memory");
    static_assert(YT == 1280 && 8 * NCONV * 128 == 196608, "constants above");
  }
  auto ask_h = [&](int tries, int mark) {
    unsigned long long due, now;
    asm volatile(
        "s_cmp_lg_u32 %2, 0\n\ts_cbranch_scc1 3f\n1:\n\tds_read_b64 %4, %6\n\ts_memrealtime %5\n\ts_waitcnt lgkmcnt(0)\n\t"
        "v_cmp_ge_u64 vcc, %5, %4\n\ts_cbranch_vccnz 2f\n\t"
        "s_sub_u32 %3, %3, 1\n\ts_cbranch_scc1 3f\n\ts_sleep 1\n\ts_cmp_lg_u32 %3, 0\n\ts_cbranch_scc1 1b\n2:\n\t"
        "buffer_load_dwordx4 %0, %7, %8, 0 offen sc1\n\tbuffer_load_dwordx4 %1, %7, %8, 0 offen offset:128 sc1\n\ts_mov_b32 %2, %9\n3:"
        : "+{v[6:9]}"(h0), "+{v[2:5]}"(h1), "+s"(h_asked), "+s"(tries), "=&v"(due), "=&s"(now)
        : "v"((unsigned)(size_t)L + SM_PRG + 48), "v"(hoff), "s"(rq), "s"(mark) : "vcc", "scc", "memory");
  };
  __builtin_amdgcn_s_setprio(0);
  ask_h(0, 1);
  TS(0, 6);
  TWB(2);
  TS(0, 7);
  TWB(3);
  TWFLUSH();
  ftag = tag;
  if (b < LATE0) {
  LAS unsigned* const selp = (LAS unsigned*)(L + SM_ROW + 256);
  unsigned t_key = 0;
  auto select_rows = [&]() {
    const SelMasks sm(lane);
    const unsigned due = __builtin_amdgcn_readfirstlane(*(const LAS int*)(L + FS_SEL));
    while ((int)((unsigned)__builtin_amdgcn_s_memrealtime() - due) < DLY_L) __builtin_amdgcn_s_sleep(1);
    unsigned long long t_ask = 0;
    select8<false>(rs, tag, lane, sm, ws, bid, t_key, [&](unsigned k, auto step) {
      if (step == 2) selp[key_slot(lane)] = k;
    });
    TSW(10, 3);
  };
  if (is_ln) {
    f32x2 wf[6], hv[6];
    const unsigned mxp = *(const LAS unsigned*)(L + SM_MX + (tid & 7) * 4);
#pragma unroll
    for (int i = 0; i < 6; i++) {
      const float t = bflo(*(const LAS u16*)(L + SM_WR + (tid >> 3) * 12 + i * 2));
      wf[i] = f32x2{t, t};
    }
    float wsum = 0.f, mxr = bflo(*(const LAS u16*)(L + SM_MX + (lane >> 2) * 2));
    unsigned one2 = 0x3f803f80u, sel0 = 0x00003f80u, sel1 = 0x3f800000u;
    asm volatile("" : "+s"(one2), "+s"(sel0), "+s"(sel1));
    if (wave == 0) {
#pragma unroll
      for (int k = 0; k < 9; k++) wsum = dot2(*(const LAS unsigned*)(L + SM_WR + (hl + 32 * k) * 4), one2, wsum);
      wsum = red32_sum(wsum);
      __builtin_amdgcn_s_setprio(2);
    }
#pragma unroll
    for (int i = 0; i < 6; i++) {
      hv[i] = -unpk(mxp);
      asm volatile("" : "+v"(hv[i]), "+v"(wf[i]));
    }
    LAS char* redw = L + SM_RED + wave * 256 + (wave >= 6 ? 128 : 0) + (lane & 7) * 32 + (lane >> 5) * 16 + ((lane >> 4) & 1) * 8;
    const LAS char* redr = L + SM_RED + ((lane >> 1) & 1) * (6 * 256 + 128) + (lane >> 2) * 16 + (lane & 1) * 8;
    unsigned kidx = (unsigned)(SEQ - 1 - 16 * b - (lane >> 2)) << 2;
    int kro = WS_KR + (lane & (8 * NKS - 1)) * 2048 + b * 16, xso = WS_XS + b * (2 * XSS) + (tid >> 3) * 12;
    asm volatile("" : "+v"(wsum), "+v"(mxr), "+v"(redw), "+v"(redr), "+v"(kidx), "+v"(kro), "+v"(xso));
    ask_h(SPIN_CAP, 1);
    TWB(4);
    {
      int n = SPIN_CAP;
      unsigned long long m;
      asm volatile(
          "s_cmp_gt_u32 %4, 3\n\ts_waitcnt vmcnt(1)\n\ts_cbranch_scc1 2f\n1:\n\ts_waitcnt vmcnt(0)\n2:\n\t"
          "v_cmp_ne_u32 vcc, %5, v9\n\tv_cmp_ne_u32 %3, %5, v5\n\ts_or_b64 vcc, vcc, %3\n\ts_cbranch_scc0 3f\n\t"
          "s_sleep 4\n\ts_mov_b64 exec, vcc\n\tbuffer_load_dwordx4 %0, %6, %7, 0 offen sc1\n\t"
          "buffer_load_dwordx4 %1, %6, %7, 0 offen offset:128 sc1\n\ts_mov_b64 exec, -1\n\ts_sub_u32 %2, %2, 1\n\ts_cbranch_scc0 1b\n\t"
          "s_waitcnt vmcnt(0)\n3:"
          : "+{v[6:9]}"(h0), "+{v[2:5]}"(h1), "+s"(n), "=&s"(m) : "s"(h_asked), "s"(tag), "v"(hoff), "s"(rq) : "vcc", "scc", "memory");
      v0 = h0;
      v1 = h1;
    }
    TS(0, 8);
    TWB(5);
    {
      f32x2 ss, qq, pp;
#pragma unroll
      for (int i = 0; i < 6; i++) {
        const unsigned t = i < 3 ? v0[i] : v1[i - 3];
        hv[i] = f32x2{dot2(t, sel0, hv[i][0]), dot2(t, sel1, hv[i][1])};
        ss = i ? ss + hv[i] : hv[i];
        qq = i ? __builtin_elementwise_fma(hv[i], hv[i], qq) : hv[i] * hv[i];
        pp = i ? __builtin_elementwise_fma(hv[i], wf[i], pp) : hv[i] * wf[i];
      }
      float s0 = ss[0], s1 = ss[1], q0 = qq[0], q1 = qq[1], p0 = pp[0], p1 = pp[1];
      asm volatile(
          "s_nop 1\n\tv_permlane32_swap_b32 %0, %1\n\tv_permlane32_swap_b32 %2, %3\n\tv_permlane32_swap_b32 %4, %5\n\t"
          "v_add_f32 %0, %0, %1\n\tv_add_f32 %2, %2, %3\n\tv_add_f32 %4, %4, %5\n\ts_nop 0\n\t"
          "v_permlane16_swap_b32 %0, %2\n\tv_add_f32_dpp %1, %4, %4 row_ror:8 row_mask:0xf bank_mask:0xf\n\t"
          "v_add_f32 %2, %0, %2\n\ts_nop 1\n\tv_add_f32_dpp %0, %2, %2 row_ror:8 row_mask:0xf bank_mask:0xf"
          : "+v"(s0), "+v"(s1), "+v"(q0), "+v"(q1), "+v"(p0), "+v"(p1));
      *(LAS f32x2*)redw = f32x2{s0, s1};
    }
    TWB(6);
    __syncthreads();
    if (wave == 0) {
      const unsigned now = (unsigned)__builtin_amdgcn_s_memrealtime();
      const volatile LAS f32x2* rp = (const volatile LAS f32x2*)redr;
      const f32x2 t0 = rp[0], t1 = rp[32], t2 = rp[64], t3 = rp[96], t4 = rp[128], t5 = rp[160];
      const f32x2 t = ((t0 + t1) + (t2 + t3)) + (t4 + t5);
      float a = t[0], p = t[1];
      asm volatile("" : "+v"(a), "+v"(p));
      a += dppf<0x4E>(a);
      p += dppf<0x4E>(p);
      p += dppf<0xB1>(p);
      const float mv = dppf<0x00>(a) * (1.0f / DM);
      const float rinv = ln_rinv(dppf<0x55>(a), mv);
      const float dl = rbf(mxr + mv) - mxr;
      const unsigned rb = pkbf(0.f, fmaf(-dl, wsum, p) * rinv);
      const unsigned key = ((rb ^ (unsigned)(((int)rb >> 31) | (int)0x80000000)) & 0xffff0000u) | kidx;
      unsigned k1 = max(key, dppu<0x141>(key)), k2 = min(key, dppu<0x141>(key)), k3;
      {
        const unsigned lo = min(k1, dppu<0x140>(k1)), hi = max(k2, dppu<0x140>(k2));
        k1 = max(k1, dppu<0x140>(k1));
        k2 = max(lo, hi);
        k3 = min(lo, hi);
      }
      {
        const unsigned lo = min(k1, dppu<0x142>(k1)), hi = max(k2, dppu<0x142>(k2)), w = max(k3, dppu<0x142>(k3));
        k1 = max(k1, dppu<0x142>(k1));
        k2 = max(lo, hi);
        k3 = max(min(lo, hi), w);
      }
      {
        const unsigned lo = min(k1, dppu<0x143>(k1)), hi = max(k2, dppu<0x143>(k2)), w = max(k3, dppu<0x143>(k3));
        k1 = max(k1, dppu<0x143>(k1));
        k2 = max(lo, hi);
        k3 = max(min(lo, hi), w);
      }
      if (lane >= 64 - 8 * NKS) stg16(rs, kro, u32x4{k1, k2 | 1u, __builtin_amdgcn_perm(k3, now, 0x07060100u), tag});
      SCHED_FENCE();
      const unsigned second = (unsigned)__builtin_amdgcn_readlane((int)k2, 63);
      if ((lane & 3) == 0) {
        LAS float* ri = (LAS float*)(L + SM_ROW + (lane >> 3) * 16 + (lane & 4));
        ri[0] = -dl * rinv;
        ri[2] = rinv;
        const unsigned fk = key | (key > second ? 0u : (key == second ? 1u : 2u));
        *(LAS unsigned*)(L + SM_ROW + 128 + lane) = fk;
        __builtin_amdgcn_raw_buffer_store_b64(u32x2{fk, tag}, rs, WS_KF + b * 128 + lane * 2, 0, SC1);
      }
      __builtin_amdgcn_s_setprio(0);
    }
    TS(0, 9);
    TWB(7);
    __syncthreads();
    {
      const f32x4 ri = *(const LAS f32x4*)(L + SM_ROW + (tid & 7) * 16);
      const u32x2 kk = *(const LAS u32x2*)(L + SM_ROW + 128 + (tid & 7) * 8);
      const f32x2 c2 = {ri[0], ri[1]}, r2 = {ri[2], ri[3]};
      unsigned wp[6];
#pragma unroll
      for (int i = 0; i < 6; i++) {
        const f32x2 w2 = __builtin_elementwise_fma(hv[i], r2, c2);
        wp[i] = pkbf(w2[0], w2[1]);
      }
#pragma unroll
      for (int i = 0; i < 2; i++)
        if (!(kk[i] & 2)) {
          u32x3 o;
#pragma unroll
          for (int k = 0; k < 3; k++) o[k] = min(__builtin_amdgcn_perm(wp[2 * k + 1], wp[2 * k], i ? 0x07060302u : 0x05040100u), 0xfffffffeu);
#pragma unroll
          for (int c = 1; c <= NC; c++) __builtin_amdgcn_raw_buffer_store_b96(o, rs, xso + (int)(kk[i] & 1) * XSS + c * (DM * 2), 0, SC1);
        }
      SCHED_FENCE();
      LAS char* dst = L + SM_T2 + (tid & 7) * T2S + (tid >> 3) * 48;
#pragma unroll
      for (int i = 0; i < 6; i += 2) *(LAS u32x4*)(dst + 8 * i) = u32x4{i < 3 ? v0[i] : v1[i - 3], wp[i], i < 2 ? v0[i + 1] : v1[i - 2], wp[i + 1]};
    }
    __syncthreads();
    const int pr = tid / 72, ck = tid - pr * 72;
    u32x4 hq[2], wq[2];
    u32x2 key = {2u, 2u};
    auto put_row = [&](int i, int slot) {
      const int o = key_addr(key[i], slot) + ck * 16;
      const u32x4 cap = {0xfffffffeu, 0xfffffffeu, 0xfffffffeu, 0xfffffffeu};
      if (key[i] & 2) {
#pragma unroll
        for (int c = 1; c <= NC; c++) stg16(rs, o + c * (DM * 2), __builtin_elementwise_min(wq[i], cap));
      }
      stg16(rs, o, __builtin_elementwise_min(hq[i], cap));
    };
    auto put_out = [&](int i) {
      u32x4 o;
#pragma unroll
      for (int k = 0; k < 4; k++) {
        const f32x2 o2 = unpk(hq[i][k]) + unpk(wq[i][k]);
        o[k] = pkbf(o2[0], o2[1]);
      }
      __builtin_amdgcn_raw_buffer_store_b128(o, ro, (16 * b + 2 * pr + i) * (DM * 2) + ck * 16, 0, OAUX);
    };
    if (wave < 9) {
      key = *(const LAS u32x2*)(L + SM_ROW + 128 + pr * 8);
#pragma unroll
      for (int k = 0; k < 4; k++) {
        const u32x4 t = *(const LAS u32x4*)(L + SM_T2 + pr * T2S + ck * 64 + k * 16);
        hq[0][k] = __builtin_amdgcn_perm(t[2], t[0], 0x05040100u);
        hq[1][k] = __builtin_amdgcn_perm(t[2], t[0], 0x07060302u);
        wq[0][k] = __builtin_amdgcn_perm(t[3], t[1], 0x05040100u);
        wq[1][k] = __builtin_amdgcn_perm(t[3], t[1], 0x07060302u);
      }
#pragma unroll
      for (int i = 0; i < 2; i++) {
        if (!(key[i] & 2)) put_row(i, 0);
        else if (EO) put_out(i);
      }
    } else if (wave == 10) {
      select_rows();
    }
    __syncthreads();
    TS(0, 10);
    if (wave < 9) {
#pragma unroll
      for (int i = 0; i < 2; i++) {
        int slot = 8;
#pragma unroll
        for (int r = 0; r < 8; r++) slot = selp[r] == key[i] ? r : slot;
        if (slot < 8) {
          if (key[i] & 2) {
            DRAIN();
            put_row(i, slot);
          }
        } else if (!EO || !(key[i] & 2)) {
          put_out(i);
        }
      }
    }
  } else {
    __syncthreads();
    if (wave == 10) select_rows();
  }
  if (is_ln && b >= RED1) {
    TS(0, 12);
    TC(0, 15);
    __builtin_amdgcn_endpgm();
  }
  {
    const int co = 8 * rb + 4 * (lane & 1);
    unsigned hv = 0u, wv = 0u, oo = 0u;
    float sg = 0.f;
    LAS float* red = (LAS float*)(L + SM_RED);
    LAS char* rw = (LAS char*)red + (wave < 10 ? wave * 128 + (lane & 7) * 16 + (lane >> 4) * 4 : lane * 8);
    int part_off = WS_PART + (tid >> 3) * (8 * DM * 4) + rb * 128 + (tid & 7) * 16;
    asm volatile("" : "+v"(rw), "+v"(part_off));
    f32x2 hf = {0.f, 0.f}, wf = {0.f, 0.f};
    int o = 0;
    auto get_rows = [&]() {
      asm volatile("buffer_load_dword %0, %2, %3, 0 offen offset:1152 sc1\n\tbuffer_load_dword %1, %2, %3, 0 offen sc1"
                   : "+v"(wv), "+v"(hv) : "v"(o), "s"(rq) : "memory");
    };
    if (wave == 10) {
      const int slot = (lane >> 1) & 7;
      const unsigned key = *(volatile LAS unsigned*)(selp + slot);
      o = key_addr(key, slot) + co;
      get_rows();
      const unsigned sb = key ^ ((int)key < 0 ? 0x80000000u : 0xffffffffu);
      sg = rbf(__builtin_amdgcn_rcpf(1.0f + __expf(-bfhi(sb))));
      oo = (unsigned)(key_row(key) * (DM * 2) + co);
      asm volatile("" : "+v"(sg), "+v"(oo));
      while ((int)((unsigned)__builtin_amdgcn_s_memrealtime() - t_key) < DLY_C) __builtin_amdgcn_s_sleep(1);
    }
    __syncthreads();
    TS(0, 11);
    if (wave < 10) {
      u32x4 v, w;
      asm volatile("buffer_load_dwordx4 %0, %2, %3, 0 offen sc1\n\ts_sleep %4\n\tbuffer_load_dwordx4 %1, %2, %3, 0 offen sc1"
                   : "=&v"(v), "=&v"(w) : "v"(part_off), "s"(rq), "n"(PPS) : "memory");
      u32x4 got = {~0u, 0u, 0u, 0u};
      _Pragma("unroll 1") for (int spin = 0; spin < SPIN_CAP; spin++) {
        asm volatile("s_waitcnt vmcnt(1)" : "+v"(v));
        if (got[0] == ~0u) got = v;
        if (__all(got[0] != ~0u)) break;
        if (got[0] == ~0u) asm volatile("buffer_load_dwordx4 %0, %1, %2, 0 offen sc1" : "+v"(v) : "v"(part_off), "s"(rq) : "memory");
        asm volatile("s_waitcnt vmcnt(1)" : "+v"(w));
        if (got[0] == ~0u) got = w;
        if (__all(got[0] != ~0u)) break;
        if (got[0] == ~0u) asm volatile("buffer_load_dwordx4 %0, %1, %2, 0 offen sc1" : "+v"(w) : "v"(part_off), "s"(rq) : "memory");
      }
#define v got
      auto r0 = __builtin_amdgcn_permlane32_swap(v[0], v[2], false, false);
      auto r1 = __builtin_amdgcn_permlane32_swap(v[1], v[3], false, false);
      const float s0 = __uint_as_float(r0[0]) + __uint_as_float(r0[1]), s1 = __uint_as_float(r1[0]) + __uint_as_float(r1[1]);
      auto r2 = __builtin_amdgcn_permlane16_swap(__float_as_uint(s0), __float_as_uint(s1), false, false);
      float a = __uint_as_float(r2[0]) + __uint_as_float(r2[1]);
      a += dppf<0x128>(a);
      *(LAS float*)rw = a;
#undef v
      asm volatile("" : : "v"(v), "v"(w));
    } else if (wave == 10) {
      _Pragma("unroll 1") for (int spin = 0; spin < SPIN_CAP; spin++) {
        asm volatile("s_waitcnt vmcnt(0)" : "+v"(wv), "+v"(hv));
        if (__all(wv != ~0u && hv != ~0u)) break;
        __builtin_amdgcn_s_sleep(4);
        get_rows();
      }
      asm volatile("s_waitcnt vmcnt(0)" : "+v"(wv), "+v"(hv));
      hf = unpk(hv);
      wf = unpk(wv);
      asm volatile("" : "+v"(hf), "+v"(wf));
    }
    __syncthreads();
    if (wave == 10 && lane < 16) {
      f32x2 p[10];
#pragma unroll
      for (int w = 0; w < 10; w++) p[w] = *(const volatile LAS f32x2*)(rw + w * 128);
      const f32x2 y = (((p[0] + p[1]) + (p[2] + p[3])) + (p[8] + p[9])) + ((p[4] + p[5]) + (p[6] + p[7]));
      const f32x2 y2 = rbf2(rbf2(y) * sg);
      const f32x2 o2 = hf + rbf2(wf + y2);
      __builtin_amdgcn_raw_buffer_store_b32(pkbf(o2[0], o2[1]), ro, oo, 0, OAUX);
      if (b == 0 && lane == 0) __builtin_amdgcn_raw_buffer_store_b32(tag, rs, WS_EPOCH, 0, SC1);
    }
  }
  TS(0, 12);
  TC(0, 15);
  asm volatile("s_endpgm" ::: "memory");
  __builtin_amdgcn_s_waitcnt(0);
  }
  }
  ffn_role(argp, ftag, L);
}

}

extern "C" void kernel_launch(void* const* d_in, const int* in_sizes, int n_in, void* d_out, int out_size,
                              void* d_ws, size_t ws_size, hipStream_t stream) {
  k_all<<<NCONV + NFFN, NTHR, 0, stream>>>((const u16*)d_in[0], (const u16*)d_in[1], (const u16*)d_in[2], (const u16*)d_in[3],
                                             (const u16*)d_in[4], (const u16*)d_in[5], (char*)d_ws, (const u16*)d_in[6],
                                             (const u16*)d_in[8], (const u16*)d_in[9], (const u16*)d_in[10], (const u16*)d_in[11],
                                             (u16*)d_out);
}
```

```cpp
#include <hip/hip_runtime.h>
#include <stdint.h>
#include <type_traits>

#define J4(a, b, c, d) (unsigned long long)((a) + 1 + (((b) + 1) << 5) + (((c) + 1) << 10) + (((d) + 1) << 15))
#define JOBS0 (J4(12, -1, -1, -1) | J4(13, -1, -1, -1) << 40)
#define JOBS1 (J4(0, 3, 8, 11) | J4(1, 4, 9, -1) << 20 | J4(2, 5, 10, -1) << 40)
#define JOBS2 (J4(6, 14, -1, -1) << 20 | J4(7, 15, -1, -1) << 40)
namespace {

constexpr int SEQ = 2048;
constexpr int DM = 576;
constexpr int NM = 128;
constexpr int FF = 4608;

constexpr int NTHR = 768;
constexpr int NFFN = 64;
constexpr int NPART = 80;
constexpr int NCONV = 192;
constexpr int NLN = 128;
constexpr int RED1 = 96;
constexpr int LATE0 = 176;

typedef unsigned short u16;
#define LAS __attribute__((address_space(3)))
#define GAS __attribute__((address_space(1)))
#define CAS __attribute__((address_space(4)))
using half8 = __attribute__((ext_vector_type(8))) _Float16;
using half2v = __attribute__((ext_vector_type(2))) _Float16;
using bf2v = __attribute__((ext_vector_type(2))) __bf16;
using bf8v = __attribute__((ext_vector_type(8))) __bf16;
using bf16v = __attribute__((ext_vector_type(16))) __bf16;
using s16x8 = __attribute__((ext_vector_type(8))) short;
using s16x4 = __attribute__((ext_vector_type(4))) short;
using f32x2 = __attribute__((ext_vector_type(2))) float;
using f32x4 = __attribute__((ext_vector_type(4))) float;
using u32x2 = __attribute__((ext_vector_type(2))) unsigned;
using u32x3 = __attribute__((ext_vector_type(3))) unsigned;
typedef u32x3 u32x3u __attribute__((aligned(4)));
using u32x4 = __attribute__((ext_vector_type(4))) unsigned;
using u64x2 = __attribute__((ext_vector_type(2))) unsigned long long;
using u64x8 = __attribute__((ext_vector_type(8))) unsigned long long;
using u64x4 = __attribute__((ext_vector_type(4))) unsigned long long;

constexpr int WS_EPOCH = 0;
constexpr int WS_KR = 131072;
constexpr int WS_KF = 4096;
constexpr int WS_ST = WS_KF + 16384;
constexpr int WS_CK = WS_ST + 16384;
constexpr int WS_UP = 262144;
constexpr int WS_ZP = WS_UP + NCONV * NLN * 128;
constexpr int WS_PART = WS_ZP + NCONV * NLN * 128;
constexpr int WS_XS = WS_PART + NPART * 8 * DM * 4;
constexpr int XSS = 4 * DM;
constexpr int WS_END = WS_XS + (2 * NLN + 8) * XSS;
static_assert(WS_CK + 16 * 2048 <= WS_KR, "ws");
constexpr int SC1 = 16;
constexpr int SPIN_CAP = 1 << 16;
constexpr int DLY_H = 225, HE5 = 5, HE4 = 25, HE3 = 40, HB5 = 72, HB4 = 48;
constexpr int DLY_K = 353, DLY_L = DLY_K + 30;
constexpr int DLY_C = 195, DLY_F = 166, PRM = 20, PEN = 50;
constexpr int KPP = 4, PPS = 6, PSL = 1, PSE = 1, LSD = 6;
constexpr int MPR = 0x1233, WPR = 0, PPR = 3, SPR = 3;
constexpr int SELW = 8, LSW = 8, NKS = 2, ED = 3, PFM = 8;

__device__ __forceinline__ float bflo(unsigned v) { return __uint_as_float(v << 16); }
__device__ __forceinline__ float bfhi(unsigned v) { return __uint_as_float(v & 0xffff0000u); }
__device__ __forceinline__ unsigned pkbf(float a, float b) {
  f32x2 f = {a, b};
  return __builtin_bit_cast(unsigned, __builtin_convertvector(f, bf2v));
}
__device__ __forceinline__ unsigned pkh(float a, float b) {
  f32x2 f = {a, b};
  return __builtin_bit_cast(unsigned, __builtin_convertvector(f, half2v));
}
__device__ __forceinline__ float rbf(float f) { return __uint_as_float(pkbf(0.f, f)); }
__device__ __forceinline__ f32x2 rbf2(f32x2 v) { return f32x2{rbf(v[0]), rbf(v[1])}; }
__device__ __forceinline__ f32x2 unpk(unsigned v) { return f32x2{bflo(v), bfhi(v)}; }
__device__ __forceinline__ float dot2(unsigned a, unsigned b, float c) {
  return __builtin_amdgcn_fdot2_f32_bf16(__builtin_bit_cast(bf2v, a), __builtin_bit_cast(bf2v, b), c, false);
}
__device__ __forceinline__ float hadd(f32x2 v) {
  float a = v[0], b = v[1];
  asm volatile("" : "+v"(a), "+v"(b));
  return a + b;
}

template <int CTRL>
__device__ __forceinline__ float dppf(float v) {
  return __int_as_float(__builtin_amdgcn_update_dpp(0, __float_as_int(v), CTRL, 0xf, 0xf, true));
}
template <int CTRL>
__device__ __forceinline__ unsigned dppu(unsigned v) {
  return (unsigned)__builtin_amdgcn_update_dpp(0, (int)v, CTRL, 0xf, 0xf, true);
}
__device__ __forceinline__ float swap16_sum(float v) {
  auto r = __builtin_amdgcn_permlane16_swap(__float_as_uint(v), __float_as_uint(v), false, false);
  return __uint_as_float(r[0]) + __uint_as_float(r[1]);
}
__device__ __forceinline__ float swap32_sum(float v) {
  auto r = __builtin_amdgcn_permlane32_swap(__float_as_uint(v), __float_as_uint(v), false, false);
  return __uint_as_float(r[0]) + __uint_as_float(r[1]);
}
__device__ __forceinline__ float red32_sum(float v) {
  v += dppf<0xB1>(v);
  v += dppf<0x4E>(v);
  v += dppf<0x141>(v);
  v += dppf<0x140>(v);
  return swap16_sum(v);
}
#define SCHED_FENCE() __builtin_amdgcn_sched_barrier(0)
#define DRAIN() asm volatile("s_waitcnt vmcnt(0)" ::: "memory")

__device__ __forceinline__ float red16_sum(float v) {
  v += dppf<0xB1>(v);
  v += dppf<0x4E>(v);
  v += dppf<0x141>(v);
  return v + dppf<0x140>(v);
}
__device__ __forceinline__ float ln_rinv(float q, float dm) {
  const float var = fmaxf(fmaf(q, 1.0f / DM, -dm * dm), 0.f);
  return __builtin_amdgcn_rcpf(rbf(__builtin_amdgcn_sqrtf(rbf(rbf(var) + 1e-5f))));
}

constexpr int TAB_STRIDE = 544;
constexpr int TAB_ROWS = 58;
constexpr int TR_M = 26, TR_F = 34, TR_H = 42;
constexpr int TABB = TAB_ROWS * TAB_STRIDE;
constexpr int SM_KC = 3 * TABB;
constexpr int RDS = 336 * 4;
constexpr int KC_RD = SM_KC;
constexpr int KC_R5 = KC_RD + 4 * RDS;
constexpr int KC_R3 = KC_R5 + 64 * 4;
constexpr int KC_R9 = KC_R3 + 64 * 4;
constexpr int KC_R7 = KC_R9 + 96 * 4;
constexpr int SM_US = KC_R7 + 96 * 4;
constexpr int UPAD = 256;
constexpr int CH2 = SM_US + (UPAD + SEQ) * 2 - SM_KC;
static_assert(SM_US + 2 * UPAD <= SM_KC + 8192 && 8192 <= CH2 && 2 * TABB + 256 < 65536, "zero fill");
constexpr int YT = 1280, YC = 3 * YT, YS = 3 * YC;
constexpr int SM_T2 = 0, T2S = 4624;
constexpr int SM_RED = SM_KC + 3 * CH2;
constexpr int SM_ROW = SM_RED + 3200;
constexpr int SM_ISC = SM_ROW + 128 + 128 + 32;
constexpr int SM_MX = SM_ISC + 16;
constexpr int SM_WR = SM_MX + 32;
constexpr int SM_DUMMY = SM_WR + DM * 2;
constexpr int SM_PRG = SM_DUMMY + 256;
constexpr int RG = 8192;
constexpr int FS_X = 18 * RG;
constexpr int FS_SEL = FS_X + 9216;
constexpr int FS_GU = FS_SEL + 16;
constexpr int SM_TOTAL = FS_GU + 8 * 160;
static_assert(SM_PRG + 64 <= SM_TOTAL && 8 * YS <= 3 * TABB && SM_ISC % 16 == 0 && CH2 % 16 == 0 && TABB % 16 == 0, "lds");

typedef f32x2 cf;
#define TM(t, a, b) "v_pk_mul_f32 v[" #t ":" #t "+1], v[" #b ":" #b "+1], v[" #a ":" #a "+1] op_sel:[1,0]\n\t"
#define TF(d, a, b, t, n) \
  "v_pk_fma_f32 v[" #d ":" #d "+1], v[" #a ":" #a "+1], v[" #b ":" #b "+1], v[" #t ":" #t "+1] op_sel:[0,0,1] op_sel_hi:[1,0,0] neg_" #n ":[0,0,1]\n\t"
#define TC16(c, v) "v_cvt_pk_f16_f32 v" #c ", v[" #v "], v[" #v "+1]\n\t"
#define TWR(c, row) "ds_write_addtid_b32 v" #c " offset:544*" #row "\n\t"
#define TAB_A \
  TC16(134, 100) TM(106, 102, 102) TM(108, 100, 104) TWR(134, 0) TF(110, 102, 102, 106, lo) TF(112, 100, 104, 108, lo) \
  TM(114, 100, 102) TM(116, 100, 110) TC16(135, 112) TF(118, 100, 102, 114, lo) TF(120, 100, 110, 116, lo) TWR(135, 8) \
  TC16(136, 118) TC16(137, 120) TM(122, 112, 110) TWR(136, 1) TM(124, 118, 104) TF(126, 112, 110, 122, lo) TWR(137, 2) \
  TF(128, 118, 104, 124, lo) TC16(134, 126) TM(130, 126, 110) TC16(135, 128) TWR(134, 10) TF(132, 126, 110, 130, lo) \
  TM(106, 118, 110) TWR(135, 9) TC16(136, 132) TF(108, 118, 110, 106, lo) TM(114, 132, 104) TWR(136, 12) TC16(137, 108) \
  TF(102, 132, 104, 114, lo) TM(116, 132, 110) TWR(137, 3) TC16(134, 102) TF(100, 132, 110, 116, lo) TM(122, 102, 104) TWR(134, 18) \
  TC16(135, 100) TF(124, 102, 104, 122, lo) TM(130, 128, 110) TWR(135, 14) TC16(136, 124) TF(106, 128, 110, 130, lo) \
  TM(118, 124, 104) TWR(136, 19) TC16(137, 106) TF(114, 124, 104, 118, lo) TM(116, 100, 104) TWR(137, 11) TC16(134, 114) \
  TF(132, 100, 104, 116, lo) TM(122, 114, 104) TWR(134, 26) TC16(135, 132) TF(102, 114, 104, 122, lo) TM(130, 120, 110) \
  TWR(135, 17) TC16(136, 102) TF(128, 120, 110, 130, lo) TM(118, 108, 110) TWR(136, 28) TC16(137, 128) TF(124, 108, 110, 118, lo) \
  TM(116, 106, 110) TWR(137, 4) TC16(134, 124) TF(100, 106, 110, 116, lo) TM(122, 112, 104) TWR(134, 5) TC16(135, 100) \
  TF(114, 112, 104, 122, lo) TM(130, 126, 104) TWR(135, 13) TC16(136, 114) TF(120, 126, 104, 130, lo) TM(118, 132, 110) \
  TWR(136, 21) TC16(137, 120) TF(108, 132, 110, 118, lo) TM(116, 102, 104) TWR(137, 20) TC16(134, 108) TF(106, 102, 104, 116, lo) \
  TM(122, 128, 110) TWR(134, 16) TC16(135, 106) TF(112, 128, 110, 122, lo) TM(130, 124, 110) TWR(135, 30) TC16(136, 112) \
  TF(126, 124, 110, 130, lo) TM(118, 100, 110) TWR(136, 6) TC16(137, 126) TF(116, 100, 110, 118, lo) TM(102, 114, 104) TWR(137, 7) \
  TC16(134, 116) TF(122, 114, 104, 102, lo) TM(128, 120, 104) TWR(134, 15) TC16(135, 122) TF(112, 120, 104, 128, lo) \
  TM(130, 132, 104) TWR(135, 23) TC16(136, 112) TF(124, 132, 104, 130, lo) TM(126, 108, 104) TWR(136, 22) TC16(137, 124) \
  TF(118, 108, 104, 126, lo) TM(110, 106, 104) TWR(137, 25) TC16(134, 118) TF(100, 106, 104, 110, lo) TWR(134, 24) TC16(135, 100) \
  TWR(135, 32)
#define TAB_B \
  "v_mov_b32 v137, 0x3c00\n\t" TWR(137, 42) TC16(134, 102) TM(106, 104, 104) TM(108, 100, 102) TWR(134, 43) \
  TF(110, 104, 104, 106, lo) TF(112, 100, 102, 108, hi) TM(114, 102, 102) TM(116, 110, 110) TF(118, 110, 110, 116, lo) \
  TF(120, 102, 102, 114, lo) TC16(135, 120) TM(122, 112, 118) TM(124, 120, 102) TWR(135, 44) TF(126, 112, 118, 122, lo) \
  TF(128, 120, 102, 124, lo) TM(130, 120, 120) TC16(136, 126) TC16(137, 128) TF(132, 120, 120, 130, lo) TWR(136, 27) \
  TM(106, 126, 104) TC16(134, 132) TWR(137, 45) TF(108, 126, 104, 106, lo) TM(100, 126, 110) TWR(134, 46) TC16(135, 108) \
  TF(116, 126, 110, 100, lo) TM(114, 108, 110) TWR(135, 29) TC16(136, 116) TF(122, 108, 110, 114, lo) TM(112, 102, 132) \
  TWR(136, 31) TC16(137, 122) TF(124, 102, 132, 112, lo) TM(130, 120, 132) TWR(137, 33) TC16(134, 124) TF(106, 120, 132, 130, lo) \
  TM(104, 128, 132) TWR(134, 47) TC16(135, 106) TF(100, 128, 132, 104, lo) TM(114, 132, 132) TWR(135, 48) TC16(136, 100) \
  TF(110, 132, 132, 114, lo) TM(112, 124, 132) TWR(136, 49) TC16(137, 110) TF(102, 124, 132, 112, lo) TM(130, 106, 132) \
  TWR(137, 50) TC16(134, 102) TF(120, 106, 132, 130, lo) TM(104, 100, 132) TWR(134, 51) TC16(135, 120) TF(128, 100, 132, 104, lo) \
  TM(114, 126, 118) TWR(135, 52) TC16(136, 128) TF(112, 126, 118, 114, lo) TM(124, 108, 118) TWR(136, 53) TC16(137, 112) \
  TF(130, 108, 118, 124, lo) TM(106, 116, 118) TWR(137, 34) TC16(134, 130) TF(104, 116, 118, 106, lo) TM(100, 122, 118) \
  TWR(134, 35) TC16(135, 104) TF(114, 122, 118, 100, lo) TM(126, 110, 132) TWR(135, 36) TC16(136, 114) TF(124, 110, 132, 126, lo) \
  TM(108, 102, 132) TWR(136, 37) TC16(137, 124) TF(106, 102, 132, 108, lo) TM(116, 120, 132) TWR(137, 54) TC16(134, 106) \
  TF(100, 120, 132, 116, lo) TM(122, 128, 132) TWR(134, 55) TC16(135, 100) TF(126, 128, 132, 122, lo) TM(110, 112, 118) \
  TWR(135, 56) TC16(136, 126) TF(124, 112, 118, 110, lo) TM(108, 130, 118) TWR(136, 57) TC16(137, 124) TF(102, 130, 118, 108, lo) \
  TM(106, 104, 118) TWR(137, 38) TC16(134, 102) TF(116, 104, 118, 106, lo) TM(120, 114, 118) TWR(134, 39) TC16(135, 116) \
  TF(100, 114, 118, 120, lo) TWR(135, 40) TC16(136, 100) TWR(136, 41)
#define TAB_CLOB "v106", "v107", "v108", "v109", "v110", "v111", "v112", "v113", "v114", "v115", "v116", "v117", "v118", "v119", "v120", "v121", "v122", "v123", "v124", "v125", "v126", "v127", "v128", "v129", "v130", "v131", "v132", "v133", "v134", "v135", "v136", "v137"
__device__ __forceinline__ unsigned pkc(cf a) { return __builtin_bit_cast(unsigned, __builtin_convertvector(a, half2v)); }
__device__ __forceinline__ cf cexp(f32x2 hi, f32x2 lo) {
  float mag = __builtin_amdgcn_exp2f(hi[0]);
  mag = fmaf(mag, lo[0], mag);
  const float fr = __builtin_amdgcn_fractf(hi[1]) + lo[1];
  return cf{__builtin_amdgcn_cosf(fr), __builtin_amdgcn_sinf(fr)} * mag;
}
__device__ __forceinline__ unsigned wave_max_u32(unsigned v) {
  v = max(v, (unsigned)__builtin_amdgcn_update_dpp(0, (int)v, 0x111, 0xf, 0xf, false));
  v = max(v, (unsigned)__builtin_amdgcn_update_dpp(0, (int)v, 0x112, 0xf, 0xf, false));
  v = max(v, (unsigned)__builtin_amdgcn_update_dpp(0, (int)v, 0x114, 0xf, 0xf, false));
  v = max(v, (unsigned)__builtin_amdgcn_update_dpp(0, (int)v, 0x118, 0xf, 0xf, false));
  v = max(v, (unsigned)__builtin_amdgcn_update_dpp(0, (int)v, 0x142, 0xa, 0xf, false));
  v = max(v, (unsigned)__builtin_amdgcn_update_dpp(0, (int)v, 0x143, 0xc, 0xf, false));
  return (unsigned)__builtin_amdgcn_readlane((int)v, 63);
}

#define MFMA16(a, b, c) __builtin_amdgcn_mfma_f32_16x16x32_f16((a), (b), (c), 0, 0, 0)
#define MFMAB(a, b, c) __builtin_amdgcn_mfma_f32_16x16x32_bf16((a), (b), (c), 0, 0, 0)

__device__ __forceinline__ half8 ld_frag(const LAS char* p) { return *(const LAS half8*)p; }
template <int O2>
__device__ __forceinline__ half8 ld_toep(const LAS unsigned* q) {
  u32x4 v = {q[0], q[1], q[O2], q[O2 + 1]};
  return __builtin_bit_cast(half8, v);
}
__device__ __forceinline__ half8 band_frag(int lane, unsigned pat) {
  unsigned t = __builtin_amdgcn_ubfe(pat << (lane & 15), 8 * (lane >> 4), 8);
  t |= t << 15;
  u32x4 v;
#pragma unroll
  for (int j = 0; j < 4; j++) v[j] = __umul24(t & (0x10001u << (2 * j)), 0x3c00u >> (2 * j));
  return __builtin_bit_cast(half8, v);
}
__device__ __forceinline__ half8 pack_win(f32x4 a, f32x4 b) {
  u32x4 v = {pkh(a[0], a[1]), pkh(a[2], a[3]), pkh(b[0], b[1]), pkh(b[2], b[3])};
  return __builtin_bit_cast(half8, v);
}

__device__ __forceinline__ void set_prio(int p) {
  if (p == 0) __builtin_amdgcn_s_setprio(0);
  if (p == 1) __builtin_amdgcn_s_setprio(1);
  if (p == 2) __builtin_amdgcn_s_setprio(2);
  if (p == 3) __builtin_amdgcn_s_setprio(3);
}
template <int V>
using IC = std::integral_constant<int, V>;
template <int S, int E, class F>
__device__ __forceinline__ void static_for(F&& f) {
  if constexpr (S < E) {
    f(std::integral_constant<int, S>{});
    static_for<S + 1, E>(f);
  }
}
#define VR(r) "v[" #r ":" #r "+3]"
#define DT(r, o) "ds_read_b64 v[" #r ":" #r "+1], v23 offset:" #o "\n\tds_read_b64 v[" #r "+2:" #r "+3], v23 offset:" #o "+8\n\t"
#define DP(r, o) "ds_read2_b32 v[" #r ":" #r "+1], v24 offset0:" #o " offset1:" #o "+1\n\t"
#define DU(r, o) "ds_read_b128 " VR(r) ", v25 offset:" #o "\n\t"
#define DW(n) "s_waitcnt lgkmcnt(" #n ")\n\t"
#define DM(c, a, b) "v_mfma_f32_16x16x32_f16 " VR(c) ", " VR(a) ", " VR(b) ", " VR(c) "\n\t"
#define DZ(c, a, b) "v_mfma_f32_16x16x32_f16 " VR(c) ", " VR(a) ", " VR(b) ", 0\n\t"
#define DC(d, w) "v_cvt_pk_f16_f32 v" #d ", v" #w ", v[" #w "+1]\n\t"
#define DY(c, t) "ds_write_b128 v26, " VR(c) " offset:11520*" #t "\n\t"
#define DA "v_add_u32 v26, 4*11520, v26\n\t"
#define DV(n) "v_mov_b32 v22, " #n "\n\t"
#define DF "ds_write_b32 v27, v22\n\t"
#define DFE "ds_read_u16 v22, v22\n\t"
#define DFP "v_lshl_or_b32 v22, v22, 16, v22\n\t"
#define DTE(p) "v_pk_mul_f16 v" #p ", v" #p ", v22\n\t"
#define DBAR "s_waitcnt vmcnt(0)\n\ts_barrier\n\t"
#define DENSE \
  DT(56, 576) DT(88, 64) DT(52, 640) DT(84, 128) DT(48, 704) DT(80, 192) DT(44, 768) DT(76, 256) DT(40, 832) DT(72, 320) DT(36, 896) \
  DT(68, 384) DT(32, 960) DT(64, 448) DT(28, 1024) DT(60, 512) DT(92, 0) DBAR DU(104, 576) DU(108, 640) DU(112, 704) DU(116, 768) DFE DW(4) \
  DZ(124, 56, 104) DU(120, 832) DU(96, 896) DZ(128, 88, 104) DW(5) DM(124, 52, 108) DU(100, 960) DU(104, 1024) DM(128, 84, 108) DU(14, 1008) \
  DW(7) DM(124, 48, 112) DU(108, 1088) DM(128, 80, 112) DW(6) DM(124, 44, 116) DU(112, 1152) DM(128, 76, 116) DFP DW(2) DM(124, 40, 120) \
  DU(116, 1216) DM(128, 72, 120) DTE(10) DZ(14, 14, 2) DTE(11) DM(124, 36, 96) DU(120, 1280) DM(128, 68, 96) DTE(12) DM(124, 32, 100) \
  DU(96, 1344) DM(128, 64, 100) DTE(13) DM(124, 28, 104) DU(100, 1408) DM(128, 60, 104) DC(144, 14) DZ(132, 92, 104) DC(145, 16) \
  DZ(136, 10, 104) DZ(18, 104, 6) DW(5) DM(128, 56, 108) DU(104, 1472) DM(132, 88, 108) DU(14, 1520) DW(6) DM(128, 52, 112) DU(108, 1536) \
  DM(132, 84, 112) DV(1) DW(6) DM(128, 48, 116) DU(112, 1600) DM(132, 80, 116) DY(124, 0) DW(7) DM(128, 44, 120) DU(116, 1664) \
  DM(132, 76, 120) DF DW(5) DM(128, 40, 96) DU(120, 1728) DM(132, 72, 96) DZ(14, 14, 2) DM(128, 36, 100) DU(96, 1792) DM(132, 68, 100) \
  DC(148, 18) DM(128, 32, 104) DU(100, 1856) DM(132, 64, 104) DC(149, 20) DW(7) DM(128, 28, 108) DU(104, 1920) DM(132, 60, 108) DC(146, 14) \
  DZ(124, 92, 108) DC(147, 16) DZ(140, 10, 108) DZ(18, 108, 6) DW(7) DM(132, 56, 112) DU(108, 1984) DM(124, 88, 112) DU(14, 2032) DW(7) \
  DM(132, 52, 116) DU(112, 2048) DM(124, 84, 116) DV(2) DW(6) DM(132, 48, 120) DU(116, 2112) DM(124, 80, 120) DY(128, 1) DW(7) \
  DM(132, 44, 96) DU(120, 2176) DM(124, 76, 96) DF DW(5) DM(132, 40, 100) DU(96, 2240) DM(124, 72, 100) DZ(14, 14, 2) DM(132, 36, 104) \
  DU(100, 2304) DM(124, 68, 104) DC(150, 18) DM(132, 32, 108) DU(104, 2368) DM(124, 64, 108) DC(151, 20) DW(7) DM(132, 28, 112) \
  DU(108, 2432) DM(124, 60, 112) DC(152, 14) DM(136, 92, 112) DC(153, 16) DZ(128, 10, 112) DZ(18, 112, 6) DW(7) DM(124, 56, 116) \
  DU(112, 2496) DM(136, 88, 116) DU(14, 2544) DW(7) DM(124, 52, 120) DU(116, 2560) DM(136, 84, 120) DV(3) DW(6) DM(124, 48, 96) \
  DU(120, 2624) DM(136, 80, 96) DY(132, 2) DW(7) DM(124, 44, 100) DU(96, 2688) DM(136, 76, 100) DF DW(5) DM(124, 40, 104) DU(100, 2752) \
  DM(136, 72, 104) DZ(14, 14, 2) DM(124, 36, 108) DU(104, 2816) DM(136, 68, 108) DC(156, 18) DM(124, 32, 112) DU(108, 2880) DM(136, 64, 112) \
  DC(157, 20) DW(7) DM(124, 28, 116) DU(112, 2944) DM(136, 60, 116) DC(154, 14) DM(140, 92, 116) DC(155, 16) DZ(132, 10, 116) DZ(18, 116, 6) \
  DW(7) DM(136, 56, 120) DU(116, 3008) DP(10, 32) DM(140, 88, 120) DP(8, 24) DW(8) DM(136, 52, 96) DU(120, 3072) DV(4) DM(140, 84, 96) \
  DC(158, 18) DP(6, 16) DW(8) DM(136, 48, 100) DU(96, 3136) DC(159, 20) DM(140, 80, 100) DY(124, 3) DW(9) DM(136, 44, 104) DU(100, 3200) DF \
  DM(140, 76, 104) DP(20, 128) DW(11) DM(136, 40, 108) DU(104, 3264) DP(18, 120) DM(140, 72, 108) DP(16, 112) DW(10) DM(136, 36, 112) \
  DU(108, 3328) DM(140, 68, 112) DP(4, 8) DM(136, 144, 8) DP(2, 0) DW(11) DM(136, 32, 116) DU(112, 3392) DM(140, 64, 116) DP(14, 104) \
  DM(140, 144, 6) DP(12, 96) DM(136, 28, 120) DU(116, 3456) DM(140, 60, 120) DA DM(128, 92, 120) DW(8) DM(140, 56, 96) DU(120, 3520) \
  DM(128, 88, 96) DM(136, 148, 18) DW(8) DM(140, 52, 100) DU(96, 3584) DM(128, 84, 100) DM(140, 148, 16) DM(140, 48, 104) DU(100, 3648) \
  DM(128, 80, 104) DV(5) DW(9) DM(140, 44, 108) DU(104, 3712) DM(128, 76, 108) DY(136, 0) DW(8) DM(140, 40, 112) DU(108, 3776) \
  DM(128, 72, 112) DF DM(128, 144, 4) DW(7) DM(140, 36, 116) DU(112, 3840) DM(128, 68, 116) DM(128, 152, 8) DW(7) DM(140, 32, 120) \
  DU(116, 3904) DM(128, 64, 120) DM(128, 148, 14) DW(7) DM(140, 28, 96) DU(120, 3968) DM(128, 60, 96) DM(132, 92, 96) DW(7) DM(128, 56, 100) \
  DU(96, 4032) DM(132, 88, 100) DM(128, 156, 18) DW(7) DM(128, 52, 104) DU(100, 4096) DM(132, 84, 104) DV(6) DM(132, 144, 2) DW(6) \
  DM(128, 48, 108) DU(104, 4160) DM(132, 80, 108) DY(140, 1) DW(6) DM(128, 44, 112) DU(108, 4224) DM(132, 76, 112) DF DW(7) DM(128, 40, 116) \
  DU(112, 4288) DM(132, 72, 116) DM(132, 152, 6) DW(7) DM(128, 36, 120) DU(116, 4352) DM(132, 68, 120) DM(132, 148, 12) DW(7) \
  DM(128, 32, 96) DU(120, 4416) DM(132, 64, 96) DM(132, 156, 16) DW(7) DM(128, 28, 100) DU(96, 4480) DM(132, 60, 100) DW(7) DM(132, 56, 104) \
  DU(100, 4544) DW(6) DM(132, 52, 108) DU(104, 4608) DV(7) DW(5) DM(132, 48, 112) DY(128, 2) DW(5) DM(132, 44, 116) DF DW(5) \
  DM(132, 40, 120) DW(4) DM(132, 36, 96) DW(3) DM(132, 32, 100) DW(2) DM(132, 28, 104) DV(8) "s_nop 6\n\t" DY(132, 3) DF
#define DENSE_CLOB "v14", "v15", "v16", "v17", "v18", "v19", "v20", "v21", "v28", "v29", "v30", "v31", "v32", "v33", "v34", "v35", "v36", "v37", "v38", "v39", "v40", "v41", "v42", "v43", "v44", "v45", "v46", "v47", "v48", "v49", "v50", "v51", "v52", "v53", "v54", "v55", "v56", "v57", "v58", "v59", "v60", "v61", "v62", "v63", "v64", "v65", "v66", "v67", "v68", "v69", "v70", "v71", "v72", "v73", "v74", "v75", "v76", "v77", "v78", "v79", "v80", "v81", "v82", "v83", "v84", "v85", "v86", "v87", "v88", "v89", "v90", "v91", "v92", "v93", "v94", "v95", "v96", "v97", "v98", "v99", "v100", "v101", "v102", "v103", "v104", "v105", "v106", "v107", "v108", "v109", "v110", "v111", "v112", "v113", "v114", "v115", "v116", "v117", "v118", "v119", "v120", "v121", "v122", "v123", "v124", "v125", "v126", "v127", "v128", "v129", "v130", "v131", "v132", "v133", "v134", "v135", "v136", "v137", "v138", "v139", "v140", "v141", "v142", "v143", "v144", "v145", "v146", "v147", "v148", "v149", "v150", "v151", "v152", "v153", "v154", "v155", "v156", "v157", "v158", "v159"

typedef __amdgpu_buffer_rsrc_t rsrc_t;
__device__ __forceinline__ u32x4 ldg16(rsrc_t rs, int off, int so = 0) {
  asm volatile("" : "+s"(so) : : "memory");
  return __builtin_amdgcn_raw_buffer_load_b128(rs, off, so, SC1);
}
__device__ __forceinline__ void stg16(rsrc_t rs, int off, u32x4 v) { __builtin_amdgcn_raw_buffer_store_b128(v, rs, off, 0, SC1); }
__device__ __forceinline__ void dma16(const char* g, unsigned lds) {
  unsigned keep;
  asm volatile("s_mov_b32 %0, m0\n\ts_mov_b32 m0, %2\n\ts_nop 0\n\tglobal_load_lds_dwordx4 %1, off\n\ts_mov_b32 m0, %0"
               : "=&s"(keep) : "v"(g), "s"(lds) : "memory");
}
__device__ __forceinline__ void dma16nt(const char* g, unsigned lds) {
  unsigned keep;
  asm volatile("s_mov_b32 %0, m0\n\ts_mov_b32 m0, %2\n\ts_nop 0\n\tglobal_load_lds_dwordx4 %1, off nt\n\ts_mov_b32 m0, %0"
               : "=&s"(keep) : "v"(g), "s"(lds) : "memory");
}
__device__ __forceinline__ void dma4(const char* g, unsigned lds) {
  unsigned keep;
  asm volatile("s_mov_b32 %0, m0\n\ts_mov_b32 m0, %2\n\ts_nop 0\n\tglobal_load_lds_dword %1, off\n\ts_mov_b32 m0, %0"
               : "=&s"(keep) : "v"(g), "s"(lds) : "memory");
}

__device__ __forceinline__ int key_row(unsigned k) { return (int)(~(k >> 2) & (SEQ - 1)); }
__device__ __forceinline__ int key_slot(int lane) { return (lane & 3) | ((lane >> 1) & 4); }
__device__ __forceinline__ int key_addr(unsigned k, int slot) {
  return WS_XS + ((k & 2) ? 2 * NLN + slot : (int)((~(k >> 5) & (2 * NLN - 2)) | (k & 1))) * XSS;
}
struct SelMasks {
  unsigned m[15];
  __device__ __forceinline__ SelMasks(int lane) {
    const unsigned b0 = -(lane & 1), b1 = -((lane >> 1) & 1), b3 = -((lane >> 3) & 1), b4 = -((lane >> 4) & 1), b5 = -(lane >> 5);
    const unsigned t[15] = {~(b0 ^ b1), ~(b1 ^ b3), ~(b0 ^ b3), ~b3, ~b1, ~b0, b3, b1, b0, ~(b3 ^ b4), ~(b1 ^ b4), ~(b0 ^ b4), ~(b3 ^ b5), ~(b1 ^ b5), ~(b0 ^ b5)};
#pragma unroll
    for (int i = 0; i < 15; i++) {
      m[i] = t[i];
      asm volatile("" : "+v"(m[i]));
    }
  }
};
template <int CTRL>
__device__ __forceinline__ unsigned cex(unsigned v, unsigned m) {
  const unsigned o = dppu<CTRL>(v);
  return max(min(v, o), min(max(v, o), m));
}
__device__ __forceinline__ unsigned clean8(unsigned v, const unsigned* m) { return cex<0xB1>(cex<0x4E>(cex<0x128>(v, m[0]), m[1]), m[2]); }
template <bool EAGER, class F>
__device__ __forceinline__ void select8(rsrc_t rs, unsigned tag, int lane, const SelMasks& sm, char* ws, int bid, unsigned& stamp, F&& use) {
  int off = WS_KR + (bid & 7) * 2048 + lane * 16, so = EAGER ? 0 : (NKS - 1) * 16384;
  unsigned m1, t1, t2, m4, third;
  u32x4 a, c, d, e;
  if (EAGER) {
    const u32x4 rq = {(unsigned)(size_t)ws, (unsigned)((size_t)ws >> 32) & 0xffffu, (unsigned)WS_END, 0x00020000u};
    unsigned long long t;
    int n = SPIN_CAP;
#define KASK(r)                                                                                                                        \
  "buffer_load_dwordx4 v[" #r ":" #r "+3], %12, %13, %11 offen sc1\n\tbuffer_load_dwordx4 v[" #r "+4:" #r "+7], %12, %13, %11 offen offset:1024 sc1\n\t" \
  "s_add_u32 %11, %11, 16384\n\ts_min_u32 %11, %11, %16\n\t"
#define KCHK(r, l)                                                                                                          \
  "s_waitcnt vmcnt(2)\n\tv_cmp_eq_u32 vcc, %14, v[" #r "+3]\n\tv_cmp_eq_u32 %9, %14, v[" #r "+7]\n\ts_and_b64 %9, %9, vcc\n\t" \
  "s_cmp_eq_u64 %9, -1\n\ts_cbranch_scc1 " #l "\n\t"
#define KUSE(r)                                                                                                             \
  "v_max_u32 %0, v[" #r "], v[" #r "+4]\n\tv_min_u32 %1, v[" #r "], v[" #r "+4]\n\tv_max_u32 %2, v[" #r "+1], v[" #r "+5]\n\t"     \
  "v_min_u32 %3, v[" #r "+1], v[" #r "+5]\n\tv_max_u32 %4, v[" #r "+2], v[" #r "+6]\n\t"
    asm volatile(KASK(148) "s_sleep %15\n1:\n\t" KASK(156) KCHK(148, 2f) KASK(148) KCHK(156, 3f) "s_sub_u32 %10, %10, 1\n\ts_cbranch_scc0 1b\n"
                 "3:\n\t" KUSE(156) "s_branch 4f\n2:\n\t" KUSE(148) "\n4:"
                 : "=&v"(m1), "=&v"(t1), "=&v"(t2), "=&v"(m4), "=&v"(third), "=&{v[148:151]}"(a), "=&{v[152:155]}"(c), "=&{v[156:159]}"(d),
                   "=&{v[160:163]}"(e), "=&s"(t), "+s"(n), "+s"(so)
                 : "v"(off), "s"(rq), "s"(tag), "n"(KPP), "n"((NKS - 1) * 16384) : "vcc", "scc", "memory");
  } else {
    _Pragma("unroll 1") for (int spin = 0; spin < SPIN_CAP; spin++) {
      a = ldg16(rs, off, so);
      c = ldg16(rs, off, so + 1024);
      if (__builtin_expect(__all(a[3] == tag && c[3] == tag), 1)) break;
      __builtin_amdgcn_s_sleep(4);
    }
    const unsigned now = (unsigned)__builtin_amdgcn_s_memrealtime();
    stamp = now - 0xffffu + wave_max_u32(max((a[2] - now - 1u) & 0xffffu, (c[2] - now - 1u) & 0xffffu));
    m1 = max(a[0], c[0]), t1 = min(a[0], c[0]), t2 = max(a[1], c[1]), m4 = min(a[1], c[1]), third = max(a[2], c[2]);
  }
  third |= 0xffffu;
  unsigned m2 = max(t1, t2), m3 = min(t1, t2);
  unsigned x = clean8(cex<0xB1>(cex<0x4E>(cex<0xB1>(m1, sm.m[0]), sm.m[1]), sm.m[2]), sm.m + 3);
  const unsigned y = clean8(cex<0xB1>(cex<0x4E>(cex<0xB1>(m2, sm.m[0]), sm.m[1]), sm.m[2]), sm.m + 6);
  x = clean8(max(x, y), sm.m + 3);
  x = clean8(max(x, dppu<0x140>(x)), sm.m + 9);
  {
    auto r = __builtin_amdgcn_permlane16_swap(x, x, false, false);
    x = clean8(max(r[0], r[1]), sm.m + 12);
    auto q = __builtin_amdgcn_permlane32_swap(x, x, false, false);
    x = max(q[0], q[1]);
  }
  use(x, IC<0>{});
  unsigned k = min(x, dppu<0xB1>(x));
  k = min(k, dppu<0x4E>(k));
  k = min(k, dppu<0x128>(k));
  if (__builtin_expect(__any(max(m3, third) > k), 0)) {
    const int slot = key_slot(lane);
    _Pragma("unroll 1") for (int r = 0; r < 8; r++) {
      k = wave_max_u32(m1);
      x = slot == r ? k : x;
      const bool e = m1 == k;
      m1 = e ? m2 : m1;
      m2 = e ? m3 : m2;
      m3 = e ? m4 : m3;
      m4 = e ? 0u : m4;
    }
    if (__any(third > k)) {
      k = 0u;
      _Pragma("unroll 1") for (int r = 0; r < 8; r++) {
        unsigned v;
        _Pragma("unroll 1") for (int spin = 0; spin < SPIN_CAP; spin++) {
          bool ok = true;
          v = 0u;
          _Pragma("unroll 4") for (int i = 0; i < 16; i++) {
            const u32x4 f = ldg16(rs, WS_KF + lane * 16, i * 1024);
            ok = ok && f[1] == tag && f[3] == tag;
            v = max(v, max(f[0] <= k - 1u ? f[0] : 0u, f[2] <= k - 1u ? f[2] : 0u));
          }
          if (__all(ok)) break;
          __builtin_amdgcn_s_sleep(1);
        }
        k = wave_max_u32(v);
        x = slot == r ? k : x;
      }
    }
    use(x, IC<1>{});
  }
  use(x, IC<2>{});
  if (EAGER) asm volatile("s_waitcnt vmcnt(9)" : : "{v[148:151]}"(a), "{v[152:155]}"(c), "{v[156:159]}"(d), "{v[160:163]}"(e) : "memory");
}

__device__ __forceinline__ int last_start(rsrc_t rs, unsigned tag, int lane, int bid, unsigned ref, u32x4 p, u32x4 q) {
  _Pragma("unroll 1") for (int spin = 0; spin < SPIN_CAP; spin++) {
    if (__all(p[1] == tag && p[3] == tag && q[1] == tag && q[3] == tag)) break;
    __builtin_amdgcn_s_sleep(2);
    p = ldg16(rs, WS_CK + (bid & 7) * 2048 + lane * 16);
    q = ldg16(rs, WS_CK + (bid & 7) * 2048 + 512 + lane * 16);
  }
  const int lag = max(max((int)(p[0] - ref), (int)(p[2] - ref)), max((int)(q[0] - ref), (int)(q[2] - ref)));
  return min((int)wave_max_u32((unsigned)max(lag + 4096, 0)) - 4096, 1000);
}
__device__ __forceinline__ int late_group(size_t wg, int bid) { return (3 - (int)(wg >> 7) - bid) & 7; }
__device__ __forceinline__ int ffn_group(size_t wg, int bid) {
  const int l = late_group(wg, bid), i = bid >> 3;
  return (bid & 7) * 9 + (bid < NFFN ? (i == l ? 8 : i) : l);
}
__device__ __forceinline__ void down_dma(size_t wd, int j, LAS char* L, int wave, int lane, int r0, int r1) {
#pragma unroll
  for (int k = 0; k < 3; k++) {
    const int slot = 3 * (wave - 9) + k, t = slot * 64 + lane, r8 = t / 72, q8 = (t - r8 * 72) ^ (r8 & 2);
    const char* p = (const char*)wd + (unsigned)((j * 64 + r8) * 1152);
    _Pragma("unroll 1") for (int i = r0; i < r1; i++)
        dma16nt(p + (unsigned)(i * 9216 + (q8 ^ ((i & 1) << 2)) * 16), (unsigned)(size_t)L + slot * 1024 + i * 9216);
  }
}
__device__ __forceinline__ unsigned stats_role(size_t argp, unsigned cpf, LAS char* L) {
  const int tid = threadIdx.x, lane = tid & 63, bid = blockIdx.x, fw = bid;
  size_t x, wsp, wd, wg;
  asm volatile(
      "s_waitcnt lgkmcnt(0)\n\ts_load_dwordx2 %0, %4, 0x0\n\ts_load_dwordx2 %1, %4, 0x30\n\ts_load_dwordx2 %2, %4, 0x58\n\t"
      "s_load_dwordx2 %3, %4, 0x48"
      : "=&s"(x), "=&s"(wsp), "=&s"(wd), "=&s"(wg) : "s"(argp));
  SCHED_FENCE();
  unsigned xo0 = (bid * 32 + (tid >> 4)) * (DM * 2) + (lane & 15) * 8;
  asm volatile("" : "+v"(xo0));
  asm volatile("s_waitcnt lgkmcnt(0)" : "+s"(x), "+s"(wsp), "+s"(wd), "+s"(wg));
  const int wave = __builtin_amdgcn_readfirstlane(tid >> 6);
  char* const ws = (char*)(GAS char*)wsp;
  const rsrc_t rs = __builtin_amdgcn_make_buffer_rsrc(ws, 0, WS_END, 0x00020000);
  unsigned tagv = WS_EPOCH;
  asm volatile("global_load_dword %0, %0, %1 sc1" : "+v"(tagv) : "s"(wsp) : "memory");
  SCHED_FENCE();
  unsigned tag;
  if (wave >= 8) {
    __builtin_amdgcn_s_barrier();
    if (wave > 8) down_dma(wd, ffn_group(wg, bid), L, wave, lane, 0, ED);
    else DRAIN();
    asm volatile("s_waitcnt vmcnt(%2)" : "+v"(tagv) : "v"(cpf), "n"(3 * ED));
    tag = __builtin_amdgcn_readfirstlane(tagv) + 1u;
    asm volatile("" : "+s"(tag));
  } else {
    u32x2 xw[9];
#pragma unroll
    for (int k = 0; k < 9; k++) xw[k] = *(GAS const u32x2*)(x + xo0 + 128 * k);
    SCHED_FENCE();
    __builtin_amdgcn_s_barrier();
    unsigned one2 = 0x3f803f80u;
    asm volatile("" : "+s"(one2));
    float s0 = 0.f, s1 = 0.f, q0 = 0.f, q1 = 0.f;
#pragma unroll
    for (int k = 0; k < 9; k++) {
      const unsigned a = xw[k][0], c = xw[k][1];
      s0 = dot2(a, one2, s0);
      q0 = dot2(a, a, q0);
      s1 = dot2(c, one2, s1);
      q1 = dot2(c, c, q1);
    }
    const float mean = red16_sum(s0 + s1) * (1.0f / DM), mbf = rbf(mean);
    float q = red16_sum(q0 + q1), dm = mean;
    if (__builtin_expect(__any(!(q * (1.0f / DM) >= mean * mean * 1.002f)), 0)) {
      f32x2 q2 = {0.f, 0.f};
#pragma unroll
      for (int k = 0; k < 18; k++) {
        const f32x2 dd = unpk(xw[k >> 1][k & 1]) - mbf;
        q2 = __builtin_elementwise_fma(dd, dd, q2);
      }
      q = red16_sum(hadd(q2));
      dm = mean - mbf;
    }
    float ri = ln_rinv(q, dm);
    asm volatile("" : "+v"(tagv), "+v"(ri));
    auto rr = __builtin_amdgcn_permlane16_swap(__float_as_uint(ri), __float_as_uint(ri), false, false);
    auto m2 = __builtin_amdgcn_permlane16_swap(__float_as_uint(mbf), __float_as_uint(mbf), false, false);
    const unsigned mp = __builtin_amdgcn_perm(m2[1], m2[0], 0x07060302u);
    if ((lane & 31) == 0) stg16(rs, WS_ST + (16 * fw + (tid >> 5)) * 16, u32x4{rr[0], rr[1], mp, tagv + 1u});
    tag = __builtin_amdgcn_readfirstlane(tagv) + 1u;
    asm volatile("" : : "v"(cpf), "s"(tag));
  }
  return tag;
}

__device__ __forceinline__ void ffn_role(size_t argp, unsigned tag, LAS char* L) {
  const int tid = threadIdx.x, lane = tid & 63, bid = blockIdx.x;
  size_t wsp, wdp;
  u64x2 gu;
  asm volatile("s_load_dwordx2 %0, %3, 0x30\n\ts_load_dwordx4 %1, %3, 0x48\n\ts_load_dwordx2 %2, %3, 0x58" : "=&s"(wsp), "=&s"(gu), "=&s"(wdp) : "s"(argp));
  SCHED_FENCE();
  const int wave = __builtin_amdgcn_readfirstlane(tid >> 6);
  const int hm = bid < NFFN ? 3 : (bid & 8 ? 2 : 1);
  const bool mine = wave < 8 && (hm >> (wave >> 2) & 1);
  const int g = lane >> 4, tq = (lane & 15) >> 2, tp = lane & 3;
  const bool full = hm == 3;
  const int gr = full ? tid >> 4 : tid >> 3;
  const int gc = full ? (lane & 7) ^ ((gr & 1) | ((gr >> 1) & 6)) : (hm & 2) * 2 + ((lane & 3) ^ ((gr >> 2) & 3));
  unsigned go = gr * (FF * 2) + gc * 16;
  asm volatile("" : "+v"(go));
  asm volatile("s_waitcnt lgkmcnt(0)" : "+s"(wsp), "+s"(gu), "+s"(wdp));
  char* const ws = (char*)(GAS char*)wsp;
  const rsrc_t rs = __builtin_amdgcn_make_buffer_rsrc(ws, 0, WS_END, 0x00020000);
  const int j = ffn_group(gu[0], bid);
  const char* gsrc = (const char*)((lane & (full ? 8 : 4)) ? gu[1] : gu[0]) + (go + j * 128);
  const unsigned lb = (unsigned)(size_t)L + wave * 1024 + 9 * RG;
  auto issue_gu = [&](int n, unsigned step) {
    if (wave < 8) {
      _Pragma("unroll 1") for (int i = 0; i < n; i++) {
        dma16nt(gsrc, lb + i * RG);
        gsrc += step;
      }
    }
  };
  __syncthreads();
  issue_gu(full ? 10 : 9, full ? 32 * FF * 2 : 64 * FF * 2);
  if (wave > 8) down_dma(wdp, j, L, wave, lane, full ? ED : (hm & 2) * 2, hm == 1 ? 4 : 8);
  s16x4 fa[18][2], fd[3][4];
  const int fr = 16 * (g >> 1) + 4 * (g & 1) + (tq & 1) + 8 * (tq >> 1), fx = (g & 1) | ((tq >> 1) << 1);
  const LAS char* gup = L + 9 * RG + (tp & 1) * 8 +
                        (full ? fr * 256 + ((wave ^ ((tq & 1) | (fx << 1))) + 8 * (tp >> 1)) * 16 : fr * 128 + (((wave & 3) ^ fx) + 4 * (tp >> 1)) * 16);
  const LAS char* gup2 = gup + 8 * RG;
  asm volatile("" : "+v"(gup), "+v"(gup2));
  auto read_gu = [&](auto ks0, auto n, auto reg0, auto sz) {
#pragma unroll
    for (int i = 0; i < n; i++)
#pragma unroll
      for (int h = 0; h < 2; h++) {
        const int o = reg0 * RG + i * sz + h * (sz / 16);
        fa[ks0 + i][h] = __builtin_amdgcn_ds_read_tr16_b64_v4i16((LAS s16x4*)(o < 8 * RG ? gup + o : gup2 + (o - 8 * RG)));
      }
  };
  auto read_down = [&]() {
    const int f = (tq >> 1) | ((g & 1) << 1);
#pragma unroll
    for (int t = 0; t < 3; t++) {
      const LAS char* p = L + (8 * g + tq + (hm == 2 ? 32 : 0)) * 1152 + ((3 * wave + t) ^ f) * 32 + tp * 8;
#pragma unroll
      for (int h = 0; h < 4; h++) {
        if (h >= 2 && !full) fd[t][h] = s16x4{0, 0, 0, 0};
        else fd[t][h] = __builtin_amdgcn_ds_read_tr16_b64_v4i16((LAS s16x4*)(p + (32 * (h >> 1) + 4 * (h & 1)) * 1152));
      }
    }
  };
  const int c15 = lane & 15;
  const LAS char* xr = L + FS_X + ((((c15 >> 3) + 2 * g) ^ (2 * tp)) + 2 * (lane & 4) + 16 * tp) * 16 + (wave < 8 ? 0 : (wave - 8) * 2048);
  LAS char* guw = L + FS_GU + (4 * (g & 1) + 2 * (g >> 1) + (c15 >> 3)) * 160 + ((wave ^ (hm == 2 ? 4 : 0)) * 8 + (lane & 7)) * 2;
  const LAS char* gur = L + FS_GU + (lane & 7) * 160 + 16 * g;
  int xo = ((lane & 7) ^ (2 * g)) * 16 + DM * 2;
  int po = WS_PART + (bid < NFFN ? bid : bid - LATE0) * (8 * DM * 4) + (3 * wave + (c15 >> 3)) * 512 + g * 128 + (lane & 7) * 16;
  int sidx = c15 < 8 ? 0x4444 : 0xeeee;
  asm volatile("" : "+v"(guw), "+v"(gur), "+v"(xo), "+v"(po), "+v"(sidx), "+v"(xr));
  if (wave < 8) *(LAS u16*)guw = 0;
  else *(LAS int*)(L + FS_SEL + 4) = 0;
  const SelMasks sm(lane);
  unsigned due = 0;
  if (full) {
    if (wave < 8) asm volatile("s_waitcnt vmcnt(2)" ::: "memory");
    __syncthreads();
    u32x4 cp, cq;
    asm volatile("s_cmp_lg_u32 %4, %5\n\ts_cbranch_scc1 1f\n\tglobal_load_dwordx4 %0, %2, %3 sc1\n\tglobal_load_dwordx4 %1, %2, %3 offset:512 sc1\n1:"
                 : "=&{v[148:151]}"(cp), "=&{v[152:155]}"(cq) : "v"(WS_CK + (8 + (bid & 7)) * 2048 + lane * 16), "s"(wsp), "s"(wave), "n"(SELW) : "scc", "memory");
    if (mine) read_gu(IC<0>{}, IC<8>{}, IC<0>{}, IC<RG>{});
    __syncthreads();
    issue_gu(8, 32 * FF * 2);
    if (wave < 8) asm volatile("s_waitcnt vmcnt(8)" ::: "memory");
    else if (wave != SELW) DRAIN();
    __syncthreads();
    if (mine) read_gu(IC<8>{}, IC<2>{}, IC<8>{}, IC<RG>{});
    read_down();
    __syncthreads();
    if (wave == SELW) {
      due = (unsigned)__builtin_amdgcn_s_memrealtime();
      asm volatile("s_waitcnt vmcnt(0)" : "+{v[148:151]}"(cp), "+{v[152:155]}"(cq));
      due += last_start(rs, tag, lane, bid, due, cp, cq);
    }
  } else {
    due = __builtin_amdgcn_readfirstlane(*(const LAS int*)(L + FS_SEL));
  }
  auto fetch = [&](int xa) {
    asm volatile(
        "s_mov_b32 m0, %2\n\t.set i_, 0\n\t.rept 9\n\ts_nop 0\n\tglobal_load_lds_dwordx4 %0, %1 offset:128*i_ sc1\n\t"
        "s_add_u32 m0, m0, 896\n\t.set i_, i_+1\n\t.endr"
        : : "v"(xa), "s"(wsp + WS_XS), "s"((unsigned)(size_t)L + FS_X) : "scc", "memory");
  };
  if (wave == SELW) {
    while ((int)((unsigned)__builtin_amdgcn_s_memrealtime() - due) < DLY_K) __builtin_amdgcn_s_sleep(1);
    unsigned unused;
    int xa;
    select8<true>(rs, tag, lane, sm, ws, bid, unused, [&](unsigned k, auto step) {
      if (step < 2) {
        asm("s_nop 1\n\tv_add_u32_dpp %0, %1, %2 quad_perm:[0,0,0,0] row_mask:0x1 bank_mask:0xf\n\t"
            "v_add_u32_dpp %0, %1, %2 quad_perm:[1,1,1,1] row_mask:0x2 bank_mask:0xf\n\t"
            "v_add_u32_dpp %0, %1, %2 quad_perm:[2,2,2,2] row_mask:0x4 bank_mask:0xf\n\t"
            "v_add_u32_dpp %0, %1, %2 quad_perm:[3,3,3,3] row_mask:0x8 bank_mask:0xf"
            : "=&v"(xa) : "v"(key_addr(step ? k : k & ~2u, key_slot(lane)) - WS_XS), "v"(xo));
        if (step) DRAIN();
        fetch(xa);
      } else {
        __builtin_amdgcn_s_barrier();
      }
    });
    xo = xa;
  } else {
    DRAIN();
    __syncthreads();
  }
  if (full) {
    if (mine) read_gu(IC<10>{}, IC<8>{}, IC<0>{}, IC<RG>{});
  } else {
    if (mine) read_gu(IC<0>{}, IC<18>{}, IC<0>{}, IC<RG / 2>{});
    read_down();
  }
  f32x4 acc[3];
  _Pragma("unroll 1") for (int spin = 1; spin < SPIN_CAP; spin++) {
    DRAIN();
    __syncthreads();
    if (mine) {
      bf8v xf[9];
#pragma unroll
      for (int i = 0; i < 9; i++) xf[i] = *(const LAS bf8v*)(xr + 1024 * i);
      SCHED_FENCE();
      f32x4 a = {0.f, 0.f, 0.f, 0.f};
#pragma unroll
      for (int i = 0; i < 9; i++) {
        const s16x8 lo = __builtin_shufflevector(fa[2 * i][0], fa[2 * i][1], 0, 1, 2, 3, 4, 5, 6, 7);
        const s16x8 hi = __builtin_shufflevector(fa[2 * i + 1][0], fa[2 * i + 1][1], 0, 1, 2, 3, 4, 5, 6, 7);
        a = __builtin_amdgcn_smfmac_f32_16x16x64_bf16(
            xf[i], __builtin_bit_cast(bf16v, __builtin_shufflevector(lo, hi, 0, 1, 2, 3, 4, 5, 6, 7, 8, 9, 10, 11, 12, 13, 14, 15)), a, sidx, 0, 0);
      }
      const float a0 = a[0], a1 = a[1], a2 = a[2], a3 = a[3];
      auto r0 = __builtin_amdgcn_permlane32_swap(__float_as_uint(a0), __float_as_uint(a2), false, false);
      auto r1 = __builtin_amdgcn_permlane32_swap(__float_as_uint(a1), __float_as_uint(a3), false, false);
      const float s0 = __uint_as_float(r0[0]) + __uint_as_float(r0[1]), s1 = __uint_as_float(r1[0]) + __uint_as_float(r1[1]);
      const float gs = __int_as_float(__builtin_amdgcn_update_dpp(__float_as_int(s0), __float_as_int(s1), 0x128, 0xf, 0xc, false));
      const float us = __int_as_float(__builtin_amdgcn_update_dpp(__float_as_int(s1), __float_as_int(s0), 0x128, 0xf, 0x3, false));
      const float gp = rbf(gs);
      const float gq = rbf(gp * __builtin_amdgcn_rcpf(1.0f + __expf(-gp)));
      *(LAS u16*)guw = (u16)pkbf(gq * rbf(us), 0.f);
    } else if (wave >= 8) {
      const u32x4 t = __builtin_elementwise_max(__builtin_elementwise_max(*(const LAS u32x4*)xr, *(const LAS u32x4*)(xr + 1024)), *(const LAS u32x4*)(xr + 2048));
      if (max(max(t[0], t[1]), max(t[2], t[3])) == ~0u) *(LAS int*)(L + FS_SEL + 4) = spin;
    }
    __syncthreads();
    const bf8v b0 = *(const LAS bf8v*)gur, b1 = *(const LAS bf8v*)(gur + 64);
    const int redo = *(const volatile LAS int*)(L + FS_SEL + 4);
    SCHED_FENCE();
    const f32x4 z4 = {0.f, 0.f, 0.f, 0.f};
#pragma unroll
    for (int t = 0; t < 3; t++) acc[t] = MFMAB(__builtin_bit_cast(bf8v, __builtin_shufflevector(fd[t][0], fd[t][1], 0, 1, 2, 3, 4, 5, 6, 7)), b0, z4);
#pragma unroll
    for (int t = 0; t < 3; t++) acc[t] = MFMAB(__builtin_bit_cast(bf8v, __builtin_shufflevector(fd[t][2], fd[t][3], 0, 1, 2, 3, 4, 5, 6, 7)), b1, acc[t]);
    asm volatile("" : "+v"(acc[0]), "+v"(acc[1]), "+v"(acc[2]));
    SCHED_FENCE();
    if (__builtin_expect(__builtin_amdgcn_readfirstlane(redo) != spin, 1)) break;
    if (wave == SELW) fetch(xo);
  }
  {
    SCHED_FENCE();
    auto put = [&](f32x4 a, int off) {
      u32x4 o = __builtin_bit_cast(u32x4, a);
      o[0] = min(o[0], 0xfffffffeu);
      stg16(rs, po + off, o);
    };
    put(c15 < 8 ? acc[0] : acc[1], 0);
    if (c15 < 8) put(acc[2], 1024);
  }
}

__global__ __launch_bounds__(NTHR, 3) void k_all(const u16*, const u16*, const u16*, const u16*, const u16*, const u16*, char*, const u16*,
                                                 const u16*, const u16*, const u16*, const u16*, u16*) {
  __shared__ __attribute__((aligned(16))) char smem[SM_TOTAL];
  LAS char* const L = (LAS char*)smem;
  const int bid = blockIdx.x;
  const size_t argp = (size_t)__builtin_amdgcn_kernarg_segment_ptr();
  u64x8 a8;
  u64x4 a4;
  size_t a1;
  unsigned cpf;
  {
    size_t pc;
    int t;
    asm volatile("s_load_dwordx16 %0, %3, 0x0\n\ts_load_dwordx8 %1, %3, 0x40\n\ts_load_dwordx2 %2, %3, 0x60" : "=&s"(a8), "=&s"(a4), "=&s"(a1) : "s"(argp));
    asm volatile(
        "s_getpc_b64 %1\n\ts_lshr_b32 %2, %3, 3\n\tv_min_u32 %0, 6, %4\n\ts_mul_i32 %2, %2, 7\n\tv_add_lshl_u32 %0, %0, %2, 7\n\t"
        "global_load_dword %0, %0, %1"
        : "=&v"(cpf), "=&s"(pc), "=&s"(t) : "s"(bid), "v"(threadIdx.x) : "scc");
  }
  SCHED_FENCE();
  const int tid = threadIdx.x, lane = tid & 63, b = bid - NFFN;
  unsigned ftag;
  if (b < 0) {
    ftag = stats_role(argp, cpf, L);
  } else {
  const int wave = __builtin_amdgcn_readfirstlane(tid >> 6);
  const bool is_ln = b < NLN;
  const int hl = lane & 31;
  const int ch = (wave & 3) < 3 ? (wave & 3) : (wave >> 2), sub = (wave & 3) < 3 ? (wave >> 2) : 3;
  const int d = 3 * b + ch;
  const int xt = 4 * b + wave, hrow = 8 * ((xt >> 1) & 127) + (lane & 1) + 2 * (lane >> 4);
  const int grp = ((xt >> 8) << 5) + ((xt & 1) << 4) + ((lane >> 1) & 7);
  unsigned xo = hrow * (DM * 4) + grp * 12, po = d * NM * 2 + lane * 4, lo = 2 * d & ~3;
  int cpo = WS_UP + (grp * 2048 + hrow) * 16;
  {
    LAS char* zc = L + (wave >> 2) * CH2 + SM_KC + 16 * (tid & 255);
    const u32x4 z = {0u, 0u, 0u, 0u};
    *(LAS u32x4*)zc = z;
    *(LAS u32x4*)(zc + 4096) = z;
  }
  LAS char* prg = L + SM_PRG + 4 * wave;
  *(LAS int*)prg = (int)(0xba5080u >> 2 * wave & 3);
  *(LAS int*)(L + SM_PRG + 52) = -1;
  asm volatile("" : "+v"(prg));
  const int xg = b & 7, sl = b >> 3, sl12 = sl < 12 ? sl : sl - 12;
  unsigned dso = 6 * b & ~3;
  asm volatile("" : "+v"(xo), "+v"(po), "+v"(lo), "+v"(cpo), "+v"(dso));
  SCHED_FENCE();
  asm volatile("s_waitcnt lgkmcnt(0)" : "+s"(a8), "+s"(a4), "+s"(a1));
  u32x3 cp[4];
  auto load_x = [&]() {
    if (wave < 4) {
#pragma unroll
      for (int i = 0; i < 4; i++) cp[i] = *(GAS const u32x3u*)(a8[0] + xo + ((i & 1) * (DM * 2) + (i >> 1) * 96));
    }
    SCHED_FENCE();
  };
  const unsigned r_ldt = *(GAS const unsigned*)(a8[5] + lo), r_lar = *(GAS const unsigned*)(a8[1] + po), r_aim = *(GAS const unsigned*)(a8[2] + po);
  const unsigned r_cr = *(GAS const unsigned*)(a8[3] + po), r_ci = *(GAS const unsigned*)(a8[4] + po);
  typedef u32x2 u32x2u __attribute__((aligned(4)));
  u32x2 r_dsk = *(GAS const u32x2u*)(a8[7] + dso);
  char* const ws = (char*)(GAS char*)a8[6];
  const rsrc_t ro = __builtin_amdgcn_make_buffer_rsrc((char*)(GAS char*)a1, 0, SEQ * DM * 2, 0x00020000);
  const rsrc_t rs = __builtin_amdgcn_make_buffer_rsrc(ws, 0, WS_END, 0x00020000);
  const unsigned tagv = __builtin_amdgcn_raw_buffer_load_b32(rs, WS_EPOCH, 0, SC1);
  SCHED_FENCE();
  load_x();
  const int rb = is_ln ? b : b - (NLN - RED1);
  LAS char *const C = L + ch * TABB, *const K = L + ch * CH2;
  const int c15 = lane & 15, g = lane >> 4;
  const int lg = late_group(a4[1], bid);
  const char* pfx = (lane & 31) < 12 ? (const char*)(sl < 12 ? a4[1] : a4[2]) + (unsigned)(((sl12 * 4 + (wave & 3)) * 12 + (lane & 31)) * (FF * 2) + xg * 1152 +
                                                                                            (lg ? lg : (lane >> 5) * 8) * 128)
                                     : (const char*)a4[3] + (unsigned)(((9 * xg + lg) * 576 + (sl * 4 + (wave & 3)) * 6 + min((lane & 31) - 12, 5)) * 128);
  asm volatile("" : "+v"(pfx));
  unsigned hs = (sub & 1) ? 0x03020c0cu : 0x01000c0cu, hs2 = (d & 1) ? 0x03020c0cu : 0x01000c0cu;
  LAS char* yb = L + ch * YC + g * 320 + c15 * 16 + (sub ? 2 * YS + YT : 0);
  const LAS char *pa, *pa2, *pb, *uf16 = K + SM_US + 2 * UPAD + 32 * c15 + 16 * g;
  LAS char *kd, *kd2;
  unsigned ks0, ks1;
  half8 bA, bB, b9, b7;
  if (sub == 0) {
    pa = C + (((unsigned)(0x33982210cdef4567ull >> (4 * c15)) & 15) + 2 * (c15 & 8)) * TAB_STRIDE + 16 * g;
    pa2 = C + (((unsigned)(0x7763554289ab0123ull >> (4 * c15)) & 15) + 2 * (c15 & 8)) * TAB_STRIDE + 16 * g;
    pb = C + (TR_H + c15) * TAB_STRIDE + 16 * g;
    kd = K + KC_RD + 2 * 544 - 16 * lane;
    kd2 = K + KC_RD + (~c15 & 3) * RDS + 2 * (535 - (c15 | 3) + 8 * g) - 64 * 16;
    uf16 -= 32 * 33;
    bA = band_frag(lane, 0x3u << 12);
    const int s = lane & 3;
    const u32x4 t = {s < 2 ? 0x0c0c0c0cu : (s == 2 ? 0x01000c0cu : 0x01000100u), s == 0 ? 0x01000c0cu : 0x01000100u,
                     s < 2 ? 0x01000100u : (s == 2 ? 0x03020100u : 0x05040302u), (unsigned)(size_t)(K + KC_RD + s * RDS + 64)};
    bB = __builtin_bit_cast(half8, t);
    ks0 = (unsigned)(size_t)(K + KC_R9 + 190 * ((c15 + 1) & 1) + 2 * (15 - c15 + 4 * g));
    b9 = band_frag(lane, 0x1ffu << 4);
    b7 = band_frag(lane, 0x7fu << 5);
    asm volatile("" : "+v"(bA), "+v"(pa), "+v"(pa2), "+v"(pb), "+v"(kd), "+v"(kd2), "+v"(ks0), "+v"(uf16), "=v"(ks1), "+v"(bB), "+v"(b9), "+v"(b7));
  } else if (sub < 3) {
    asm volatile("" : "=v"(bA), "=v"(pa), "=v"(pa2), "=v"(pb), "=v"(kd), "=v"(kd2), "=v"(ks0), "=v"(uf16), "=v"(ks1), "=v"(bB));
  } else {
    pa = C + (TR_H + 15 - c15) * TAB_STRIDE + 16 * g;
    pa2 = K + KC_R5 + 2 * (158 - 64 - 2 * c15 + 4 * g) - 128;
    pb = C + (TR_M + c15) * TAB_STRIDE + 16 * g;
    kd = K + (c15 < 8 ? ((c15 & 1) ? KC_R5 : KC_R3) + 2 * (80 - 8 * (c15 & 6) + 4 * g) : KC_R9 + 2 * (120 - 8 * c15 + 2 * g));
    kd2 = kd + (c15 < 8 ? 4 : KC_R7 - KC_R9);
    ks0 = c15 < 8 ? 0x03020100u : 0x05040100u;
    ks1 = c15 < 8 ? 0x07060504u : 0x07060302u;
    uf16 -= 16 * c15;
    bA = band_frag(lane, 0x1fu << 6);
    bB = band_frag(lane, 0x7u << 11);
    asm volatile("" : "+v"(bA), "+v"(pa), "+v"(pa2), "+v"(pb), "+v"(kd), "+v"(kd2), "+v"(ks0), "+v"(uf16), "+v"(ks1), "+v"(bB));
  }
  const unsigned pe = sub < 2 ? 0u : 3u << 23;
  f32x2 kh = {-1.44269502f, 0.159154936f}, kl = {-1.92596299e-8f, 7.13048051e-9f}, k1 = {128.f * 0.693147181f, 128.f}, ln2 = {0.693147181f, 128.f};
  const unsigned pi = sub < 2 ? 0x3f800000u : 0xc1000000u;
  u32x2 k0u = {0x3f317218u + pe, pi}, p0u = {0x3f800000u + pe, pi};
  const int vt = tid - 256;
  int uoff = WS_UP + b * 16384 + vt * 16, soff = WS_ST + vt * 16;
  asm volatile("" : "+v"(yb), "+v"(uoff), "+v"(soff));
  asm volatile("s_mov_b32 m0, %8" : "+s"(hs), "+s"(hs2), "+s"(kh), "+s"(kl), "+s"(k0u), "+s"(k1), "+s"(ln2), "+s"(p0u)
               : "s"((unsigned)(size_t)C + (sub & 1) * 256));
  SCHED_FENCE();
  SCHED_FENCE();
  const unsigned tag = __builtin_amdgcn_readfirstlane(tagv) + 1u;
  auto half = [&](unsigned v) { return __uint_as_float(__builtin_amdgcn_perm(v, v, hs)); };
  f32x2 f_l = {half(r_lar), __uint_as_float(__builtin_amdgcn_perm(r_ldt, r_ldt, hs2))}, f_c = {half(r_cr), half(r_ci)};
  float f_aim = half(r_aim);
  unsigned f_cm;
  asm volatile("v_max3_f32 %0, |%1|, |%2|, |%3|\n\tv_max_f32 %0, %0, |%4|" : "=&v"(f_cm) : "v"(r_cr), "v"(r_ci), "v"(r_cr << 16), "v"(r_ci << 16));
  asm volatile("" : "+v"(f_l), "+v"(f_aim), "+v"(f_c), "+v"(r_dsk) : "v"(cpf));
  SCHED_FENCE();
  SCHED_FENCE();
  if (wave < 4) {
    u32x4 pc[4];
#pragma unroll
    for (int i = 0; i < 12; i++) {
      const int r = i / 6 * 2, k = i % 6 / 2;
      const unsigned t = __builtin_amdgcn_perm(cp[r + 1][k], cp[r][k], (i & 1) ? 0x07060302u : 0x05040100u);
      pc[i / 3][i % 3] = t;
      pc[i / 3][3] = tag;
    }
#pragma unroll
    for (int i = 0; i < 4; i++) stg16(rs, cpo + (i >> 1) * (16 * 1024 * 16) + (i & 1) * 16384, pc[i]);
    if (is_ln && tid >= 184) dma16((const char*)a4[0] + (tid - 184) * 16, (unsigned)(size_t)L + SM_WR + (wave - 3) * 1024 + 128);
    dma4(pfx, (unsigned)(size_t)L + SM_DUMMY);
  }
  SCHED_FENCE();
  u32x4 v0, v1, st0, st1;
  const u32x4 rq = {(unsigned)a8[6], (unsigned)(a8[6] >> 32) & 0xffffu, (unsigned)WS_END, 0x00020000u};
  asm volatile("" : "=v"(v0), "=v"(v1), "=v"(st0), "=v"(st1));
  auto ask = [&](u32x4& p, u32x4& q, int off) {
    asm volatile("buffer_load_dwordx4 %0, %2, %3, 0 offen sc1\n\tbuffer_load_dwordx4 %1, %2, %3, %4 offen sc1"
                 : "+v"(p), "+v"(q) : "v"(off), "s"(rq), "s"(8192) : "memory");
  };
  auto polls = [&](int k) {
    if (wave >= 4 && (wave < 8 ? 0 : 1) == k) ask(v0, v1, uoff);
    if (wave >= 4 && (wave < 8 ? 0 : 1) == k) ask(st0, st1, soff);
    SCHED_FENCE();
  };
  auto get = [&](bool both) {
    _Pragma("unroll 1") for (int spin = 0; spin < SPIN_CAP; spin++) {
      const bool ok = both ? v0[3] == tag && v1[3] == tag : st0[3] == tag && st1[3] == tag;
      if (__builtin_expect(__all(ok), 1)) break;
      __builtin_amdgcn_s_sleep(4);
      if (both) {
        asm volatile("s_waitcnt vmcnt(0)" : "+v"(st0), "+v"(st1));
        if (!ok) ask(v0, v1, uoff);
      }
      if (st0[3] != tag || st1[3] != tag) ask(st0, st1, soff);
      if (both) asm volatile("s_waitcnt vmcnt(0)" : "+v"(v0), "+v"(v1), "+v"(st0), "+v"(st1));
      else asm volatile("s_waitcnt vmcnt(0)" : "+v"(st0), "+v"(st1));
    }
  };
  {
    const f32x2 l2e = -f32x2{kh[0], kh[0]}, th = f_l * l2e;
    const f32x2 tl = __builtin_elementwise_fma(f_l, -f32x2{kl[0], kl[0]}, __builtin_elementwise_fma(f_l, l2e, -th)) * ln2[0];
    const f32x2 e0 = {__builtin_amdgcn_exp2f(th[0]), __builtin_amdgcn_exp2f(th[1])}, ex = __builtin_elementwise_fma(e0, tl, e0);
    const unsigned eb = pkbf(ex[0], ex[1]);
    const float dt = bfhi(eb);
    const f32x2 av = f32x2{bflo(eb), f_aim} * dt;
    const f32x2 hi = av * kh, lo = __builtin_elementwise_fma(av, kl, __builtin_elementwise_fma(av, kh, -hi));
    int se = 385 - (int)(wave_max_u32(f_cm) >> 23) - (int)((unsigned)__builtin_amdgcn_readfirstlane(eb) >> 23);
    se = max(se, 2);
    asm("" : "+s"(se));
    se = min(se, 252);
    const float dts = dt * __uint_as_float((unsigned)(se + 1) << 23);
    cf cc = f_c * dts;
    cf ea = cexp(hi * __builtin_bit_cast(f32x2, p0u), lo * __builtin_bit_cast(f32x2, k0u)), eb2 = cexp(hi * 128.f, lo * k1);
    SCHED_FENCE();
    asm volatile("s_cmp_lt_u32 %3, 2\n\ts_cbranch_scc0 1f\n\t" TAB_A "s_branch 2f\n1:\n\t" TAB_B "\n2:"
                 : "+{v[100:101]}"(cc), "+{v[102:103]}"(ea), "+{v[104:105]}"(eb2) : "s"(sub) : TAB_CLOB, "scc", "memory");
    if (sub == 2) *(volatile LAS unsigned*)(L + SM_ISC + 4 * ch) = (unsigned)(254 - se) << 23;
  }
  SCHED_FENCE();
  polls(1);
  const f32x4 zero4 = {0.f, 0.f, 0.f, 0.f};
  f32x2 sb[2][3];
  if (sub == 0) {
    {
      half8 fa[8], fc[8], fb[8];
#pragma unroll
      for (int ks = 0; ks < 4; ks++) { fa[ks] = ld_frag(pa + 64 * ks); fc[ks] = ld_frag(pa2 + 64 * ks); }
      asm volatile("s_waitcnt lgkmcnt(0)" ::: "memory");
      __syncthreads();
#pragma unroll
      for (int ks = 0; ks < 4; ks++) fb[ks] = ld_frag(pb + 64 * ks);
      SCHED_FENCE();
#pragma unroll
      for (int ks = 4; ks < 8; ks++) { fa[ks] = ld_frag(pa + 64 * ks); fb[ks] = ld_frag(pb + 64 * ks); fc[ks] = ld_frag(pa2 + 64 * ks); }
      SCHED_FENCE();
      SCHED_FENCE();
      f32x4 f = zero4, e = zero4;
#pragma unroll
      for (int ks = 0; ks < 8; ks++) { f = MFMA16(fa[ks], fb[ks], f); e = MFMA16(fc[ks], fb[ks], e); }
      const unsigned d0 = pkh(f[0], f[1]);
      const unsigned d1 = pkh(f[2], f[3]), d2 = pkh(e[0], e[1]), d3 = pkh(e[2], e[3]);
      const unsigned p0 = dppu<0x138>(d0), p1 = dppu<0x138>(d1);
      *(volatile LAS u32x4*)kd = u32x4{d0, d1, d2, d3};
      const unsigned a1 = __builtin_amdgcn_alignbit(d2, d1, 16), a2 = __builtin_amdgcn_alignbit(d3, d2, 16), a3 = __builtin_amdgcn_alignbit(p0, d3, 16);
      *(volatile LAS u32x4*)(kd + RDS) = u32x4{__builtin_amdgcn_alignbit(d1, d0, 16), a1, a2, a3};
      *(volatile LAS u32x4*)(kd + 2 * RDS) = u32x4{d1, d2, d3, p0};
      *(volatile LAS u32x4*)(kd + 3 * RDS) = u32x4{a1, a2, a3, __builtin_amdgcn_alignbit(p1, p0, 16)};
      const unsigned q0 = dppu<0xFF>(d0), q1 = dppu<0xFF>(d1);
      const u32x4 t = __builtin_bit_cast(u32x4, bB);
      if (lane >= 60)
        *(volatile LAS u32x4*)(size_t)t[3] =
            u32x4{0u, __builtin_amdgcn_perm(q1, q0, t[0]), __builtin_amdgcn_perm(q1, q0, t[1]), __builtin_amdgcn_perm(q1, q0, t[2])};
    }
    {
      const LAS char* fe = K + KC_R5 + 64;
      asm volatile(DENSE : "+{v[2:5]}"(b9), "+{v[6:9]}"(b7), "+{v[10:13]}"(bA), "+{v22}"(fe), "+{v26}"(yb)
                   : "{v23}"(kd2), "{v24}"(ks0), "{v25}"(uf16), "{v27}"(prg) : DENSE_CLOB, "memory");
    }
  } else {
  asm volatile("s_waitcnt lgkmcnt(0)" ::: "memory");
  __syncthreads();
  polls(0);
  half8 td[6];
  auto mid_td = [&]() {
    const LAS unsigned* t5 = (const LAS unsigned*)pa2;
#pragma unroll
    for (int e = 0; e < 3; e++) {
      td[e] = ld_toep<8>(t5 + 16 * (2 - e));
      td[3 + e] = ld_toep<8>(t5 + (KC_R3 - KC_R5) / 4 + 16 * (2 - e));
    }
  };
  if (wave == 3) {
    half8 fa[24], fb[24];
    f32x4 f[3] = {zero4, zero4, zero4};
    constexpr int PF = PFM;
#pragma unroll
    for (int i = 0; i < PF; i++) { fa[i] = ld_frag(pa + (i >> 3) * TABB + 64 * (i & 7)); fb[i] = ld_frag(pb + (i >> 3) * TABB + 64 * (i & 7)); }
    SCHED_FENCE();
    auto put = [&](int c) {
      const unsigned d0 = pkh(f[c][0], f[c][1]), d1 = pkh(f[c][2], f[c][3]);
      const unsigned w0 = __builtin_amdgcn_perm(d1, d0, ks0), w1 = __builtin_amdgcn_perm(d1, d0, ks1);
      *(LAS unsigned*)(kd + c * CH2) = w0;
      *(LAS unsigned*)(kd2 + c * CH2) = w1;
      if (c15 >= 8) {
        *(LAS u16*)(kd + c * CH2 + 190) = (u16)w0;
        *(LAS u16*)(kd + c * CH2 + 192) = (u16)(w0 >> 16);
        *(LAS u16*)(kd + c * CH2 + (KC_R7 - KC_R9) + 190) = (u16)w1;
        *(LAS u16*)(kd + c * CH2 + (KC_R7 - KC_R9) + 192) = (u16)(w1 >> 16);
      }
    };
    static_for<0, 24>([&](auto ic) {
      constexpr int i = decltype(ic)::value, n = i + PF;
      f[i >> 3] = MFMA16(fa[i], fb[i], f[i >> 3]);
      SCHED_FENCE();
      if constexpr (n < 24) { fa[n] = ld_frag(pa + (n >> 3) * TABB + 64 * (n & 7)); fb[n] = ld_frag(pb + (n >> 3) * TABB + 64 * (n & 7)); }
      if constexpr (i == 14) {
        put(0);
        asm volatile("" ::: "memory");
        mid_td();
      }
      if constexpr (i == 18) put(1);
      SCHED_FENCE();
    });
    put(2);
    __builtin_amdgcn_s_waitcnt(0x0F70);
  }
  SCHED_FENCE();
  if (wave >= 4) {
    f32x2 dsk[3], xf[2][3];
    LAS char* dst = L + SM_US + 2 * UPAD + 4 * vt;
#pragma unroll
    for (int c = 0; c < 3; c++) {
      dsk[c][0] = __uint_as_float(__builtin_amdgcn_perm(r_dsk[1], r_dsk[0], 0x01000c0cu + 0x02020000u * (c + (b & 1))));
      asm volatile("" : "+v"(dsk[c]), "+v"(dst));
    }
    asm volatile("s_waitcnt vmcnt(2)" : "+v"(v0), "+v"(v1));
    get(true);
#pragma unroll
    for (int c = 0; c < 3; c++) {
      xf[0][c] = unpk(v0[c]);
      xf[1][c] = unpk(v1[c]);
      asm volatile("" : "+v"(xf[0][c]), "+v"(xf[1][c]));
    }
    SCHED_FENCE();
    asm volatile("s_waitcnt vmcnt(0)" : "+v"(st0), "+v"(st1));
    get(false);
    f32x2 uf[2][3];
#pragma unroll
    for (int i = 0; i < 2; i++) {
      const u32x4& st = i ? st1 : st0;
      const f32x2 ri = {__uint_as_float(st[0]), __uint_as_float(st[1])};
      const f32x2 cc = -unpk(st[2]) * ri;
#pragma unroll
      for (int c = 0; c < 3; c++) {
        uf[i][c] = __builtin_elementwise_fma(xf[i][c], ri, cc);
        *(LAS unsigned*)(dst + i * 2048 + c * CH2) = pkc(uf[i][c]);
      }
    }
    SCHED_FENCE();
#pragma unroll
    for (int i = 0; i < 2; i++)
#pragma unroll
      for (int c = 0; c < 3; c++) {
        sb[i][c] = __builtin_elementwise_fma(uf[i][c], f32x2{dsk[c][0], dsk[c][0]}, xf[i][c]);
        asm volatile("" : "+v"(sb[i][c]));
      }
    if (is_ln && (vt >> 3) == (b & 63)) *(LAS unsigned*)(L + SM_MX + (vt & 7) * 4) = b < 64 ? st0[2] : st1[2];
  }
  LAS char* sbw = L + ((wave >> 1) - 2) * YS + ((vt >> 1) & 3) * 320 + ((vt >> 3) & 15) * 16 + (vt & 1) * 8;
  int two = sub == 3 ? 2 : 8;
  asm volatile("" : "+v"(sbw), "+v"(two));
  auto put_sb = [&]() {
#pragma unroll
    for (int c = 0; c < 6; c++) *(volatile LAS f32x2*)(sbw + c % 3 * YC + 2 * YT + c / 3 * 4 * YS) = sb[c / 3][c % 3];
    *(volatile LAS int*)prg = two;
  };
  if (sub < 3) __builtin_amdgcn_s_setprio(SPR);
  else __builtin_amdgcn_s_setprio(MPR & 3);
  __syncthreads();
  {
    if (sub < 3) {
      unsigned long long t0 = __builtin_amdgcn_s_memrealtime();
      if (wave == LSW && lane < 16) __builtin_amdgcn_raw_buffer_store_b64(u32x2{(unsigned)t0, tag}, rs, WS_CK + lane * 2048 + b * 8, 0, SC1);
      if (is_ln && sub == 2) {
        const u32x4 ones = {~0u, ~0u, ~0u, ~0u};
        const int o = (vt & 255) * 16;
#pragma unroll
        for (int i = 0; i < 3; i++) stg16(rs, WS_PART + b * 11520 + o + i * 3072, ones);
        if (o < 2304) stg16(rs, WS_PART + b * 11520 + o + 9216, ones);
#pragma unroll
        for (int i = 0; i < 2 * XSS; i += 3072)
          if (i + 3072 <= 2 * XSS || o < 2 * XSS - i) stg16(rs, WS_XS + b * (2 * XSS) + o + i, ones);
        if (b < 8 * XSS / 3072) stg16(rs, WS_XS + 2 * NLN * XSS + b * 3072 + o, ones);
      }
      SCHED_FENCE();
      __builtin_amdgcn_s_setprio(0);
      put_sb();
      if (wave == LSW) {
        __builtin_amdgcn_s_sleep(LSD);
        const int lag = last_start(rs, tag, lane, bid, (unsigned)t0, u32x4{0u, ~tag, 0u, 0u}, u32x4{0u, 0u, 0u, 0u});
        t0 += lag;
        *(volatile LAS unsigned long long*)(L + SM_PRG + 48) =
            t0 + (DLY_H - (b >= 96 ? 0 : (b >= HB5 ? HE5 : (b >= HB4 ? HE4 : HE3))));
        *(LAS int*)(L + FS_SEL) = (int)t0;
      }
    } else {
      if (wave != 3) {
        put_sb();
        mid_td();
      }
      f32x4 a2[6], w[4];
#pragma unroll
      for (int i = 0; i < 6; i++) a2[i] = zero4;
      half8 uw[12];
#pragma unroll
      for (int jj = 0; jj < 6; jj++) uw[jj] = ld_frag(uf16 - 48 + 256 * jj);
      SCHED_FENCE();
      w[0] = MFMA16(uw[0], bA, zero4); w[1] = MFMA16(uw[1], bA, zero4);
      w[2] = MFMA16(uw[0], bB, zero4); w[3] = MFMA16(uw[1], bB, zero4);
      half8 X5 = pack_win(w[0], w[1]), X3 = pack_win(w[2], w[3]);
      SCHED_FENCE();
#pragma unroll
      for (int jj = 0; jj < 6; jj++) {
        if (jj < 5) {
          w[0] = MFMA16(uw[2 * jj + 2], bA, zero4);
          if (jj < 3) uw[2 * jj + 6] = ld_frag(uf16 - 48 + 256 * (2 * jj + 6));
          SCHED_FENCE();
          w[1] = MFMA16(uw[2 * jj + 3], bA, zero4);
          if (jj < 3) uw[2 * jj + 7] = ld_frag(uf16 - 48 + 256 * (2 * jj + 7));
          SCHED_FENCE();
          w[2] = MFMA16(uw[2 * jj + 2], bB, zero4);
        }
        if (jj > 0) *(volatile LAS f32x4*)(yb + (jj - 1) * YS) = a2[jj - 1];
        SCHED_FENCE();
        if (jj < 5) w[3] = MFMA16(uw[2 * jj + 3], bB, zero4);
        if (jj > 0) *(volatile LAS int*)prg = 2 + jj;
        if (jj > 0 && jj < 5 && (MPR >> 4 * jj & 3) != (MPR >> 4 * (jj - 1) & 3)) set_prio(MPR >> 4 * jj & 3);
        SCHED_FENCE();
        u32x4 n5, n3;
#pragma unroll
        for (int q = 0; q < 6; q++) {
          const int e = q >> 1;
          if (jj + e < 6) a2[jj + e] = MFMA16((q & 1) ? X3 : X5, td[3 * (q & 1) + e], a2[jj + e]);
          if (jj < 5 && q < 4) {
            u32x4& nn = q < 2 ? n5 : n3;
            nn[2 * (q & 1)] = pkh(w[q][0], w[q][1]);
            nn[2 * (q & 1) + 1] = pkh(w[q][2], w[q][3]);
          }
          SCHED_FENCE();
        }
        if (jj < 5) {
          X5 = __builtin_bit_cast(half8, n5);
          X3 = __builtin_bit_cast(half8, n3);
        }
      }
      *(volatile LAS f32x4*)(yb + 5 * YS) = a2[5];
      *(volatile LAS int*)prg = 8;
    }
  }
  }
  u32x4 h0, h1;
  int h_asked = b / NLN, hoff = WS_ZP + b * (NCONV * 128) + (tid >> 3) * 256 + (tid & 7) * 16;
  asm volatile("" : "={v[6:9]}"(h0), "={v[2:5]}"(h1), "+v"(hoff));
  unsigned jobs = (unsigned)((wave < 4 ? JOBS0 : (wave < 8 ? JOBS1 : JOBS2)) >> 20 * (wave & 3)) & 0xfffffu;
  if (jobs) {
    set_prio(PPR);
    const LAS char* pp = L + SM_PRG + 4 * min(lane, 11);
    int seen = *(const volatile LAS int*)pp;
    const u32x4 isc = *(const LAS u32x4*)(L + SM_ISC);
    unsigned p, t, n = SPIN_CAP;
    const LAS char* yp = L + ((lane >> 1) & 3) * 320 + (lane >> 3) * 16 + (lane & 1) * 8;
    const int zo = WS_ZP + ((lane >> 3) * NCONV + b) * 128 + (lane & 7) * 16;
#define PR3(r, o)                                                                                       \
  "ds_read_b64 v[" #r ":" #r "+1], v10 offset:" #o "\n\tds_read_b64 v[" #r "+2:" #r "+3], v10 offset:" #o "+3840\n\t" \
  "ds_read_b64 v[" #r "+4:" #r "+5], v10 offset:" #o "+7680\n\t"
#define PF3(r)                                                                                                 \
  "v_fmac_f32 v16, v12, v[" #r "]\n\tv_fmac_f32 v17, v12, v[" #r "+1]\n\tv_fmac_f32 v18, v13, v[" #r "+2]\n\t" \
  "v_fmac_f32 v19, v13, v[" #r "+3]\n\tv_fmac_f32 v20, v14, v[" #r "+4]\n\tv_fmac_f32 v21, v14, v[" #r "+5]\n\t"
#define PASK(l)                                                                                                              \
  "ds_read_b64 v[34:35], %19\n\ts_setprio %16\n\ts_memrealtime %7\n\ts_cmp_lg_u32 %6, 0\n\ts_waitcnt lgkmcnt(0)\n\ts_cbranch_scc1 " #l "f\n\t"       \
  "v_cmp_ge_u64 vcc, %7, v[34:35]\n\ts_cbranch_vccz " #l "f\n\tbuffer_load_dwordx4 %4, %18, %12, 0 offen sc1\n\t"                  \
  "buffer_load_dwordx4 %5, %18, %12, 0 offen offset:128 sc1\n\ts_mov_b32 %6, 4\n" #l ":\n\t"
    unsigned long long now;
    asm volatile(
        "v_mov_b32 v11, %20\n\tv_mov_b32 v37, %13\n"
        "1:\n\ts_and_b32 %1, %0, 31\n\ts_sub_u32 %1, %1, 1\n\ts_lshr_b32 %2, %1, 1\n\ts_lshl_b32 vcc_lo, %1, 7\n\ts_mul_i32 vcc_hi, %2, 11520-256\n\t"
        "s_add_u32 vcc_lo, vcc_lo, vcc_hi\n\tv_add_u32 v10, vcc_lo, %8\n\ts_mul_i32 %1, %1, 196608\n"
        "2:\n\tv_cmp_gt_i32 vcc, %2, v11\n\ts_cbranch_vccz 3f\n\ts_sleep %14\n\tds_read_b32 v11, %10\n\ts_sub_u32 %3, %3, 1\n\t"
        "s_cmp_gt_i32 %3, 0\n\ts_waitcnt lgkmcnt(0)\n\ts_cbranch_scc1 2b\n"
        "3:\n\ts_setprio %17\n\tds_read_b32 v11, %10\n\t" PR3(16, 2560) PR3(22, 0) "s_cmp_lt_u32 %2, 2\n\ts_cbranch_scc1 4f\n\t" PR3(28, 1280) "\n"
        "4:\n\t" PASK(7) "v_cmp_ge_i32 vcc, %2, v11\n\ts_cbranch_vccz 5f\n\ts_cmp_lt_u32 %2, 2\n\ts_cbranch_scc1 8f\n\t"
        "s_and_b32 vcc_lo, vcc_lo, 0x777\n\ts_cbranch_scc0 9f\n"
        "8:\n\ts_sleep %15\n\ts_sub_u32 %3, %3, 1\n\ts_cmp_gt_i32 %3, 0\n\ts_cbranch_scc1 3b\n"
        "9:\n\t" PF3(22) "\n"
        "10:\n\ts_setprio %17\n\tds_read_b32 v11, %10\n\t" PR3(28, 1280) PASK(11) "v_cmp_ge_i32 vcc, %2, v11\n\ts_cbranch_vccz 12f\n\t"
        "s_sub_u32 %3, %3, 1\n\ts_cmp_gt_i32 %3, 0\n\ts_cbranch_scc1 10b\n\ts_branch 12f\n"
        "5:\n\t" PF3(22) "s_cmp_lt_u32 %2, 2\n\ts_cbranch_scc1 6f\n12:\n\t" PF3(28) "\n"
        "6:\n\tv_cvt_pk_bf16_f32 v34, v16, v17\n\tv_cvt_pk_bf16_f32 v35, v18, v19\n\tv_cvt_pk_bf16_f32 v36, v20, v21\n\t"
        "s_lshr_b32 %0, %0, 5\n\tbuffer_store_dwordx4 v[34:37], %9, %12, %1 offen sc1\n\ts_cbranch_scc1 1b\n\ts_nop 1"
        : "+s"(jobs), "=&s"(p), "=&s"(t), "+s"(n), "+{v[6:9]}"(h0), "+{v[2:5]}"(h1), "+s"(h_asked), "=&s"(now)
        : "v"(yp), "v"(zo), "v"(pp), "{v[12:15]}"(isc), "s"(rq), "s"(tag), "n"(PSL), "n"(PSE), "n"(PPR), "n"(WPR),
          "v"(hoff), "v"(L + SM_PRG + 48), "v"(seen)
        : "v10", "v11", "v16", "v17", "v18", "v19", "v20", "v21", "v22", "v23", "v24", "v25", "v26", "v27", "v28", "v29", "v30", "v31", "v32",
          "v33", "v34", "v35", "v36", "v37", "vcc", "scc", "memory");
    static_assert(YT == 1280 && 8 * NCONV * 128 == 196608, "constants above");
  }
  auto ask_h = [&](int tries, int mark) {
    unsigned long long due, now;
    asm volatile(
        "s_cmp_lg_u32 %2, 0\n\ts_cbranch_scc1 3f\n1:\n\tds_read_b64 %4, %6\n\ts_memrealtime %5\n\ts_waitcnt lgkmcnt(0)\n\t"
        "v_cmp_ge_u64 vcc, %5, %4\n\ts_cbranch_vccnz 2f\n\t"
        "s_sub_u32 %3, %3, 1\n\ts_cbranch_scc1 3f\n\ts_sleep 1\n\ts_cmp_lg_u32 %3, 0\n\ts_cbranch_scc1 1b\n2:\n\t"
        "buffer_load_dwordx4 %0, %7, %8, 0 offen sc1\n\tbuffer_load_dwordx4 %1, %7, %8, 0 offen offset:128 sc1\n\ts_mov_b32 %2, %9\n3:"
        : "+{v[6:9]}"(h0), "+{v[2:5]}"(h1), "+s"(h_asked), "+s"(tries), "=&v"(due), "=&s"(now)
        : "v"((unsigned)(size_t)L + SM_PRG + 48), "v"(hoff), "s"(rq), "s"(mark) : "vcc", "scc", "memory");
  };
  __builtin_amdgcn_s_setprio(0);
  ask_h(0, 1);
  ftag = tag;
  if (b < LATE0) {
  LAS unsigned* const selp = (LAS unsigned*)(L + SM_ROW + 256);
  unsigned t_key = 0;
  auto select_rows = [&]() {
    const SelMasks sm(lane);
    const unsigned due = __builtin_amdgcn_readfirstlane(*(const LAS int*)(L + FS_SEL));
    while ((int)((unsigned)__builtin_amdgcn_s_memrealtime() - due) < DLY_L) __builtin_amdgcn_s_sleep(1);
    select8<false>(rs, tag, lane, sm, ws, bid, t_key, [&](unsigned k, auto step) {
      if (step == 2) selp[key_slot(lane)] = k;
    });
  };
  if (is_ln) {
    f32x2 wf[6], hv[6];
    const unsigned mxp = *(const LAS unsigned*)(L + SM_MX + (tid & 7) * 4);
#pragma unroll
    for (int i = 0; i < 6; i++) {
      const float t = bflo(*(const LAS u16*)(L + SM_WR + (tid >> 3) * 12 + i * 2));
      wf[i] = f32x2{t, t};
    }
    float wsum = 0.f, mxr = bflo(*(const LAS u16*)(L + SM_MX + (lane >> 2) * 2));
    unsigned one2 = 0x3f803f80u, sel0 = 0x00003f80u, sel1 = 0x3f800000u;
    asm volatile("" : "+s"(one2), "+s"(sel0), "+s"(sel1));
    if (wave == 0) {
#pragma unroll
      for (int k = 0; k < 9; k++) wsum = dot2(*(const LAS unsigned*)(L + SM_WR + (hl + 32 * k) * 4), one2, wsum);
      wsum = red32_sum(wsum);
      __builtin_amdgcn_s_setprio(2);
    }
#pragma unroll
    for (int i = 0; i < 6; i++) {
      hv[i] = -unpk(mxp);
      asm volatile("" : "+v"(hv[i]), "+v"(wf[i]));
    }
    LAS char* redw = L + SM_RED + ((lane & 7) * 8 + (lane >> 5) * 4 + ((lane >> 4) & 1)) * 48 + wave * 8 + (wave >= 6 ? 48 : 0);
    const LAS char* redr = L + SM_RED + lane * 48;
    unsigned kidx = (unsigned)(SEQ - 1 - 16 * b - (lane >> 2)) << 2;
    int kro = WS_KR + (lane & (8 * NKS - 1)) * 2048 + b * 16, xso = WS_XS + b * (2 * XSS) + (tid >> 3) * 12;
    asm volatile("" : "+v"(wsum), "+v"(mxr), "+v"(redw), "+v"(redr), "+v"(kidx), "+v"(kro), "+v"(xso));
    ask_h(SPIN_CAP, 1);
    {
      int n = SPIN_CAP;
      unsigned long long m;
      asm volatile(
          "s_cmp_gt_u32 %4, 3\n\ts_waitcnt vmcnt(1)\n\ts_cbranch_scc1 2f\n1:\n\ts_waitcnt vmcnt(0)\n2:\n\t"
          "v_cmp_ne_u32 vcc, %5, v9\n\tv_cmp_ne_u32 %3, %5, v5\n\ts_or_b64 vcc, vcc, %3\n\ts_cbranch_scc0 3f\n\t"
          "s_sleep 4\n\ts_mov_b64 exec, vcc\n\tbuffer_load_dwordx4 %0, %6, %7, 0 offen sc1\n\t"
          "buffer_load_dwordx4 %1, %6, %7, 0 offen offset:128 sc1\n\ts_mov_b64 exec, -1\n\ts_sub_u32 %2, %2, 1\n\ts_cbranch_scc0 1b\n\t"
          "s_waitcnt vmcnt(0)\n3:"
          : "+{v[6:9]}"(h0), "+{v[2:5]}"(h1), "+s"(n), "=&s"(m) : "s"(h_asked), "s"(tag), "v"(hoff), "s"(rq) : "vcc", "scc", "memory");
      v0 = h0;
      v1 = h1;
    }
    {
      f32x2 ss, qq, pp;
#pragma unroll
      for (int i = 0; i < 6; i++) {
        const unsigned t = i < 3 ? v0[i] : v1[i - 3];
        hv[i] = f32x2{dot2(t, sel0, hv[i][0]), dot2(t, sel1, hv[i][1])};
        ss = i ? ss + hv[i] : hv[i];
        qq = i ? __builtin_elementwise_fma(hv[i], hv[i], qq) : hv[i] * hv[i];
        pp = i ? __builtin_elementwise_fma(hv[i], wf[i], pp) : hv[i] * wf[i];
      }
      float s0 = ss[0], s1 = ss[1], q0 = qq[0], q1 = qq[1], p0 = pp[0], p1 = pp[1];
      asm volatile(
          "s_nop 1\n\tv_permlane32_swap_b32 %0, %1\n\tv_permlane32_swap_b32 %2, %3\n\tv_permlane32_swap_b32 %4, %5\n\t"
          "v_add_f32 %0, %0, %1\n\tv_add_f32 %2, %2, %3\n\tv_add_f32 %4, %4, %5\n\ts_nop 0\n\t"
          "v_permlane16_swap_b32 %0, %2\n\tv_add_f32_dpp %1, %4, %4 row_ror:8 row_mask:0xf bank_mask:0xf\n\t"
          "v_add_f32 %2, %0, %2\n\ts_nop 1\n\tv_add_f32_dpp %0, %2, %2 row_ror:8 row_mask:0xf bank_mask:0xf"
          : "+v"(s0), "+v"(s1), "+v"(q0), "+v"(q1), "+v"(p0), "+v"(p1));
      *(LAS f32x2*)redw = f32x2{s0, s1};
    }
    __syncthreads();
    if (wave == 0) {
      unsigned long long clk;
      asm volatile("s_memrealtime %0" : "=s"(clk) : : "memory");
      const volatile LAS f32x4* rp = (const volatile LAS f32x4*)redr;
      const f32x4 t0 = rp[0], t1 = rp[1], t2 = rp[2];
      const f32x2 t = ((f32x2{t0[0], t0[1]} + f32x2{t0[2], t0[3]}) + (f32x2{t1[0], t1[1]} + f32x2{t1[2], t1[3]})) + (f32x2{t2[0], t2[1]} + f32x2{t2[2], t2[3]});
      float a = t[0], p = t[1];
      asm volatile("s_waitcnt lgkmcnt(0)" : "+v"(a), "+v"(p), "+s"(clk));
      const unsigned now = (unsigned)clk;
      a += dppf<0x4E>(a);
      p += dppf<0x4E>(p);
      p += dppf<0xB1>(p);
      const float mv = dppf<0x00>(a) * (1.0f / DM);
      const float rinv = ln_rinv(dppf<0x55>(a), mv);
      const float dl = rbf(mxr + mv) - mxr;
      const unsigned rb = pkbf(0.f, fmaf(-dl, wsum, p) * rinv);
      const unsigned key = ((rb ^ (unsigned)(((int)rb >> 31) | (int)0x80000000)) & 0xffff0000u) | kidx;
      unsigned k1 = max(key, dppu<0x141>(key)), k2 = min(key, dppu<0x141>(key)), k3;
      {
        const unsigned lo = min(k1, dppu<0x140>(k1)), hi = max(k2, dppu<0x140>(k2));
        k1 = max(k1, dppu<0x140>(k1));
        k2 = max(lo, hi);
        k3 = min(lo, hi);
      }
      {
        const unsigned lo = min(k1, dppu<0x142>(k1)), hi = max(k2, dppu<0x142>(k2)), w = max(k3, dppu<0x142>(k3));
        k1 = max(k1, dppu<0x142>(k1));
        k2 = max(lo, hi);
        k3 = max(min(lo, hi), w);
      }
      {
        const unsigned lo = min(k1, dppu<0x143>(k1)), hi = max(k2, dppu<0x143>(k2)), w = max(k3, dppu<0x143>(k3));
        k1 = max(k1, dppu<0x143>(k1));
        k2 = max(lo, hi);
        k3 = max(min(lo, hi), w);
      }
      if (lane >= 64 - 8 * NKS) stg16(rs, kro, u32x4{k1, k2 | 1u, __builtin_amdgcn_perm(k3, now, 0x07060100u), tag});
      SCHED_FENCE();
      const unsigned second = (unsigned)__builtin_amdgcn_readlane((int)k2, 63);
      if ((lane & 3) == 0) {
        LAS float* ri = (LAS float*)(L + SM_ROW + (lane >> 3) * 16 + (lane & 4));
        ri[0] = -dl * rinv;
        ri[2] = rinv;
        const unsigned fk = key | (key > second ? 0u : (key == second ? 1u : 2u));
        *(LAS unsigned*)(L + SM_ROW + 128 + lane) = fk;
        __builtin_amdgcn_raw_buffer_store_b64(u32x2{fk, tag}, rs, WS_KF + b * 128 + lane * 2, 0, SC1);
      }
      __builtin_amdgcn_s_setprio(0);
    }
    __syncthreads();
    {
      const f32x4 ri = *(const LAS f32x4*)(L + SM_ROW + (tid & 7) * 16);
      const u32x2 kk = *(const LAS u32x2*)(L + SM_ROW + 128 + (tid & 7) * 8);
      const f32x2 c2 = {ri[0], ri[1]}, r2 = {ri[2], ri[3]};
      unsigned wp[6];
#pragma unroll
      for (int i = 0; i < 6; i++) {
        const f32x2 w2 = __builtin_elementwise_fma(hv[i], r2, c2);
        wp[i] = pkbf(w2[0], w2[1]);
      }
#pragma unroll
      for (int i = 0; i < 2; i++)
        if (!(kk[i] & 2)) {
          u32x3 o;
#pragma unroll
          for (int k = 0; k < 3; k++) o[k] = min(__builtin_amdgcn_perm(wp[2 * k + 1], wp[2 * k], i ? 0x07060302u : 0x05040100u), 0xfffffffeu);
          __builtin_amdgcn_raw_buffer_store_b96(o, rs, xso + (int)(kk[i] & 1) * XSS + DM * 2, 0, SC1);
        }
      SCHED_FENCE();
      LAS char* dst = L + SM_T2 + (tid & 7) * T2S + (tid >> 3) * 48;
#pragma unroll
      for (int i = 0; i < 6; i += 2) *(LAS u32x4*)(dst + 8 * i) = u32x4{i < 3 ? v0[i] : v1[i - 3], wp[i], i < 2 ? v0[i + 1] : v1[i - 2], wp[i + 1]};
    }
    __syncthreads();
    const int pr = tid / 72, ck = tid - pr * 72;
    u32x4 hq[2], wq[2];
    u32x2 key = {2u, 2u};
    auto put_row = [&](int i, int slot) {
      const int o = key_addr(key[i], slot) + ck * 16;
      const u32x4 cap = {0xfffffffeu, 0xfffffffeu, 0xfffffffeu, 0xfffffffeu};
      if (key[i] & 2) stg16(rs, o + DM * 2, __builtin_elementwise_min(wq[i], cap));
      stg16(rs, o, __builtin_elementwise_min(hq[i], cap));
    };
    auto put_out = [&](int i) {
      u32x4 o;
#pragma unroll
      for (int k = 0; k < 4; k++) {
        const f32x2 o2 = unpk(hq[i][k]) + unpk(wq[i][k]);
        o[k] = pkbf(o2[0], o2[1]);
      }
      __builtin_amdgcn_raw_buffer_store_b128(o, ro, (16 * b + 2 * pr + i) * (DM * 2) + ck * 16, 0, SC1);
    };
    if (wave < 9) {
      key = *(const LAS u32x2*)(L + SM_ROW + 128 + pr * 8);
#pragma unroll
      for (int k = 0; k < 4; k++) {
        const u32x4 t = *(const LAS u32x4*)(L + SM_T2 + pr * T2S + ck * 64 + k * 16);
        hq[0][k] = __builtin_amdgcn_perm(t[2], t[0], 0x05040100u);
        hq[1][k] = __builtin_amdgcn_perm(t[2], t[0], 0x07060302u);
        wq[0][k] = __builtin_amdgcn_perm(t[3], t[1], 0x05040100u);
        wq[1][k] = __builtin_amdgcn_perm(t[3], t[1], 0x07060302u);
      }
#pragma unroll
      for (int i = 0; i < 2; i++) {
        if (!(key[i] & 2)) put_row(i, 0);
        else put_out(i);
      }
    } else if (wave == 10) {
      select_rows();
    }
    __syncthreads();
    if (wave < 9) {
#pragma unroll
      for (int i = 0; i < 2; i++) {
        int slot = 8;
#pragma unroll
        for (int r = 0; r < 8; r++) slot = selp[r] == key[i] ? r : slot;
        if (slot < 8) {
          if (key[i] & 2) {
            DRAIN();
            put_row(i, slot);
          }
        } else if (!(key[i] & 2)) {
          put_out(i);
        }
      }
    }
  } else {
    __syncthreads();
    if (wave == 10) select_rows();
  }
  if (is_ln && b >= RED1) {
    __builtin_amdgcn_endpgm();
  }
  {
    const int co = 8 * rb + 4 * (lane & 1);
    unsigned hv = 0u, wv = 0u, oo = 0u;
    float sg = 0.f;
    LAS float* red = (LAS float*)(L + SM_RED);
    LAS char* rw = (LAS char*)red + (wave < 10 ? wave * 128 + (lane & 7) * 16 + (lane >> 4) * 4 : lane * 8);
    int part_off = WS_PART + (tid >> 3) * (8 * DM * 4) + rb * 128 + (tid & 7) * 16;
    asm volatile("" : "+v"(rw), "+v"(part_off));
    f32x2 hf = {0.f, 0.f}, wf = {0.f, 0.f};
    int o = 0;
    auto get_rows = [&]() {
      asm volatile("buffer_load_dword %0, %2, %3, 0 offen offset:1152 sc1\n\tbuffer_load_dword %1, %2, %3, 0 offen sc1"
                   : "+v"(wv), "+v"(hv) : "v"(o), "s"(rq) : "memory");
    };
    if (wave == 10) {
      const int slot = (lane >> 1) & 7;
      const unsigned key = *(volatile LAS unsigned*)(selp + slot);
      o = key_addr(key, slot) + co;
      get_rows();
      const unsigned sb = key ^ ((int)key < 0 ? 0x80000000u : 0xffffffffu);
      sg = rbf(__builtin_amdgcn_rcpf(1.0f + __expf(-bfhi(sb))));
      oo = (unsigned)(key_row(key) * (DM * 2) + co);
      asm volatile("" : "+v"(sg), "+v"(oo));
      const int look = __builtin_amdgcn_readfirstlane(*(const LAS int*)(L + FS_SEL)) + DLY_K - (int)t_key;
      const int due = min(max(look + DLY_F, look < PRM ? DLY_C + PEN : DLY_C), 1000);
      while ((int)((unsigned)__builtin_amdgcn_s_memrealtime() - t_key) < due) __builtin_amdgcn_s_sleep(1);
    }
    __syncthreads();
    if (wave < 10) {
      u32x4 v, w;
      asm volatile("buffer_load_dwordx4 %0, %2, %3, 0 offen sc1\n\ts_sleep %4\n\tbuffer_load_dwordx4 %1, %2, %3, 0 offen sc1"
                   : "=&v"(v), "=&v"(w) : "v"(part_off), "s"(rq), "n"(PPS) : "memory");
      u32x4 got = {~0u, 0u, 0u, 0u};
      _Pragma("unroll 1") for (int spin = 0; spin < SPIN_CAP; spin++) {
        asm volatile("s_waitcnt vmcnt(1)" : "+v"(v));
        if (got[0] == ~0u) got = v;
        if (__all(got[0] != ~0u)) break;
        if (got[0] == ~0u) asm volatile("buffer_load_dwordx4 %0, %1, %2, 0 offen sc1" : "+v"(v) : "v"(part_off), "s"(rq) : "memory");
        asm volatile("s_waitcnt vmcnt(1)" : "+v"(w));
        if (got[0] == ~0u) got = w;
        if (__all(got[0] != ~0u)) break;
        if (got[0] == ~0u) asm volatile("buffer_load_dwordx4 %0, %1, %2, 0 offen sc1" : "+v"(w) : "v"(part_off), "s"(rq) : "memory");
      }
#define v got
      auto r0 = __builtin_amdgcn_permlane32_swap(v[0], v[2], false, false);
      auto r1 = __builtin_amdgcn_permlane32_swap(v[1], v[3], false, false);
      const float s0 = __uint_as_float(r0[0]) + __uint_as_float(r0[1]), s1 = __uint_as_float(r1[0]) + __uint_as_float(r1[1]);
      auto r2 = __builtin_amdgcn_permlane16_swap(__float_as_uint(s0), __float_as_uint(s1), false, false);
      float a = __uint_as_float(r2[0]) + __uint_as_float(r2[1]);
      a += dppf<0x128>(a);
      *(LAS float*)rw = a;
#undef v
      asm volatile("" : : "v"(v), "v"(w));
    } else if (wave == 10) {
      _Pragma("unroll 1") for (int spin = 0; spin < SPIN_CAP; spin++) {
        asm volatile("s_waitcnt vmcnt(0)" : "+v"(wv), "+v"(hv));
        if (__all(wv != ~0u && hv != ~0u)) break;
        __builtin_amdgcn_s_sleep(4);
        get_rows();
      }
      asm volatile("s_waitcnt vmcnt(0)" : "+v"(wv), "+v"(hv));
      hf = unpk(hv);
      wf = unpk(wv);
      asm volatile("" : "+v"(hf), "+v"(wf));
    }
    __syncthreads();
    if (wave == 10 && lane < 16) {
      f32x2 p[10];
#pragma unroll
      for (int w = 0; w < 10; w++) p[w] = *(const volatile LAS f32x2*)(rw + w * 128);
      const f32x2 y = (((p[0] + p[1]) + (p[2] + p[3])) + (p[8] + p[9])) + ((p[4] + p[5]) + (p[6] + p[7]));
      const f32x2 y2 = rbf2(rbf2(y) * sg);
      const f32x2 o2 = hf + rbf2(wf + y2);
      __builtin_amdgcn_raw_buffer_store_b32(pkbf(o2[0], o2[1]), ro, oo, 0, SC1);
      if (b == 0 && lane == 0) __builtin_amdgcn_raw_buffer_store_b32(tag, rs, WS_EPOCH, 0, SC1);
    }
  }
  asm volatile("s_endpgm" ::: "memory");
  __builtin_amdgcn_s_waitcnt(0);
  }
  }
  ffn_role(argp, ftag, L);
}

}

extern "C" void kernel_launch(void* const* d_in, const int* in_sizes, int n_in, void* d_out, int out_size,
                              void* d_ws, size_t ws_size, hipStream_t stream) {
  k_all<<<NCONV + NFFN, NTHR, 0, stream>>>((const u16*)d_in[0], (const u16*)d_in[1], (const u16*)d_in[2], (const u16*)d_in[3],
                                             (const u16*)d_in[4], (const u16*)d_in[5], (char*)d_ws, (const u16*)d_in[6],
                                             (const u16*)d_in[8], (const u16*)d_in[9], (const u16*)d_in[10], (const u16*)d_in[11],
                                             (u16*)d_out);
}
```

```cpp
#include <hip/hip_runtime.h>
#include <stdint.h>
#include <type_traits>

#define J4(a, b, c, d) (unsigned long long)((a) + 1 + (((b) + 1) << 5) + (((c) + 1) << 10) + (((d) + 1) << 15))
#define JOBS0 (J4(12, -1, -1, -1) | J4(13, -1, -1, -1) << 40)
#define JOBS1 (J4(0, 3, 8, 11) | J4(1, 4, 9, -1) << 20 | J4(2, 5, 10, -1) << 40)
#define JOBS2 (J4(6, 14, -1, -1) << 20 | J4(7, 15, -1, -1) << 40)
namespace {

constexpr int SEQ = 2048;
constexpr int DM = 576;
constexpr int NM = 128;
constexpr int FF = 4608;

constexpr int NTHR = 768;
constexpr int NFFN = 64;
constexpr int NPART = 80;
constexpr int NCONV = 192;
constexpr int NLN = 128;
constexpr int RED1 = 96;
constexpr int LATE0 = 176;

typedef unsigned short u16;
#define LAS __attribute__((address_space(3)))
#define GAS __attribute__((address_space(1)))
#define CAS __attribute__((address_space(4)))
using half8 = __attribute__((ext_vector_type(8))) _Float16;
using half2v = __attribute__((ext_vector_type(2))) _Float16;
using bf2v = __attribute__((ext_vector_type(2))) __bf16;
using bf8v = __attribute__((ext_vector_type(8))) __bf16;
using bf16v = __attribute__((ext_vector_type(16))) __bf16;
using s16x8 = __attribute__((ext_vector_type(8))) short;
using s16x4 = __attribute__((ext_vector_type(4))) short;
using f32x2 = __attribute__((ext_vector_type(2))) float;
using f32x4 = __attribute__((ext_vector_type(4))) float;
using u32x2 = __attribute__((ext_vector_type(2))) unsigned;
using u32x3 = __attribute__((ext_vector_type(3))) unsigned;
typedef u32x3 u32x3u __attribute__((aligned(4)));
using u32x4 = __attribute__((ext_vector_type(4))) unsigned;
using u64x2 = __attribute__((ext_vector_type(2))) unsigned long long;
using u64x8 = __attribute__((ext_vector_type(8))) unsigned long long;
using u64x4 = __attribute__((ext_vector_type(4))) unsigned long long;

constexpr int WS_EPOCH = 0;
constexpr int WS_KR = 131072;
constexpr int WS_KF = 4096;
constexpr int WS_ST = WS_KF + 16384;
constexpr int WS_CK = WS_ST + 16384;
constexpr int WS_UP = 262144;
constexpr int WS_ZP = WS_UP + NCONV * NLN * 128;
constexpr int WS_PART = WS_ZP + NCONV * NLN * 128;
constexpr int WS_XS = WS_PART + NPART * 8 * DM * 4;
constexpr int XSS = 4 * DM;
constexpr int WS_END = WS_XS + (2 * NLN + 8) * XSS;
static_assert(WS_CK + 16 * 2048 <= WS_KR, "ws");
constexpr int SC1 = 16;
constexpr int SPIN_CAP = 1 << 16;
constexpr int DLY_H = 223, HE5 = 5, HE4 = 25, HE3 = 40, HB5 = 72, HB4 = 48;
constexpr int DLY_K = 353, DLY_L = DLY_K + 30;
constexpr int DLY_C = 195, DLY_F = 168, PRM = 20, PEN = 50;
constexpr int KPP = 4, PPS = 6, PSL = 1, PSE = 1, LSD = 6;
constexpr int MPR = 0x1233, WPR = 0, PPR = 3, SPR = 3;
constexpr int SELW = 8, LSW = 8, NKS = 2, ED = 3, PFM = 8;

__device__ __forceinline__ float bflo(unsigned v) { return __uint_as_float(v << 16); }
__device__ __forceinline__ float bfhi(unsigned v) { return __uint_as_float(v & 0xffff0000u); }
__device__ __forceinline__ unsigned pkbf(float a, float b) {
  f32x2 f = {a, b};
  return __builtin_bit_cast(unsigned, __builtin_convertvector(f, bf2v));
}
__device__ __forceinline__ unsigned pkh(float a, float b) {
  f32x2 f = {a, b};
  return __builtin_bit_cast(unsigned, __builtin_convertvector(f, half2v));
}
__device__ __forceinline__ float rbf(float f) { return __uint_as_float(pkbf(0.f, f)); }
__device__ __forceinline__ f32x2 rbf2(f32x2 v) { return f32x2{rbf(v[0]), rbf(v[1])}; }
__device__ __forceinline__ f32x2 unpk(unsigned v) { return f32x2{bflo(v), bfhi(v)}; }
__device__ __forceinline__ float dot2(unsigned a, unsigned b, float c) {
  return __builtin_amdgcn_fdot2_f32_bf16(__builtin_bit_cast(bf2v, a), __builtin_bit_cast(bf2v, b), c, false);
}
__device__ __forceinline__ float hadd(f32x2 v) {
  float a = v[0], b = v[1];
  asm volatile("" : "+v"(a), "+v"(b));
  return a + b;
}

template <int CTRL>
__device__ __forceinline__ float dppf(float v) {
  return __int_as_float(__builtin_amdgcn_update_dpp(0, __float_as_int(v), CTRL, 0xf, 0xf, true));
}
template <int CTRL>
__device__ __forceinline__ unsigned dppu(unsigned v) {
  return (unsigned)__builtin_amdgcn_update_dpp(0, (int)v, CTRL, 0xf, 0xf, true);
}
__device__ __forceinline__ float swap16_sum(float v) {
  auto r = __builtin_amdgcn_permlane16_swap(__float_as_uint(v), __float_as_uint(v), false, false);
  return __uint_as_float(r[0]) + __uint_as_float(r[1]);
}
__device__ __forceinline__ float swap32_sum(float v) {
  auto r = __builtin_amdgcn_permlane32_swap(__float_as_uint(v), __float_as_uint(v), false, false);
  return __uint_as_float(r[0]) + __uint_as_float(r[1]);
}
__device__ __forceinline__ float red32_sum(float v) {
  v += dppf<0xB1>(v);
  v += dppf<0x4E>(v);
  v += dppf<0x141>(v);
  v += dppf<0x140>(v);
  return swap16_sum(v);
}
#define SCHED_FENCE() __builtin_amdgcn_sched_barrier(0)
#define DRAIN() asm volatile("s_waitcnt vmcnt(0)" ::: "memory")

__device__ __forceinline__ float red16_sum(float v) {
  v += dppf<0xB1>(v);
  v += dppf<0x4E>(v);
  v += dppf<0x141>(v);
  return v + dppf<0x140>(v);
}
__device__ __forceinline__ float ln_rinv(float q, float dm) {
  const float var = fmaxf(fmaf(q, 1.0f / DM, -dm * dm), 0.f);
  return __builtin_amdgcn_rcpf(rbf(__builtin_amdgcn_sqrtf(rbf(rbf(var) + 1e-5f))));
}

constexpr int TAB_STRIDE = 544;
constexpr int TAB_ROWS = 58;
constexpr int TR_M = 26, TR_F = 34, TR_H = 42;
constexpr int TABB = TAB_ROWS * TAB_STRIDE;
constexpr int SM_KC = 3 * TABB;
constexpr int RDS = 336 * 4;
constexpr int KC_RD = SM_KC;
constexpr int KC_R5 = KC_RD + 4 * RDS;
constexpr int KC_R3 = KC_R5 + 64 * 4;
constexpr int KC_R9 = KC_R3 + 64 * 4;
constexpr int KC_R7 = KC_R9 + 96 * 4;
constexpr int SM_US = KC_R7 + 96 * 4;
constexpr int UPAD = 256;
constexpr int CH2 = SM_US + (UPAD + SEQ) * 2 - SM_KC;
static_assert(SM_US + 2 * UPAD <= SM_KC + 8192 && 8192 <= CH2 && 2 * TABB + 256 < 65536, "zero fill");
constexpr int YT = 1280, YC = 3 * YT, YS = 3 * YC;
constexpr int SM_T2 = 0, T2S = 4624;
constexpr int SM_RED = SM_KC + 3 * CH2;
constexpr int SM_ROW = SM_RED + 3200;
constexpr int SM_ISC = SM_ROW + 128 + 128 + 32;
constexpr int SM_MX = SM_ISC + 16;
constexpr int SM_WR = SM_MX + 32;
constexpr int SM_DUMMY = SM_WR + DM * 2;
constexpr int SM_PRG = SM_DUMMY + 256;
constexpr int RG = 8192;
constexpr int FS_X = 18 * RG;
constexpr int FS_SEL = FS_X + 9216;
constexpr int FS_GU = FS_SEL + 16;
constexpr int SM_TOTAL = FS_GU + 8 * 160;
static_assert(SM_PRG + 64 <= SM_TOTAL && 8 * YS <= 3 * TABB && SM_ISC % 16 == 0 && CH2 % 16 == 0 && TABB % 16 == 0, "lds");

typedef f32x2 cf;
#define TM(t, a, b) "v_pk_mul_f32 v[" #t ":" #t "+1], v[" #b ":" #b "+1], v[" #a ":" #a "+1] op_sel:[1,0]\n\t"
#define TF(d, a, b, t, n) \
  "v_pk_fma_f32 v[" #d ":" #d "+1], v[" #a ":" #a "+1], v[" #b ":" #b "+1], v[" #t ":" #t "+1] op_sel:[0,0,1] op_sel_hi:[1,0,0] neg_" #n ":[0,0,1]\n\t"
#define TC16(c, v) "v_cvt_pk_f16_f32 v" #c ", v[" #v "], v[" #v "+1]\n\t"
#define TWR(c, row) "ds_write_addtid_b32 v" #c " offset:544*" #row "\n\t"
#define TAB_A \
  TC16(134, 100) TM(106, 102, 102) TM(108, 100, 104) TWR(134, 0) TF(110, 102, 102, 106, lo) TF(112, 100, 104, 108, lo) \
  TM(114, 100, 102) TM(116, 100, 110) TC16(135, 112) TF(118, 100, 102, 114, lo) TF(120, 100, 110, 116, lo) TWR(135, 8) \
  TC16(136, 118) TC16(137, 120) TM(122, 112, 110) TWR(136, 1) TM(124, 118, 104) TF(126, 112, 110, 122, lo) TWR(137, 2) \
  TF(128, 118, 104, 124, lo) TC16(134, 126) TM(130, 126, 110) TC16(135, 128) TWR(134, 10) TF(132, 126, 110, 130, lo) \
  TM(106, 118, 110) TWR(135, 9) TC16(136, 132) TF(108, 118, 110, 106, lo) TM(114, 132, 104) TWR(136, 12) TC16(137, 108) \
  TF(102, 132, 104, 114, lo) TM(116, 132, 110) TWR(137, 3) TC16(134, 102) TF(100, 132, 110, 116, lo) TM(122, 102, 104) TWR(134, 18) \
  TC16(135, 100) TF(124, 102, 104, 122, lo) TM(130, 128, 110) TWR(135, 14) TC16(136, 124) TF(106, 128, 110, 130, lo) \
  TM(118, 124, 104) TWR(136, 19) TC16(137, 106) TF(114, 124, 104, 118, lo) TM(116, 100, 104) TWR(137, 11) TC16(134, 114) \
  TF(132, 100, 104, 116, lo) TM(122, 114, 104) TWR(134, 26) TC16(135, 132) TF(102, 114, 104, 122, lo) TM(130, 120, 110) \
  TWR(135, 17) TC16(136, 102) TF(128, 120, 110, 130, lo) TM(118, 108, 110) TWR(136, 28) TC16(137, 128) TF(124, 108, 110, 118, lo) \
  TM(116, 106, 110) TWR(137, 4) TC16(134, 124) TF(100, 106, 110, 116, lo) TM(122, 112, 104) TWR(134, 5) TC16(135, 100) \
  TF(114, 112, 104, 122, lo) TM(130, 126, 104) TWR(135, 13) TC16(136, 114) TF(120, 126, 104, 130, lo) TM(118, 132, 110) \
  TWR(136, 21) TC16(137, 120) TF(108, 132, 110, 118, lo) TM(116, 102, 104) TWR(137, 20) TC16(134, 108) TF(106, 102, 104, 116, lo) \
  TM(122, 128, 110) TWR(134, 16) TC16(135, 106) TF(112, 128, 110, 122, lo) TM(130, 124, 110) TWR(135, 30) TC16(136, 112) \
  TF(126, 124, 110, 130, lo) TM(118, 100, 110) TWR(136, 6) TC16(137, 126) TF(116, 100, 110, 118, lo) TM(102, 114, 104) TWR(137, 7) \
  TC16(134, 116) TF(122, 114, 104, 102, lo) TM(128, 120, 104) TWR(134, 15) TC16(135, 122) TF(112, 120, 104, 128, lo) \
  TM(130, 132, 104) TWR(135, 23) TC16(136, 112) TF(124, 132, 104, 130, lo) TM(126, 108, 104) TWR(136, 22) TC16(137, 124) \
  TF(118, 108, 104, 126, lo) TM(110, 106, 104) TWR(137, 25) TC16(134, 118) TF(100, 106, 104, 110, lo) TWR(134, 24) TC16(135, 100) \
  TWR(135, 32)
#define TAB_B \
  "v_mov_b32 v137, 0x3c00\n\t" TWR(137, 42) TC16(134, 102) TM(106, 104, 104) TM(108, 100, 102) TWR(134, 43) \
  TF(110, 104, 104, 106, lo) TF(112, 100, 102, 108, hi) TM(114, 102, 102) TM(116, 110, 110) TF(118, 110, 110, 116, lo) \
  TF(120, 102, 102, 114, lo) TC16(135, 120) TM(122, 112, 118) TM(124, 120, 102) TWR(135, 44) TF(126, 112, 118, 122, lo) \
  TF(128, 120, 102, 124, lo) TM(130, 120, 120) TC16(136, 126) TC16(137, 128) TF(132, 120, 120, 130, lo) TWR(136, 27) \
  TM(106, 126, 104) TC16(134, 132) TWR(137, 45) TF(108, 126, 104, 106, lo) TM(100, 126, 110) TWR(134, 46) TC16(135, 108) \
  TF(116, 126, 110, 100, lo) TM(114, 108, 110) TWR(135, 29) TC16(136, 116) TF(122, 108, 110, 114, lo) TM(112, 102, 132) \
  TWR(136, 31) TC16(137, 122) TF(124, 102, 132, 112, lo) TM(130, 120, 132) TWR(137, 33) TC16(134, 124) TF(106, 120, 132, 130, lo) \
  TM(104, 128, 132) TWR(134, 47) TC16(135, 106) TF(100, 128, 132, 104, lo) TM(114, 132, 132) TWR(135, 48) TC16(136, 100) \
  TF(110, 132, 132, 114, lo) TM(112, 124, 132) TWR(136, 49) TC16(137, 110) TF(102, 124, 132, 112, lo) TM(130, 106, 132) \
  TWR(137, 50) TC16(134, 102) TF(120, 106, 132, 130, lo) TM(104, 100, 132) TWR(134, 51) TC16(135, 120) TF(128, 100, 132, 104, lo) \
  TM(114, 126, 118) TWR(135, 52) TC16(136, 128) TF(112, 126, 118, 114, lo) TM(124, 108, 118) TWR(136, 53) TC16(137, 112) \
  TF(130, 108, 118, 124, lo) TM(106, 116, 118) TWR(137, 34) TC16(134, 130) TF(104, 116, 118, 106, lo) TM(100, 122, 118) \
  TWR(134, 35) TC16(135, 104) TF(114, 122, 118, 100, lo) TM(126, 110, 132) TWR(135, 36) TC16(136, 114) TF(124, 110, 132, 126, lo) \
  TM(108, 102, 132) TWR(136, 37) TC16(137, 124) TF(106, 102, 132, 108, lo) TM(116, 120, 132) TWR(137, 54) TC16(134, 106) \
  TF(100, 120, 132, 116, lo) TM(122, 128, 132) TWR(134, 55) TC16(135, 100) TF(126, 128, 132, 122, lo) TM(110, 112, 118) \
  TWR(135, 56) TC16(136, 126) TF(124, 112, 118, 110, lo) TM(108, 130, 118) TWR(136, 57) TC16(137, 124) TF(102, 130, 118, 108, lo) \
  TM(106, 104, 118) TWR(137, 38) TC16(134, 102) TF(116, 104, 118, 106, lo) TM(120, 114, 118) TWR(134, 39) TC16(135, 116) \
  TF(100, 114, 118, 120, lo) TWR(135, 40) TC16(136, 100) TWR(136, 41)
#define TAB_CLOB "v106", "v107", "v108", "v109", "v110", "v111", "v112", "v113", "v114", "v115", "v116", "v117", "v118", "v119", "v120", "v121", "v122", "v123", "v124", "v125", "v126", "v127", "v128", "v129", "v130", "v131", "v132", "v133", "v134", "v135", "v136", "v137"
__device__ __forceinline__ unsigned pkc(cf a) { return __builtin_bit_cast(unsigned, __builtin_convertvector(a, half2v)); }
__device__ __forceinline__ cf cexp(f32x2 hi, f32x2 lo) {
  float mag = __builtin_amdgcn_exp2f(hi[0]);
  mag = fmaf(mag, lo[0], mag);
  const float fr = __builtin_amdgcn_fractf(hi[1]) + lo[1];
  return cf{__builtin_amdgcn_cosf(fr), __builtin_amdgcn_sinf(fr)} * mag;
}
__device__ __forceinline__ unsigned wave_max_u32(unsigned v) {
  v = max(v, (unsigned)__builtin_amdgcn_update_dpp(0, (int)v, 0x111, 0xf, 0xf, false));
  v = max(v, (unsigned)__builtin_amdgcn_update_dpp(0, (int)v, 0x112, 0xf, 0xf, false));
  v = max(v, (unsigned)__builtin_amdgcn_update_dpp(0, (int)v, 0x114, 0xf, 0xf, false));
  v = max(v, (unsigned)__builtin_amdgcn_update_dpp(0, (int)v, 0x118, 0xf, 0xf, false));
  v = max(v, (unsigned)__builtin_amdgcn_update_dpp(0, (int)v, 0x142, 0xa, 0xf, false));
  v = max(v, (unsigned)__builtin_amdgcn_update_dpp(0, (int)v, 0x143, 0xc, 0xf, false));
  return (unsigned)__builtin_amdgcn_readlane((int)v, 63);
}

#define MFMA16(a, b, c) __builtin_amdgcn_mfma_f32_16x16x32_f16((a), (b), (c), 0, 0, 0)
#define MFMAB(a, b, c) __builtin_amdgcn_mfma_f32_16x16x32_bf16((a), (b), (c), 0, 0, 0)

__device__ __forceinline__ half8 ld_frag(const LAS char* p) { return *(const LAS half8*)p; }
template <int O2>
__device__ __forceinline__ half8 ld_toep(const LAS unsigned* q) {
  u32x4 v = {q[0], q[1], q[O2], q[O2 + 1]};
  return __builtin_bit_cast(half8, v);
}
__device__ __forceinline__ half8 band_frag(int lane, unsigned pat) {
  unsigned t = __builtin_amdgcn_ubfe(pat << (lane & 15), 8 * (lane >> 4), 8);
  t |= t << 15;
  u32x4 v;
#pragma unroll
  for (int j = 0; j < 4; j++) v[j] = __umul24(t & (0x10001u << (2 * j)), 0x3c00u >> (2 * j));
  return __builtin_bit_cast(half8, v);
}
__device__ __forceinline__ half8 pack_win(f32x4 a, f32x4 b) {
  u32x4 v = {pkh(a[0], a[1]), pkh(a[2], a[3]), pkh(b[0], b[1]), pkh(b[2], b[3])};
  return __builtin_bit_cast(half8, v);
}

__device__ __forceinline__ void set_prio(int p) {
  if (p == 0) __builtin_amdgcn_s_setprio(0);
  if (p == 1) __builtin_amdgcn_s_setprio(1);
  if (p == 2) __builtin_amdgcn_s_setprio(2);
  if (p == 3) __builtin_amdgcn_s_setprio(3);
}
template <int V>
using IC = std::integral_constant<int, V>;
template <int S, int E, class F>
__device__ __forceinline__ void static_for(F&& f) {
  if constexpr (S < E) {
    f(std::integral_constant<int, S>{});
    static_for<S + 1, E>(f);
  }
}
#define VR(r) "v[" #r ":" #r "+3]"
#define DT(r, o) "ds_read_b64 v[" #r ":" #r "+1], v23 offset:" #o "\n\tds_read_b64 v[" #r "+2:" #r "+3], v23 offset:" #o "+8\n\t"
#define DP(r, o) "ds_read2_b32 v[" #r ":" #r "+1], v24 offset0:" #o " offset1:" #o "+1\n\t"
#define DU(r, o) "ds_read_b128 " VR(r) ", v25 offset:" #o "\n\t"
#define DW(n) "s_waitcnt lgkmcnt(" #n ")\n\t"
#define DM(c, a, b) "v_mfma_f32_16x16x32_f16 " VR(c) ", " VR(a) ", " VR(b) ", " VR(c) "\n\t"
#define DZ(c, a, b) "v_mfma_f32_16x16x32_f16 " VR(c) ", " VR(a) ", " VR(b) ", 0\n\t"
#define DC(d, w) "v_cvt_pk_f16_f32 v" #d ", v" #w ", v[" #w "+1]\n\t"
#define DY(c, t) "ds_write_b128 v26, " VR(c) " offset:11520*" #t "\n\t"
#define DA "v_add_u32 v26, 4*11520, v26\n\t"
#define DV(n) "v_mov_b32 v22, " #n "\n\t"
#define DF "ds_write_b32 v27, v22\n\t"
#define DFE "ds_read_u16 v22, v22\n\t"
#define DFP "v_lshl_or_b32 v22, v22, 16, v22\n\t"
#define DTE(p) "v_pk_mul_f16 v" #p ", v" #p ", v22\n\t"
#define DBAR "s_waitcnt vmcnt(0)\n\ts_barrier\n\t"
#define DENSE \
  DT(56, 576) DT(88, 64) DT(52, 640) DT(84, 128) DT(48, 704) DT(80, 192) DT(44, 768) DT(76, 256) DT(40, 832) DT(72, 320) DT(36, 896) \
  DT(68, 384) DT(32, 960) DT(64, 448) DT(28, 1024) DT(60, 512) DT(92, 0) DBAR DU(104, 576) DU(108, 640) DU(112, 704) DU(116, 768) DFE DW(4) \
  DZ(124, 56, 104) DU(120, 832) DU(96, 896) DZ(128, 88, 104) DW(5) DM(124, 52, 108) DU(100, 960) DU(104, 1024) DM(128, 84, 108) DU(14, 1008) \
  DW(7) DM(124, 48, 112) DU(108, 1088) DM(128, 80, 112) DW(6) DM(124, 44, 116) DU(112, 1152) DM(128, 76, 116) DFP DW(2) DM(124, 40, 120) \
  DU(116, 1216) DM(128, 72, 120) DTE(10) DZ(14, 14, 2) DTE(11) DM(124, 36, 96) DU(120, 1280) DM(128, 68, 96) DTE(12) DM(124, 32, 100) \
  DU(96, 1344) DM(128, 64, 100) DTE(13) DM(124, 28, 104) DU(100, 1408) DM(128, 60, 104) DC(144, 14) DZ(132, 92, 104) DC(145, 16) \
  DZ(136, 10, 104) DZ(18, 104, 6) DW(5) DM(128, 56, 108) DU(104, 1472) DM(132, 88, 108) DU(14, 1520) DW(6) DM(128, 52, 112) DU(108, 1536) \
  DM(132, 84, 112) DV(1) DW(6) DM(128, 48, 116) DU(112, 1600) DM(132, 80, 116) DY(124, 0) DW(7) DM(128, 44, 120) DU(116, 1664) \
  DM(132, 76, 120) DF DW(5) DM(128, 40, 96) DU(120, 1728) DM(132, 72, 96) DZ(14, 14, 2) DM(128, 36, 100) DU(96, 1792) DM(132, 68, 100) \
  DC(148, 18) DM(128, 32, 104) DU(100, 1856) DM(132, 64, 104) DC(149, 20) DW(7) DM(128, 28, 108) DU(104, 1920) DM(132, 60, 108) DC(146, 14) \
  DZ(124, 92, 108) DC(147, 16) DZ(140, 10, 108) DZ(18, 108, 6) DW(7) DM(132, 56, 112) DU(108, 1984) DM(124, 88, 112) DU(14, 2032) DW(7) \
  DM(132, 52, 116) DU(112, 2048) DM(124, 84, 116) DV(2) DW(6) DM(132, 48, 120) DU(116, 2112) DM(124, 80, 120) DY(128, 1) DW(7) \
  DM(132, 44, 96) DU(120, 2176) DM(124, 76, 96) DF DW(5) DM(132, 40, 100) DU(96, 2240) DM(124, 72, 100) DZ(14, 14, 2) DM(132, 36, 104) \
  DU(100, 2304) DM(124, 68, 104) DC(150, 18) DM(132, 32, 108) DU(104, 2368) DM(124, 64, 108) DC(151, 20) DW(7) DM(132, 28, 112) \
  DU(108, 2432) DM(124, 60, 112) DC(152, 14) DM(136, 92, 112) DC(153, 16) DZ(128, 10, 112) DZ(18, 112, 6) DW(7) DM(124, 56, 116) \
  DU(112, 2496) DM(136, 88, 116) DU(14, 2544) DW(7) DM(124, 52, 120) DU(116, 2560) DM(136, 84, 120) DV(3) DW(6) DM(124, 48, 96) \
  DU(120, 2624) DM(136, 80, 96) DY(132, 2) DW(7) DM(124, 44, 100) DU(96, 2688) DM(136, 76, 100) DF DW(5) DM(124, 40, 104) DU(100, 2752) \
  DM(136, 72, 104) DZ(14, 14, 2) DM(124, 36, 108) DU(104, 2816) DM(136, 68, 108) DC(156, 18) DM(124, 32, 112) DU(108, 2880) DM(136, 64, 112) \
  DC(157, 20) DW(7) DM(124, 28, 116) DU(112, 2944) DM(136, 60, 116) DC(154, 14) DM(140, 92, 116) DC(155, 16) DZ(132, 10, 116) DZ(18, 116, 6) \
  DW(7) DM(136, 56, 120) DU(116, 3008) DP(10, 32) DM(140, 88, 120) DP(8, 24) DW(8) DM(136, 52, 96) DU(120, 3072) DV(4) DM(140, 84, 96) \
  DC(158, 18) DP(6, 16) DW(8) DM(136, 48, 100) DU(96, 3136) DC(159, 20) DM(140, 80, 100) DY(124, 3) DW(9) DM(136, 44, 104) DU(100, 3200) DF \
  DM(140, 76, 104) DP(20, 128) DW(11) DM(136, 40, 108) DU(104, 3264) DP(18, 120) DM(140, 72, 108) DP(16, 112) DW(10) DM(136, 36, 112) \
  DU(108, 3328) DM(140, 68, 112) DP(4, 8) DM(136, 144, 8) DP(2, 0) DW(11) DM(136, 32, 116) DU(112, 3392) DM(140, 64, 116) DP(14, 104) \
  DM(140, 144, 6) DP(12, 96) DM(136, 28, 120) DU(116, 3456) DM(140, 60, 120) DA DM(128, 92, 120) DW(8) DM(140, 56, 96) DU(120, 3520) \
  DM(128, 88, 96) DM(136, 148, 18) DW(8) DM(140, 52, 100) DU(96, 3584) DM(128, 84, 100) DM(140, 148, 16) DM(140, 48, 104) DU(100, 3648) \
  DM(128, 80, 104) DV(5) DW(9) DM(140, 44, 108) DU(104, 3712) DM(128, 76, 108) DY(136, 0) DW(8) DM(140, 40, 112) DU(108, 3776) \
  DM(128, 72, 112) DF DM(128, 144, 4) DW(7) DM(140, 36, 116) DU(112, 3840) DM(128, 68, 116) DM(128, 152, 8) DW(7) DM(140, 32, 120) \
  DU(116, 3904) DM(128, 64, 120) DM(128, 148, 14) DW(7) DM(140, 28, 96) DU(120, 3968) DM(128, 60, 96) DM(132, 92, 96) DW(7) DM(128, 56, 100) \
  DU(96, 4032) DM(132, 88, 100) DM(128, 156, 18) DW(7) DM(128, 52, 104) DU(100, 4096) DM(132, 84, 104) DV(6) DM(132, 144, 2) DW(6) \
  DM(128, 48, 108) DU(104, 4160) DM(132, 80, 108) DY(140, 1) DW(6) DM(128, 44, 112) DU(108, 4224) DM(132, 76, 112) DF DW(7) DM(128, 40, 116) \
  DU(112, 4288) DM(132, 72, 116) DM(132, 152, 6) DW(7) DM(128, 36, 120) DU(116, 4352) DM(132, 68, 120) DM(132, 148, 12) DW(7) \
  DM(128, 32, 96) DU(120, 4416) DM(132, 64, 96) DM(132, 156, 16) DW(7) DM(128, 28, 100) DU(96, 4480) DM(132, 60, 100) DW(7) DM(132, 56, 104) \
  DU(100, 4544) DW(6) DM(132, 52, 108) DU(104, 4608) DV(7) DW(5) DM(132, 48, 112) DY(128, 2) DW(5) DM(132, 44, 116) DF DW(5) \
  DM(132, 40, 120) DW(4) DM(132, 36, 96) DW(3) DM(132, 32, 100) DW(2) DM(132, 28, 104) DV(8) "s_nop 6\n\t" DY(132, 3) DF
#define DENSE_CLOB "v14", "v15", "v16", "v17", "v18", "v19", "v20", "v21", "v28", "v29", "v30", "v31", "v32", "v33", "v34", "v35", "v36", "v37", "v38", "v39", "v40", "v41", "v42", "v43", "v44", "v45", "v46", "v47", "v48", "v49", "v50", "v51", "v52", "v53", "v54", "v55", "v56", "v57", "v58", "v59", "v60", "v61", "v62", "v63", "v64", "v65", "v66", "v67", "v68", "v69", "v70", "v71", "v72", "v73", "v74", "v75", "v76", "v77", "v78", "v79", "v80", "v81", "v82", "v83", "v84", "v85", "v86", "v87", "v88", "v89", "v90", "v91", "v92", "v93", "v94", "v95", "v96", "v97", "v98", "v99", "v100", "v101", "v102", "v103", "v104", "v105", "v106", "v107", "v108", "v109", "v110", "v111", "v112", "v113", "v114", "v115", "v116", "v117", "v118", "v119", "v120", "v121", "v122", "v123", "v124", "v125", "v126", "v127", "v128", "v129", "v130", "v131", "v132", "v133", "v134", "v135", "v136", "v137", "v138", "v139", "v140", "v141", "v142", "v143", "v144", "v145", "v146", "v147", "v148", "v149", "v150", "v151", "v152", "v153", "v154", "v155", "v156", "v157", "v158", "v159"

typedef __amdgpu_buffer_rsrc_t rsrc_t;
__device__ __forceinline__ u32x4 ldg16(rsrc_t rs, int off, int so = 0) {
  asm volatile("" : "+s"(so) : : "memory");
  return __builtin_amdgcn_raw_buffer_load_b128(rs, off, so, SC1);
}
__device__ __forceinline__ void stg16(rsrc_t rs, int off, u32x4 v) { __builtin_amdgcn_raw_buffer_store_b128(v, rs, off, 0, SC1); }
__device__ __forceinline__ void dma16(const char* g, unsigned lds) {
  unsigned keep;
  asm volatile("s_mov_b32 %0, m0\n\ts_mov_b32 m0, %2\n\ts_nop 0\n\tglobal_load_lds_dwordx4 %1, off\n\ts_mov_b32 m0, %0"
               : "=&s"(keep) : "v"(g), "s"(lds) : "memory");
}
__device__ __forceinline__ void dma16nt(const char* g, unsigned lds) {
  unsigned keep;
  asm volatile("s_mov_b32 %0, m0\n\ts_mov_b32 m0, %2\n\ts_nop 0\n\tglobal_load_lds_dwordx4 %1, off nt\n\ts_mov_b32 m0, %0"
               : "=&s"(keep) : "v"(g), "s"(lds) : "memory");
}
__device__ __forceinline__ void dma4(const char* g, unsigned lds) {
  unsigned keep;
  asm volatile("s_mov_b32 %0, m0\n\ts_mov_b32 m0, %2\n\ts_nop 0\n\tglobal_load_lds_dword %1, off\n\ts_mov_b32 m0, %0"
               : "=&s"(keep) : "v"(g), "s"(lds) : "memory");
}

__device__ __forceinline__ int key_row(unsigned k) { return (int)(~(k >> 2) & (SEQ - 1)); }
__device__ __forceinline__ int key_slot(int lane) { return (lane & 3) | ((lane >> 1) & 4); }
__device__ __forceinline__ int key_addr(unsigned k, int slot) {
  return WS_XS + ((k & 2) ? 2 * NLN + slot : (int)((~(k >> 5) & (2 * NLN - 2)) | (k & 1))) * XSS;
}
struct SelMasks {
  unsigned m[15];
  __device__ __forceinline__ SelMasks(int lane) {
    const unsigned b0 = -(lane & 1), b1 = -((lane >> 1) & 1), b3 = -((lane >> 3) & 1), b4 = -((lane >> 4) & 1), b5 = -(lane >> 5);
    const unsigned t[15] = {~(b0 ^ b1), ~(b1 ^ b3), ~(b0 ^ b3), ~b3, ~b1, ~b0, b3, b1, b0, ~(b3 ^ b4), ~(b1 ^ b4), ~(b0 ^ b4), ~(b3 ^ b5), ~(b1 ^ b5), ~(b0 ^ b5)};
#pragma unroll
    for (int i = 0; i < 15; i++) {
      m[i] = t[i];
      asm volatile("" : "+v"(m[i]));
    }
  }
};
template <int CTRL>
__device__ __forceinline__ unsigned cex(unsigned v, unsigned m) {
  const unsigned o = dppu<CTRL>(v);
  return max(min(v, o), min(max(v, o), m));
}
__device__ __forceinline__ unsigned clean8(unsigned v, const unsigned* m) { return cex<0xB1>(cex<0x4E>(cex<0x128>(v, m[0]), m[1]), m[2]); }
template <bool EAGER, class F>
__device__ __forceinline__ void select8(rsrc_t rs, unsigned tag, int lane, const SelMasks& sm, char* ws, int bid, unsigned& stamp, F&& use) {
  int off = WS_KR + (bid & 7) * 2048 + lane * 16, so = EAGER ? 0 : (NKS - 1) * 16384;
  unsigned m1, t1, t2, m4, third;
  u32x4 a, c, d, e;
  if (EAGER) {
    const u32x4 rq = {(unsigned)(size_t)ws, (unsigned)((size_t)ws >> 32) & 0xffffu, (unsigned)WS_END, 0x00020000u};
    unsigned long long t;
    int n = SPIN_CAP;
#define KASK(r)                                                                                                                        \
  "buffer_load_dwordx4 v[" #r ":" #r "+3], %12, %13, %11 offen sc1\n\tbuffer_load_dwordx4 v[" #r "+4:" #r "+7], %12, %13, %11 offen offset:1024 sc1\n\t" \
  "s_add_u32 %11, %11, 16384\n\ts_min_u32 %11, %11, %16\n\t"
#define KCHK(r, l)                                                                                                          \
  "s_waitcnt vmcnt(2)\n\tv_cmp_eq_u32 vcc, %14, v[" #r "+3]\n\tv_cmp_eq_u32 %9, %14, v[" #r "+7]\n\ts_and_b64 %9, %9, vcc\n\t" \
  "s_cmp_eq_u64 %9, -1\n\ts_cbranch_scc1 " #l "\n\t"
#define KUSE(r)                                                                                                             \
  "v_max_u32 %0, v[" #r "], v[" #r "+4]\n\tv_min_u32 %1, v[" #r "], v[" #r "+4]\n\tv_max_u32 %2, v[" #r "+1], v[" #r "+5]\n\t"     \
  "v_min_u32 %3, v[" #r "+1], v[" #r "+5]\n\tv_max_u32 %4, v[" #r "+2], v[" #r "+6]\n\t"
    asm volatile(KASK(148) "s_sleep %15\n1:\n\t" KASK(156) KCHK(148, 2f) KASK(148) KCHK(156, 3f) "s_sub_u32 %10, %10, 1\n\ts_cbranch_scc0 1b\n"
                 "3:\n\t" KUSE(156) "s_branch 4f\n2:\n\t" KUSE(148) "\n4:"
                 : "=&v"(m1), "=&v"(t1), "=&v"(t2), "=&v"(m4), "=&v"(third), "=&{v[148:151]}"(a), "=&{v[152:155]}"(c), "=&{v[156:159]}"(d),
                   "=&{v[160:163]}"(e), "=&s"(t), "+s"(n), "+s"(so)
                 : "v"(off), "s"(rq), "s"(tag), "n"(KPP), "n"((NKS - 1) * 16384) : "vcc", "scc", "memory");
  } else {
    _Pragma("unroll 1") for (int spin = 0; spin < SPIN_CAP; spin++) {
      a = ldg16(rs, off, so);
      c = ldg16(rs, off, so + 1024);
      if (__builtin_expect(__all(a[3] == tag && c[3] == tag), 1)) break;
      __builtin_amdgcn_s_sleep(4);
    }
    const unsigned now = (unsigned)__builtin_amdgcn_s_memrealtime();
    stamp = now - 0xffffu + wave_max_u32(max((a[2] - now - 1u) & 0xffffu, (c[2] - now - 1u) & 0xffffu));
    m1 = max(a[0], c[0]), t1 = min(a[0], c[0]), t2 = max(a[1], c[1]), m4 = min(a[1], c[1]), third = max(a[2], c[2]);
  }
  third |= 0xffffu;
  unsigned m2 = max(t1, t2), m3 = min(t1, t2);
  unsigned x = clean8(cex<0xB1>(cex<0x4E>(cex<0xB1>(m1, sm.m[0]), sm.m[1]), sm.m[2]), sm.m + 3);
  const unsigned y = clean8(cex<0xB1>(cex<0x4E>(cex<0xB1>(m2, sm.m[0]), sm.m[1]), sm.m[2]), sm.m + 6);
  x = clean8(max(x, y), sm.m + 3);
  x = clean8(max(x, dppu<0x140>(x)), sm.m + 9);
  {
    auto r = __builtin_amdgcn_permlane16_swap(x, x, false, false);
    x = clean8(max(r[0], r[1]), sm.m + 12);
    auto q = __builtin_amdgcn_permlane32_swap(x, x, false, false);
    x = max(q[0], q[1]);
  }
  use(x, IC<0>{});
  unsigned k = min(x, dppu<0xB1>(x));
  k = min(k, dppu<0x4E>(k));
  k = min(k, dppu<0x128>(k));
  if (__builtin_expect(__any(max(m3, third) > k), 0)) {
    const int slot = key_slot(lane);
    _Pragma("unroll 1") for (int r = 0; r < 8; r++) {
      k = wave_max_u32(m1);
      x = slot == r ? k : x;
      const bool e = m1 == k;
      m1 = e ? m2 : m1;
      m2 = e ? m3 : m2;
      m3 = e ? m4 : m3;
      m4 = e ? 0u : m4;
    }
    if (__any(third > k)) {
      k = 0u;
      _Pragma("unroll 1") for (int r = 0; r < 8; r++) {
        unsigned v;
        _Pragma("unroll 1") for (int spin = 0; spin < SPIN_CAP; spin++) {
          bool ok = true;
          v = 0u;
          _Pragma("unroll 4") for (int i = 0; i < 16; i++) {
            const u32x4 f = ldg16(rs, WS_KF + lane * 16, i * 1024);
            ok = ok && f[1] == tag && f[3] == tag;
            v = max(v, max(f[0] <= k - 1u ? f[0] : 0u, f[2] <= k - 1u ? f[2] : 0u));
          }
          if (__all(ok)) break;
          __builtin_amdgcn_s_sleep(1);
        }
        k = wave_max_u32(v);
        x = slot == r ? k : x;
      }
    }
    use(x, IC<1>{});
  }
  use(x, IC<2>{});
  if (EAGER) asm volatile("s_waitcnt vmcnt(9)" : : "{v[148:151]}"(a), "{v[152:155]}"(c), "{v[156:159]}"(d), "{v[160:163]}"(e) : "memory");
}

__device__ __forceinline__ int last_start(rsrc_t rs, unsigned tag, int lane, int bid, unsigned ref, u32x4 p, u32x4 q) {
  _Pragma("unroll 1") for (int spin = 0; spin < SPIN_CAP; spin++) {
    if (__all(p[1] == tag && p[3] == tag && q[1] == tag && q[3] == tag)) break;
    __builtin_amdgcn_s_sleep(2);
    p = ldg16(rs, WS_CK + (bid & 7) * 2048 + lane * 16);
    q = ldg16(rs, WS_CK + (bid & 7) * 2048 + 512 + lane * 16);
  }
  const int lag = max(max((int)(p[0] - ref), (int)(p[2] - ref)), max((int)(q[0] - ref), (int)(q[2] - ref)));
  return min((int)wave_max_u32((unsigned)max(lag + 4096, 0)) - 4096, 1000);
}
__device__ __forceinline__ int late_group(size_t wg, int bid) { return (3 - (int)(wg >> 7) - bid) & 7; }
__device__ __forceinline__ int ffn_group(size_t wg, int bid) {
  const int l = late_group(wg, bid), i = bid >> 3;
  return (bid & 7) * 9 + (bid < NFFN ? (i == l ? 8 : i) : l);
}
__device__ __forceinline__ void down_dma(size_t wd, int j, LAS char* L, int wave, int lane, int r0, int r1) {
#pragma unroll
  for (int k = 0; k < 3; k++) {
    const int slot = 3 * (wave - 9) + k, t = slot * 64 + lane, r8 = t / 72, q8 = (t - r8 * 72) ^ (r8 & 2);
    const char* p = (const char*)wd + (unsigned)((j * 64 + r8) * 1152);
    _Pragma("unroll 1") for (int i = r0; i < r1; i++)
        dma16nt(p + (unsigned)(i * 9216 + (q8 ^ ((i & 1) << 2)) * 16), (unsigned)(size_t)L + slot * 1024 + i * 9216);
  }
}
__device__ __forceinline__ unsigned stats_role(size_t argp, unsigned cpf, LAS char* L) {
  const int tid = threadIdx.x, lane = tid & 63, bid = blockIdx.x, fw = bid;
  size_t x, wsp, wd, wg;
  asm volatile(
      "s_waitcnt lgkmcnt(0)\n\ts_load_dwordx2 %0, %4, 0x0\n\ts_load_dwordx2 %1, %4, 0x30\n\ts_load_dwordx2 %2, %4, 0x58\n\t"
      "s_load_dwordx2 %3, %4, 0x48"
      : "=&s"(x), "=&s"(wsp), "=&s"(wd), "=&s"(wg) : "s"(argp));
  SCHED_FENCE();
  unsigned xo0 = (bid * 32 + (tid >> 4)) * (DM * 2) + (lane & 15) * 8;
  asm volatile("" : "+v"(xo0));
  asm volatile("s_waitcnt lgkmcnt(0)" : "+s"(x), "+s"(wsp), "+s"(wd), "+s"(wg));
  const int wave = __builtin_amdgcn_readfirstlane(tid >> 6);
  char* const ws = (char*)(GAS char*)wsp;
  const rsrc_t rs = __builtin_amdgcn_make_buffer_rsrc(ws, 0, WS_END, 0x00020000);
  unsigned tagv = WS_EPOCH;
  asm volatile("global_load_dword %0, %0, %1 sc1" : "+v"(tagv) : "s"(wsp) : "memory");
  SCHED_FENCE();
  unsigned tag;
  if (wave >= 8) {
    __builtin_amdgcn_s_barrier();
    if (wave > 8) down_dma(wd, ffn_group(wg, bid), L, wave, lane, 0, ED);
    else DRAIN();
    asm volatile("s_waitcnt vmcnt(%2)" : "+v"(tagv) : "v"(cpf), "n"(3 * ED));
    tag = __builtin_amdgcn_readfirstlane(tagv) + 1u;
    asm volatile("" : "+s"(tag));
  } else {
    u32x2 xw[9];
#pragma unroll
    for (int k = 0; k < 9; k++) xw[k] = *(GAS const u32x2*)(x + xo0 + 128 * k);
    SCHED_FENCE();
    __builtin_amdgcn_s_barrier();
    unsigned one2 = 0x3f803f80u;
    asm volatile("" : "+s"(one2));
    float s0 = 0.f, s1 = 0.f, q0 = 0.f, q1 = 0.f;
#pragma unroll
    for (int k = 0; k < 9; k++) {
      const unsigned a = xw[k][0], c = xw[k][1];
      s0 = dot2(a, one2, s0);
      q0 = dot2(a, a, q0);
      s1 = dot2(c, one2, s1);
      q1 = dot2(c, c, q1);
    }
    const float mean = red16_sum(s0 + s1) * (1.0f / DM), mbf = rbf(mean);
    float q = red16_sum(q0 + q1), dm = mean;
    if (__builtin_expect(__any(!(q * (1.0f / DM) >= mean * mean * 1.002f)), 0)) {
      f32x2 q2 = {0.f, 0.f};
#pragma unroll
      for (int k = 0; k < 18; k++) {
        const f32x2 dd = unpk(xw[k >> 1][k & 1]) - mbf;
        q2 = __builtin_elementwise_fma(dd, dd, q2);
      }
      q = red16_sum(hadd(q2));
      dm = mean - mbf;
    }
    float ri = ln_rinv(q, dm);
    asm volatile("" : "+v"(tagv), "+v"(ri));
    auto rr = __builtin_amdgcn_permlane16_swap(__float_as_uint(ri), __float_as_uint(ri), false, false);
    auto m2 = __builtin_amdgcn_permlane16_swap(__float_as_uint(mbf), __float_as_uint(mbf), false, false);
    const unsigned mp = __builtin_amdgcn_perm(m2[1], m2[0], 0x07060302u);
    if ((lane & 31) == 0) stg16(rs, WS_ST + (16 * fw + (tid >> 5)) * 16, u32x4{rr[0], rr[1], mp, tagv + 1u});
    tag = __builtin_amdgcn_readfirstlane(tagv) + 1u;
    asm volatile("" : : "v"(cpf), "s"(tag));
  }
  return tag;
}

__device__ __forceinline__ void ffn_role(size_t argp, unsigned tag, LAS char* L) {
  const int tid = threadIdx.x, lane = tid & 63, bid = blockIdx.x;
  size_t wsp, wdp;
  u64x2 gu;
  asm volatile("s_load_dwordx2 %0, %3, 0x30\n\ts_load_dwordx4 %1, %3, 0x48\n\ts_load_dwordx2 %2, %3, 0x58" : "=&s"(wsp), "=&s"(gu), "=&s"(wdp) : "s"(argp));
  SCHED_FENCE();
  const int wave = __builtin_amdgcn_readfirstlane(tid >> 6);
  const int hm = bid < NFFN ? 3 : (bid & 8 ? 2 : 1);
  const bool mine = wave < 8 && (hm >> (wave >> 2) & 1);
  const int g = lane >> 4, tq = (lane & 15) >> 2, tp = lane & 3;
  const bool full = hm == 3;
  const int gr = full ? tid >> 4 : tid >> 3;
  const int gc = full ? (lane & 7) ^ ((gr & 1) | ((gr >> 1) & 6)) : (hm & 2) * 2 + ((lane & 3) ^ ((gr >> 2) & 3));
  unsigned go = gr * (FF * 2) + gc * 16;
  asm volatile("" : "+v"(go));
  asm volatile("s_waitcnt lgkmcnt(0)" : "+s"(wsp), "+s"(gu), "+s"(wdp));
  char* const ws = (char*)(GAS char*)wsp;
  const rsrc_t rs = __builtin_amdgcn_make_buffer_rsrc(ws, 0, WS_END, 0x00020000);
  const int j = ffn_group(gu[0], bid);
  const char* gsrc = (const char*)((lane & (full ? 8 : 4)) ? gu[1] : gu[0]) + (go + j * 128);
  const unsigned lb = (unsigned)(size_t)L + wave * 1024 + 9 * RG;
  auto issue_gu = [&](int n, unsigned step) {
    if (wave < 8) {
      _Pragma("unroll 1") for (int i = 0; i < n; i++) {
        dma16nt(gsrc, lb + i * RG);
        gsrc += step;
      }
    }
  };
  __syncthreads();
  issue_gu(full ? 10 : 9, full ? 32 * FF * 2 : 64 * FF * 2);
  if (wave > 8) down_dma(wdp, j, L, wave, lane, full ? ED : (hm & 2) * 2, hm == 1 ? 4 : 8);
  s16x4 fa[18][2], fd[3][4];
  const int fr = 16 * (g >> 1) + 4 * (g & 1) + (tq & 1) + 8 * (tq >> 1), fx = (g & 1) | ((tq >> 1) << 1);
  const LAS char* gup = L + 9 * RG + (tp & 1) * 8 +
                        (full ? fr * 256 + ((wave ^ ((tq & 1) | (fx << 1))) + 8 * (tp >> 1)) * 16 : fr * 128 + (((wave & 3) ^ fx) + 4 * (tp >> 1)) * 16);
  const LAS char* gup2 = gup + 8 * RG;
  asm volatile("" : "+v"(gup), "+v"(gup2));
  auto read_gu = [&](auto ks0, auto n, auto reg0, auto sz) {
#pragma unroll
    for (int i = 0; i < n; i++)
#pragma unroll
      for (int h = 0; h < 2; h++) {
        const int o = reg0 * RG + i * sz + h * (sz / 16);
        fa[ks0 + i][h] = __builtin_amdgcn_ds_read_tr16_b64_v4i16((LAS s16x4*)(o < 8 * RG ? gup + o : gup2 + (o - 8 * RG)));
      }
  };
  auto read_down = [&]() {
    const int f = (tq >> 1) | ((g & 1) << 1);
#pragma unroll
    for (int t = 0; t < 3; t++) {
      const LAS char* p = L + (8 * g + tq + (hm == 2 ? 32 : 0)) * 1152 + ((3 * wave + t) ^ f) * 32 + tp * 8;
#pragma unroll
      for (int h = 0; h < 4; h++) {
        if (h >= 2 && !full) fd[t][h] = s16x4{0, 0, 0, 0};
        else fd[t][h] = __builtin_amdgcn_ds_read_tr16_b64_v4i16((LAS s16x4*)(p + (32 * (h >> 1) + 4 * (h & 1)) * 1152));
      }
    }
  };
  const int c15 = lane & 15;
  const LAS char* xr = L + FS_X + ((((c15 >> 3) + 2 * g) ^ (2 * tp)) + 2 * (lane & 4) + 16 * tp) * 16 + (wave < 8 ? 0 : (wave - 8) * 2048);
  LAS char* guw = L + FS_GU + (4 * (g & 1) + 2 * (g >> 1) + (c15 >> 3)) * 160 + ((wave ^ (hm == 2 ? 4 : 0)) * 8 + (lane & 7)) * 2;
  const LAS char* gur = L + FS_GU + (lane & 7) * 160 + 16 * g;
  int xo = ((lane & 7) ^ (2 * g)) * 16 + DM * 2;
  int po = WS_PART + (bid < NFFN ? bid : bid - LATE0) * (8 * DM * 4) + (3 * wave + (c15 >> 3)) * 512 + g * 128 + (lane & 7) * 16;
  int sidx = c15 < 8 ? 0x4444 : 0xeeee;
  asm volatile("" : "+v"(guw), "+v"(gur), "+v"(xo), "+v"(po), "+v"(sidx), "+v"(xr));
  if (wave < 8) *(LAS u16*)guw = 0;
  else *(LAS int*)(L + FS_SEL + 4) = 0;
  const SelMasks sm(lane);
  unsigned due = 0;
  if (full) {
    if (wave < 8) asm volatile("s_waitcnt vmcnt(2)" ::: "memory");
    __syncthreads();
    u32x4 cp, cq;
    asm volatile("s_cmp_lg_u32 %4, %5\n\ts_cbranch_scc1 1f\n\tglobal_load_dwordx4 %0, %2, %3 sc1\n\tglobal_load_dwordx4 %1, %2, %3 offset:512 sc1\n1:"
                 : "=&{v[148:151]}"(cp), "=&{v[152:155]}"(cq) : "v"(WS_CK + (8 + (bid & 7)) * 2048 + lane * 16), "s"(wsp), "s"(wave), "n"(SELW) : "scc", "memory");
    if (mine) read_gu(IC<0>{}, IC<8>{}, IC<0>{}, IC<RG>{});
    __syncthreads();
    issue_gu(8, 32 * FF * 2);
    if (wave < 8) asm volatile("s_waitcnt vmcnt(8)" ::: "memory");
    else if (wave != SELW) DRAIN();
    __syncthreads();
    if (mine) read_gu(IC<8>{}, IC<2>{}, IC<8>{}, IC<RG>{});
    read_down();
    __syncthreads();
    if (wave == SELW) {
      due = (unsigned)__builtin_amdgcn_s_memrealtime();
      asm volatile("s_waitcnt vmcnt(0)" : "+{v[148:151]}"(cp), "+{v[152:155]}"(cq));
      due += last_start(rs, tag, lane, bid, due, cp, cq);
    }
  } else {
    due = __builtin_amdgcn_readfirstlane(*(const LAS int*)(L + FS_SEL));
  }
  auto fetch = [&](int xa) {
    asm volatile(
        "s_mov_b32 m0, %2\n\t.set i_, 0\n\t.rept 9\n\ts_nop 0\n\tglobal_load_lds_dwordx4 %0, %1 offset:128*i_ sc1\n\t"
        "s_add_u32 m0, m0, 896\n\t.set i_, i_+1\n\t.endr"
        : : "v"(xa), "s"(wsp + WS_XS), "s"((unsigned)(size_t)L + FS_X) : "scc", "memory");
  };
  if (wave == SELW) {
    while ((int)((unsigned)__builtin_amdgcn_s_memrealtime() - due) < DLY_K) __builtin_amdgcn_s_sleep(1);
    unsigned unused;
    int xa;
    select8<true>(rs, tag, lane, sm, ws, bid, unused, [&](unsigned k, auto step) {
      if (step < 2) {
        asm("s_nop 1\n\tv_add_u32_dpp %0, %1, %2 quad_perm:[0,0,0,0] row_mask:0x1 bank_mask:0xf\n\t"
            "v_add_u32_dpp %0, %1, %2 quad_perm:[1,1,1,1] row_mask:0x2 bank_mask:0xf\n\t"
            "v_add_u32_dpp %0, %1, %2 quad_perm:[2,2,2,2] row_mask:0x4 bank_mask:0xf\n\t"
            "v_add_u32_dpp %0, %1, %2 quad_perm:[3,3,3,3] row_mask:0x8 bank_mask:0xf"
            : "=&v"(xa) : "v"(key_addr(step ? k : k & ~2u, key_slot(lane)) - WS_XS), "v"(xo));
        if (step) DRAIN();
        fetch(xa);
      } else {
        __builtin_amdgcn_s_barrier();
      }
    });
    xo = xa;
  } else {
    DRAIN();
    __syncthreads();
  }
  if (full) {
    if (mine) read_gu(IC<10>{}, IC<8>{}, IC<0>{}, IC<RG>{});
  } else {
    if (mine) read_gu(IC<0>{}, IC<18>{}, IC<0>{}, IC<RG / 2>{});
    read_down();
  }
  f32x4 acc[3];
  _Pragma("unroll 1") for (int spin = 1; spin < SPIN_CAP; spin++) {
    DRAIN();
    __syncthreads();
    if (mine) {
      bf8v xf[9];
#pragma unroll
      for (int i = 0; i < 9; i++) xf[i] = *(const LAS bf8v*)(xr + 1024 * i);
      SCHED_FENCE();
      f32x4 a = {0.f, 0.f, 0.f, 0.f};
#pragma unroll
      for (int i = 0; i < 9; i++) {
        const s16x8 lo = __builtin_shufflevector(fa[2 * i][0], fa[2 * i][1], 0, 1, 2, 3, 4, 5, 6, 7);
        const s16x8 hi = __builtin_shufflevector(fa[2 * i + 1][0], fa[2 * i + 1][1], 0, 1, 2, 3, 4, 5, 6, 7);
        a = __builtin_amdgcn_smfmac_f32_16x16x64_bf16(
            xf[i], __builtin_bit_cast(bf16v, __builtin_shufflevector(lo, hi, 0, 1, 2, 3, 4, 5, 6, 7, 8, 9, 10, 11, 12, 13, 14, 15)), a, sidx, 0, 0);
      }
      const float a0 = a[0], a1 = a[1], a2 = a[2], a3 = a[3];
      auto r0 = __builtin_amdgcn_permlane32_swap(__float_as_uint(a0), __float_as_uint(a2), false, false);
      auto r1 = __builtin_amdgcn_permlane32_swap(__float_as_uint(a1), __float_as_uint(a3), false, false);
      const float s0 = __uint_as_float(r0[0]) + __uint_as_float(r0[1]), s1 = __uint_as_float(r1[0]) + __uint_as_float(r1[1]);
      const float gs = __int_as_float(__builtin_amdgcn_update_dpp(__float_as_int(s0), __float_as_int(s1), 0x128, 0xf, 0xc, false));
      const float us = __int_as_float(__builtin_amdgcn_update_dpp(__float_as_int(s1), __float_as_int(s0), 0x128, 0xf, 0x3, false));
      const float gp = rbf(gs);
      const float gq = rbf(gp * __builtin_amdgcn_rcpf(1.0f + __expf(-gp)));
      *(LAS u16*)guw = (u16)pkbf(gq * rbf(us), 0.f);
    } else if (wave >= 8) {
      const u32x4 t = __builtin_elementwise_max(__builtin_elementwise_max(*(const LAS u32x4*)xr, *(const LAS u32x4*)(xr + 1024)), *(const LAS u32x4*)(xr + 2048));
      if (max(max(t[0], t[1]), max(t[2], t[3])) == ~0u) *(LAS int*)(L + FS_SEL + 4) = spin;
    }
    __syncthreads();
    const bf8v b0 = *(const LAS bf8v*)gur, b1 = *(const LAS bf8v*)(gur + 64);
    const int redo = *(const volatile LAS int*)(L + FS_SEL + 4);
    SCHED_FENCE();
    const f32x4 z4 = {0.f, 0.f, 0.f, 0.f};
#pragma unroll
    for (int t = 0; t < 3; t++) acc[t] = MFMAB(__builtin_bit_cast(bf8v, __builtin_shufflevector(fd[t][0], fd[t][1], 0, 1, 2, 3, 4, 5, 6, 7)), b0, z4);
#pragma unroll
    for (int t = 0; t < 3; t++) acc[t] = MFMAB(__builtin_bit_cast(bf8v, __builtin_shufflevector(fd[t][2], fd[t][3], 0, 1, 2, 3, 4, 5, 6, 7)), b1, acc[t]);
    asm volatile("" : "+v"(acc[0]), "+v"(acc[1]), "+v"(acc[2]));
    SCHED_FENCE();
    if (__builtin_expect(__builtin_amdgcn_readfirstlane(redo) != spin, 1)) break;
    if (wave == SELW) fetch(xo);
  }
  {
    SCHED_FENCE();
    auto put = [&](f32x4 a, int off) {
      u32x4 o = __builtin_bit_cast(u32x4, a);
      o[0] = min(o[0], 0xfffffffeu);
      stg16(rs, po + off, o);
    };
    put(c15 < 8 ? acc[0] : acc[1], 0);
    if (c15 < 8) put(acc[2], 1024);
  }
}

__global__ __launch_bounds__(NTHR, 3) void k_all(const u16*, const u16*, const u16*, const u16*, const u16*, const u16*, char*, const u16*,
                                                 const u16*, const u16*, const u16*, const u16*, u16*) {
  __shared__ __attribute__((aligned(16))) char smem[SM_TOTAL];
  LAS char* const L = (LAS char*)smem;
  const int bid = blockIdx.x;
  const size_t argp = (size_t)__builtin_amdgcn_kernarg_segment_ptr();
  u64x8 a8;
  u64x4 a4;
  size_t a1;
  unsigned cpf;
  {
    size_t pc;
    int t;
    asm volatile("s_load_dwordx16 %0, %3, 0x0\n\ts_load_dwordx8 %1, %3, 0x40\n\ts_load_dwordx2 %2, %3, 0x60" : "=&s"(a8), "=&s"(a4), "=&s"(a1) : "s"(argp));
    asm volatile(
        "s_getpc_b64 %1\n\ts_lshr_b32 %2, %3, 3\n\tv_min_u32 %0, 6, %4\n\ts_mul_i32 %2, %2, 7\n\tv_add_lshl_u32 %0, %0, %2, 7\n\t"
        "global_load_dword %0, %0, %1"
        : "=&v"(cpf), "=&s"(pc), "=&s"(t) : "s"(bid), "v"(threadIdx.x) : "scc");
  }
  SCHED_FENCE();
  const int tid = threadIdx.x, lane = tid & 63, b = bid - NFFN;
  unsigned ftag;
  if (b < 0) {
    ftag = stats_role(argp, cpf, L);
  } else {
  const int wave = __builtin_amdgcn_readfirstlane(tid >> 6);
  const bool is_ln = b < NLN;
  const int hl = lane & 31;
  const int ch = (wave & 3) < 3 ? (wave & 3) : (wave >> 2), sub = (wave & 3) < 3 ? (wave >> 2) : 3;
  const int d = 3 * b + ch;
  const int xt = 4 * b + wave, hrow = 8 * ((xt >> 1) & 127) + (lane & 1) + 2 * (lane >> 4);
  const int grp = ((xt >> 8) << 5) + ((xt & 1) << 4) + ((lane >> 1) & 7);
  unsigned xo = hrow * (DM * 4) + grp * 12, po = d * NM * 2 + lane * 4, lo = 2 * d & ~3;
  int cpo = WS_UP + (grp * 2048 + hrow) * 16;
  {
    LAS char* zc = L + (wave >> 2) * CH2 + SM_KC + 16 * (tid & 255);
    const u32x4 z = {0u, 0u, 0u, 0u};
    *(LAS u32x4*)zc = z;
    *(LAS u32x4*)(zc + 4096) = z;
  }
  LAS char* prg = L + SM_PRG + 4 * wave;
  *(LAS int*)prg = (int)(0xba5080u >> 2 * wave & 3);
  *(LAS int*)(L + SM_PRG + 52) = -1;
  asm volatile("" : "+v"(prg));
  const int xg = b & 7, sl = b >> 3, sl12 = sl < 12 ? sl : sl - 12;
  unsigned dso = 6 * b & ~3;
  asm volatile("" : "+v"(xo), "+v"(po), "+v"(lo), "+v"(cpo), "+v"(dso));
  SCHED_FENCE();
  asm volatile("s_waitcnt lgkmcnt(0)" : "+s"(a8), "+s"(a4), "+s"(a1));
  u32x3 cp[4];
  auto load_x = [&]() {
    if (wave < 4) {
#pragma unroll
      for (int i = 0; i < 4; i++) cp[i] = *(GAS const u32x3u*)(a8[0] + xo + ((i & 1) * (DM * 2) + (i >> 1) * 96));
    }
    SCHED_FENCE();
  };
  const unsigned r_ldt = *(GAS const unsigned*)(a8[5] + lo), r_lar = *(GAS const unsigned*)(a8[1] + po), r_aim = *(GAS const unsigned*)(a8[2] + po);
  const unsigned r_cr = *(GAS const unsigned*)(a8[3] + po), r_ci = *(GAS const unsigned*)(a8[4] + po);
  typedef u32x2 u32x2u __attribute__((aligned(4)));
  u32x2 r_dsk = *(GAS const u32x2u*)(a8[7] + dso);
  char* const ws = (char*)(GAS char*)a8[6];
  const rsrc_t ro = __builtin_amdgcn_make_buffer_rsrc((char*)(GAS char*)a1, 0, SEQ * DM * 2, 0x00020000);
  const rsrc_t rs = __builtin_amdgcn_make_buffer_rsrc(ws, 0, WS_END, 0x00020000);
  const unsigned tagv = __builtin_amdgcn_raw_buffer_load_b32(rs, WS_EPOCH, 0, SC1);
  SCHED_FENCE();
  load_x();
  const int rb = is_ln ? b : b - (NLN - RED1);
  LAS char *const C = L + ch * TABB, *const K = L + ch * CH2;
  const int c15 = lane & 15, g = lane >> 4;
  const int lg = late_group(a4[1], bid);
  const char* pfx = (lane & 31) < 12 ? (const char*)(sl < 12 ? a4[1] : a4[2]) + (unsigned)(((sl12 * 4 + (wave & 3)) * 12 + (lane & 31)) * (FF * 2) + xg * 1152 +
                                                                                            (lg ? lg : (lane >> 5) * 8) * 128)
                                     : (const char*)a4[3] + (unsigned)(((9 * xg + lg) * 576 + (sl * 4 + (wave & 3)) * 6 + min((lane & 31) - 12, 5)) * 128);
  asm volatile("" : "+v"(pfx));
  unsigned hs = (sub & 1) ? 0x03020c0cu : 0x01000c0cu, hs2 = (d & 1) ? 0x03020c0cu : 0x01000c0cu;
  LAS char* yb = L + ch * YC + g * 320 + c15 * 16 + (sub ? 2 * YS + YT : 0);
  const LAS char *pa, *pa2, *pb, *uf16 = K + SM_US + 2 * UPAD + 32 * c15 + 16 * g;
  LAS char *kd, *kd2;
  unsigned ks0, ks1;
  half8 bA, bB, b9, b7;
  if (sub == 0) {
    pa = C + (((unsigned)(0x33982210cdef4567ull >> (4 * c15)) & 15) + 2 * (c15 & 8)) * TAB_STRIDE + 16 * g;
    pa2 = C + (((unsigned)(0x7763554289ab0123ull >> (4 * c15)) & 15) + 2 * (c15 & 8)) * TAB_STRIDE + 16 * g;
    pb = C + (TR_H + c15) * TAB_STRIDE + 16 * g;
    kd = K + KC_RD + 2 * 544 - 16 * lane;
    kd2 = K + KC_RD + (~c15 & 3) * RDS + 2 * (535 - (c15 | 3) + 8 * g) - 64 * 16;
    uf16 -= 32 * 33;
    bA = band_frag(lane, 0x3u << 12);
    const int s = lane & 3;
    const u32x4 t = {s < 2 ? 0x0c0c0c0cu : (s == 2 ? 0x01000c0cu : 0x01000100u), s == 0 ? 0x01000c0cu : 0x01000100u,
                     s < 2 ? 0x01000100u : (s == 2 ? 0x03020100u : 0x05040302u), (unsigned)(size_t)(K + KC_RD + s * RDS + 64)};
    bB = __builtin_bit_cast(half8, t);
    ks0 = (unsigned)(size_t)(K + KC_R9 + 190 * ((c15 + 1) & 1) + 2 * (15 - c15 + 4 * g));
    b9 = band_frag(lane, 0x1ffu << 4);
    b7 = band_frag(lane, 0x7fu << 5);
    asm volatile("" : "+v"(bA), "+v"(pa), "+v"(pa2), "+v"(pb), "+v"(kd), "+v"(kd2), "+v"(ks0), "+v"(uf16), "=v"(ks1), "+v"(bB), "+v"(b9), "+v"(b7));
  } else if (sub < 3) {
    asm volatile("" : "=v"(bA), "=v"(pa), "=v"(pa2), "=v"(pb), "=v"(kd), "=v"(kd2), "=v"(ks0), "=v"(uf16), "=v"(ks1), "=v"(bB));
  } else {
    pa = C + (TR_H + 15 - c15) * TAB_STRIDE + 16 * g;
    pa2 = K + KC_R5 + 2 * (158 - 64 - 2 * c15 + 4 * g) - 128;
    pb = C + (TR_M + c15) * TAB_STRIDE + 16 * g;
    kd = K + (c15 < 8 ? ((c15 & 1) ? KC_R5 : KC_R3) + 2 * (80 - 8 * (c15 & 6) + 4 * g) : KC_R9 + 2 * (120 - 8 * c15 + 2 * g));
    kd2 = kd + (c15 < 8 ? 4 : KC_R7 - KC_R9);
    ks0 = c15 < 8 ? 0x03020100u : 0x05040100u;
    ks1 = c15 < 8 ? 0x07060504u : 0x07060302u;
    uf16 -= 16 * c15;
    bA = band_frag(lane, 0x1fu << 6);
    bB = band_frag(lane, 0x7u << 11);
    asm volatile("" : "+v"(bA), "+v"(pa), "+v"(pa2), "+v"(pb), "+v"(kd), "+v"(kd2), "+v"(ks0), "+v"(uf16), "+v"(ks1), "+v"(bB));
  }
  const unsigned pe = sub < 2 ? 0u : 3u << 23;
  f32x2 kh = {-1.44269502f, 0.159154936f}, kl = {-1.92596299e-8f, 7.13048051e-9f}, k1 = {128.f * 0.693147181f, 128.f}, ln2 = {0.693147181f, 128.f};
  const unsigned pi = sub < 2 ? 0x3f800000u : 0xc1000000u;
  u32x2 k0u = {0x3f317218u + pe, pi}, p0u = {0x3f800000u + pe, pi};
  const int vt = tid - 256;
  int uoff = WS_UP + b * 16384 + vt * 16, soff = WS_ST + vt * 16;
  asm volatile("" : "+v"(yb), "+v"(uoff), "+v"(soff));
  asm volatile("s_mov_b32 m0, %8" : "+s"(hs), "+s"(hs2), "+s"(kh), "+s"(kl), "+s"(k0u), "+s"(k1), "+s"(ln2), "+s"(p0u)
               : "s"((unsigned)(size_t)C + (sub & 1) * 256));
  SCHED_FENCE();
  SCHED_FENCE();
  const unsigned tag = __builtin_amdgcn_readfirstlane(tagv) + 1u;
  auto half = [&](unsigned v) { return __uint_as_float(__builtin_amdgcn_perm(v, v, hs)); };
  f32x2 f_l = {half(r_lar), __uint_as_float(__builtin_amdgcn_perm(r_ldt, r_ldt, hs2))}, f_c = {half(r_cr), half(r_ci)};
  float f_aim = half(r_aim);
  unsigned f_cm;
  asm volatile("v_max3_f32 %0, |%1|, |%2|, |%3|\n\tv_max_f32 %0, %0, |%4|" : "=&v"(f_cm) : "v"(r_cr), "v"(r_ci), "v"(r_cr << 16), "v"(r_ci << 16));
  asm volatile("" : "+v"(f_l), "+v"(f_aim), "+v"(f_c), "+v"(r_dsk) : "v"(cpf));
  SCHED_FENCE();
  SCHED_FENCE();
  if (wave < 4) {
    u32x4 pc[4];
#pragma unroll
    for (int i = 0; i < 12; i++) {
      const int r = i / 6 * 2, k = i % 6 / 2;
      const unsigned t = __builtin_amdgcn_perm(cp[r + 1][k], cp[r][k], (i & 1) ? 0x07060302u : 0x05040100u);
      pc[i / 3][i % 3] = t;
      pc[i / 3][3] = tag;
    }
#pragma unroll
    for (int i = 0; i < 4; i++) stg16(rs, cpo + (i >> 1) * (16 * 1024 * 16) + (i & 1) * 16384, pc[i]);
    if (is_ln && tid >= 184) dma16((const char*)a4[0] + (tid - 184) * 16, (unsigned)(size_t)L + SM_WR + (wave - 3) * 1024 + 128);
    dma4(pfx, (unsigned)(size_t)L + SM_DUMMY);
  }
  SCHED_FENCE();
  u32x4 v0, v1, st0, st1;
  const u32x4 rq = {(unsigned)a8[6], (unsigned)(a8[6] >> 32) & 0xffffu, (unsigned)WS_END, 0x00020000u};
  asm volatile("" : "=v"(v0), "=v"(v1), "=v"(st0), "=v"(st1));
  auto ask = [&](u32x4& p, u32x4& q, int off) {
    asm volatile("buffer_load_dwordx4 %0, %2, %3, 0 offen sc1\n\tbuffer_load_dwordx4 %1, %2, %3, %4 offen sc1"
                 : "+v"(p), "+v"(q) : "v"(off), "s"(rq), "s"(8192) : "memory");
  };
  auto polls = [&](int k) {
    if (wave >= 4 && (wave < 8 ? 0 : 1) == k) ask(v0, v1, uoff);
    if (wave >= 4 && (wave < 8 ? 0 : 1) == k) ask(st0, st1, soff);
    SCHED_FENCE();
  };
  auto get = [&](bool both) {
    _Pragma("unroll 1") for (int spin = 0; spin < SPIN_CAP; spin++) {
      const bool ok = both ? v0[3] == tag && v1[3] == tag : st0[3] == tag && st1[3] == tag;
      if (__builtin_expect(__all(ok), 1)) break;
      __builtin_amdgcn_s_sleep(4);
      if (both) {
        asm volatile("s_waitcnt vmcnt(0)" : "+v"(st0), "+v"(st1));
        if (!ok) ask(v0, v1, uoff);
      }
      if (st0[3] != tag || st1[3] != tag) ask(st0, st1, soff);
      if (both) asm volatile("s_waitcnt vmcnt(0)" : "+v"(v0), "+v"(v1), "+v"(st0), "+v"(st1));
      else asm volatile("s_waitcnt vmcnt(0)" : "+v"(st0), "+v"(st1));
    }
  };
  {
    const f32x2 l2e = -f32x2{kh[0], kh[0]}, th = f_l * l2e;
    const f32x2 tl = __builtin_elementwise_fma(f_l, -f32x2{kl[0], kl[0]}, __builtin_elementwise_fma(f_l, l2e, -th)) * ln2[0];
    const f32x2 e0 = {__builtin_amdgcn_exp2f(th[0]), __builtin_amdgcn_exp2f(th[1])}, ex = __builtin_elementwise_fma(e0, tl, e0);
    const unsigned eb = pkbf(ex[0], ex[1]);
    const float dt = bfhi(eb);
    const f32x2 av = f32x2{bflo(eb), f_aim} * dt;
    const f32x2 hi = av * kh, lo = __builtin_elementwise_fma(av, kl, __builtin_elementwise_fma(av, kh, -hi));
    int se = 385 - (int)(wave_max_u32(f_cm) >> 23) - (int)((unsigned)__builtin_amdgcn_readfirstlane(eb) >> 23);
    se = max(se, 2);
    asm("" : "+s"(se));
    se = min(se, 252);
    const float dts = dt * __uint_as_float((unsigned)(se + 1) << 23);
    cf cc = f_c * dts;
    cf ea = cexp(hi * __builtin_bit_cast(f32x2, p0u), lo * __builtin_bit_cast(f32x2, k0u)), eb2 = cexp(hi * 128.f, lo * k1);
    SCHED_FENCE();
    asm volatile("s_cmp_lt_u32 %3, 2\n\ts_cbranch_scc0 1f\n\t" TAB_A "s_branch 2f\n1:\n\t" TAB_B "\n2:"
                 : "+{v[100:101]}"(cc), "+{v[102:103]}"(ea), "+{v[104:105]}"(eb2) : "s"(sub) : TAB_CLOB, "scc", "memory");
    if (sub == 2) *(volatile LAS unsigned*)(L + SM_ISC + 4 * ch) = (unsigned)(254 - se) << 23;
  }
  SCHED_FENCE();
  polls(1);
  const f32x4 zero4 = {0.f, 0.f, 0.f, 0.f};
  f32x2 sb[2][3];
  if (sub == 0) {
    {
      half8 fa[8], fc[8], fb[8];
#pragma unroll
      for (int ks = 0; ks < 4; ks++) { fa[ks] = ld_frag(pa + 64 * ks); fc[ks] = ld_frag(pa2 + 64 * ks); }
      asm volatile("s_waitcnt lgkmcnt(0)" ::: "memory");
      __syncthreads();
#pragma unroll
      for (int ks = 0; ks < 4; ks++) fb[ks] = ld_frag(pb + 64 * ks);
      SCHED_FENCE();
#pragma unroll
      for (int ks = 4; ks < 8; ks++) { fa[ks] = ld_frag(pa + 64 * ks); fb[ks] = ld_frag(pb + 64 * ks); fc[ks] = ld_frag(pa2 + 64 * ks); }
      SCHED_FENCE();
      SCHED_FENCE();
      f32x4 f = zero4, e = zero4;
#pragma unroll
      for (int ks = 0; ks < 8; ks++) { f = MFMA16(fa[ks], fb[ks], f); e = MFMA16(fc[ks], fb[ks], e); }
      const unsigned d0 = pkh(f[0], f[1]);
      const unsigned d1 = pkh(f[2], f[3]), d2 = pkh(e[0], e[1]), d3 = pkh(e[2], e[3]);
      const unsigned p0 = dppu<0x138>(d0), p1 = dppu<0x138>(d1);
      *(volatile LAS u32x4*)kd = u32x4{d0, d1, d2, d3};
      const unsigned a1 = __builtin_amdgcn_alignbit(d2, d1, 16), a2 = __builtin_amdgcn_alignbit(d3, d2, 16), a3 = __builtin_amdgcn_alignbit(p0, d3, 16);
      *(volatile LAS u32x4*)(kd + RDS) = u32x4{__builtin_amdgcn_alignbit(d1, d0, 16), a1, a2, a3};
      *(volatile LAS u32x4*)(kd + 2 * RDS) = u32x4{d1, d2, d3, p0};
      *(volatile LAS u32x4*)(kd + 3 * RDS) = u32x4{a1, a2, a3, __builtin_amdgcn_alignbit(p1, p0, 16)};
      const unsigned q0 = dppu<0xFF>(d0), q1 = dppu<0xFF>(d1);
      const u32x4 t = __builtin_bit_cast(u32x4, bB);
      if (lane >= 60)
        *(volatile LAS u32x4*)(size_t)t[3] =
            u32x4{0u, __builtin_amdgcn_perm(q1, q0, t[0]), __builtin_amdgcn_perm(q1, q0, t[1]), __builtin_amdgcn_perm(q1, q0, t[2])};
    }
    {
      const LAS char* fe = K + KC_R5 + 64;
      asm volatile(DENSE : "+{v[2:5]}"(b9), "+{v[6:9]}"(b7), "+{v[10:13]}"(bA), "+{v22}"(fe), "+{v26}"(yb)
                   : "{v23}"(kd2), "{v24}"(ks0), "{v25}"(uf16), "{v27}"(prg) : DENSE_CLOB, "memory");
    }
  } else {
  asm volatile("s_waitcnt lgkmcnt(0)" ::: "memory");
  __syncthreads();
  polls(0);
  half8 td[6];
  auto mid_td = [&]() {
    const LAS unsigned* t5 = (const LAS unsigned*)pa2;
#pragma unroll
    for (int e = 0; e < 3; e++) {
      td[e] = ld_toep<8>(t5 + 16 * (2 - e));
      td[3 + e] = ld_toep<8>(t5 + (KC_R3 - KC_R5) / 4 + 16 * (2 - e));
    }
  };
  if (wave == 3) {
    half8 fa[24], fb[24];
    f32x4 f[3] = {zero4, zero4, zero4};
    constexpr int PF = PFM;
#pragma unroll
    for (int i = 0; i < PF; i++) { fa[i] = ld_frag(pa + (i >> 3) * TABB + 64 * (i & 7)); fb[i] = ld_frag(pb + (i >> 3) * TABB + 64 * (i & 7)); }
    SCHED_FENCE();
    auto put = [&](int c) {
      const unsigned d0 = pkh(f[c][0], f[c][1]), d1 = pkh(f[c][2], f[c][3]);
      const unsigned w0 = __builtin_amdgcn_perm(d1, d0, ks0), w1 = __builtin_amdgcn_perm(d1, d0, ks1);
      *(LAS unsigned*)(kd + c * CH2) = w0;
      *(LAS unsigned*)(kd2 + c * CH2) = w1;
      if (c15 >= 8) {
        *(LAS u16*)(kd + c * CH2 + 190) = (u16)w0;
        *(LAS u16*)(kd + c * CH2 + 192) = (u16)(w0 >> 16);
        *(LAS u16*)(kd + c * CH2 + (KC_R7 - KC_R9) + 190) = (u16)w1;
        *(LAS u16*)(kd + c * CH2 + (KC_R7 - KC_R9) + 192) = (u16)(w1 >> 16);
      }
    };
    static_for<0, 24>([&](auto ic) {
      constexpr int i = decltype(ic)::value, n = i + PF;
      f[i >> 3] = MFMA16(fa[i], fb[i], f[i >> 3]);
      SCHED_FENCE();
      if constexpr (n < 24) { fa[n] = ld_frag(pa + (n >> 3) * TABB + 64 * (n & 7)); fb[n] = ld_frag(pb + (n >> 3) * TABB + 64 * (n & 7)); }
      if constexpr (i == 14) {
        put(0);
        asm volatile("" ::: "memory");
        mid_td();
      }
      if constexpr (i == 18) put(1);
      SCHED_FENCE();
    });
    put(2);
    __builtin_amdgcn_s_waitcnt(0x0F70);
  }
  SCHED_FENCE();
  LAS char* sbw;
  int two;
  if (wave >= 4) {
    sbw = L + ((wave >> 1) - 2) * YS + ((vt >> 1) & 3) * 320 + ((vt >> 3) & 15) * 16 + (vt & 1) * 8;
    two = sub == 3 ? 2 : 8;
    LAS char* mxw = L + ((vt >> 3) == (b & 63) ? SM_MX + (vt & 7) * 4 : SM_DUMMY + lane * 4);
    asm volatile("" : "+v"(sbw), "+v"(two), "+v"(mxw));
    f32x2 dsk[3], xf[2][3];
    LAS char* dst = L + SM_US + 2 * UPAD + 4 * vt;
#pragma unroll
    for (int c = 0; c < 3; c++) {
      dsk[c][0] = __uint_as_float(__builtin_amdgcn_perm(r_dsk[1], r_dsk[0], 0x01000c0cu + 0x02020000u * (c + (b & 1))));
      asm volatile("" : "+v"(dsk[c]), "+v"(dst));
    }
    asm volatile("s_waitcnt vmcnt(2)" : "+v"(v0), "+v"(v1));
    get(true);
#pragma unroll
    for (int c = 0; c < 3; c++) {
      xf[0][c] = unpk(v0[c]);
      xf[1][c] = unpk(v1[c]);
      asm volatile("" : "+v"(xf[0][c]), "+v"(xf[1][c]));
    }
    SCHED_FENCE();
    asm volatile("s_waitcnt vmcnt(0)" : "+v"(st0), "+v"(st1));
    get(false);
    f32x2 uf[2][3];
#pragma unroll
    for (int i = 0; i < 2; i++) {
      const u32x4& st = i ? st1 : st0;
      const f32x2 ri = {__uint_as_float(st[0]), __uint_as_float(st[1])};
      const f32x2 cc = -unpk(st[2]) * ri;
#pragma unroll
      for (int c = 0; c < 3; c++) {
        uf[i][c] = __builtin_elementwise_fma(xf[i][c], ri, cc);
        *(LAS unsigned*)(dst + i * 2048 + c * CH2) = pkc(uf[i][c]);
      }
    }
    SCHED_FENCE();
#pragma unroll
    for (int i = 0; i < 2; i++)
#pragma unroll
      for (int c = 0; c < 3; c++) {
        sb[i][c] = __builtin_elementwise_fma(uf[i][c], f32x2{dsk[c][0], dsk[c][0]}, xf[i][c]);
        asm volatile("" : "+v"(sb[i][c]));
      }
    *(LAS unsigned*)mxw = b < 64 ? st0[2] : st1[2];
  } else {
    asm volatile("" : "=v"(sbw), "=v"(two));
  }
  auto put_sb = [&]() {
#pragma unroll
    for (int c = 0; c < 6; c++) *(volatile LAS f32x2*)(sbw + c % 3 * YC + 2 * YT + c / 3 * 4 * YS) = sb[c / 3][c % 3];
    *(volatile LAS int*)prg = two;
  };
  if (sub < 3) __builtin_amdgcn_s_setprio(SPR);
  else __builtin_amdgcn_s_setprio(MPR & 3);
  __syncthreads();
  {
    if (sub < 3) {
      unsigned long long t0 = __builtin_amdgcn_s_memrealtime();
      if (wave == LSW && lane < 16) __builtin_amdgcn_raw_buffer_store_b64(u32x2{(unsigned)t0, tag}, rs, WS_CK + lane * 2048 + b * 8, 0, SC1);
      if (is_ln && sub == 2) {
        const u32x4 ones = {~0u, ~0u, ~0u, ~0u};
        const int o = (vt & 255) * 16;
#pragma unroll
        for (int i = 0; i < 3; i++) stg16(rs, WS_PART + b * 11520 + o + i * 3072, ones);
        if (o < 2304) stg16(rs, WS_PART + b * 11520 + o + 9216, ones);
#pragma unroll
        for (int i = 0; i < 2 * XSS; i += 3072)
          if (i + 3072 <= 2 * XSS || o < 2 * XSS - i) stg16(rs, WS_XS + b * (2 * XSS) + o + i, ones);
        if (b < 8 * XSS / 3072) stg16(rs, WS_XS + 2 * NLN * XSS + b * 3072 + o, ones);
      }
      SCHED_FENCE();
      __builtin_amdgcn_s_setprio(0);
      put_sb();
      if (wave == LSW) {
        __builtin_amdgcn_s_sleep(LSD);
        const int lag = last_start(rs, tag, lane, bid, (unsigned)t0, u32x4{0u, ~tag, 0u, 0u}, u32x4{0u, 0u, 0u, 0u});
        t0 += lag;
        *(volatile LAS unsigned long long*)(L + SM_PRG + 48) =
            t0 + (DLY_H - (b >= 96 ? 0 : (b >= HB5 ? HE5 : (b >= HB4 ? HE4 : HE3))));
        *(LAS int*)(L + FS_SEL) = (int)t0;
      }
    } else {
      if (wave != 3) {
        put_sb();
        mid_td();
      }
      f32x4 a2[6], w[4];
#pragma unroll
      for (int i = 0; i < 6; i++) a2[i] = zero4;
      half8 uw[12];
#pragma unroll
      for (int jj = 0; jj < 6; jj++) uw[jj] = ld_frag(uf16 - 48 + 256 * jj);
      SCHED_FENCE();
      w[0] = MFMA16(uw[0], bA, zero4); w[1] = MFMA16(uw[1], bA, zero4);
      w[2] = MFMA16(uw[0], bB, zero4); w[3] = MFMA16(uw[1], bB, zero4);
      half8 X5 = pack_win(w[0], w[1]), X3 = pack_win(w[2], w[3]);
      SCHED_FENCE();
#pragma unroll
      for (int jj = 0; jj < 6; jj++) {
        if (jj < 5) {
          w[0] = MFMA16(uw[2 * jj + 2], bA, zero4);
          if (jj < 3) uw[2 * jj + 6] = ld_frag(uf16 - 48 + 256 * (2 * jj + 6));
          SCHED_FENCE();
          w[1] = MFMA16(uw[2 * jj + 3], bA, zero4);
          if (jj < 3) uw[2 * jj + 7] = ld_frag(uf16 - 48 + 256 * (2 * jj + 7));
          SCHED_FENCE();
          w[2] = MFMA16(uw[2 * jj + 2], bB, zero4);
        }
        if (jj > 0) *(volatile LAS f32x4*)(yb + (jj - 1) * YS) = a2[jj - 1];
        SCHED_FENCE();
        if (jj < 5) w[3] = MFMA16(uw[2 * jj + 3], bB, zero4);
        if (jj > 0) *(volatile LAS int*)prg = 2 + jj;
        if (jj > 0 && jj < 5 && (MPR >> 4 * jj & 3) != (MPR >> 4 * (jj - 1) & 3)) set_prio(MPR >> 4 * jj & 3);
        SCHED_FENCE();
        u32x4 n5, n3;
#pragma unroll
        for (int q = 0; q < 6; q++) {
          const int e = q >> 1;
          if (jj + e < 6) a2[jj + e] = MFMA16((q & 1) ? X3 : X5, td[3 * (q & 1) + e], a2[jj + e]);
          if (jj < 5 && q < 4) {
            u32x4& nn = q < 2 ? n5 : n3;
            nn[2 * (q & 1)] = pkh(w[q][0], w[q][1]);
            nn[2 * (q & 1) + 1] = pkh(w[q][2], w[q][3]);
          }
          SCHED_FENCE();
        }
        if (jj < 5) {
          X5 = __builtin_bit_cast(half8, n5);
          X3 = __builtin_bit_cast(half8, n3);
        }
      }
      *(volatile LAS f32x4*)(yb + 5 * YS) = a2[5];
      *(volatile LAS int*)prg = 8;
    }
  }
  }
  u32x4 h0, h1;
  int h_asked = b / NLN, hoff = WS_ZP + b * (NCONV * 128) + (tid >> 3) * 256 + (tid & 7) * 16;
  asm volatile("" : "={v[6:9]}"(h0), "={v[2:5]}"(h1), "+v"(hoff));
  unsigned jobs = (unsigned)((wave < 4 ? JOBS0 : (wave < 8 ? JOBS1 : JOBS2)) >> 20 * (wave & 3)) & 0xfffffu;
  if (jobs) {
    set_prio(PPR);
    const LAS char* pp = L + SM_PRG + 4 * min(lane, 11);
    int seen = *(const volatile LAS int*)pp;
    const u32x4 isc = *(const LAS u32x4*)(L + SM_ISC);
    unsigned p, t, n = SPIN_CAP;
    const LAS char* yp = L + ((lane >> 1) & 3) * 320 + (lane >> 3) * 16 + (lane & 1) * 8;
    const int zo = WS_ZP + ((lane >> 3) * NCONV + b) * 128 + (lane & 7) * 16;
#define PR3(r, o)                                                                                       \
  "ds_read_b64 v[" #r ":" #r "+1], v10 offset:" #o "\n\tds_read_b64 v[" #r "+2:" #r "+3], v10 offset:" #o "+3840\n\t" \
  "ds_read_b64 v[" #r "+4:" #r "+5], v10 offset:" #o "+7680\n\t"
#define PF3(r)                                                                                                 \
  "v_fmac_f32 v16, v12, v[" #r "]\n\tv_fmac_f32 v17, v12, v[" #r "+1]\n\tv_fmac_f32 v18, v13, v[" #r "+2]\n\t" \
  "v_fmac_f32 v19, v13, v[" #r "+3]\n\tv_fmac_f32 v20, v14, v[" #r "+4]\n\tv_fmac_f32 v21, v14, v[" #r "+5]\n\t"
#define PASK(l)                                                                                                              \
  "ds_read_b64 v[34:35], %19\n\ts_setprio %16\n\ts_memrealtime %7\n\ts_cmp_lg_u32 %6, 0\n\ts_waitcnt lgkmcnt(0)\n\ts_cbranch_scc1 " #l "f\n\t"       \
  "v_cmp_ge_u64 vcc, %7, v[34:35]\n\ts_cbranch_vccz " #l "f\n\tbuffer_load_dwordx4 %4, %18, %12, 0 offen sc1\n\t"                  \
  "buffer_load_dwordx4 %5, %18, %12, 0 offen offset:128 sc1\n\ts_mov_b32 %6, 4\n" #l ":\n\t"
    unsigned long long now;
    asm volatile(
        "v_mov_b32 v11, %20\n\tv_mov_b32 v37, %13\n"
        "1:\n\ts_and_b32 %1, %0, 31\n\ts_sub_u32 %1, %1, 1\n\ts_lshr_b32 %2, %1, 1\n\ts_lshl_b32 vcc_lo, %1, 7\n\ts_mul_i32 vcc_hi, %2, 11520-256\n\t"
        "s_add_u32 vcc_lo, vcc_lo, vcc_hi\n\tv_add_u32 v10, vcc_lo, %8\n\ts_mul_i32 %1, %1, 196608\n"
        "2:\n\tv_cmp_gt_i32 vcc, %2, v11\n\ts_cbranch_vccz 3f\n\ts_sleep %14\n\tds_read_b32 v11, %10\n\ts_sub_u32 %3, %3, 1\n\t"
        "s_cmp_gt_i32 %3, 0\n\ts_waitcnt lgkmcnt(0)\n\ts_cbranch_scc1 2b\n"
        "3:\n\ts_setprio %17\n\tds_read_b32 v11, %10\n\t" PR3(16, 2560) PR3(22, 0) "s_cmp_lt_u32 %2, 2\n\ts_cbranch_scc1 4f\n\t" PR3(28, 1280) "\n"
        "4:\n\t" PASK(7) "v_cmp_ge_i32 vcc, %2, v11\n\ts_cbranch_vccz 5f\n\ts_cmp_lt_u32 %2, 2\n\ts_cbranch_scc1 8f\n\t"
        "s_and_b32 vcc_lo, vcc_lo, 0x777\n\ts_cbranch_scc0 9f\n"
        "8:\n\ts_sleep %15\n\ts_sub_u32 %3, %3, 1\n\ts_cmp_gt_i32 %3, 0\n\ts_cbranch_scc1 3b\n"
        "9:\n\t" PF3(22) "\n"
        "10:\n\ts_setprio %17\n\tds_read_b32 v11, %10\n\t" PR3(28, 1280) PASK(11) "v_cmp_ge_i32 vcc, %2, v11\n\ts_cbranch_vccz 12f\n\t"
        "s_sub_u32 %3, %3, 1\n\ts_cmp_gt_i32 %3, 0\n\ts_cbranch_scc1 10b\n\ts_branch 12f\n"
        "5:\n\t" PF3(22) "s_cmp_lt_u32 %2, 2\n\ts_cbranch_scc1 6f\n12:\n\t" PF3(28) "\n"
        "6:\n\tv_cvt_pk_bf16_f32 v34, v16, v17\n\tv_cvt_pk_bf16_f32 v35, v18, v19\n\tv_cvt_pk_bf16_f32 v36, v20, v21\n\t"
        "s_lshr_b32 %0, %0, 5\n\tbuffer_store_dwordx4 v[34:37], %9, %12, %1 offen sc1\n\ts_cbranch_scc1 1b\n\ts_nop 1"
        : "+s"(jobs), "=&s"(p), "=&s"(t), "+s"(n), "+{v[6:9]}"(h0), "+{v[2:5]}"(h1), "+s"(h_asked), "=&s"(now)
        : "v"(yp), "v"(zo), "v"(pp), "{v[12:15]}"(isc), "s"(rq), "s"(tag), "n"(PSL), "n"(PSE), "n"(PPR), "n"(WPR),
          "v"(hoff), "v"(L + SM_PRG + 48), "v"(seen)
        : "v10", "v11", "v16", "v17", "v18", "v19", "v20", "v21", "v22", "v23", "v24", "v25", "v26", "v27", "v28", "v29", "v30", "v31", "v32",
          "v33", "v34", "v35", "v36", "v37", "vcc", "scc", "memory");
    static_assert(YT == 1280 && 8 * NCONV * 128 == 196608, "constants above");
  }
  auto ask_h = [&](int tries, int mark) {
    unsigned long long due, now;
    asm volatile(
        "s_cmp_lg_u32 %2, 0\n\ts_cbranch_scc1 3f\n1:\n\tds_read_b64 %4, %6\n\ts_memrealtime %5\n\ts_waitcnt lgkmcnt(0)\n\t"
        "v_cmp_ge_u64 vcc, %5, %4\n\ts_cbranch_vccnz 2f\n\t"
        "s_sub_u32 %3, %3, 1\n\ts_cbranch_scc1 3f\n\ts_sleep 1\n\ts_cmp_lg_u32 %3, 0\n\ts_cbranch_scc1 1b\n2:\n\t"
        "buffer_load_dwordx4 %0, %7, %8, 0 offen sc1\n\tbuffer_load_dwordx4 %1, %7, %8, 0 offen offset:128 sc1\n\ts_mov_b32 %2, %9\n3:"
        : "+{v[6:9]}"(h0), "+{v[2:5]}"(h1), "+s"(h_asked), "+s"(tries), "=&v"(due), "=&s"(now)
        : "v"((unsigned)(size_t)L + SM_PRG + 48), "v"(hoff), "s"(rq), "s"(mark) : "vcc", "scc", "memory");
  };
  __builtin_amdgcn_s_setprio(0);
  ask_h(0, 1);
  ftag = tag;
  if (b < LATE0) {
  LAS unsigned* const selp = (LAS unsigned*)(L + SM_ROW + 256);
  unsigned t_key = 0;
  auto select_rows = [&]() {
    const SelMasks sm(lane);
    const unsigned due = __builtin_amdgcn_readfirstlane(*(const LAS int*)(L + FS_SEL));
    while ((int)((unsigned)__builtin_amdgcn_s_memrealtime() - due) < DLY_L) __builtin_amdgcn_s_sleep(1);
    select8<false>(rs, tag, lane, sm, ws, bid, t_key, [&](unsigned k, auto step) {
      if (step == 2) selp[key_slot(lane)] = k;
    });
  };
  if (is_ln) {
    f32x2 wf[6], hv[6];
    const unsigned mxp = *(const LAS unsigned*)(L + SM_MX + (tid & 7) * 4);
#pragma unroll
    for (int i = 0; i < 6; i++) {
      const float t = bflo(*(const LAS u16*)(L + SM_WR + (tid >> 3) * 12 + i * 2));
      wf[i] = f32x2{t, t};
    }
    float wsum = 0.f, mxr = bflo(*(const LAS u16*)(L + SM_MX + (lane >> 2) * 2));
    unsigned one2 = 0x3f803f80u, sel0 = 0x00003f80u, sel1 = 0x3f800000u;
    asm volatile("" : "+s"(one2), "+s"(sel0), "+s"(sel1));
    if (wave == 0) {
#pragma unroll
      for (int k = 0; k < 9; k++) wsum = dot2(*(const LAS unsigned*)(L + SM_WR + (hl + 32 * k) * 4), one2, wsum);
      wsum = red32_sum(wsum);
      __builtin_amdgcn_s_setprio(2);
    }
#pragma unroll
    for (int i = 0; i < 6; i++) {
      hv[i] = -unpk(mxp);
      asm volatile("" : "+v"(hv[i]), "+v"(wf[i]));
    }
    LAS char* redw = L + SM_RED + ((lane & 7) * 8 + (lane >> 5) * 4 + ((lane >> 4) & 1)) * 48 + wave * 8 + (wave >= 6 ? 48 : 0);
    const LAS char* redr = L + SM_RED + lane * 48;
    unsigned kidx = (unsigned)(SEQ - 1 - 16 * b - (lane >> 2)) << 2;
    int kro = WS_KR + (lane & (8 * NKS - 1)) * 2048 + b * 16, xso = WS_XS + b * (2 * XSS) + (tid >> 3) * 12;
    asm volatile("" : "+v"(wsum), "+v"(mxr), "+v"(redw), "+v"(redr), "+v"(kidx), "+v"(kro), "+v"(xso));
    ask_h(SPIN_CAP, 1);
    {
      int n = SPIN_CAP;
      unsigned long long m;
      asm volatile(
          "s_cmp_gt_u32 %4, 3\n\ts_waitcnt vmcnt(1)\n\ts_cbranch_scc1 2f\n1:\n\ts_waitcnt vmcnt(0)\n2:\n\t"
          "v_cmp_ne_u32 vcc, %5, v9\n\tv_cmp_ne_u32 %3, %5, v5\n\ts_or_b64 vcc, vcc, %3\n\ts_cbranch_scc0 3f\n\t"
          "s_sleep 4\n\ts_mov_b64 exec, vcc\n\tbuffer_load_dwordx4 %0, %6, %7, 0 offen sc1\n\t"
          "buffer_load_dwordx4 %1, %6, %7, 0 offen offset:128 sc1\n\ts_mov_b64 exec, -1\n\ts_sub_u32 %2, %2, 1\n\ts_cbranch_scc0 1b\n\t"
          "s_waitcnt vmcnt(0)\n3:"
          : "+{v[6:9]}"(h0), "+{v[2:5]}"(h1), "+s"(n), "=&s"(m) : "s"(h_asked), "s"(tag), "v"(hoff), "s"(rq) : "vcc", "scc", "memory");
      v0 = h0;
      v1 = h1;
    }
    {
      f32x2 ss, qq, pp;
#pragma unroll
      for (int i = 0; i < 6; i++) {
        const unsigned t = i < 3 ? v0[i] : v1[i - 3];
        hv[i] = f32x2{dot2(t, sel0, hv[i][0]), dot2(t, sel1, hv[i][1])};
        ss = i ? ss + hv[i] : hv[i];
        qq = i ? __builtin_elementwise_fma(hv[i], hv[i], qq) : hv[i] * hv[i];
        pp = i ? __builtin_elementwise_fma(hv[i], wf[i], pp) : hv[i] * wf[i];
      }
      float s0 = ss[0], s1 = ss[1], q0 = qq[0], q1 = qq[1], p0 = pp[0], p1 = pp[1];
      asm volatile(
          "s_nop 1\n\tv_permlane32_swap_b32 %0, %1\n\tv_permlane32_swap_b32 %2, %3\n\tv_permlane32_swap_b32 %4, %5\n\t"
          "v_add_f32 %0, %0, %1\n\tv_add_f32 %2, %2, %3\n\tv_add_f32 %4, %4, %5\n\ts_nop 0\n\t"
          "v_permlane16_swap_b32 %0, %2\n\tv_add_f32_dpp %1, %4, %4 row_ror:8 row_mask:0xf bank_mask:0xf\n\t"
          "v_add_f32 %2, %0, %2\n\ts_nop 1\n\tv_add_f32_dpp %0, %2, %2 row_ror:8 row_mask:0xf bank_mask:0xf"
          : "+v"(s0), "+v"(s1), "+v"(q0), "+v"(q1), "+v"(p0), "+v"(p1));
      *(LAS f32x2*)redw = f32x2{s0, s1};
    }
    __syncthreads();
    if (wave == 0) {
      unsigned long long clk;
      asm volatile("s_memrealtime %0" : "=s"(clk) : : "memory");
      const volatile LAS f32x4* rp = (const volatile LAS f32x4*)redr;
      const f32x4 t0 = rp[0], t1 = rp[1], t2 = rp[2];
      const f32x2 t = ((f32x2{t0[0], t0[1]} + f32x2{t0[2], t0[3]}) + (f32x2{t1[0], t1[1]} + f32x2{t1[2], t1[3]})) + (f32x2{t2[0], t2[1]} + f32x2{t2[2], t2[3]});
      float a = t[0], p = t[1];
      asm volatile("s_waitcnt lgkmcnt(0)" : "+v"(a), "+v"(p), "+s"(clk));
      const unsigned now = (unsigned)clk;
      a += dppf<0x4E>(a);
      p += dppf<0x4E>(p);
      p += dppf<0xB1>(p);
      const float mv = dppf<0x00>(a) * (1.0f / DM);
      const float rinv = ln_rinv(dppf<0x55>(a), mv);
      const float dl = rbf(mxr + mv) - mxr;
      const unsigned rb = pkbf(0.f, fmaf(-dl, wsum, p) * rinv);
      const unsigned key = ((rb ^ (unsigned)(((int)rb >> 31) | (int)0x80000000)) & 0xffff0000u) | kidx;
      unsigned k1 = max(key, dppu<0x141>(key)), k2 = min(key, dppu<0x141>(key)), k3;
      {
        const unsigned lo = min(k1, dppu<0x140>(k1)), hi = max(k2, dppu<0x140>(k2));
        k1 = max(k1, dppu<0x140>(k1));
        k2 = max(lo, hi);
        k3 = min(lo, hi);
      }
      {
        const unsigned lo = min(k1, dppu<0x142>(k1)), hi = max(k2, dppu<0x142>(k2)), w = max(k3, dppu<0x142>(k3));
        k1 = max(k1, dppu<0x142>(k1));
        k2 = max(lo, hi);
        k3 = max(min(lo, hi), w);
      }
      {
        const unsigned lo = min(k1, dppu<0x143>(k1)), hi = max(k2, dppu<0x143>(k2)), w = max(k3, dppu<0x143>(k3));
        k1 = max(k1, dppu<0x143>(k1));
        k2 = max(lo, hi);
        k3 = max(min(lo, hi), w);
      }
      if (lane >= 64 - 8 * NKS) stg16(rs, kro, u32x4{k1, k2 | 1u, __builtin_amdgcn_perm(k3, now, 0x07060100u), tag});
      SCHED_FENCE();
      const unsigned second = (unsigned)__builtin_amdgcn_readlane((int)k2, 63);
      if ((lane & 3) == 0) {
        LAS float* ri = (LAS float*)(L + SM_ROW + (lane >> 3) * 16 + (lane & 4));
        ri[0] = -dl * rinv;
        ri[2] = rinv;
        const unsigned fk = key | (key > second ? 0u : (key == second ? 1u : 2u));
        *(LAS unsigned*)(L + SM_ROW + 128 + lane) = fk;
        __builtin_amdgcn_raw_buffer_store_b64(u32x2{fk, tag}, rs, WS_KF + b * 128 + lane * 2, 0, SC1);
      }
      __builtin_amdgcn_s_setprio(0);
    }
    __syncthreads();
    {
      const f32x4 ri = *(const LAS f32x4*)(L + SM_ROW + (tid & 7) * 16);
      const u32x2 kk = *(const LAS u32x2*)(L + SM_ROW + 128 + (tid & 7) * 8);
      const f32x2 c2 = {ri[0], ri[1]}, r2 = {ri[2], ri[3]};
      unsigned wp[6];
#pragma unroll
      for (int i = 0; i < 6; i++) {
        const f32x2 w2 = __builtin_elementwise_fma(hv[i], r2, c2);
        wp[i] = pkbf(w2[0], w2[1]);
      }
#pragma unroll
      for (int i = 0; i < 2; i++)
        if (!(kk[i] & 2)) {
          u32x3 o;
#pragma unroll
          for (int k = 0; k < 3; k++) o[k] = min(__builtin_amdgcn_perm(wp[2 * k + 1], wp[2 * k], i ? 0x07060302u : 0x05040100u), 0xfffffffeu);
          __builtin_amdgcn_raw_buffer_store_b96(o, rs, xso + (int)(kk[i] & 1) * XSS + DM * 2, 0, SC1);
        }
      SCHED_FENCE();
      LAS char* dst = L + SM_T2 + (tid & 7) * T2S + (tid >> 3) * 48;
#pragma unroll
      for (int i = 0; i < 6; i += 2) *(LAS u32x4*)(dst + 8 * i) = u32x4{i < 3 ? v0[i] : v1[i - 3], wp[i], i < 2 ? v0[i + 1] : v1[i - 2], wp[i + 1]};
    }
    __syncthreads();
    const int pr = tid / 72, ck = tid - pr * 72;
    u32x4 hq[2], wq[2];
    u32x2 key = {2u, 2u};
    auto put_row = [&](int i, int slot) {
      const int o = key_addr(key[i], slot) + ck * 16;
      const u32x4 cap = {0xfffffffeu, 0xfffffffeu, 0xfffffffeu, 0xfffffffeu};
      if (key[i] & 2) stg16(rs, o + DM * 2, __builtin_elementwise_min(wq[i], cap));
      stg16(rs, o, __builtin_elementwise_min(hq[i], cap));
    };
    auto put_out = [&](int i) {
      u32x4 o;
#pragma unroll
      for (int k = 0; k < 4; k++) {
        const f32x2 o2 = unpk(hq[i][k]) + unpk(wq[i][k]);
        o[k] = pkbf(o2[0], o2[1]);
      }
      __builtin_amdgcn_raw_buffer_store_b128(o, ro, (16 * b + 2 * pr + i) * (DM * 2) + ck * 16, 0, SC1);
    };
    if (wave < 9) {
      key = *(const LAS u32x2*)(L + SM_ROW + 128 + pr * 8);
#pragma unroll
      for (int k = 0; k < 4; k++) {
        const u32x4 t = *(const LAS u32x4*)(L + SM_T2 + pr * T2S + ck * 64 + k * 16);
        hq[0][k] = __builtin_amdgcn_perm(t[2], t[0], 0x05040100u);
        hq[1][k] = __builtin_amdgcn_perm(t[2], t[0], 0x07060302u);
        wq[0][k] = __builtin_amdgcn_perm(t[3], t[1], 0x05040100u);
        wq[1][k] = __builtin_amdgcn_perm(t[3], t[1], 0x07060302u);
      }
#pragma unroll
      for (int i = 0; i < 2; i++) {
        if (!(key[i] & 2)) put_row(i, 0);
        else put_out(i);
      }
    } else if (wave == 10) {
      select_rows();
    }
    __syncthreads();
    if (wave < 9) {
#pragma unroll
      for (int i = 0; i < 2; i++) {
        int slot = 8;
#pragma unroll
        for (int r = 0; r < 8; r++) slot = selp[r] == key[i] ? r : slot;
        if (slot < 8) {
          if (key[i] & 2) {
            DRAIN();
            put_row(i, slot);
          }
        } else if (!(key[i] & 2)) {
          put_out(i);
        }
      }
    }
  } else {
    __syncthreads();
    if (wave == 10) select_rows();
  }
  if (is_ln && b >= RED1) {
    __builtin_amdgcn_endpgm();
  }
  {
    const int co = 8 * rb + 4 * (lane & 1);
    unsigned hv = 0u, wv = 0u, oo = 0u;
    float sg = 0.f;
    LAS float* red = (LAS float*)(L + SM_RED);
    LAS char* rw = (LAS char*)red + (wave < 10 ? wave * 128 + (lane & 7) * 16 + (lane >> 4) * 4 : lane * 8);
    int part_off = WS_PART + (tid >> 3) * (8 * DM * 4) + rb * 128 + (tid & 7) * 16;
    asm volatile("" : "+v"(rw), "+v"(part_off));
    f32x2 hf = {0.f, 0.f}, wf = {0.f, 0.f};
    int o = 0;
    auto get_rows = [&]() {
      asm volatile("buffer_load_dword %0, %2, %3, 0 offen offset:1152 sc1\n\tbuffer_load_dword %1, %2, %3, 0 offen sc1"
                   : "+v"(wv), "+v"(hv) : "v"(o), "s"(rq) : "memory");
    };
    if (wave == 10) {
      const int slot = (lane >> 1) & 7;
      const unsigned key = *(volatile LAS unsigned*)(selp + slot);
      o = key_addr(key, slot) + co;
      get_rows();
      const unsigned sb = key ^ ((int)key < 0 ? 0x80000000u : 0xffffffffu);
      sg = rbf(__builtin_amdgcn_rcpf(1.0f + __expf(-bfhi(sb))));
      oo = (unsigned)(key_row(key) * (DM * 2) + co);
      asm volatile("" : "+v"(sg), "+v"(oo));
      const int look = __builtin_amdgcn_readfirstlane(*(const LAS int*)(L + FS_SEL)) + DLY_K - (int)t_key;
      const int due = min(max(look + DLY_F, look < PRM ? DLY_C + PEN : DLY_C), 1000);
      while ((int)((unsigned)__builtin_amdgcn_s_memrealtime() - t_key) < due) __builtin_amdgcn_s_sleep(1);
    }
    __syncthreads();
    if (wave < 10) {
      u32x4 v, w;
      asm volatile("buffer_load_dwordx4 %0, %2, %3, 0 offen sc1\n\ts_sleep %4\n\tbuffer_load_dwordx4 %1, %2, %3, 0 offen sc1"
                   : "=&v"(v), "=&v"(w) : "v"(part_off), "s"(rq), "n"(PPS) : "memory");
      u32x4 got = {~0u, 0u, 0u, 0u};
      _Pragma("unroll 1") for (int spin = 0; spin < SPIN_CAP; spin++) {
        asm volatile("s_waitcnt vmcnt(1)" : "+v"(v));
        if (got[0] == ~0u) got = v;
        if (__all(got[0] != ~0u)) break;
        if (got[0] == ~0u) asm volatile("buffer_load_dwordx4 %0, %1, %2, 0 offen sc1" : "+v"(v) : "v"(part_off), "s"(rq) : "memory");
        asm volatile("s_waitcnt vmcnt(1)" : "+v"(w));
        if (got[0] == ~0u) got = w;
        if (__all(got[0] != ~0u)) break;
        if (got[0] == ~0u) asm volatile("buffer_load_dwordx4 %0, %1, %2, 0 offen sc1" : "+v"(w) : "v"(part_off), "s"(rq) : "memory");
      }
#define v got
      auto r0 = __builtin_amdgcn_permlane32_swap(v[0], v[2], false, false);
      auto r1 = __builtin_amdgcn_permlane32_swap(v[1], v[3], false, false);
      const float s0 = __uint_as_float(r0[0]) + __uint_as_float(r0[1]), s1 = __uint_as_float(r1[0]) + __uint_as_float(r1[1]);
      auto r2 = __builtin_amdgcn_permlane16_swap(__float_as_uint(s0), __float_as_uint(s1), false, false);
      float a = __uint_as_float(r2[0]) + __uint_as_float(r2[1]);
      a += dppf<0x128>(a);
      *(LAS float*)rw = a;
#undef v
      asm volatile("" : : "v"(v), "v"(w));
    } else if (wave == 10) {
      _Pragma("unroll 1") for (int spin = 0; spin < SPIN_CAP; spin++) {
        asm volatile("s_waitcnt vmcnt(0)" : "+v"(wv), "+v"(hv));
        if (__all(wv != ~0u && hv != ~0u)) break;
        __builtin_amdgcn_s_sleep(4);
        get_rows();
      }
      asm volatile("s_waitcnt vmcnt(0)" : "+v"(wv), "+v"(hv));
      hf = unpk(hv);
      wf = unpk(wv);
      asm volatile("" : "+v"(hf), "+v"(wf));
    }
    __syncthreads();
    if (wave == 10 && lane < 16) {
      f32x2 p[10];
#pragma unroll
      for (int w = 0; w < 10; w++) p[w] = *(const volatile LAS f32x2*)(rw + w * 128);
      const f32x2 y = (((p[0] + p[1]) + (p[2] + p[3])) + (p[8] + p[9])) + ((p[4] + p[5]) + (p[6] + p[7]));
      const f32x2 y2 = rbf2(rbf2(y) * sg);
      const f32x2 o2 = hf + rbf2(wf + y2);
      __builtin_amdgcn_raw_buffer_store_b32(pkbf(o2[0], o2[1]), ro, oo, 0, SC1);
      if (b == 0 && lane == 0) __builtin_amdgcn_raw_buffer_store_b32(tag, rs, WS_EPOCH, 0, SC1);
    }
  }
  asm volatile("s_endpgm" ::: "memory");
  __builtin_amdgcn_s_waitcnt(0);
  }
  }
  ffn_role(argp, ftag, L);
}

}

extern "C" void kernel_launch(void* const* d_in, const int* in_sizes, int n_in, void* d_out, int out_size,
                              void* d_ws, size_t ws_size, hipStream_t stream) {
  k_all<<<NCONV + NFFN, NTHR, 0, stream>>>((const u16*)d_in[0], (const u16*)d_in[1], (const u16*)d_in[2], (const u16*)d_in[3],
                                             (const u16*)d_in[4], (const u16*)d_in[5], (char*)d_ws, (const u16*)d_in[6],
                                             (const u16*)d_in[8], (const u16*)d_in[9], (const u16*)d_in[10], (const u16*)d_in[11],
                                             (u16*)d_out);
}
```
